```python
import torch

def _softmax(x, axis=-1):
    return torch.softmax(x, dim=axis)

def _gelu(x):
    return torch.nn.functional.gelu(x, approximate='tanh')

def _silu(x):
    return torch.nn.functional.silu(x)

def _sigmoid(x):
    return torch.sigmoid(x)

def _layernorm(x, axis=-1, eps=1e-05):
    m = x.mean(dim=axis, keepdim=True)
    v = x.var(dim=axis, keepdim=True, unbiased=False)
    return (x - m) / torch.sqrt(v + eps)

def _scatter_add(tgt, idx, src, axis=0):
    return tgt.clone().index_add_(axis, idx, src)

def _segment_sum(src, seg, n):
    out = torch.zeros((n,) + src.shape[1:], dtype=src.dtype)
    return out.index_add_(0, seg, src)

def _pad(x, pw):
    flat = tuple((v for pair in reversed(pw) for v in pair))
    return torch.nn.functional.pad(x, flat)
kernel_name = 'adaLN_cond__sliding_block_cached__geglu_ffn__prompt'

def setup_inputs():

    _w = lambda a: (lambda t: t.to(torch.bfloat16) if t.is_floating_point() else t)(torch.as_tensor(a))

    def _A_setup():
        B, Sq, D = (1, 2048, 512)
        Hq, Hkv, Sk, Dh = (32, 8, 2048, 128)
        assert Hq % Hkv == 0, (Hq, Hkv)
        return {'x': _w((lambda *s: torch.randn(*s, dtype=torch.bfloat16) * 0.1)(B, Sq, D)), 'k_cache': _w((lambda *s: torch.randn(*s, dtype=torch.bfloat16) * 0.8)(B, Hkv, Sk, Dh)), 'v_cache': _w((lambda *s: torch.randn(*s, dtype=torch.bfloat16) * 1.6)(B, Hkv, Sk, Dh)), 'wq': _w((lambda *s: torch.randn(*s, dtype=torch.bfloat16) * 0.8)(D, Hq * Dh) * D ** (-0.5)), 'wo': _w((lambda *s: torch.randn(*s, dtype=torch.bfloat16) * 1.6)(Hq * Dh, D) * (Hq * Dh) ** (-0.5))}
    _A_in = _A_setup()

    def _B_setup():
        B, S, D, F = (1, 2048, 512, 1792)
        return {'x': _w((lambda *s: torch.randn(*s, dtype=torch.bfloat16) * 0.1)(B, S, D)), 'w_gate': _w((lambda *s: torch.randn(*s, dtype=torch.bfloat16) * 0.2)(D, F)), 'w_up': _w((lambda *s: torch.randn(*s, dtype=torch.bfloat16) * 0.2)(D, F)), 'w_down': _w((lambda *s: torch.randn(*s, dtype=torch.bfloat16) * 0.2)(F, D))}
    _B_in = _B_setup()
    return {'A_x': _A_in['x'], 'A_k_cache': _A_in['k_cache'], 'A_v_cache': _A_in['v_cache'], 'A_wq': _A_in['wq'], 'A_wo': _A_in['wo'], 'B_x': _B_in['x'], 'B_w_gate': _B_in['w_gate'], 'B_w_up': _B_in['w_up'], 'B_w_down': _B_in['w_down'], 'c': _w((lambda *s: torch.randn(*s, dtype=torch.bfloat16) * 0.8)(1, 512) * 8.0), 'w_mod': _w((lambda *s: torch.randn(*s, dtype=torch.bfloat16) * 0.8)(512, 6 * 512) * 512 ** (-0.5) * 8.0)}

def reference(A_x, A_k_cache, A_v_cache, A_wq, A_wo, B_x, B_w_gate, B_w_up, B_w_down, c, w_mod):

    def _A_ref(x, k_cache, v_cache, wq, wo):
        B, Sq, D = x.shape
        Hq, Dh = (32, 128)
        Hkv, Sk = (k_cache.shape[1], k_cache.shape[2])
        W = min(4096, max(256, Sk // 4))
        rep = Hq // Hkv
        q = (lambda x, *d: x.permute(*d))((x @ wq).reshape(B, Sq, Hq, Dh), 0, 2, 1, 3)
        k = (lambda x, n, axis: torch.repeat_interleave(x, n, dim=axis))(k_cache[:, :, Sk - W:, :], rep, axis=1)
        v = (lambda x, n, axis: torch.repeat_interleave(x, n, dim=axis))(v_cache[:, :, Sk - W:, :], rep, axis=1)
        scores = torch.einsum('bhqd,bhkd->bhqk', q, k) * Dh ** (-0.5)
        p = _softmax(scores, axis=-1)
        o = torch.einsum('bhqk,bhkd->bhqd', p, v)
        return (lambda x, *d: x.permute(*d))(o, 0, 2, 1, 3).reshape(B, Sq, Hq * Dh) @ wo

    def _B_ref(x, w_gate, w_up, w_down):
        return _gelu(x @ w_gate) * (x @ w_up) @ w_down
    x = A_x
    mod = (_silu(c) @ w_mod).reshape(c.shape[0], 6, 1, -1)
    sa, ca, ga, sb, cb, gb = (mod[:, 0].clone(), mod[:, 1].clone(), mod[:, 2].clone(), mod[:, 3].clone(), mod[:, 4].clone(), mod[:, 5].clone())
    h = _layernorm(x, axis=-1) * (1 + ca) + sa
    x = x + ga * _A_ref(h, A_k_cache, A_v_cache, A_wq, A_wo)
    h = _layernorm(x, axis=-1) * (1 + cb) + sb
    x = x + gb * _B_ref(h, B_w_gate, B_w_up, B_w_down)
    return x
```

```cpp
#include <hip/hip_runtime.h>
#include <hip/hip_bf16.h>
typedef unsigned short u16;
__device__ __forceinline__ float b2f(u16 v){ return __uint_as_float(((unsigned)v)<<16); }
__device__ __forceinline__ u16 f2b(float f){ unsigned u=__float_as_uint(f); return (u16)((u+0x7FFF+((u>>16)&1))>>16); }
#define RND 0
__device__ __forceinline__ float rb(float f){ return RND ? b2f(f2b(f)) : f; }

__global__ void gemm_k(const float* A, int lda, const u16* B, int ldb, float* C, int ldc, int M, int N, int K){
  __shared__ float As[16][17], Bs[16][17];
  int tx=threadIdx.x&15, ty=threadIdx.x>>4;
  int row=blockIdx.y*16+ty, col=blockIdx.x*16+tx;
  float acc=0;
  for(int k0=0;k0<K;k0+=16){
    As[ty][tx]=A[(long)row*lda+k0+tx];
    Bs[ty][tx]=b2f(B[(long)(k0+ty)*ldb+col]);
    __syncthreads();
    for(int k=0;k<16;k++) acc+=As[ty][k]*Bs[k][tx];
    __syncthreads();
  }
  C[(long)row*ldc+col]=rb(acc);
}
__global__ void mod_k(const u16* c, const u16* w, float* mod){
  int n=blockIdx.x*blockDim.x+threadIdx.x;
  float acc=0;
  for(int k=0;k<512;k++){ float x=b2f(c[k]); float s=rb(x/(1.f+expf(-x))); acc+=s*b2f(w[k*3072+n]); }
  mod[n]=rb(acc);
}
__global__ void ln_k(const float* x, const float* sh, const float* sc, float* h){
  int r=blockIdx.x; __shared__ float red[512];
  int t=threadIdx.x; float v=x[r*512+t];
  red[t]=v; __syncthreads();
  for(int s=256;s>0;s>>=1){ if(t<s) red[t]+=red[t+s]; __syncthreads(); }
  float mf=red[0]/512.f; __syncthreads();
  float d=v-mf; red[t]=d*d; __syncthreads();
  for(int s=256;s>0;s>>=1){ if(t<s) red[t]+=red[t+s]; __syncthreads(); }
  float var=rb(red[0]/512.f); float m=rb(mf);
  float num=rb(v-m); float den=rb(sqrtf(rb(var+1e-5f)));
  float ln=rb(num/den);
  float o=rb(rb(ln*rb(1.f+sc[t]))+sh[t]);
  h[r*512+t]=o;
}
__global__ void cvt_k(const u16* a, float* b, int n){ int i=blockIdx.x*blockDim.x+threadIdx.x; if(i<n) b[i]=b2f(a[i]); }
__global__ void attn_k(const float* q, const u16* K, const u16* V, float* o){
  int r=blockIdx.x, h=blockIdx.y, g=h>>2; int t=threadIdx.x;
  __shared__ float qs[128]; __shared__ float p[512]; __shared__ float red[512];
  if(t<128) qs[t]=q[(long)r*4096+h*128+t];
  __syncthreads();
  const u16* kr=K+((long)g*2048+1536+t)*128; float s=0;
  for(int d=0;d<128;d++) s+=qs[d]*b2f(kr[d]);
  s=rb(rb(s)*(RND? b2f(f2b(0.08838834764831845f)) : 0.08838834764831845f));
  red[t]=s; __syncthreads();
  for(int st=256;st>0;st>>=1){ if(t<st) red[t]=fmaxf(red[t],red[t+st]); __syncthreads(); }
  float m=red[0]; __syncthreads();
  float e=expf(s-m); red[t]=e; __syncthreads();
  for(int st=256;st>0;st>>=1){ if(t<st) red[t]+=red[t+st]; __syncthreads(); }
  p[t]=rb(e/red[0]); __syncthreads();
  if(t<128){ float a=0; const u16* vr=V+((long)g*2048+1536)*128+t; for(int k=0;k<512;k++) a+=p[k]*b2f(vr[k*128]); o[(long)r*4096+h*128+t]=rb(a); }
}
__global__ void res_k(const float* x, const float* g, const float* a, float* o, u16* ob, int n){
  int i=blockIdx.x*blockDim.x+threadIdx.x; float v=rb(x[i]+rb(g[i&511]*a[i])); o[i]=v; if(ob) ob[i]=f2b(v);
}
__global__ void act_k(const float* g, const float* u, float* o, int n){
  int i=blockIdx.x*blockDim.x+threadIdx.x; float x=g[i];
  float ge=rb(0.5f*x*(1.f+tanhf(0.7978845608028654f*(x+0.044715f*x*x*x))));
  o[i]=rb(ge*u[i]);
}
extern "C" void kernel_launch(void* const* d_in, const int* in_sizes, int n_in, void* d_out, int out_size, void* d_ws, size_t ws_size, hipStream_t stream){
  const u16 *x=(const u16*)d_in[0],*K=(const u16*)d_in[1],*V=(const u16*)d_in[2],*wq=(const u16*)d_in[3],*wo=(const u16*)d_in[4];
  const u16 *wg=(const u16*)d_in[6],*wu=(const u16*)d_in[7],*wd=(const u16*)d_in[8],*c=(const u16*)d_in[9],*wm=(const u16*)d_in[10];
  float* ws=(float*)d_ws;
  float* mod=ws; float* xf=mod+4096; float* h=xf+2048*512; float* q=h+2048*512; float* o=q+2048*4096; float* at=o+2048*4096; float* x1=at+2048*512;
  float* gt=x1+2048*512; float* up=gt+2048*1792; float* ac=up+2048*1792; float* dn=ac+2048*1792; float* fin=dn+2048*512;
  mod_k<<<12,256,0,stream>>>(c,wm,mod);
  cvt_k<<<4096,256,0,stream>>>(x,xf,2048*512);
  ln_k<<<2048,512,0,stream>>>(xf,mod,mod+512,h);
  gemm_k<<<dim3(256,128),256,0,stream>>>(h,512,wq,4096,q,4096,2048,4096,512);
  attn_k<<<dim3(2048,32),512,0,stream>>>(q,K,V,o);
  gemm_k<<<dim3(32,128),256,0,stream>>>(o,4096,wo,512,at,512,2048,512,4096);
  res_k<<<4096,256,0,stream>>>(xf,mod+1024,at,x1,nullptr,2048*512);
  ln_k<<<2048,512,0,stream>>>(x1,mod+1536,mod+2048,h);
  gemm_k<<<dim3(112,128),256,0,stream>>>(h,512,wg,1792,gt,1792,2048,1792,512);
  gemm_k<<<dim3(112,128),256,0,stream>>>(h,512,wu,1792,up,1792,2048,1792,512);
  act_k<<<2048*1792/256,256,0,stream>>>(gt,up,ac,2048*1792);
  gemm_k<<<dim3(32,128),256,0,stream>>>(ac,1792,wd,512,dn,512,2048,512,1792);
  res_k<<<4096,256,0,stream>>>(x1,mod+2560,dn,fin,(u16*)d_out,2048*512);
}
```

```cpp
#include <hip/hip_runtime.h>
__global__ __launch_bounds__(256,1) void main_kernel(const void* x,const void* k,const void* v,const void* wq,const void* wo,const void* wg,const void* wu,const void* wd,void* out,void* ws,const void* c,const void* wm){
  asm volatile(
"s_load_dwordx16 s[44:59], s[4:5], 0x0\n"
"s_load_dwordx8 s[60:67], s[4:5], 0x40\n"
"v_lshrrev_b32 v16, 6, v0\n"
"v_and_b32 v22, 63, v0\n"
"v_readfirstlane_b32 s7, v16\n"
"v_lshlrev_b32 v1, 4, v22\n"
"v_and_b32 v20, 15, v22\n"
"v_lshrrev_b32 v21, 4, v22\n"
"s_lshr_b32 s11, s6, 3\n"
"s_lshl_b32 s11, s11, 12\n"
"v_and_b32 v17, 0xc0, v0\n"
"v_lshlrev_b32 v17, 4, v17\n"
"s_getpc_b64 s[68:69]\n"
"L_pc%=:\n"
"v_add3_u32 v17, v17, s11, v1\n"
"v_min_u32 v17, L_end%=-L_pc%=-16, v17\n"
"global_load_dwordx4 a[24:27], v17, s[68:69]\n"
"s_lshr_b32 s11, s6, 4\n"
"s_lshl_b32 s11, s11, 3\n"
"s_and_b32 s12, s6, 7\n"
"s_or_b32 s77, s11, s12\n"
"s_sub_u32 s11, s77, 64\n"
"s_and_b32 s12, s11, 31\n"
"s_add_u32 s12, s12, 16\n"
"s_lshr_b32 s13, s11, 5\n"
"s_and_b32 s14, s77, 15\n"
"s_lshr_b32 s15, s77, 4\n"
"s_cmp_lt_u32 s77, 64\n"
"s_cselect_b32 s72, s14, s12\n"
"s_cselect_b32 s73, s15, s13\n"
"s_cselect_b32 s13, 7, 8\n"
"s_lshl_b32 s78, s73, s13\n"
"v_lshrrev_b32 v16, 3, v0\n"
"s_mov_b32 s11, 0x1800\n"
"v_and_b32 v17, 7, v0\n"
"v_lshlrev_b32 v17, 4, v17\n"
"v_mad_u32_u24 v17, v16, s11, v17\n"
"v_lshlrev_b32 v16, 1, v16\n"
"s_and_b32 s11, s6, 7\n"
"s_lshr_b32 s12, s6, 3\n"
"s_and_b32 s13, s11, 7\n"
"s_lshl_b32 s13, s13, 0\n"
"s_and_b32 s14, s12, 0\n"
"s_add_u32 s8, s13, s14\n"
"s_lshr_b32 s13, s11, 3\n"
"s_lshl_b32 s13, s13, 5\n"
"s_lshr_b32 s14, s12, 0\n"
"s_add_u32 s9, s13, s14\n"
"s_and_b32 s82, s14, 15\n"
"s_mul_i32 s11, s78, 0x1800\n"
"s_lshl_b32 s12, s72, 7\n"
"s_add_u32 s11, s11, s12\n"
"s_lshl_b32 s12, s78, 1\n"
"v_mov_b32 v126, v17\n"
"v_mov_b32 v127, v16\n"
"s_mov_b32 s75, s11\n"
"s_mov_b32 s76, s12\n"
"s_lshl_b32 s40, s7, 12\n"
"v_lshlrev_b32 v16, 10, v21\n"
"v_add_u32 v17, 0, v21\n"
"v_xor_b32 v17, v17, v20\n"
"v_lshl_add_u32 v17, v17, 4, v16\n"
"v_add_u32 v104, 0, v17\n"
"v_add_u32 v17, 4, v21\n"
"v_xor_b32 v17, v17, v20\n"
"v_lshl_add_u32 v17, v17, 4, v16\n"
"v_add_u32 v105, 0xc00, v17\n"
"v_add_u32 v17, 8, v21\n"
"v_xor_b32 v17, v17, v20\n"
"v_lshl_add_u32 v17, v17, 4, v16\n"
"v_add_u32 v106, 0x1800, v17\n"
"v_add_u32 v17, 12, v21\n"
"v_xor_b32 v17, v17, v20\n"
"v_lshl_add_u32 v17, v17, 4, v16\n"
"v_add_u32 v107, 0x2400, v17\n"
"s_lshl_b32 s13, s7, 14\n"
"s_add_u32 s13, s13, 0x10000\n"
"v_lshlrev_b32 v16, 8, v20\n"
"v_add_u32 v16, s13, v16\n"
"v_add_u32 v17, 0, v21\n"
"v_xor_b32 v17, v17, v20\n"
"v_lshl_add_u32 v28, v17, 4, v16\n"
"v_add_u32 v17, 4, v21\n"
"v_xor_b32 v17, v17, v20\n"
"v_lshl_add_u32 v29, v17, 4, v16\n"
"v_add_u32 v17, 8, v21\n"
"v_xor_b32 v17, v17, v20\n"
"v_lshl_add_u32 v30, v17, 4, v16\n"
"v_add_u32 v17, 12, v21\n"
"v_xor_b32 v17, v17, v20\n"
"v_lshl_add_u32 v31, v17, 4, v16\n"
"s_waitcnt lgkmcnt(0)\n"
"s_add_u32 s28, s62, 0\n"
"s_addc_u32 s29, s63, 0\n"
"s_and_b32 s29, s29, 0xffff\n"
"s_mov_b32 s30, 0x8000000\n"
"s_mov_b32 s31, 0x20000\n"
"s_add_u32 s20, s66, 0\n"
"s_addc_u32 s21, s67, 0\n"
"s_and_b32 s21, s21, 0xffff\n"
"s_mov_b32 s22, 0x300000\n"
"s_mov_b32 s23, 0x20000\n"
"s_add_u32 s24, s64, 0\n"
"s_addc_u32 s25, s65, 0\n"
"s_and_b32 s25, s25, 0xffff\n"
"s_mov_b32 s26, 0x400\n"
"s_mov_b32 s27, 0x20000\n"
"s_lshl_b32 s11, s9, 16\n"
"s_add_u32 s36, s44, s11\n"
"s_addc_u32 s37, s45, 0\n"
"s_and_b32 s37, s37, 0xffff\n"
"s_mov_b32 s38, 0x10000\n"
"s_mov_b32 s39, 0x20000\n"
"s_lshr_b32 s11, s6, 3\n"
"s_lshl_b32 s11, s11, 12\n"
"s_lshl_b32 s15, s7, 10\n"
"s_add_u32 s11, s11, s15\n"
"s_min_u32 s11, s11, L_end%=-L_pc%=-1024\n"
"s_add_u32 s44, s68, s11\n"
"s_addc_u32 s45, s69, 0\n"
"buffer_load_dword v125, off, s[28:31], 0 sc1\n"
"s_bitcmp1_b32 s6, 3\n"
"s_cbranch_scc0 L_early%=\n"
"s_cmp_lt_u32 s77, 64\n"
"s_cbranch_scc0 L_late2%=\n"
"s_add_u32 s11, s75, 0\n"
"buffer_load_dwordx4 v[32:35], v126, s[20:23], s11 offen\n"
"s_add_u32 s11, s75, 0x30000\n"
"buffer_load_dwordx4 v[36:39], v126, s[20:23], s11 offen\n"
"s_add_u32 s11, s75, 0x60000\n"
"buffer_load_dwordx4 v[40:43], v126, s[20:23], s11 offen\n"
"s_add_u32 s11, s75, 0x90000\n"
"buffer_load_dwordx4 v[44:47], v126, s[20:23], s11 offen\n"
"buffer_load_ushort v64, v127, s[24:27], s76 offen\n"
"buffer_load_ushort v65, v127, s[24:27], s76 offen offset:64\n"
"buffer_load_ushort v66, v127, s[24:27], s76 offen offset:128\n"
"buffer_load_ushort v67, v127, s[24:27], s76 offen offset:192\n"
"s_mov_b32 s33, 0x5040100\n"
"s_mov_b32 s34, 0x7060302\n"
"s_mov_b32 s43, 0x6f800000\n"
"s_mul_i32 s11, s73, 0x6000\n"
"s_lshl_b32 s12, s72, 9\n"
"s_add_u32 s11, s11, s12\n"
"s_add_u32 s74, s11, 0x20000\n"
"s_and_b32 s11, s6, 7\n"
"s_lshr_b32 s12, s6, 3\n"
"s_and_b32 s13, s11, 7\n"
"s_lshl_b32 s13, s13, 0\n"
"s_and_b32 s14, s12, 0\n"
"s_add_u32 s80, s13, s14\n"
"s_lshr_b32 s13, s11, 3\n"
"s_lshl_b32 s13, s13, 5\n"
"s_lshr_b32 s14, s12, 0\n"
"s_add_u32 s81, s13, s14\n"
"s_and_b32 s83, s14, 15\n"
"s_min_u32 s87, s80, 6\n"
"s_lshl_b32 s84, s9, 19\n"
"s_lshl_b32 s15, s8, 11\n"
"s_add_u32 s84, s84, s15\n"
"s_lshl_b32 s15, s7, 9\n"
"s_add_u32 s84, s84, s15\n"
"s_add_u32 s84, s84, 0x100000\n"
"s_lshl_b32 s85, s81, 19\n"
"s_lshl_b32 s15, s80, 11\n"
"s_add_u32 s85, s85, s15\n"
"s_lshl_b32 s15, s7, 9\n"
"s_add_u32 s85, s85, s15\n"
"s_add_u32 s85, s85, 0x1800000\n"
"s_lshl_b32 s86, s9, 16\n"
"s_lshl_b32 s15, s8, 13\n"
"s_add_u32 s86, s86, s15\n"
"s_lshl_b32 s15, s7, 9\n"
"s_add_u32 s86, s86, s15\n"
"s_add_u32 s86, s86, 0x1400000\n"
"s_lshl_b32 s11, s8, 10\n"
"s_lshl_b32 s15, s7, 8\n"
"s_add_u32 s11, s11, s15\n"
"s_add_u32 s16, s50, s11\n"
"s_addc_u32 s17, s51, 0\n"
"s_and_b32 s17, s17, 0xffff\n"
"s_mov_b32 s18, 0x3fe100\n"
"s_mov_b32 s19, 0x20000\n"
"v_lshlrev_b32 v16, 16, v21\n"
"v_lshl_add_u32 v16, v20, 4, v16\n"
"v_mov_b32 v8, v16\n"
"v_add_u32 v9, 0x2000, v16\n"
"v_add_u32 v10, 0x4000, v16\n"
"v_add_u32 v11, 0x6000, v16\n"
"v_add_u32 v12, 0x8000, v16\n"
"v_add_u32 v13, 0xa000, v16\n"
"v_add_u32 v14, 0xc000, v16\n"
"v_add_u32 v15, 0xe000, v16\n"
"v_lshlrev_b32 v16, 8, v21\n"
"v_add_u32 v16, s40, v16\n"
"v_add_u32 v17, 4, v21\n"
"v_xor_b32 v17, v17, v20\n"
"v_and_b32 v17, 15, v17\n"
"v_lshl_add_u32 v2, v17, 4, v16\n"
"v_add_u32 v17, 8, v21\n"
"v_xor_b32 v17, v17, v20\n"
"v_and_b32 v17, 15, v17\n"
"v_lshl_add_u32 v3, v17, 4, v16\n"
"v_add_u32 v17, 12, v21\n"
"v_xor_b32 v17, v17, v20\n"
"v_and_b32 v17, 15, v17\n"
"v_lshl_add_u32 v4, v17, 4, v16\n"
"v_add_u32 v17, 16, v21\n"
"v_xor_b32 v17, v17, v20\n"
"v_and_b32 v17, 15, v17\n"
"v_lshl_add_u32 v5, v17, 4, v16\n"
"v_bfe_u32 v16, v22, 2, 3\n"
"v_and_b32 v17, 3, v22\n"
"v_lshlrev_b32 v16, 8, v16\n"
"v_lshl_add_u32 v16, v17, 4, v16\n"
"v_lshrrev_b32 v17, 5, v22\n"
"v_lshl_add_u32 v16, v17, 6, v16\n"
"v_add_u32 v16, s40, v16\n"
"v_add_u32 v6, 0x400, v16\n"
"v_add_u32 v7, 0x80, v16\n"
"s_mov_b32 s88, 0x3f803f80\n"
"s_mov_b32 s89, 0xffff0000\n"
"s_mov_b32 s90, 0x3b000000\n"
"v_and_b32 v16, 7, v22\n"
"v_lshlrev_b32 v16, 5, v16\n"
"v_lshrrev_b32 v17, 3, v22\n"
"v_lshl_add_u32 v16, v17, 2, v16\n"
"s_lshl_b32 s11, s7, 8\n"
"s_add_u32 s11, s11, 0x27800\n"
"v_add_u32 v16, s11, v16\n"
"v_lshlrev_b32 v18, 2, v22\n"
"v_add_u32 v18, 0x27800, v18\n"
"v_lshlrev_b32 v17, 3, v22\n"
"v_and_b32 v19, 8, v22\n"
"v_cmp_eq_u32 vcc, 0, v19\n"
"s_waitcnt vmcnt(3)\n"
"v_lshlrev_b32 v64, 16, v64\n"
"s_waitcnt vmcnt(2)\n"
"v_lshlrev_b32 v65, 16, v65\n"
"s_waitcnt vmcnt(1)\n"
"v_lshlrev_b32 v66, 16, v66\n"
"s_waitcnt vmcnt(0)\n"
"v_lshlrev_b32 v67, 16, v67\n"
"v_mul_f32 v72, 0xbfb8aa3b, v64\n"
"v_mul_f32 v73, 0xbfb8aa3b, v65\n"
"v_mul_f32 v74, 0xbfb8aa3b, v66\n"
"v_mul_f32 v75, 0xbfb8aa3b, v67\n"
"v_exp_f32 v72, v72\n"
"v_exp_f32 v73, v73\n"
"v_exp_f32 v74, v74\n"
"v_exp_f32 v75, v75\n"
"v_add_f32 v72, 1.0, v72\n"
"v_add_f32 v73, 1.0, v73\n"
"v_add_f32 v74, 1.0, v74\n"
"v_add_f32 v75, 1.0, v75\n"
"v_rcp_f32 v72, v72\n"
"v_rcp_f32 v73, v73\n"
"v_rcp_f32 v74, v74\n"
"v_rcp_f32 v75, v75\n"
"v_mul_f32 v72, v64, v72\n"
"v_mul_f32 v73, v65, v73\n"
"v_mul_f32 v74, v66, v74\n"
"v_mul_f32 v75, v67, v75\n"
"v_cvt_pk_bf16_f32 v72, v72, 0\n"
"v_cvt_pk_bf16_f32 v73, v73, 0\n"
"v_cvt_pk_bf16_f32 v74, v74, 0\n"
"v_cvt_pk_bf16_f32 v75, v75, 0\n"
"v_lshlrev_b32 v80, 16, v72\n"
"v_lshlrev_b32 v81, 16, v73\n"
"v_lshlrev_b32 v82, 16, v74\n"
"v_lshlrev_b32 v83, 16, v75\n"
"v_dot2_f32_bf16 v112, v32, v72, 0\n"
"v_dot2_f32_bf16 v113, v32, v80, 0\n"
"v_dot2_f32_bf16 v114, v33, v72, 0\n"
"v_dot2_f32_bf16 v115, v33, v80, 0\n"
"v_dot2_f32_bf16 v116, v34, v72, 0\n"
"v_dot2_f32_bf16 v117, v34, v80, 0\n"
"v_dot2_f32_bf16 v118, v35, v72, 0\n"
"v_dot2_f32_bf16 v119, v35, v80, 0\n"
"v_dot2_f32_bf16 v112, v36, v73, v112\n"
"v_dot2_f32_bf16 v113, v36, v81, v113\n"
"v_dot2_f32_bf16 v114, v37, v73, v114\n"
"v_dot2_f32_bf16 v115, v37, v81, v115\n"
"v_dot2_f32_bf16 v116, v38, v73, v116\n"
"v_dot2_f32_bf16 v117, v38, v81, v117\n"
"v_dot2_f32_bf16 v118, v39, v73, v118\n"
"v_dot2_f32_bf16 v119, v39, v81, v119\n"
"v_dot2_f32_bf16 v112, v40, v74, v112\n"
"v_dot2_f32_bf16 v113, v40, v82, v113\n"
"v_dot2_f32_bf16 v114, v41, v74, v114\n"
"v_dot2_f32_bf16 v115, v41, v82, v115\n"
"v_dot2_f32_bf16 v116, v42, v74, v116\n"
"v_dot2_f32_bf16 v117, v42, v82, v117\n"
"v_dot2_f32_bf16 v118, v43, v74, v118\n"
"v_dot2_f32_bf16 v119, v43, v82, v119\n"
"v_dot2_f32_bf16 v112, v44, v75, v112\n"
"v_dot2_f32_bf16 v113, v44, v83, v113\n"
"v_dot2_f32_bf16 v114, v45, v75, v114\n"
"v_dot2_f32_bf16 v115, v45, v83, v115\n"
"v_dot2_f32_bf16 v116, v46, v75, v116\n"
"v_dot2_f32_bf16 v117, v46, v83, v117\n"
"v_dot2_f32_bf16 v118, v47, v75, v118\n"
"v_dot2_f32_bf16 v119, v47, v83, v119\n"
"v_permlane32_swap_b32 v112, v116\n"
"v_permlane32_swap_b32 v113, v117\n"
"v_permlane32_swap_b32 v114, v118\n"
"v_permlane32_swap_b32 v115, v119\n"
"v_add_f32 v112, v112, v116\n"
"v_add_f32 v113, v113, v117\n"
"v_add_f32 v114, v114, v118\n"
"v_add_f32 v115, v115, v119\n"
"s_nop 0\n"
"v_permlane16_swap_b32 v112, v114\n"
"v_permlane16_swap_b32 v113, v115\n"
"v_add_f32 v112, v112, v114\n"
"v_add_f32 v113, v113, v115\n"
"s_nop 0\n"
"v_add_f32_dpp v112, v112, v112 row_ror:8 row_mask:0xf bank_mask:0xf\n"
"v_add_f32_dpp v113, v113, v113 row_ror:8 row_mask:0xf bank_mask:0xf\n"
"v_cndmask_b32 v112, v113, v112, vcc\n"
"ds_write_b32 v16, v112\n"
"v_readfirstlane_b32 s10, v125\n"
"s_waitcnt lgkmcnt(0)\n"
"s_barrier\n"
"ds_read_b32 v112, v18\n"
"ds_read_b32 v113, v18 offset:256\n"
"ds_read_b32 v114, v18 offset:512\n"
"ds_read_b32 v115, v18 offset:768\n"
"s_add_u32 s10, s10, 1\n"
"s_waitcnt lgkmcnt(2)\n"
"v_add_f32 v112, v112, v113\n"
"s_waitcnt lgkmcnt(0)\n"
"v_add_f32 v114, v114, v115\n"
"v_add_f32 v112, v112, v114\n"
"v_mov_b32 v113, s10\n"
"s_cmp_lg_u32 s7, 0\n"
"s_cbranch_scc1 L_mgu%=\n"
"buffer_store_dwordx2 v[112:113], v17, s[28:31], s74 offen sc1\n"
"L_mgu%=:\n"
"s_lshl_b32 s13, s7, 14\n"
"s_add_u32 s13, s13, 0x10000\n"
"s_add_u32 m0, s13, 0\n"
"s_lshl_b32 s11, s7, 8\n"
"buffer_load_dwordx4 v104, s[36:39], s11 offen lds\n"
"buffer_load_dwordx4 v105, s[36:39], s11 offen offset:1024 lds\n"
"buffer_load_dwordx4 v106, s[36:39], s11 offen offset:2048 lds\n"
"buffer_load_dwordx4 v107, s[36:39], s11 offen offset:3072 lds\n"
"s_add_u32 m0, s13, 0x1000\n"
"s_lshl_b32 s11, s7, 8\n"
"s_add_u32 s11, s11, 0x4000\n"
"buffer_load_dwordx4 v104, s[36:39], s11 offen lds\n"
"buffer_load_dwordx4 v105, s[36:39], s11 offen offset:1024 lds\n"
"buffer_load_dwordx4 v106, s[36:39], s11 offen offset:2048 lds\n"
"buffer_load_dwordx4 v107, s[36:39], s11 offen offset:3072 lds\n"
"s_add_u32 m0, s13, 0x2000\n"
"s_lshl_b32 s11, s7, 8\n"
"s_add_u32 s11, s11, 0x8000\n"
"buffer_load_dwordx4 v104, s[36:39], s11 offen lds\n"
"buffer_load_dwordx4 v105, s[36:39], s11 offen offset:1024 lds\n"
"buffer_load_dwordx4 v106, s[36:39], s11 offen offset:2048 lds\n"
"buffer_load_dwordx4 v107, s[36:39], s11 offen offset:3072 lds\n"
"s_add_u32 m0, s13, 0x3000\n"
"s_lshl_b32 s11, s7, 8\n"
"s_add_u32 s11, s11, 0xc000\n"
"buffer_load_dwordx4 v104, s[36:39], s11 offen lds\n"
"buffer_load_dwordx4 v105, s[36:39], s11 offen offset:1024 lds\n"
"buffer_load_dwordx4 v106, s[36:39], s11 offen offset:2048 lds\n"
"buffer_load_dwordx4 v107, s[36:39], s11 offen offset:3072 lds\n"
"s_branch L_j0%=\n"
"L_late2%=:\n"
"s_add_u32 s11, s75, 0\n"
"buffer_load_dwordx4 v[32:35], v126, s[20:23], s11 offen\n"
"s_add_u32 s11, s75, 0x30000\n"
"buffer_load_dwordx4 v[36:39], v126, s[20:23], s11 offen\n"
"s_add_u32 s11, s75, 0x60000\n"
"buffer_load_dwordx4 v[40:43], v126, s[20:23], s11 offen\n"
"s_add_u32 s11, s75, 0x90000\n"
"buffer_load_dwordx4 v[44:47], v126, s[20:23], s11 offen\n"
"s_add_u32 s11, s75, 0xc0000\n"
"buffer_load_dwordx4 v[48:51], v126, s[20:23], s11 offen\n"
"s_add_u32 s11, s75, 0xf0000\n"
"buffer_load_dwordx4 v[52:55], v126, s[20:23], s11 offen\n"
"s_add_u32 s11, s75, 0x120000\n"
"buffer_load_dwordx4 v[56:59], v126, s[20:23], s11 offen\n"
"s_add_u32 s11, s75, 0x150000\n"
"buffer_load_dwordx4 v[60:63], v126, s[20:23], s11 offen\n"
"buffer_load_ushort v64, v127, s[24:27], s76 offen\n"
"buffer_load_ushort v65, v127, s[24:27], s76 offen offset:64\n"
"buffer_load_ushort v66, v127, s[24:27], s76 offen offset:128\n"
"buffer_load_ushort v67, v127, s[24:27], s76 offen offset:192\n"
"buffer_load_ushort v68, v127, s[24:27], s76 offen offset:256\n"
"buffer_load_ushort v69, v127, s[24:27], s76 offen offset:320\n"
"buffer_load_ushort v70, v127, s[24:27], s76 offen offset:384\n"
"buffer_load_ushort v71, v127, s[24:27], s76 offen offset:448\n"
"s_mov_b32 s33, 0x5040100\n"
"s_mov_b32 s34, 0x7060302\n"
"s_mov_b32 s43, 0x6f800000\n"
"s_mul_i32 s11, s73, 0x6000\n"
"s_lshl_b32 s12, s72, 9\n"
"s_add_u32 s11, s11, s12\n"
"s_add_u32 s74, s11, 0x20000\n"
"s_and_b32 s11, s6, 7\n"
"s_lshr_b32 s12, s6, 3\n"
"s_and_b32 s13, s11, 7\n"
"s_lshl_b32 s13, s13, 0\n"
"s_and_b32 s14, s12, 0\n"
"s_add_u32 s80, s13, s14\n"
"s_lshr_b32 s13, s11, 3\n"
"s_lshl_b32 s13, s13, 5\n"
"s_lshr_b32 s14, s12, 0\n"
"s_add_u32 s81, s13, s14\n"
"s_and_b32 s83, s14, 15\n"
"s_min_u32 s87, s80, 6\n"
"s_lshl_b32 s84, s9, 19\n"
"s_lshl_b32 s15, s8, 11\n"
"s_add_u32 s84, s84, s15\n"
"s_lshl_b32 s15, s7, 9\n"
"s_add_u32 s84, s84, s15\n"
"s_add_u32 s84, s84, 0x100000\n"
"s_lshl_b32 s85, s81, 19\n"
"s_lshl_b32 s15, s80, 11\n"
"s_add_u32 s85, s85, s15\n"
"s_lshl_b32 s15, s7, 9\n"
"s_add_u32 s85, s85, s15\n"
"s_add_u32 s85, s85, 0x1800000\n"
"s_lshl_b32 s86, s9, 16\n"
"s_lshl_b32 s15, s8, 13\n"
"s_add_u32 s86, s86, s15\n"
"s_lshl_b32 s15, s7, 9\n"
"s_add_u32 s86, s86, s15\n"
"s_add_u32 s86, s86, 0x1400000\n"
"s_lshl_b32 s11, s8, 10\n"
"s_lshl_b32 s15, s7, 8\n"
"s_add_u32 s11, s11, s15\n"
"s_add_u32 s16, s50, s11\n"
"s_addc_u32 s17, s51, 0\n"
"s_and_b32 s17, s17, 0xffff\n"
"s_mov_b32 s18, 0x3fe100\n"
"s_mov_b32 s19, 0x20000\n"
"v_lshlrev_b32 v16, 16, v21\n"
"v_lshl_add_u32 v16, v20, 4, v16\n"
"v_mov_b32 v8, v16\n"
"v_add_u32 v9, 0x2000, v16\n"
"v_add_u32 v10, 0x4000, v16\n"
"v_add_u32 v11, 0x6000, v16\n"
"v_add_u32 v12, 0x8000, v16\n"
"v_add_u32 v13, 0xa000, v16\n"
"v_add_u32 v14, 0xc000, v16\n"
"v_add_u32 v15, 0xe000, v16\n"
"v_lshlrev_b32 v16, 8, v21\n"
"v_add_u32 v16, s40, v16\n"
"v_add_u32 v17, 4, v21\n"
"v_xor_b32 v17, v17, v20\n"
"v_and_b32 v17, 15, v17\n"
"v_lshl_add_u32 v2, v17, 4, v16\n"
"v_add_u32 v17, 8, v21\n"
"v_xor_b32 v17, v17, v20\n"
"v_and_b32 v17, 15, v17\n"
"v_lshl_add_u32 v3, v17, 4, v16\n"
"v_add_u32 v17, 12, v21\n"
"v_xor_b32 v17, v17, v20\n"
"v_and_b32 v17, 15, v17\n"
"v_lshl_add_u32 v4, v17, 4, v16\n"
"v_add_u32 v17, 16, v21\n"
"v_xor_b32 v17, v17, v20\n"
"v_and_b32 v17, 15, v17\n"
"v_lshl_add_u32 v5, v17, 4, v16\n"
"v_bfe_u32 v16, v22, 2, 3\n"
"v_and_b32 v17, 3, v22\n"
"v_lshlrev_b32 v16, 8, v16\n"
"v_lshl_add_u32 v16, v17, 4, v16\n"
"v_lshrrev_b32 v17, 5, v22\n"
"v_lshl_add_u32 v16, v17, 6, v16\n"
"v_add_u32 v16, s40, v16\n"
"v_add_u32 v6, 0x400, v16\n"
"v_add_u32 v7, 0x80, v16\n"
"s_mov_b32 s88, 0x3f803f80\n"
"s_mov_b32 s89, 0xffff0000\n"
"s_mov_b32 s90, 0x3b000000\n"
"v_and_b32 v16, 7, v22\n"
"v_lshlrev_b32 v16, 5, v16\n"
"v_lshrrev_b32 v17, 3, v22\n"
"v_lshl_add_u32 v16, v17, 2, v16\n"
"s_lshl_b32 s11, s7, 8\n"
"s_add_u32 s11, s11, 0x27800\n"
"v_add_u32 v16, s11, v16\n"
"v_lshlrev_b32 v18, 2, v22\n"
"v_add_u32 v18, 0x27800, v18\n"
"v_lshlrev_b32 v17, 3, v22\n"
"v_and_b32 v19, 8, v22\n"
"v_cmp_eq_u32 vcc, 0, v19\n"
"s_waitcnt vmcnt(7)\n"
"v_lshlrev_b32 v64, 16, v64\n"
"s_waitcnt vmcnt(6)\n"
"v_lshlrev_b32 v65, 16, v65\n"
"s_waitcnt vmcnt(5)\n"
"v_lshlrev_b32 v66, 16, v66\n"
"s_waitcnt vmcnt(4)\n"
"v_lshlrev_b32 v67, 16, v67\n"
"s_waitcnt vmcnt(3)\n"
"v_lshlrev_b32 v68, 16, v68\n"
"s_waitcnt vmcnt(2)\n"
"v_lshlrev_b32 v69, 16, v69\n"
"s_waitcnt vmcnt(1)\n"
"v_lshlrev_b32 v70, 16, v70\n"
"s_waitcnt vmcnt(0)\n"
"v_lshlrev_b32 v71, 16, v71\n"
"v_mul_f32 v72, 0xbfb8aa3b, v64\n"
"v_mul_f32 v73, 0xbfb8aa3b, v65\n"
"v_mul_f32 v74, 0xbfb8aa3b, v66\n"
"v_mul_f32 v75, 0xbfb8aa3b, v67\n"
"v_mul_f32 v76, 0xbfb8aa3b, v68\n"
"v_mul_f32 v77, 0xbfb8aa3b, v69\n"
"v_mul_f32 v78, 0xbfb8aa3b, v70\n"
"v_mul_f32 v79, 0xbfb8aa3b, v71\n"
"v_exp_f32 v72, v72\n"
"v_exp_f32 v73, v73\n"
"v_exp_f32 v74, v74\n"
"v_exp_f32 v75, v75\n"
"v_exp_f32 v76, v76\n"
"v_exp_f32 v77, v77\n"
"v_exp_f32 v78, v78\n"
"v_exp_f32 v79, v79\n"
"v_add_f32 v72, 1.0, v72\n"
"v_add_f32 v73, 1.0, v73\n"
"v_add_f32 v74, 1.0, v74\n"
"v_add_f32 v75, 1.0, v75\n"
"v_add_f32 v76, 1.0, v76\n"
"v_add_f32 v77, 1.0, v77\n"
"v_add_f32 v78, 1.0, v78\n"
"v_add_f32 v79, 1.0, v79\n"
"v_rcp_f32 v72, v72\n"
"v_rcp_f32 v73, v73\n"
"v_rcp_f32 v74, v74\n"
"v_rcp_f32 v75, v75\n"
"v_rcp_f32 v76, v76\n"
"v_rcp_f32 v77, v77\n"
"v_rcp_f32 v78, v78\n"
"v_rcp_f32 v79, v79\n"
"v_mul_f32 v72, v64, v72\n"
"v_mul_f32 v73, v65, v73\n"
"v_mul_f32 v74, v66, v74\n"
"v_mul_f32 v75, v67, v75\n"
"v_mul_f32 v76, v68, v76\n"
"v_mul_f32 v77, v69, v77\n"
"v_mul_f32 v78, v70, v78\n"
"v_mul_f32 v79, v71, v79\n"
"v_cvt_pk_bf16_f32 v72, v72, 0\n"
"v_cvt_pk_bf16_f32 v73, v73, 0\n"
"v_cvt_pk_bf16_f32 v74, v74, 0\n"
"v_cvt_pk_bf16_f32 v75, v75, 0\n"
"v_cvt_pk_bf16_f32 v76, v76, 0\n"
"v_cvt_pk_bf16_f32 v77, v77, 0\n"
"v_cvt_pk_bf16_f32 v78, v78, 0\n"
"v_cvt_pk_bf16_f32 v79, v79, 0\n"
"v_lshlrev_b32 v80, 16, v72\n"
"v_lshlrev_b32 v81, 16, v73\n"
"v_lshlrev_b32 v82, 16, v74\n"
"v_lshlrev_b32 v83, 16, v75\n"
"v_lshlrev_b32 v84, 16, v76\n"
"v_lshlrev_b32 v85, 16, v77\n"
"v_lshlrev_b32 v86, 16, v78\n"
"v_lshlrev_b32 v87, 16, v79\n"
"v_dot2_f32_bf16 v112, v32, v72, 0\n"
"v_dot2_f32_bf16 v113, v32, v80, 0\n"
"v_dot2_f32_bf16 v114, v33, v72, 0\n"
"v_dot2_f32_bf16 v115, v33, v80, 0\n"
"v_dot2_f32_bf16 v116, v34, v72, 0\n"
"v_dot2_f32_bf16 v117, v34, v80, 0\n"
"v_dot2_f32_bf16 v118, v35, v72, 0\n"
"v_dot2_f32_bf16 v119, v35, v80, 0\n"
"v_dot2_f32_bf16 v112, v36, v73, v112\n"
"v_dot2_f32_bf16 v113, v36, v81, v113\n"
"v_dot2_f32_bf16 v114, v37, v73, v114\n"
"v_dot2_f32_bf16 v115, v37, v81, v115\n"
"v_dot2_f32_bf16 v116, v38, v73, v116\n"
"v_dot2_f32_bf16 v117, v38, v81, v117\n"
"v_dot2_f32_bf16 v118, v39, v73, v118\n"
"v_dot2_f32_bf16 v119, v39, v81, v119\n"
"v_dot2_f32_bf16 v112, v40, v74, v112\n"
"v_dot2_f32_bf16 v113, v40, v82, v113\n"
"v_dot2_f32_bf16 v114, v41, v74, v114\n"
"v_dot2_f32_bf16 v115, v41, v82, v115\n"
"v_dot2_f32_bf16 v116, v42, v74, v116\n"
"v_dot2_f32_bf16 v117, v42, v82, v117\n"
"v_dot2_f32_bf16 v118, v43, v74, v118\n"
"v_dot2_f32_bf16 v119, v43, v82, v119\n"
"v_dot2_f32_bf16 v112, v44, v75, v112\n"
"v_dot2_f32_bf16 v113, v44, v83, v113\n"
"v_dot2_f32_bf16 v114, v45, v75, v114\n"
"v_dot2_f32_bf16 v115, v45, v83, v115\n"
"v_dot2_f32_bf16 v116, v46, v75, v116\n"
"v_dot2_f32_bf16 v117, v46, v83, v117\n"
"v_dot2_f32_bf16 v118, v47, v75, v118\n"
"v_dot2_f32_bf16 v119, v47, v83, v119\n"
"v_dot2_f32_bf16 v112, v48, v76, v112\n"
"v_dot2_f32_bf16 v113, v48, v84, v113\n"
"v_dot2_f32_bf16 v114, v49, v76, v114\n"
"v_dot2_f32_bf16 v115, v49, v84, v115\n"
"v_dot2_f32_bf16 v116, v50, v76, v116\n"
"v_dot2_f32_bf16 v117, v50, v84, v117\n"
"v_dot2_f32_bf16 v118, v51, v76, v118\n"
"v_dot2_f32_bf16 v119, v51, v84, v119\n"
"v_dot2_f32_bf16 v112, v52, v77, v112\n"
"v_dot2_f32_bf16 v113, v52, v85, v113\n"
"v_dot2_f32_bf16 v114, v53, v77, v114\n"
"v_dot2_f32_bf16 v115, v53, v85, v115\n"
"v_dot2_f32_bf16 v116, v54, v77, v116\n"
"v_dot2_f32_bf16 v117, v54, v85, v117\n"
"v_dot2_f32_bf16 v118, v55, v77, v118\n"
"v_dot2_f32_bf16 v119, v55, v85, v119\n"
"v_dot2_f32_bf16 v112, v56, v78, v112\n"
"v_dot2_f32_bf16 v113, v56, v86, v113\n"
"v_dot2_f32_bf16 v114, v57, v78, v114\n"
"v_dot2_f32_bf16 v115, v57, v86, v115\n"
"v_dot2_f32_bf16 v116, v58, v78, v116\n"
"v_dot2_f32_bf16 v117, v58, v86, v117\n"
"v_dot2_f32_bf16 v118, v59, v78, v118\n"
"v_dot2_f32_bf16 v119, v59, v86, v119\n"
"v_dot2_f32_bf16 v112, v60, v79, v112\n"
"v_dot2_f32_bf16 v113, v60, v87, v113\n"
"v_dot2_f32_bf16 v114, v61, v79, v114\n"
"v_dot2_f32_bf16 v115, v61, v87, v115\n"
"v_dot2_f32_bf16 v116, v62, v79, v116\n"
"v_dot2_f32_bf16 v117, v62, v87, v117\n"
"v_dot2_f32_bf16 v118, v63, v79, v118\n"
"v_dot2_f32_bf16 v119, v63, v87, v119\n"
"v_permlane32_swap_b32 v112, v116\n"
"v_permlane32_swap_b32 v113, v117\n"
"v_permlane32_swap_b32 v114, v118\n"
"v_permlane32_swap_b32 v115, v119\n"
"v_add_f32 v112, v112, v116\n"
"v_add_f32 v113, v113, v117\n"
"v_add_f32 v114, v114, v118\n"
"v_add_f32 v115, v115, v119\n"
"s_nop 0\n"
"v_permlane16_swap_b32 v112, v114\n"
"v_permlane16_swap_b32 v113, v115\n"
"v_add_f32 v112, v112, v114\n"
"v_add_f32 v113, v113, v115\n"
"s_nop 0\n"
"v_add_f32_dpp v112, v112, v112 row_ror:8 row_mask:0xf bank_mask:0xf\n"
"v_add_f32_dpp v113, v113, v113 row_ror:8 row_mask:0xf bank_mask:0xf\n"
"v_cndmask_b32 v112, v113, v112, vcc\n"
"ds_write_b32 v16, v112\n"
"v_readfirstlane_b32 s10, v125\n"
"s_waitcnt lgkmcnt(0)\n"
"s_barrier\n"
"ds_read_b32 v112, v18\n"
"ds_read_b32 v113, v18 offset:256\n"
"ds_read_b32 v114, v18 offset:512\n"
"ds_read_b32 v115, v18 offset:768\n"
"s_add_u32 s10, s10, 1\n"
"s_waitcnt lgkmcnt(2)\n"
"v_add_f32 v112, v112, v113\n"
"s_waitcnt lgkmcnt(0)\n"
"v_add_f32 v114, v114, v115\n"
"v_add_f32 v112, v112, v114\n"
"v_mov_b32 v113, s10\n"
"s_cmp_lg_u32 s7, 0\n"
"s_cbranch_scc1 L_mgn%=\n"
"buffer_store_dwordx2 v[112:113], v17, s[28:31], s74 offen sc1\n"
"L_mgn%=:\n"
"s_lshl_b32 s13, s7, 14\n"
"s_add_u32 s13, s13, 0x10000\n"
"s_add_u32 m0, s13, 0\n"
"s_lshl_b32 s11, s7, 8\n"
"buffer_load_dwordx4 v104, s[36:39], s11 offen lds\n"
"buffer_load_dwordx4 v105, s[36:39], s11 offen offset:1024 lds\n"
"buffer_load_dwordx4 v106, s[36:39], s11 offen offset:2048 lds\n"
"buffer_load_dwordx4 v107, s[36:39], s11 offen offset:3072 lds\n"
"s_add_u32 m0, s13, 0x1000\n"
"s_lshl_b32 s11, s7, 8\n"
"s_add_u32 s11, s11, 0x4000\n"
"buffer_load_dwordx4 v104, s[36:39], s11 offen lds\n"
"buffer_load_dwordx4 v105, s[36:39], s11 offen offset:1024 lds\n"
"buffer_load_dwordx4 v106, s[36:39], s11 offen offset:2048 lds\n"
"buffer_load_dwordx4 v107, s[36:39], s11 offen offset:3072 lds\n"
"s_add_u32 m0, s13, 0x2000\n"
"s_lshl_b32 s11, s7, 8\n"
"s_add_u32 s11, s11, 0x8000\n"
"buffer_load_dwordx4 v104, s[36:39], s11 offen lds\n"
"buffer_load_dwordx4 v105, s[36:39], s11 offen offset:1024 lds\n"
"buffer_load_dwordx4 v106, s[36:39], s11 offen offset:2048 lds\n"
"buffer_load_dwordx4 v107, s[36:39], s11 offen offset:3072 lds\n"
"s_add_u32 m0, s13, 0x3000\n"
"s_lshl_b32 s11, s7, 8\n"
"s_add_u32 s11, s11, 0xc000\n"
"buffer_load_dwordx4 v104, s[36:39], s11 offen lds\n"
"buffer_load_dwordx4 v105, s[36:39], s11 offen offset:1024 lds\n"
"buffer_load_dwordx4 v106, s[36:39], s11 offen offset:2048 lds\n"
"buffer_load_dwordx4 v107, s[36:39], s11 offen offset:3072 lds\n"
"s_branch L_j0%=\n"
"L_early%=:\n"
"s_lshl_b32 s13, s7, 14\n"
"s_add_u32 s13, s13, 0x10000\n"
"s_add_u32 m0, s13, 0\n"
"s_lshl_b32 s11, s7, 8\n"
"buffer_load_dwordx4 v104, s[36:39], s11 offen lds\n"
"buffer_load_dwordx4 v105, s[36:39], s11 offen offset:1024 lds\n"
"buffer_load_dwordx4 v106, s[36:39], s11 offen offset:2048 lds\n"
"buffer_load_dwordx4 v107, s[36:39], s11 offen offset:3072 lds\n"
"s_add_u32 m0, s13, 0x1000\n"
"s_lshl_b32 s11, s7, 8\n"
"s_add_u32 s11, s11, 0x4000\n"
"buffer_load_dwordx4 v104, s[36:39], s11 offen lds\n"
"buffer_load_dwordx4 v105, s[36:39], s11 offen offset:1024 lds\n"
"buffer_load_dwordx4 v106, s[36:39], s11 offen offset:2048 lds\n"
"buffer_load_dwordx4 v107, s[36:39], s11 offen offset:3072 lds\n"
"s_add_u32 m0, s13, 0x2000\n"
"s_lshl_b32 s11, s7, 8\n"
"s_add_u32 s11, s11, 0x8000\n"
"buffer_load_dwordx4 v104, s[36:39], s11 offen lds\n"
"buffer_load_dwordx4 v105, s[36:39], s11 offen offset:1024 lds\n"
"buffer_load_dwordx4 v106, s[36:39], s11 offen offset:2048 lds\n"
"buffer_load_dwordx4 v107, s[36:39], s11 offen offset:3072 lds\n"
"s_mov_b32 s33, 0x5040100\n"
"s_mov_b32 s34, 0x7060302\n"
"s_mov_b32 s43, 0x6f800000\n"
"s_mul_i32 s11, s73, 0x6000\n"
"s_lshl_b32 s12, s72, 9\n"
"s_add_u32 s11, s11, s12\n"
"s_add_u32 s74, s11, 0x20000\n"
"s_and_b32 s11, s6, 7\n"
"s_lshr_b32 s12, s6, 3\n"
"s_and_b32 s13, s11, 7\n"
"s_lshl_b32 s13, s13, 0\n"
"s_and_b32 s14, s12, 0\n"
"s_add_u32 s80, s13, s14\n"
"s_lshr_b32 s13, s11, 3\n"
"s_lshl_b32 s13, s13, 5\n"
"s_lshr_b32 s14, s12, 0\n"
"s_add_u32 s81, s13, s14\n"
"s_and_b32 s83, s14, 15\n"
"s_min_u32 s87, s80, 6\n"
"s_lshl_b32 s84, s9, 19\n"
"s_lshl_b32 s15, s8, 11\n"
"s_add_u32 s84, s84, s15\n"
"s_lshl_b32 s15, s7, 9\n"
"s_add_u32 s84, s84, s15\n"
"s_add_u32 s84, s84, 0x100000\n"
"s_lshl_b32 s85, s81, 19\n"
"s_lshl_b32 s15, s80, 11\n"
"s_add_u32 s85, s85, s15\n"
"s_lshl_b32 s15, s7, 9\n"
"s_add_u32 s85, s85, s15\n"
"s_add_u32 s85, s85, 0x1800000\n"
"s_lshl_b32 s86, s9, 16\n"
"s_lshl_b32 s15, s8, 13\n"
"s_add_u32 s86, s86, s15\n"
"s_lshl_b32 s15, s7, 9\n"
"s_add_u32 s86, s86, s15\n"
"s_add_u32 s86, s86, 0x1400000\n"
"s_lshl_b32 s11, s8, 10\n"
"s_lshl_b32 s15, s7, 8\n"
"s_add_u32 s11, s11, s15\n"
"s_add_u32 s16, s50, s11\n"
"s_addc_u32 s17, s51, 0\n"
"s_and_b32 s17, s17, 0xffff\n"
"s_mov_b32 s18, 0x3fe100\n"
"s_mov_b32 s19, 0x20000\n"
"v_lshlrev_b32 v16, 16, v21\n"
"v_lshl_add_u32 v16, v20, 4, v16\n"
"v_mov_b32 v8, v16\n"
"v_add_u32 v9, 0x2000, v16\n"
"v_add_u32 v10, 0x4000, v16\n"
"v_add_u32 v11, 0x6000, v16\n"
"v_add_u32 v12, 0x8000, v16\n"
"v_add_u32 v13, 0xa000, v16\n"
"v_add_u32 v14, 0xc000, v16\n"
"v_add_u32 v15, 0xe000, v16\n"
"v_lshlrev_b32 v16, 8, v21\n"
"v_add_u32 v16, s40, v16\n"
"v_add_u32 v17, 4, v21\n"
"v_xor_b32 v17, v17, v20\n"
"v_and_b32 v17, 15, v17\n"
"v_lshl_add_u32 v2, v17, 4, v16\n"
"v_add_u32 v17, 8, v21\n"
"v_xor_b32 v17, v17, v20\n"
"v_and_b32 v17, 15, v17\n"
"v_lshl_add_u32 v3, v17, 4, v16\n"
"v_add_u32 v17, 12, v21\n"
"v_xor_b32 v17, v17, v20\n"
"v_and_b32 v17, 15, v17\n"
"v_lshl_add_u32 v4, v17, 4, v16\n"
"v_add_u32 v17, 16, v21\n"
"v_xor_b32 v17, v17, v20\n"
"v_and_b32 v17, 15, v17\n"
"v_lshl_add_u32 v5, v17, 4, v16\n"
"v_bfe_u32 v16, v22, 2, 3\n"
"v_and_b32 v17, 3, v22\n"
"v_lshlrev_b32 v16, 8, v16\n"
"v_lshl_add_u32 v16, v17, 4, v16\n"
"v_lshrrev_b32 v17, 5, v22\n"
"v_lshl_add_u32 v16, v17, 6, v16\n"
"v_add_u32 v16, s40, v16\n"
"v_add_u32 v6, 0x400, v16\n"
"v_add_u32 v7, 0x80, v16\n"
"s_mov_b32 s88, 0x3f803f80\n"
"s_mov_b32 s89, 0xffff0000\n"
"s_mov_b32 s90, 0x3b000000\n"
"s_lshl_b32 s13, s7, 14\n"
"s_add_u32 s13, s13, 0x10000\n"
"s_add_u32 m0, s13, 0x3000\n"
"s_lshl_b32 s11, s7, 8\n"
"s_add_u32 s11, s11, 0xc000\n"
"buffer_load_dwordx4 v104, s[36:39], s11 offen lds\n"
"buffer_load_dwordx4 v105, s[36:39], s11 offen offset:1024 lds\n"
"buffer_load_dwordx4 v106, s[36:39], s11 offen offset:2048 lds\n"
"buffer_load_dwordx4 v107, s[36:39], s11 offen offset:3072 lds\n"
"s_waitcnt vmcnt(16)\n"
"v_readfirstlane_b32 s10, v125\n"
"s_nop 0\n"
"s_add_u32 s10, s10, 1\n"
"L_j0%=:\n"
"s_lshl_b32 s11, s6, 7\n"
"s_add_u32 s11, s11, 0x40000\n"
"v_mov_b32 v16, s10\n"
"buffer_store_dword v16, off, s[28:31], s11 sc1\n"
"s_lshl_b32 s11, s8, 19\n"
"s_add_u32 s12, s11, 0x60000\n"
"s_add_u32 s20, s46, s12\n"
"s_addc_u32 s21, s47, 0\n"
"s_and_b32 s21, s21, 0xffff\n"
"s_mov_b32 s22, 0x20000\n"
"s_mov_b32 s23, 0x20000\n"
"s_add_u32 s12, s11, 0x5fc00\n"
"s_add_u32 s24, s48, s12\n"
"s_addc_u32 s25, s49, 0\n"
"s_and_b32 s25, s25, 0xffff\n"
"s_mov_b32 s26, 0x20400\n"
"s_mov_b32 s27, 0x20000\n"
"v_mov_b32 v24, s88\n"
"v_mov_b32 v25, s88\n"
"v_mov_b32 v26, s88\n"
"v_mov_b32 v27, s88\n"
"v_lshlrev_b32 v16, 5, v20\n"
"v_add_u32 v16, 0x27000, v16\n"
"s_lshl_b32 s11, s7, 3\n"
"v_add_u32 v17, s11, v16\n"
"v_lshrrev_b32 v18, 2, v20\n"
"v_cmp_eq_u32 vcc, v18, v21\n"
"v_lshlrev_b32 v19, 3, v22\n"
"v_add_u32 v19, 0x20000, v19\n"
"v_cndmask_b32 v17, v19, v17, vcc\n"
"v_and_b32 v18, 1, v20\n"
"v_cmp_eq_u32 s[64:65], 1, v18\n"
"v_and_b32 v18, 2, v20\n"
"v_cmp_eq_u32 s[66:67], 2, v18\n"
"s_waitcnt vmcnt(13)\n"
"ds_read_b128 v[192:195], v28\n"
"ds_read_b128 v[196:199], v29\n"
"ds_read_b128 v[200:203], v30\n"
"ds_read_b128 v[204:207], v31\n"
"s_waitcnt vmcnt(9)\n"
"ds_read_b128 v[208:211], v28 offset:4096\n"
"ds_read_b128 v[212:215], v29 offset:4096\n"
"ds_read_b128 v[216:219], v30 offset:4096\n"
"ds_read_b128 v[220:223], v31 offset:4096\n"
"s_waitcnt lgkmcnt(7)\n"
"v_mfma_f32_16x16x32_bf16 v[96:99], v[24:27], v[192:195], 0\n"
"v_lshlrev_b32 v128, 16, v192\n"
"v_and_b32 v129, s89, v192\n"
"v_mfma_f32_16x16x32_bf16 v[100:103], v[192:195], v[192:195], 0\n"
"v_lshlrev_b32 v130, 16, v193\n"
"v_and_b32 v131, s89, v193\n"
"s_waitcnt lgkmcnt(6)\n"
"v_mfma_f32_16x16x32_bf16 v[96:99], v[24:27], v[196:199], v[96:99]\n"
"v_lshlrev_b32 v132, 16, v194\n"
"v_and_b32 v133, s89, v194\n"
"v_mfma_f32_16x16x32_bf16 v[100:103], v[196:199], v[196:199], v[100:103]\n"
"v_lshlrev_b32 v134, 16, v195\n"
"v_and_b32 v135, s89, v195\n"
"s_waitcnt lgkmcnt(5)\n"
"v_mfma_f32_16x16x32_bf16 v[96:99], v[24:27], v[200:203], v[96:99]\n"
"v_lshlrev_b32 v136, 16, v196\n"
"v_and_b32 v137, s89, v196\n"
"v_mfma_f32_16x16x32_bf16 v[100:103], v[200:203], v[200:203], v[100:103]\n"
"v_lshlrev_b32 v138, 16, v197\n"
"v_and_b32 v139, s89, v197\n"
"s_waitcnt lgkmcnt(4)\n"
"v_mfma_f32_16x16x32_bf16 v[96:99], v[24:27], v[204:207], v[96:99]\n"
"v_lshlrev_b32 v140, 16, v198\n"
"v_and_b32 v141, s89, v198\n"
"v_mfma_f32_16x16x32_bf16 v[100:103], v[204:207], v[204:207], v[100:103]\n"
"v_lshlrev_b32 v142, 16, v199\n"
"v_and_b32 v143, s89, v199\n"
"v_lshlrev_b32 v144, 16, v200\n"
"v_and_b32 v145, s89, v200\n"
"v_lshlrev_b32 v146, 16, v201\n"
"v_and_b32 v147, s89, v201\n"
"v_lshlrev_b32 v148, 16, v202\n"
"v_and_b32 v149, s89, v202\n"
"v_lshlrev_b32 v150, 16, v203\n"
"v_and_b32 v151, s89, v203\n"
"v_lshlrev_b32 v152, 16, v204\n"
"v_and_b32 v153, s89, v204\n"
"v_lshlrev_b32 v154, 16, v205\n"
"v_and_b32 v155, s89, v205\n"
"v_lshlrev_b32 v156, 16, v206\n"
"v_and_b32 v157, s89, v206\n"
"v_lshlrev_b32 v158, 16, v207\n"
"v_and_b32 v159, s89, v207\n"
"s_waitcnt vmcnt(5)\n"
"ds_read_b128 v[224:227], v28 offset:8192\n"
"ds_read_b128 v[228:231], v29 offset:8192\n"
"ds_read_b128 v[232:235], v30 offset:8192\n"
"ds_read_b128 v[236:239], v31 offset:8192\n"
"s_waitcnt lgkmcnt(7)\n"
"v_mfma_f32_16x16x32_bf16 v[104:107], v[24:27], v[208:211], 0\n"
"v_lshlrev_b32 v160, 16, v208\n"
"v_and_b32 v161, s89, v208\n"
"v_mfma_f32_16x16x32_bf16 v[108:111], v[208:211], v[208:211], 0\n"
"v_lshlrev_b32 v162, 16, v209\n"
"v_and_b32 v163, s89, v209\n"
"s_waitcnt lgkmcnt(6)\n"
"v_mfma_f32_16x16x32_bf16 v[104:107], v[24:27], v[212:215], v[104:107]\n"
"v_lshlrev_b32 v164, 16, v210\n"
"v_and_b32 v165, s89, v210\n"
"v_mfma_f32_16x16x32_bf16 v[108:111], v[212:215], v[212:215], v[108:111]\n"
"v_lshlrev_b32 v166, 16, v211\n"
"v_and_b32 v167, s89, v211\n"
"s_waitcnt lgkmcnt(5)\n"
"v_mfma_f32_16x16x32_bf16 v[104:107], v[24:27], v[216:219], v[104:107]\n"
"v_lshlrev_b32 v168, 16, v212\n"
"v_and_b32 v169, s89, v212\n"
"v_mfma_f32_16x16x32_bf16 v[108:111], v[216:219], v[216:219], v[108:111]\n"
"v_lshlrev_b32 v170, 16, v213\n"
"v_and_b32 v171, s89, v213\n"
"s_waitcnt lgkmcnt(4)\n"
"v_mfma_f32_16x16x32_bf16 v[104:107], v[24:27], v[220:223], v[104:107]\n"
"v_lshlrev_b32 v172, 16, v214\n"
"v_and_b32 v173, s89, v214\n"
"v_mfma_f32_16x16x32_bf16 v[108:111], v[220:223], v[220:223], v[108:111]\n"
"v_lshlrev_b32 v174, 16, v215\n"
"v_and_b32 v175, s89, v215\n"
"v_lshlrev_b32 v176, 16, v216\n"
"v_and_b32 v177, s89, v216\n"
"v_lshlrev_b32 v178, 16, v217\n"
"v_and_b32 v179, s89, v217\n"
"v_lshlrev_b32 v180, 16, v218\n"
"v_and_b32 v181, s89, v218\n"
"v_lshlrev_b32 v182, 16, v219\n"
"v_and_b32 v183, s89, v219\n"
"v_lshlrev_b32 v184, 16, v220\n"
"v_and_b32 v185, s89, v220\n"
"v_lshlrev_b32 v186, 16, v221\n"
"v_and_b32 v187, s89, v221\n"
"v_lshlrev_b32 v188, 16, v222\n"
"v_and_b32 v189, s89, v222\n"
"v_lshlrev_b32 v190, 16, v223\n"
"v_and_b32 v191, s89, v223\n"
"s_waitcnt vmcnt(1)\n"
"ds_read_b128 v[240:243], v28 offset:12288\n"
"ds_read_b128 v[244:247], v29 offset:12288\n"
"ds_read_b128 v[248:251], v30 offset:12288\n"
"ds_read_b128 v[252:255], v31 offset:12288\n"
"s_waitcnt lgkmcnt(7)\n"
"v_mfma_f32_16x16x32_bf16 v[112:115], v[24:27], v[224:227], 0\n"
"v_lshlrev_b32 v192, 16, v224\n"
"v_and_b32 v193, s89, v224\n"
"v_mfma_f32_16x16x32_bf16 v[116:119], v[224:227], v[224:227], 0\n"
"v_lshlrev_b32 v194, 16, v225\n"
"v_and_b32 v195, s89, v225\n"
"s_waitcnt lgkmcnt(6)\n"
"v_mfma_f32_16x16x32_bf16 v[112:115], v[24:27], v[228:231], v[112:115]\n"
"v_lshlrev_b32 v196, 16, v226\n"
"v_and_b32 v197, s89, v226\n"
"v_mfma_f32_16x16x32_bf16 v[116:119], v[228:231], v[228:231], v[116:119]\n"
"v_lshlrev_b32 v198, 16, v227\n"
"v_and_b32 v199, s89, v227\n"
"s_waitcnt lgkmcnt(5)\n"
"v_mfma_f32_16x16x32_bf16 v[112:115], v[24:27], v[232:235], v[112:115]\n"
"v_lshlrev_b32 v200, 16, v228\n"
"v_and_b32 v201, s89, v228\n"
"v_mfma_f32_16x16x32_bf16 v[116:119], v[232:235], v[232:235], v[116:119]\n"
"v_lshlrev_b32 v202, 16, v229\n"
"v_and_b32 v203, s89, v229\n"
"s_waitcnt lgkmcnt(4)\n"
"v_mfma_f32_16x16x32_bf16 v[112:115], v[24:27], v[236:239], v[112:115]\n"
"v_lshlrev_b32 v204, 16, v230\n"
"v_and_b32 v205, s89, v230\n"
"v_mfma_f32_16x16x32_bf16 v[116:119], v[236:239], v[236:239], v[116:119]\n"
"v_lshlrev_b32 v206, 16, v231\n"
"v_and_b32 v207, s89, v231\n"
"s_waitcnt lgkmcnt(3)\n"
"v_mfma_f32_16x16x32_bf16 v[120:123], v[24:27], v[240:243], 0\n"
"v_lshlrev_b32 v208, 16, v232\n"
"v_and_b32 v209, s89, v232\n"
"v_mfma_f32_16x16x32_bf16 v[124:127], v[240:243], v[240:243], 0\n"
"v_lshlrev_b32 v210, 16, v233\n"
"v_and_b32 v211, s89, v233\n"
"s_waitcnt lgkmcnt(2)\n"
"v_mfma_f32_16x16x32_bf16 v[120:123], v[24:27], v[244:247], v[120:123]\n"
"v_lshlrev_b32 v212, 16, v234\n"
"v_and_b32 v213, s89, v234\n"
"v_mfma_f32_16x16x32_bf16 v[124:127], v[244:247], v[244:247], v[124:127]\n"
"v_lshlrev_b32 v214, 16, v235\n"
"v_and_b32 v215, s89, v235\n"
"s_waitcnt lgkmcnt(1)\n"
"v_mfma_f32_16x16x32_bf16 v[120:123], v[24:27], v[248:251], v[120:123]\n"
"v_lshlrev_b32 v216, 16, v236\n"
"v_and_b32 v217, s89, v236\n"
"v_mfma_f32_16x16x32_bf16 v[124:127], v[248:251], v[248:251], v[124:127]\n"
"v_lshlrev_b32 v218, 16, v237\n"
"v_and_b32 v219, s89, v237\n"
"s_waitcnt lgkmcnt(0)\n"
"v_mfma_f32_16x16x32_bf16 v[120:123], v[24:27], v[252:255], v[120:123]\n"
"v_lshlrev_b32 v220, 16, v238\n"
"v_and_b32 v221, s89, v238\n"
"v_mfma_f32_16x16x32_bf16 v[124:127], v[252:255], v[252:255], v[124:127]\n"
"v_lshlrev_b32 v222, 16, v239\n"
"v_and_b32 v223, s89, v239\n"
"v_lshlrev_b32 v224, 16, v240\n"
"v_and_b32 v225, s89, v240\n"
"v_lshlrev_b32 v226, 16, v241\n"
"v_and_b32 v227, s89, v241\n"
"v_cndmask_b32 v100, v100, v101, s[64:65]\n"
"v_cndmask_b32 v102, v102, v103, s[64:65]\n"
"v_cndmask_b32 v97, v100, v102, s[66:67]\n"
"ds_write_b64 v17, v[96:97]\n"
"v_lshlrev_b32 v228, 16, v242\n"
"v_and_b32 v229, s89, v242\n"
"v_lshlrev_b32 v230, 16, v243\n"
"v_and_b32 v231, s89, v243\n"
"v_cndmask_b32 v108, v108, v109, s[64:65]\n"
"v_cndmask_b32 v110, v110, v111, s[64:65]\n"
"v_cndmask_b32 v105, v108, v110, s[66:67]\n"
"ds_write_b64 v17, v[104:105] offset:512\n"
"v_lshlrev_b32 v232, 16, v244\n"
"v_and_b32 v233, s89, v244\n"
"v_lshlrev_b32 v234, 16, v245\n"
"v_and_b32 v235, s89, v245\n"
"v_cndmask_b32 v116, v116, v117, s[64:65]\n"
"v_cndmask_b32 v118, v118, v119, s[64:65]\n"
"v_cndmask_b32 v113, v116, v118, s[66:67]\n"
"ds_write_b64 v17, v[112:113] offset:1024\n"
"v_lshlrev_b32 v236, 16, v246\n"
"v_and_b32 v237, s89, v246\n"
"v_lshlrev_b32 v238, 16, v247\n"
"v_and_b32 v239, s89, v247\n"
"v_cndmask_b32 v124, v124, v125, s[64:65]\n"
"v_cndmask_b32 v126, v126, v127, s[64:65]\n"
"v_cndmask_b32 v121, v124, v126, s[66:67]\n"
"ds_write_b64 v17, v[120:121] offset:1536\n"
"v_lshlrev_b32 v23, 4, v0\n"
"s_mov_b32 s12, 0x20000\n"
"buffer_load_dwordx4 v[64:67], v23, s[28:31], s12 offen sc1\n"
"s_mov_b32 s12, 0x21000\n"
"buffer_load_dwordx4 v[68:71], v23, s[28:31], s12 offen sc1\n"
"s_mov_b32 s12, 0x26000\n"
"buffer_load_dwordx4 v[72:75], v23, s[28:31], s12 offen sc1\n"
"s_mov_b32 s12, 0x27000\n"
"buffer_load_dwordx4 v[76:79], v23, s[28:31], s12 offen sc1\n"
"s_mov_b32 s12, 0x2c000\n"
"buffer_load_dwordx4 v[80:83], v23, s[28:31], s12 offen sc1\n"
"s_mov_b32 s12, 0x2d000\n"
"buffer_load_dwordx4 v[84:87], v23, s[28:31], s12 offen sc1\n"
"s_mov_b32 s12, 0x32000\n"
"buffer_load_dwordx4 v[88:91], v23, s[28:31], s12 offen sc1\n"
"s_mov_b32 s12, 0x33000\n"
"buffer_load_dwordx4 v[92:95], v23, s[28:31], s12 offen sc1\n"
"s_waitcnt lgkmcnt(0)\n"
"s_barrier\n"
"ds_read_b128 v[96:99], v16\n"
"ds_read_b128 v[100:103], v16 offset:16\n"
"ds_read_b128 v[104:107], v16 offset:512\n"
"ds_read_b128 v[108:111], v16 offset:528\n"
"ds_read_b128 v[112:115], v16 offset:1024\n"
"ds_read_b128 v[116:119], v16 offset:1040\n"
"ds_read_b128 v[120:123], v16 offset:1536\n"
"ds_read_b128 v[124:127], v16 offset:1552\n"
"v_lshlrev_b32 v240, 16, v248\n"
"v_and_b32 v241, s89, v248\n"
"v_lshlrev_b32 v242, 16, v249\n"
"v_and_b32 v243, s89, v249\n"
"v_lshlrev_b32 v244, 16, v250\n"
"v_and_b32 v245, s89, v250\n"
"v_lshlrev_b32 v246, 16, v251\n"
"v_and_b32 v247, s89, v251\n"
"v_lshlrev_b32 v248, 16, v252\n"
"v_and_b32 v249, s89, v252\n"
"v_lshlrev_b32 v250, 16, v253\n"
"v_and_b32 v251, s89, v253\n"
"v_lshlrev_b32 v252, 16, v254\n"
"v_and_b32 v253, s89, v254\n"
"v_lshlrev_b32 v254, 16, v255\n"
"v_and_b32 v255, s89, v255\n"
"s_lshl_b32 s35, s82, 18\n"
"buffer_load_dwordx4 v[32:35], v8, s[16:19], s35 offen sc1\n"
"buffer_load_dwordx4 v[36:39], v9, s[16:19], s35 offen sc1\n"
"buffer_load_dwordx4 v[40:43], v10, s[16:19], s35 offen sc1\n"
"buffer_load_dwordx4 v[44:47], v11, s[16:19], s35 offen sc1\n"
"buffer_load_dwordx4 v[48:51], v12, s[16:19], s35 offen sc1\n"
"buffer_load_dwordx4 v[52:55], v13, s[16:19], s35 offen sc1\n"
"buffer_load_dwordx4 v[56:59], v14, s[16:19], s35 offen sc1\n"
"buffer_load_dwordx4 v[60:63], v15, s[16:19], s35 offen sc1\n"
"s_waitcnt lgkmcnt(7)\n"
"v_pk_add_f32 v[96:97], v[96:97], v[98:99]\n"
"s_waitcnt lgkmcnt(5)\n"
"v_pk_add_f32 v[104:105], v[104:105], v[106:107]\n"
"s_waitcnt lgkmcnt(3)\n"
"v_pk_add_f32 v[112:113], v[112:113], v[114:115]\n"
"s_waitcnt lgkmcnt(1)\n"
"v_pk_add_f32 v[120:121], v[120:121], v[122:123]\n"
"v_pk_add_f32 v[100:101], v[100:101], v[102:103]\n"
"v_pk_add_f32 v[108:109], v[108:109], v[110:111]\n"
"v_pk_add_f32 v[116:117], v[116:117], v[118:119]\n"
"s_waitcnt lgkmcnt(0)\n"
"v_pk_add_f32 v[124:125], v[124:125], v[126:127]\n"
"v_pk_add_f32 v[96:97], v[96:97], v[100:101]\n"
"v_pk_add_f32 v[104:105], v[104:105], v[108:109]\n"
"v_pk_add_f32 v[112:113], v[112:113], v[116:117]\n"
"v_pk_add_f32 v[120:121], v[120:121], v[124:125]\n"
"v_mul_f32 v96, 0x3b000000, v96\n"
"v_mul_f32 v104, 0x3b000000, v104\n"
"v_mul_f32 v112, 0x3b000000, v112\n"
"v_mul_f32 v120, 0x3b000000, v120\n"
"v_mul_f32 v98, v96, v96\n"
"v_mul_f32 v106, v104, v104\n"
"v_mul_f32 v114, v112, v112\n"
"v_mul_f32 v122, v120, v120\n"
"v_fma_f32 v98, v97, s90, -v98\n"
"v_fma_f32 v106, v105, s90, -v106\n"
"v_fma_f32 v114, v113, s90, -v114\n"
"v_fma_f32 v122, v121, s90, -v122\n"
"v_max_f32 v98, 0, v98\n"
"v_max_f32 v106, 0, v106\n"
"v_max_f32 v114, 0, v114\n"
"v_max_f32 v122, 0, v122\n"
"v_add_f32 v98, 0x3727c5ac, v98\n"
"v_add_f32 v106, 0x3727c5ac, v106\n"
"v_add_f32 v114, 0x3727c5ac, v114\n"
"v_add_f32 v122, 0x3727c5ac, v122\n"
"v_rsq_f32 v24, v98\n"
"v_rsq_f32 v26, v106\n"
"v_rsq_f32 v28, v114\n"
"v_rsq_f32 v30, v122\n"
"v_mul_f32 v25, -v96, v24\n"
"v_mul_f32 v27, -v104, v26\n"
"v_mul_f32 v29, -v112, v28\n"
"v_mul_f32 v31, -v120, v30\n"
"v_pk_fma_f32 v[128:129], v[128:129], v[24:25], v[24:25] op_sel:[0,0,1] op_sel_hi:[1,0,1]\n"
"v_pk_fma_f32 v[130:131], v[130:131], v[24:25], v[24:25] op_sel:[0,0,1] op_sel_hi:[1,0,1]\n"
"v_pk_fma_f32 v[132:133], v[132:133], v[24:25], v[24:25] op_sel:[0,0,1] op_sel_hi:[1,0,1]\n"
"v_pk_fma_f32 v[134:135], v[134:135], v[24:25], v[24:25] op_sel:[0,0,1] op_sel_hi:[1,0,1]\n"
"v_pk_fma_f32 v[136:137], v[136:137], v[24:25], v[24:25] op_sel:[0,0,1] op_sel_hi:[1,0,1]\n"
"v_pk_fma_f32 v[138:139], v[138:139], v[24:25], v[24:25] op_sel:[0,0,1] op_sel_hi:[1,0,1]\n"
"v_pk_fma_f32 v[140:141], v[140:141], v[24:25], v[24:25] op_sel:[0,0,1] op_sel_hi:[1,0,1]\n"
"v_pk_fma_f32 v[142:143], v[142:143], v[24:25], v[24:25] op_sel:[0,0,1] op_sel_hi:[1,0,1]\n"
"v_pk_fma_f32 v[144:145], v[144:145], v[24:25], v[24:25] op_sel:[0,0,1] op_sel_hi:[1,0,1]\n"
"v_pk_fma_f32 v[146:147], v[146:147], v[24:25], v[24:25] op_sel:[0,0,1] op_sel_hi:[1,0,1]\n"
"v_pk_fma_f32 v[148:149], v[148:149], v[24:25], v[24:25] op_sel:[0,0,1] op_sel_hi:[1,0,1]\n"
"v_pk_fma_f32 v[150:151], v[150:151], v[24:25], v[24:25] op_sel:[0,0,1] op_sel_hi:[1,0,1]\n"
"v_pk_fma_f32 v[152:153], v[152:153], v[24:25], v[24:25] op_sel:[0,0,1] op_sel_hi:[1,0,1]\n"
"v_pk_fma_f32 v[154:155], v[154:155], v[24:25], v[24:25] op_sel:[0,0,1] op_sel_hi:[1,0,1]\n"
"v_pk_fma_f32 v[156:157], v[156:157], v[24:25], v[24:25] op_sel:[0,0,1] op_sel_hi:[1,0,1]\n"
"v_pk_fma_f32 v[158:159], v[158:159], v[24:25], v[24:25] op_sel:[0,0,1] op_sel_hi:[1,0,1]\n"
"v_pk_fma_f32 v[160:161], v[160:161], v[26:27], v[26:27] op_sel:[0,0,1] op_sel_hi:[1,0,1]\n"
"v_pk_fma_f32 v[162:163], v[162:163], v[26:27], v[26:27] op_sel:[0,0,1] op_sel_hi:[1,0,1]\n"
"v_pk_fma_f32 v[164:165], v[164:165], v[26:27], v[26:27] op_sel:[0,0,1] op_sel_hi:[1,0,1]\n"
"v_pk_fma_f32 v[166:167], v[166:167], v[26:27], v[26:27] op_sel:[0,0,1] op_sel_hi:[1,0,1]\n"
"v_pk_fma_f32 v[168:169], v[168:169], v[26:27], v[26:27] op_sel:[0,0,1] op_sel_hi:[1,0,1]\n"
"v_pk_fma_f32 v[170:171], v[170:171], v[26:27], v[26:27] op_sel:[0,0,1] op_sel_hi:[1,0,1]\n"
"v_pk_fma_f32 v[172:173], v[172:173], v[26:27], v[26:27] op_sel:[0,0,1] op_sel_hi:[1,0,1]\n"
"v_pk_fma_f32 v[174:175], v[174:175], v[26:27], v[26:27] op_sel:[0,0,1] op_sel_hi:[1,0,1]\n"
"v_pk_fma_f32 v[176:177], v[176:177], v[26:27], v[26:27] op_sel:[0,0,1] op_sel_hi:[1,0,1]\n"
"v_pk_fma_f32 v[178:179], v[178:179], v[26:27], v[26:27] op_sel:[0,0,1] op_sel_hi:[1,0,1]\n"
"v_pk_fma_f32 v[180:181], v[180:181], v[26:27], v[26:27] op_sel:[0,0,1] op_sel_hi:[1,0,1]\n"
"v_pk_fma_f32 v[182:183], v[182:183], v[26:27], v[26:27] op_sel:[0,0,1] op_sel_hi:[1,0,1]\n"
"v_pk_fma_f32 v[184:185], v[184:185], v[26:27], v[26:27] op_sel:[0,0,1] op_sel_hi:[1,0,1]\n"
"v_pk_fma_f32 v[186:187], v[186:187], v[26:27], v[26:27] op_sel:[0,0,1] op_sel_hi:[1,0,1]\n"
"v_pk_fma_f32 v[188:189], v[188:189], v[26:27], v[26:27] op_sel:[0,0,1] op_sel_hi:[1,0,1]\n"
"v_pk_fma_f32 v[190:191], v[190:191], v[26:27], v[26:27] op_sel:[0,0,1] op_sel_hi:[1,0,1]\n"
"v_pk_fma_f32 v[192:193], v[192:193], v[28:29], v[28:29] op_sel:[0,0,1] op_sel_hi:[1,0,1]\n"
"v_pk_fma_f32 v[194:195], v[194:195], v[28:29], v[28:29] op_sel:[0,0,1] op_sel_hi:[1,0,1]\n"
"v_pk_fma_f32 v[196:197], v[196:197], v[28:29], v[28:29] op_sel:[0,0,1] op_sel_hi:[1,0,1]\n"
"v_pk_fma_f32 v[198:199], v[198:199], v[28:29], v[28:29] op_sel:[0,0,1] op_sel_hi:[1,0,1]\n"
"v_pk_fma_f32 v[200:201], v[200:201], v[28:29], v[28:29] op_sel:[0,0,1] op_sel_hi:[1,0,1]\n"
"v_pk_fma_f32 v[202:203], v[202:203], v[28:29], v[28:29] op_sel:[0,0,1] op_sel_hi:[1,0,1]\n"
"v_pk_fma_f32 v[204:205], v[204:205], v[28:29], v[28:29] op_sel:[0,0,1] op_sel_hi:[1,0,1]\n"
"v_pk_fma_f32 v[206:207], v[206:207], v[28:29], v[28:29] op_sel:[0,0,1] op_sel_hi:[1,0,1]\n"
"v_pk_fma_f32 v[208:209], v[208:209], v[28:29], v[28:29] op_sel:[0,0,1] op_sel_hi:[1,0,1]\n"
"v_pk_fma_f32 v[210:211], v[210:211], v[28:29], v[28:29] op_sel:[0,0,1] op_sel_hi:[1,0,1]\n"
"v_pk_fma_f32 v[212:213], v[212:213], v[28:29], v[28:29] op_sel:[0,0,1] op_sel_hi:[1,0,1]\n"
"v_pk_fma_f32 v[214:215], v[214:215], v[28:29], v[28:29] op_sel:[0,0,1] op_sel_hi:[1,0,1]\n"
"v_pk_fma_f32 v[216:217], v[216:217], v[28:29], v[28:29] op_sel:[0,0,1] op_sel_hi:[1,0,1]\n"
"v_pk_fma_f32 v[218:219], v[218:219], v[28:29], v[28:29] op_sel:[0,0,1] op_sel_hi:[1,0,1]\n"
"v_pk_fma_f32 v[220:221], v[220:221], v[28:29], v[28:29] op_sel:[0,0,1] op_sel_hi:[1,0,1]\n"
"v_pk_fma_f32 v[222:223], v[222:223], v[28:29], v[28:29] op_sel:[0,0,1] op_sel_hi:[1,0,1]\n"
"v_pk_fma_f32 v[224:225], v[224:225], v[30:31], v[30:31] op_sel:[0,0,1] op_sel_hi:[1,0,1]\n"
"v_pk_fma_f32 v[226:227], v[226:227], v[30:31], v[30:31] op_sel:[0,0,1] op_sel_hi:[1,0,1]\n"
"v_pk_fma_f32 v[228:229], v[228:229], v[30:31], v[30:31] op_sel:[0,0,1] op_sel_hi:[1,0,1]\n"
"v_pk_fma_f32 v[230:231], v[230:231], v[30:31], v[30:31] op_sel:[0,0,1] op_sel_hi:[1,0,1]\n"
"v_pk_fma_f32 v[232:233], v[232:233], v[30:31], v[30:31] op_sel:[0,0,1] op_sel_hi:[1,0,1]\n"
"v_pk_fma_f32 v[234:235], v[234:235], v[30:31], v[30:31] op_sel:[0,0,1] op_sel_hi:[1,0,1]\n"
"v_pk_fma_f32 v[236:237], v[236:237], v[30:31], v[30:31] op_sel:[0,0,1] op_sel_hi:[1,0,1]\n"
"v_pk_fma_f32 v[238:239], v[238:239], v[30:31], v[30:31] op_sel:[0,0,1] op_sel_hi:[1,0,1]\n"
"v_pk_fma_f32 v[240:241], v[240:241], v[30:31], v[30:31] op_sel:[0,0,1] op_sel_hi:[1,0,1]\n"
"v_pk_fma_f32 v[242:243], v[242:243], v[30:31], v[30:31] op_sel:[0,0,1] op_sel_hi:[1,0,1]\n"
"v_pk_fma_f32 v[244:245], v[244:245], v[30:31], v[30:31] op_sel:[0,0,1] op_sel_hi:[1,0,1]\n"
"v_pk_fma_f32 v[246:247], v[246:247], v[30:31], v[30:31] op_sel:[0,0,1] op_sel_hi:[1,0,1]\n"
"v_pk_fma_f32 v[248:249], v[248:249], v[30:31], v[30:31] op_sel:[0,0,1] op_sel_hi:[1,0,1]\n"
"v_pk_fma_f32 v[250:251], v[250:251], v[30:31], v[30:31] op_sel:[0,0,1] op_sel_hi:[1,0,1]\n"
"v_pk_fma_f32 v[252:253], v[252:253], v[30:31], v[30:31] op_sel:[0,0,1] op_sel_hi:[1,0,1]\n"
"v_pk_fma_f32 v[254:255], v[254:255], v[30:31], v[30:31] op_sel:[0,0,1] op_sel_hi:[1,0,1]\n"
"v_lshlrev_b32 v29, 5, v21\n"
"s_lshl_b32 s11, s7, 9\n"
"s_add_u32 s11, s11, 0x24000\n"
"v_add_u32 v29, s11, v29\n"
"s_lshl_b32 s11, s7, 2\n"
"s_sub_u32 s11, s11, s82\n"
"s_add_u32 s15, s11, 0\n"
"s_and_b32 s15, s15, 15\n"
"s_lshl_b32 s15, s15, 12\n"
"v_add_u32 v25, s15, v1\n"
"s_add_u32 s15, s11, 1\n"
"s_and_b32 s15, s15, 15\n"
"s_lshl_b32 s15, s15, 12\n"
"v_add_u32 v26, s15, v1\n"
"s_add_u32 s15, s11, 2\n"
"s_and_b32 s15, s15, 15\n"
"s_lshl_b32 s15, s15, 12\n"
"v_add_u32 v27, s15, v1\n"
"s_add_u32 s15, s11, 3\n"
"s_and_b32 s15, s15, 15\n"
"s_lshl_b32 s15, s15, 12\n"
"v_add_u32 v28, s15, v1\n"
"s_mov_b32 s13, 0x10000\n"
"L_mga%=:\n"
"s_waitcnt vmcnt(15)\n"
"v_xor_b32 v16, s10, v65\n"
"v_bitop3_b32 v16, v67, s10, v16 bitop3:0xbe\n"
"s_waitcnt vmcnt(14)\n"
"v_bitop3_b32 v16, v69, s10, v16 bitop3:0xbe\n"
"v_bitop3_b32 v16, v71, s10, v16 bitop3:0xbe\n"
"s_waitcnt vmcnt(13)\n"
"v_bitop3_b32 v16, v73, s10, v16 bitop3:0xbe\n"
"v_bitop3_b32 v16, v75, s10, v16 bitop3:0xbe\n"
"s_waitcnt vmcnt(12)\n"
"v_bitop3_b32 v16, v77, s10, v16 bitop3:0xbe\n"
"v_bitop3_b32 v16, v79, s10, v16 bitop3:0xbe\n"
"s_waitcnt vmcnt(11)\n"
"v_bitop3_b32 v16, v81, s10, v16 bitop3:0xbe\n"
"v_bitop3_b32 v16, v83, s10, v16 bitop3:0xbe\n"
"s_waitcnt vmcnt(10)\n"
"v_bitop3_b32 v16, v85, s10, v16 bitop3:0xbe\n"
"v_bitop3_b32 v16, v87, s10, v16 bitop3:0xbe\n"
"s_waitcnt vmcnt(9)\n"
"v_bitop3_b32 v16, v89, s10, v16 bitop3:0xbe\n"
"v_bitop3_b32 v16, v91, s10, v16 bitop3:0xbe\n"
"s_waitcnt vmcnt(8)\n"
"v_bitop3_b32 v16, v93, s10, v16 bitop3:0xbe\n"
"v_bitop3_b32 v16, v95, s10, v16 bitop3:0xbe\n"
"v_cmp_ne_u32 vcc, 0, v16\n"
"s_nop 0\n"
"s_cbranch_vccz L_mgda%=\n"
"s_cmp_eq_u32 s13, 0\n"
"s_cbranch_scc1 L_mgda%=\n"
"s_sub_u32 s13, s13, 1\n"
"s_mov_b32 s12, 0x20000\n"
"buffer_load_dwordx4 v[64:67], v23, s[28:31], s12 offen sc1\n"
"s_mov_b32 s12, 0x21000\n"
"buffer_load_dwordx4 v[68:71], v23, s[28:31], s12 offen sc1\n"
"s_mov_b32 s12, 0x26000\n"
"buffer_load_dwordx4 v[72:75], v23, s[28:31], s12 offen sc1\n"
"s_mov_b32 s12, 0x27000\n"
"buffer_load_dwordx4 v[76:79], v23, s[28:31], s12 offen sc1\n"
"s_mov_b32 s12, 0x2c000\n"
"buffer_load_dwordx4 v[80:83], v23, s[28:31], s12 offen sc1\n"
"s_mov_b32 s12, 0x2d000\n"
"buffer_load_dwordx4 v[84:87], v23, s[28:31], s12 offen sc1\n"
"s_mov_b32 s12, 0x32000\n"
"buffer_load_dwordx4 v[88:91], v23, s[28:31], s12 offen sc1\n"
"s_mov_b32 s12, 0x33000\n"
"buffer_load_dwordx4 v[92:95], v23, s[28:31], s12 offen sc1\n"
"s_waitcnt vmcnt(0)\n"
"s_branch L_mga%=\n"
"L_mgda%=:\n"
"v_lshrrev_b32 v16, 1, v23\n"
"v_add_u32 v16, 0x24000, v16\n"
"s_mov_b32 s13, 0xffff0000\n"
"v_add_f32 v64, v64, v72\n"
"v_add_f32 v80, v80, v88\n"
"v_add_f32 v66, v66, v74\n"
"v_add_f32 v82, v82, v90\n"
"v_add_f32 v68, v68, v76\n"
"v_add_f32 v84, v84, v92\n"
"v_add_f32 v70, v70, v78\n"
"v_add_f32 v86, v86, v94\n"
"v_add_f32 v64, v64, v80\n"
"v_add_f32 v66, v66, v82\n"
"v_add_f32 v68, v68, v84\n"
"v_add_f32 v70, v70, v86\n"
"v_cvt_pk_bf16_f32 v72, v64, v66\n"
"v_lshlrev_b32 v76, 16, v72\n"
"v_and_b32 v77, s13, v72\n"
"v_mul_f32 v76, 0x3e0293ee, v76\n"
"v_mul_f32 v77, 0x3e0293ee, v77\n"
"ds_write_b64 v16, v[76:77] offset:2048\n"
"v_cvt_pk_bf16_f32 v73, v68, v70\n"
"v_lshlrev_b32 v78, 16, v73\n"
"v_and_b32 v79, s13, v73\n"
"v_add_f32 v78, 1.0, v78\n"
"v_add_f32 v79, 1.0, v79\n"
"v_cvt_pk_bf16_f32 v73, v78, v79\n"
"v_lshlrev_b32 v78, 16, v73\n"
"v_and_b32 v79, s13, v73\n"
"v_mul_f32 v78, 0x3e0293ee, v78\n"
"v_mul_f32 v79, 0x3e0293ee, v79\n"
"ds_write_b64 v16, v[78:79]\n"
"s_waitcnt lgkmcnt(0)\n"
"s_barrier\n"
"ds_read_b128 v[96:99], v29\n"
"ds_read_b128 v[104:107], v29 offset:2048\n"
"ds_read_b128 v[100:103], v29 offset:16\n"
"ds_read_b128 v[108:111], v29 offset:2064\n"
"ds_read_b128 v[112:115], v29 offset:128\n"
"ds_read_b128 v[120:123], v29 offset:2176\n"
"ds_read_b128 v[116:119], v29 offset:144\n"
"ds_read_b128 v[124:127], v29 offset:2192\n"
"s_waitcnt lgkmcnt(6)\n"
"v_pk_fma_f32 v[128:129], v[128:129], v[96:97], v[104:105]\n"
"v_pk_fma_f32 v[130:131], v[130:131], v[98:99], v[106:107]\n"
"s_waitcnt lgkmcnt(4)\n"
"v_pk_fma_f32 v[132:133], v[132:133], v[100:101], v[108:109]\n"
"v_pk_fma_f32 v[134:135], v[134:135], v[102:103], v[110:111]\n"
"v_cvt_pk_bf16_f32 v128, v128, v129\n"
"v_cvt_pk_bf16_f32 v129, v130, v131\n"
"v_cvt_pk_bf16_f32 v130, v132, v133\n"
"v_cvt_pk_bf16_f32 v131, v134, v135\n"
"ds_write_b128 v25, v[128:131]\n"
"v_pk_fma_f32 v[160:161], v[160:161], v[96:97], v[104:105]\n"
"v_pk_fma_f32 v[162:163], v[162:163], v[98:99], v[106:107]\n"
"v_pk_fma_f32 v[164:165], v[164:165], v[100:101], v[108:109]\n"
"v_pk_fma_f32 v[166:167], v[166:167], v[102:103], v[110:111]\n"
"v_cvt_pk_bf16_f32 v160, v160, v161\n"
"v_cvt_pk_bf16_f32 v161, v162, v163\n"
"v_cvt_pk_bf16_f32 v162, v164, v165\n"
"v_cvt_pk_bf16_f32 v163, v166, v167\n"
"ds_write_b128 v25, v[160:163] offset:1024\n"
"v_pk_fma_f32 v[192:193], v[192:193], v[96:97], v[104:105]\n"
"v_pk_fma_f32 v[194:195], v[194:195], v[98:99], v[106:107]\n"
"v_pk_fma_f32 v[196:197], v[196:197], v[100:101], v[108:109]\n"
"v_pk_fma_f32 v[198:199], v[198:199], v[102:103], v[110:111]\n"
"v_cvt_pk_bf16_f32 v192, v192, v193\n"
"v_cvt_pk_bf16_f32 v193, v194, v195\n"
"v_cvt_pk_bf16_f32 v194, v196, v197\n"
"v_cvt_pk_bf16_f32 v195, v198, v199\n"
"ds_write_b128 v25, v[192:195] offset:2048\n"
"v_pk_fma_f32 v[224:225], v[224:225], v[96:97], v[104:105]\n"
"v_pk_fma_f32 v[226:227], v[226:227], v[98:99], v[106:107]\n"
"v_pk_fma_f32 v[228:229], v[228:229], v[100:101], v[108:109]\n"
"v_pk_fma_f32 v[230:231], v[230:231], v[102:103], v[110:111]\n"
"ds_read_b128 v[96:99], v29 offset:256\n"
"ds_read_b128 v[104:107], v29 offset:2304\n"
"ds_read_b128 v[100:103], v29 offset:272\n"
"ds_read_b128 v[108:111], v29 offset:2320\n"
"v_cvt_pk_bf16_f32 v224, v224, v225\n"
"v_cvt_pk_bf16_f32 v225, v226, v227\n"
"v_cvt_pk_bf16_f32 v226, v228, v229\n"
"v_cvt_pk_bf16_f32 v227, v230, v231\n"
"ds_write_b128 v25, v[224:227] offset:3072\n"
"s_waitcnt lgkmcnt(10)\n"
"v_pk_fma_f32 v[136:137], v[136:137], v[112:113], v[120:121]\n"
"v_pk_fma_f32 v[138:139], v[138:139], v[114:115], v[122:123]\n"
"s_waitcnt lgkmcnt(8)\n"
"v_pk_fma_f32 v[140:141], v[140:141], v[116:117], v[124:125]\n"
"v_pk_fma_f32 v[142:143], v[142:143], v[118:119], v[126:127]\n"
"v_cvt_pk_bf16_f32 v136, v136, v137\n"
"v_cvt_pk_bf16_f32 v137, v138, v139\n"
"v_cvt_pk_bf16_f32 v138, v140, v141\n"
"v_cvt_pk_bf16_f32 v139, v142, v143\n"
"ds_write_b128 v26, v[136:139]\n"
"s_add_u32 s35, s35, 0x40000\n"
"s_and_b32 s35, s35, 0x3fffff\n"
"v_pk_fma_f32 v[168:169], v[168:169], v[112:113], v[120:121]\n"
"v_pk_fma_f32 v[170:171], v[170:171], v[114:115], v[122:123]\n"
"v_pk_fma_f32 v[172:173], v[172:173], v[116:117], v[124:125]\n"
"v_pk_fma_f32 v[174:175], v[174:175], v[118:119], v[126:127]\n"
"v_cvt_pk_bf16_f32 v168, v168, v169\n"
"v_cvt_pk_bf16_f32 v169, v170, v171\n"
"v_cvt_pk_bf16_f32 v170, v172, v173\n"
"v_cvt_pk_bf16_f32 v171, v174, v175\n"
"ds_write_b128 v26, v[168:171] offset:1024\n"
"buffer_load_dwordx4 v[64:67], v8, s[16:19], s35 offen sc1\n"
"v_pk_fma_f32 v[200:201], v[200:201], v[112:113], v[120:121]\n"
"v_pk_fma_f32 v[202:203], v[202:203], v[114:115], v[122:123]\n"
"v_pk_fma_f32 v[204:205], v[204:205], v[116:117], v[124:125]\n"
"v_pk_fma_f32 v[206:207], v[206:207], v[118:119], v[126:127]\n"
"v_cvt_pk_bf16_f32 v200, v200, v201\n"
"v_cvt_pk_bf16_f32 v201, v202, v203\n"
"v_cvt_pk_bf16_f32 v202, v204, v205\n"
"v_cvt_pk_bf16_f32 v203, v206, v207\n"
"ds_write_b128 v26, v[200:203] offset:2048\n"
"buffer_load_dwordx4 v[68:71], v9, s[16:19], s35 offen sc1\n"
"v_pk_fma_f32 v[232:233], v[232:233], v[112:113], v[120:121]\n"
"v_pk_fma_f32 v[234:235], v[234:235], v[114:115], v[122:123]\n"
"v_pk_fma_f32 v[236:237], v[236:237], v[116:117], v[124:125]\n"
"v_pk_fma_f32 v[238:239], v[238:239], v[118:119], v[126:127]\n"
"ds_read_b128 v[112:115], v29 offset:384\n"
"ds_read_b128 v[120:123], v29 offset:2432\n"
"ds_read_b128 v[116:119], v29 offset:400\n"
"ds_read_b128 v[124:127], v29 offset:2448\n"
"v_cvt_pk_bf16_f32 v232, v232, v233\n"
"v_cvt_pk_bf16_f32 v233, v234, v235\n"
"v_cvt_pk_bf16_f32 v234, v236, v237\n"
"v_cvt_pk_bf16_f32 v235, v238, v239\n"
"ds_write_b128 v26, v[232:235] offset:3072\n"
"buffer_load_dwordx4 v[72:75], v10, s[16:19], s35 offen sc1\n"
"s_waitcnt lgkmcnt(11)\n"
"v_pk_fma_f32 v[144:145], v[144:145], v[96:97], v[104:105]\n"
"v_pk_fma_f32 v[146:147], v[146:147], v[98:99], v[106:107]\n"
"s_waitcnt lgkmcnt(9)\n"
"v_pk_fma_f32 v[148:149], v[148:149], v[100:101], v[108:109]\n"
"v_pk_fma_f32 v[150:151], v[150:151], v[102:103], v[110:111]\n"
"v_cvt_pk_bf16_f32 v144, v144, v145\n"
"v_cvt_pk_bf16_f32 v145, v146, v147\n"
"v_cvt_pk_bf16_f32 v146, v148, v149\n"
"v_cvt_pk_bf16_f32 v147, v150, v151\n"
"ds_write_b128 v27, v[144:147]\n"
"buffer_load_dwordx4 v[76:79], v11, s[16:19], s35 offen sc1\n"
"v_pk_fma_f32 v[176:177], v[176:177], v[96:97], v[104:105]\n"
"v_pk_fma_f32 v[178:179], v[178:179], v[98:99], v[106:107]\n"
"v_pk_fma_f32 v[180:181], v[180:181], v[100:101], v[108:109]\n"
"v_pk_fma_f32 v[182:183], v[182:183], v[102:103], v[110:111]\n"
"v_cvt_pk_bf16_f32 v176, v176, v177\n"
"v_cvt_pk_bf16_f32 v177, v178, v179\n"
"v_cvt_pk_bf16_f32 v178, v180, v181\n"
"v_cvt_pk_bf16_f32 v179, v182, v183\n"
"ds_write_b128 v27, v[176:179] offset:1024\n"
"buffer_load_dwordx4 v[80:83], v12, s[16:19], s35 offen sc1\n"
"v_pk_fma_f32 v[208:209], v[208:209], v[96:97], v[104:105]\n"
"v_pk_fma_f32 v[210:211], v[210:211], v[98:99], v[106:107]\n"
"v_pk_fma_f32 v[212:213], v[212:213], v[100:101], v[108:109]\n"
"v_pk_fma_f32 v[214:215], v[214:215], v[102:103], v[110:111]\n"
"v_cvt_pk_bf16_f32 v208, v208, v209\n"
"v_cvt_pk_bf16_f32 v209, v210, v211\n"
"v_cvt_pk_bf16_f32 v210, v212, v213\n"
"v_cvt_pk_bf16_f32 v211, v214, v215\n"
"ds_write_b128 v27, v[208:211] offset:2048\n"
"buffer_load_dwordx4 v[84:87], v13, s[16:19], s35 offen sc1\n"
"v_pk_fma_f32 v[240:241], v[240:241], v[96:97], v[104:105]\n"
"v_pk_fma_f32 v[242:243], v[242:243], v[98:99], v[106:107]\n"
"v_pk_fma_f32 v[244:245], v[244:245], v[100:101], v[108:109]\n"
"v_pk_fma_f32 v[246:247], v[246:247], v[102:103], v[110:111]\n"
"v_cvt_pk_bf16_f32 v240, v240, v241\n"
"v_cvt_pk_bf16_f32 v241, v242, v243\n"
"v_cvt_pk_bf16_f32 v242, v244, v245\n"
"v_cvt_pk_bf16_f32 v243, v246, v247\n"
"ds_write_b128 v27, v[240:243] offset:3072\n"
"buffer_load_dwordx4 v[88:91], v14, s[16:19], s35 offen sc1\n"
"s_waitcnt lgkmcnt(7)\n"
"v_pk_fma_f32 v[152:153], v[152:153], v[112:113], v[120:121]\n"
"v_pk_fma_f32 v[154:155], v[154:155], v[114:115], v[122:123]\n"
"s_waitcnt lgkmcnt(5)\n"
"v_pk_fma_f32 v[156:157], v[156:157], v[116:117], v[124:125]\n"
"v_pk_fma_f32 v[158:159], v[158:159], v[118:119], v[126:127]\n"
"v_cvt_pk_bf16_f32 v152, v152, v153\n"
"v_cvt_pk_bf16_f32 v153, v154, v155\n"
"v_cvt_pk_bf16_f32 v154, v156, v157\n"
"v_cvt_pk_bf16_f32 v155, v158, v159\n"
"ds_write_b128 v28, v[152:155]\n"
"buffer_load_dwordx4 v[92:95], v15, s[16:19], s35 offen sc1\n"
"v_pk_fma_f32 v[184:185], v[184:185], v[112:113], v[120:121]\n"
"v_pk_fma_f32 v[186:187], v[186:187], v[114:115], v[122:123]\n"
"v_pk_fma_f32 v[188:189], v[188:189], v[116:117], v[124:125]\n"
"v_pk_fma_f32 v[190:191], v[190:191], v[118:119], v[126:127]\n"
"v_cvt_pk_bf16_f32 v184, v184, v185\n"
"v_cvt_pk_bf16_f32 v185, v186, v187\n"
"v_cvt_pk_bf16_f32 v186, v188, v189\n"
"v_cvt_pk_bf16_f32 v187, v190, v191\n"
"ds_write_b128 v28, v[184:187] offset:1024\n"
"v_pk_fma_f32 v[216:217], v[216:217], v[112:113], v[120:121]\n"
"v_pk_fma_f32 v[218:219], v[218:219], v[114:115], v[122:123]\n"
"v_pk_fma_f32 v[220:221], v[220:221], v[116:117], v[124:125]\n"
"v_pk_fma_f32 v[222:223], v[222:223], v[118:119], v[126:127]\n"
"v_cvt_pk_bf16_f32 v216, v216, v217\n"
"v_cvt_pk_bf16_f32 v217, v218, v219\n"
"v_cvt_pk_bf16_f32 v218, v220, v221\n"
"v_cvt_pk_bf16_f32 v219, v222, v223\n"
"ds_write_b128 v28, v[216:219] offset:2048\n"
"v_pk_fma_f32 v[248:249], v[248:249], v[112:113], v[120:121]\n"
"v_pk_fma_f32 v[250:251], v[250:251], v[114:115], v[122:123]\n"
"v_pk_fma_f32 v[252:253], v[252:253], v[116:117], v[124:125]\n"
"v_pk_fma_f32 v[254:255], v[254:255], v[118:119], v[126:127]\n"
"v_cvt_pk_bf16_f32 v248, v248, v249\n"
"v_cvt_pk_bf16_f32 v249, v250, v251\n"
"v_cvt_pk_bf16_f32 v250, v252, v253\n"
"v_cvt_pk_bf16_f32 v251, v254, v255\n"
"ds_write_b128 v28, v[248:251] offset:3072\n"
"s_waitcnt lgkmcnt(0)\n"
"s_barrier\n"
"s_and_b32 s11, s9, 1\n"
"s_mul_i32 s11, s11, 5\n"
"L_stg%=:\n"
"s_cmp_eq_u32 s11, 0\n"
"s_cbranch_scc1 L_stgd%=\n"
"s_sleep 8\n"
"s_sub_u32 s11, s11, 1\n"
"s_branch L_stg%=\n"
"L_stgd%=:\n"
"ds_read_b128 a[224:227], v1\n"
"ds_read_b128 a[228:231], v1 offset:1024\n"
"ds_read_b128 a[232:235], v1 offset:2048\n"
"ds_read_b128 a[236:239], v1 offset:3072\n"
"s_waitcnt vmcnt(8)\n"
"v_perm_b32 v16, v36, v32, s33\n"
"v_perm_b32 v17, v44, v40, s33\n"
"v_perm_b32 v18, v52, v48, s33\n"
"v_perm_b32 v19, v60, v56, s33\n"
"v_perm_b32 v20, v36, v32, s34\n"
"v_perm_b32 v21, v44, v40, s34\n"
"v_perm_b32 v22, v52, v48, s34\n"
"v_perm_b32 v23, v60, v56, s34\n"
"; q ks=0\n"
"s_waitcnt lgkmcnt(0)\n"
"v_mfma_f32_16x16x32_bf16 v[128:131], v[16:19], a[224:227], 0\n"
"v_perm_b32 v24, v37, v33, s33\n"
"s_add_u32 s35, s35, 0x40000\n"
"s_and_b32 s35, s35, 0x3fffff\n"
"v_mfma_f32_16x16x32_bf16 v[132:135], v[16:19], a[228:231], 0\n"
"buffer_load_dwordx4 v[96:99], v8, s[16:19], s35 offen sc1\n"
"v_perm_b32 v25, v45, v41, s33\n"
"v_mfma_f32_16x16x32_bf16 v[192:195], v[16:19], a[232:235], 0\n"
"v_perm_b32 v26, v53, v49, s33\n"
"v_mfma_f32_16x16x32_bf16 v[196:199], v[16:19], a[236:239], 0\n"
"buffer_load_dwordx4 v[100:103], v9, s[16:19], s35 offen sc1\n"
"v_perm_b32 v27, v61, v57, s33\n"
"v_mfma_f32_16x16x32_bf16 v[136:139], v[20:23], a[224:227], 0\n"
"v_perm_b32 v28, v37, v33, s34\n"
"v_mfma_f32_16x16x32_bf16 v[140:143], v[20:23], a[228:231], 0\n"
"buffer_load_dwordx4 v[104:107], v10, s[16:19], s35 offen sc1\n"
"v_perm_b32 v29, v45, v41, s34\n"
"v_mfma_f32_16x16x32_bf16 v[200:203], v[20:23], a[232:235], 0\n"
"v_perm_b32 v30, v53, v49, s34\n"
"v_mfma_f32_16x16x32_bf16 v[204:207], v[20:23], a[236:239], 0\n"
"buffer_load_dwordx4 v[108:111], v11, s[16:19], s35 offen sc1\n"
"v_perm_b32 v31, v61, v57, s34\n"
"v_mfma_f32_16x16x32_bf16 v[144:147], v[24:27], a[224:227], 0\n"
"v_perm_b32 v16, v38, v34, s33\n"
"v_mfma_f32_16x16x32_bf16 v[148:151], v[24:27], a[228:231], 0\n"
"ds_read_b128 a[240:243], v1 offset:4096\n"
"v_perm_b32 v17, v46, v42, s33\n"
"v_mfma_f32_16x16x32_bf16 v[208:211], v[24:27], a[232:235], 0\n"
"buffer_load_dwordx4 v[112:115], v12, s[16:19], s35 offen sc1\n"
"v_perm_b32 v18, v54, v50, s33\n"
"v_mfma_f32_16x16x32_bf16 v[212:215], v[24:27], a[236:239], 0\n"
"buffer_load_dwordx4 v[116:119], v13, s[16:19], s35 offen sc1\n"
"v_perm_b32 v19, v62, v58, s33\n"
"v_mfma_f32_16x16x32_bf16 v[152:155], v[28:31], a[224:227], 0\n"
"v_perm_b32 v20, v38, v34, s34\n"
"v_mfma_f32_16x16x32_bf16 v[156:159], v[28:31], a[228:231], 0\n"
"ds_read_b128 a[244:247], v1 offset:5120\n"
"v_perm_b32 v21, v46, v42, s34\n"
"v_mfma_f32_16x16x32_bf16 v[216:219], v[28:31], a[232:235], 0\n"
"buffer_load_dwordx4 v[120:123], v14, s[16:19], s35 offen sc1\n"
"v_perm_b32 v22, v54, v50, s34\n"
"v_mfma_f32_16x16x32_bf16 v[220:223], v[28:31], a[236:239], 0\n"
"buffer_load_dwordx4 v[124:127], v15, s[16:19], s35 offen sc1\n"
"v_perm_b32 v23, v62, v58, s34\n"
"v_mfma_f32_16x16x32_bf16 v[160:163], v[16:19], a[224:227], 0\n"
"v_perm_b32 v24, v39, v35, s33\n"
"v_mfma_f32_16x16x32_bf16 v[164:167], v[16:19], a[228:231], 0\n"
"ds_read_b128 a[248:251], v1 offset:6144\n"
"v_perm_b32 v25, v47, v43, s33\n"
"v_mfma_f32_16x16x32_bf16 v[224:227], v[16:19], a[232:235], 0\n"
"v_perm_b32 v26, v55, v51, s33\n"
"v_mfma_f32_16x16x32_bf16 v[228:231], v[16:19], a[236:239], 0\n"
"v_perm_b32 v27, v63, v59, s33\n"
"v_mfma_f32_16x16x32_bf16 v[168:171], v[20:23], a[224:227], 0\n"
"v_perm_b32 v28, v39, v35, s34\n"
"v_mfma_f32_16x16x32_bf16 v[172:175], v[20:23], a[228:231], 0\n"
"ds_read_b128 a[252:255], v1 offset:7168\n"
"v_perm_b32 v29, v47, v43, s34\n"
"v_mfma_f32_16x16x32_bf16 v[232:235], v[20:23], a[232:235], 0\n"
"v_perm_b32 v30, v55, v51, s34\n"
"v_mfma_f32_16x16x32_bf16 v[236:239], v[20:23], a[236:239], 0\n"
"v_perm_b32 v31, v63, v59, s34\n"
"s_add_u32 s35, s35, 0x40000\n"
"s_and_b32 s35, s35, 0x3fffff\n"
"v_mfma_f32_16x16x32_bf16 v[176:179], v[24:27], a[224:227], 0\n"
"buffer_load_dwordx4 v[32:35], v8, s[16:19], s35 offen sc1\n"
"s_waitcnt vmcnt(9)\n"
"v_perm_b32 v16, v68, v64, s33\n"
"v_mfma_f32_16x16x32_bf16 v[180:183], v[24:27], a[228:231], 0\n"
"v_perm_b32 v17, v76, v72, s33\n"
"v_mfma_f32_16x16x32_bf16 v[240:243], v[24:27], a[232:235], 0\n"
"v_perm_b32 v18, v84, v80, s33\n"
"v_mfma_f32_16x16x32_bf16 v[244:247], v[24:27], a[236:239], 0\n"
"buffer_load_dwordx4 v[36:39], v9, s[16:19], s35 offen sc1\n"
"v_perm_b32 v19, v92, v88, s33\n"
"v_mfma_f32_16x16x32_bf16 v[184:187], v[28:31], a[224:227], 0\n"
"v_perm_b32 v20, v68, v64, s34\n"
"v_mfma_f32_16x16x32_bf16 v[188:191], v[28:31], a[228:231], 0\n"
"v_perm_b32 v21, v76, v72, s34\n"
"v_mfma_f32_16x16x32_bf16 v[248:251], v[28:31], a[232:235], 0\n"
"buffer_load_dwordx4 v[40:43], v10, s[16:19], s35 offen sc1\n"
"v_perm_b32 v22, v84, v80, s34\n"
"v_mfma_f32_16x16x32_bf16 v[252:255], v[28:31], a[236:239], 0\n"
"v_perm_b32 v23, v92, v88, s34\n"
"; q ks=1\n"
"s_waitcnt lgkmcnt(0)\n"
"v_mfma_f32_16x16x32_bf16 v[128:131], v[16:19], a[240:243], v[128:131]\n"
"v_perm_b32 v24, v69, v65, s33\n"
"v_mfma_f32_16x16x32_bf16 v[132:135], v[16:19], a[244:247], v[132:135]\n"
"buffer_load_dwordx4 v[44:47], v11, s[16:19], s35 offen sc1\n"
"v_perm_b32 v25, v77, v73, s33\n"
"v_mfma_f32_16x16x32_bf16 v[192:195], v[16:19], a[248:251], v[192:195]\n"
"v_perm_b32 v26, v85, v81, s33\n"
"v_mfma_f32_16x16x32_bf16 v[196:199], v[16:19], a[252:255], v[196:199]\n"
"v_perm_b32 v27, v93, v89, s33\n"
"v_mfma_f32_16x16x32_bf16 v[136:139], v[20:23], a[240:243], v[136:139]\n"
"buffer_load_dwordx4 v[48:51], v12, s[16:19], s35 offen sc1\n"
"v_perm_b32 v28, v69, v65, s34\n"
"v_mfma_f32_16x16x32_bf16 v[140:143], v[20:23], a[244:247], v[140:143]\n"
"v_perm_b32 v29, v77, v73, s34\n"
"v_mfma_f32_16x16x32_bf16 v[200:203], v[20:23], a[248:251], v[200:203]\n"
"v_perm_b32 v30, v85, v81, s34\n"
"v_mfma_f32_16x16x32_bf16 v[204:207], v[20:23], a[252:255], v[204:207]\n"
"buffer_load_dwordx4 v[52:55], v13, s[16:19], s35 offen sc1\n"
"v_perm_b32 v31, v93, v89, s34\n"
"v_mfma_f32_16x16x32_bf16 v[144:147], v[24:27], a[240:243], v[144:147]\n"
"v_perm_b32 v16, v70, v66, s33\n"
"v_mfma_f32_16x16x32_bf16 v[148:151], v[24:27], a[244:247], v[148:151]\n"
"ds_read_b128 a[224:227], v1 offset:8192\n"
"v_perm_b32 v17, v78, v74, s33\n"
"v_mfma_f32_16x16x32_bf16 v[208:211], v[24:27], a[248:251], v[208:211]\n"
"buffer_load_dwordx4 v[56:59], v14, s[16:19], s35 offen sc1\n"
"v_perm_b32 v18, v86, v82, s33\n"
"v_mfma_f32_16x16x32_bf16 v[212:215], v[24:27], a[252:255], v[212:215]\n"
"v_perm_b32 v19, v94, v90, s33\n"
"v_mfma_f32_16x16x32_bf16 v[152:155], v[28:31], a[240:243], v[152:155]\n"
"v_perm_b32 v20, v70, v66, s34\n"
"v_mfma_f32_16x16x32_bf16 v[156:159], v[28:31], a[244:247], v[156:159]\n"
"buffer_load_dwordx4 v[60:63], v15, s[16:19], s35 offen sc1\n"
"v_perm_b32 v21, v78, v74, s34\n"
"v_mfma_f32_16x16x32_bf16 v[216:219], v[28:31], a[248:251], v[216:219]\n"
"ds_read_b128 a[228:231], v1 offset:9216\n"
"v_perm_b32 v22, v86, v82, s34\n"
"v_mfma_f32_16x16x32_bf16 v[220:223], v[28:31], a[252:255], v[220:223]\n"
"v_perm_b32 v23, v94, v90, s34\n"
"v_mfma_f32_16x16x32_bf16 v[160:163], v[16:19], a[240:243], v[160:163]\n"
"v_perm_b32 v24, v71, v67, s33\n"
"v_mfma_f32_16x16x32_bf16 v[164:167], v[16:19], a[244:247], v[164:167]\n"
"ds_read_b128 a[232:235], v1 offset:10240\n"
"v_perm_b32 v25, v79, v75, s33\n"
"v_mfma_f32_16x16x32_bf16 v[224:227], v[16:19], a[248:251], v[224:227]\n"
"v_perm_b32 v26, v87, v83, s33\n"
"v_mfma_f32_16x16x32_bf16 v[228:231], v[16:19], a[252:255], v[228:231]\n"
"v_perm_b32 v27, v95, v91, s33\n"
"v_mfma_f32_16x16x32_bf16 v[168:171], v[20:23], a[240:243], v[168:171]\n"
"v_perm_b32 v28, v71, v67, s34\n"
"v_mfma_f32_16x16x32_bf16 v[172:175], v[20:23], a[244:247], v[172:175]\n"
"ds_read_b128 a[236:239], v1 offset:11264\n"
"v_perm_b32 v29, v79, v75, s34\n"
"v_mfma_f32_16x16x32_bf16 v[232:235], v[20:23], a[248:251], v[232:235]\n"
"v_perm_b32 v30, v87, v83, s34\n"
"v_mfma_f32_16x16x32_bf16 v[236:239], v[20:23], a[252:255], v[236:239]\n"
"v_perm_b32 v31, v95, v91, s34\n"
"s_add_u32 s35, s35, 0x40000\n"
"s_and_b32 s35, s35, 0x3fffff\n"
"v_mfma_f32_16x16x32_bf16 v[176:179], v[24:27], a[240:243], v[176:179]\n"
"buffer_load_dwordx4 v[64:67], v8, s[16:19], s35 offen sc1\n"
"s_waitcnt vmcnt(9)\n"
"v_perm_b32 v16, v100, v96, s33\n"
"v_mfma_f32_16x16x32_bf16 v[180:183], v[24:27], a[244:247], v[180:183]\n"
"v_perm_b32 v17, v108, v104, s33\n"
"v_mfma_f32_16x16x32_bf16 v[240:243], v[24:27], a[248:251], v[240:243]\n"
"v_perm_b32 v18, v116, v112, s33\n"
"v_mfma_f32_16x16x32_bf16 v[244:247], v[24:27], a[252:255], v[244:247]\n"
"buffer_load_dwordx4 v[68:71], v9, s[16:19], s35 offen sc1\n"
"v_perm_b32 v19, v124, v120, s33\n"
"v_mfma_f32_16x16x32_bf16 v[184:187], v[28:31], a[240:243], v[184:187]\n"
"v_perm_b32 v20, v100, v96, s34\n"
"v_mfma_f32_16x16x32_bf16 v[188:191], v[28:31], a[244:247], v[188:191]\n"
"v_perm_b32 v21, v108, v104, s34\n"
"v_mfma_f32_16x16x32_bf16 v[248:251], v[28:31], a[248:251], v[248:251]\n"
"buffer_load_dwordx4 v[72:75], v10, s[16:19], s35 offen sc1\n"
"v_perm_b32 v22, v116, v112, s34\n"
"v_mfma_f32_16x16x32_bf16 v[252:255], v[28:31], a[252:255], v[252:255]\n"
"v_perm_b32 v23, v124, v120, s34\n"
"; q ks=2\n"
"s_waitcnt lgkmcnt(0)\n"
"v_mfma_f32_16x16x32_bf16 v[128:131], v[16:19], a[224:227], v[128:131]\n"
"v_perm_b32 v24, v101, v97, s33\n"
"v_mfma_f32_16x16x32_bf16 v[132:135], v[16:19], a[228:231], v[132:135]\n"
"buffer_load_dwordx4 v[76:79], v11, s[16:19], s35 offen sc1\n"
"v_perm_b32 v25, v109, v105, s33\n"
"v_mfma_f32_16x16x32_bf16 v[192:195], v[16:19], a[232:235], v[192:195]\n"
"v_perm_b32 v26, v117, v113, s33\n"
"v_mfma_f32_16x16x32_bf16 v[196:199], v[16:19], a[236:239], v[196:199]\n"
"v_perm_b32 v27, v125, v121, s33\n"
"v_mfma_f32_16x16x32_bf16 v[136:139], v[20:23], a[224:227], v[136:139]\n"
"buffer_load_dwordx4 v[80:83], v12, s[16:19], s35 offen sc1\n"
"v_perm_b32 v28, v101, v97, s34\n"
"v_mfma_f32_16x16x32_bf16 v[140:143], v[20:23], a[228:231], v[140:143]\n"
"v_perm_b32 v29, v109, v105, s34\n"
"v_mfma_f32_16x16x32_bf16 v[200:203], v[20:23], a[232:235], v[200:203]\n"
"v_perm_b32 v30, v117, v113, s34\n"
"v_mfma_f32_16x16x32_bf16 v[204:207], v[20:23], a[236:239], v[204:207]\n"
"buffer_load_dwordx4 v[84:87], v13, s[16:19], s35 offen sc1\n"
"v_perm_b32 v31, v125, v121, s34\n"
"v_mfma_f32_16x16x32_bf16 v[144:147], v[24:27], a[224:227], v[144:147]\n"
"v_perm_b32 v16, v102, v98, s33\n"
"v_mfma_f32_16x16x32_bf16 v[148:151], v[24:27], a[228:231], v[148:151]\n"
"ds_read_b128 a[240:243], v1 offset:12288\n"
"v_perm_b32 v17, v110, v106, s33\n"
"v_mfma_f32_16x16x32_bf16 v[208:211], v[24:27], a[232:235], v[208:211]\n"
"buffer_load_dwordx4 v[88:91], v14, s[16:19], s35 offen sc1\n"
"v_perm_b32 v18, v118, v114, s33\n"
"v_mfma_f32_16x16x32_bf16 v[212:215], v[24:27], a[236:239], v[212:215]\n"
"v_perm_b32 v19, v126, v122, s33\n"
"v_mfma_f32_16x16x32_bf16 v[152:155], v[28:31], a[224:227], v[152:155]\n"
"v_perm_b32 v20, v102, v98, s34\n"
"v_mfma_f32_16x16x32_bf16 v[156:159], v[28:31], a[228:231], v[156:159]\n"
"buffer_load_dwordx4 v[92:95], v15, s[16:19], s35 offen sc1\n"
"v_perm_b32 v21, v110, v106, s34\n"
"v_mfma_f32_16x16x32_bf16 v[216:219], v[28:31], a[232:235], v[216:219]\n"
"ds_read_b128 a[244:247], v1 offset:13312\n"
"v_perm_b32 v22, v118, v114, s34\n"
"v_mfma_f32_16x16x32_bf16 v[220:223], v[28:31], a[236:239], v[220:223]\n"
"v_perm_b32 v23, v126, v122, s34\n"
"v_mfma_f32_16x16x32_bf16 v[160:163], v[16:19], a[224:227], v[160:163]\n"
"v_perm_b32 v24, v103, v99, s33\n"
"v_mfma_f32_16x16x32_bf16 v[164:167], v[16:19], a[228:231], v[164:167]\n"
"ds_read_b128 a[248:251], v1 offset:14336\n"
"v_perm_b32 v25, v111, v107, s33\n"
"v_mfma_f32_16x16x32_bf16 v[224:227], v[16:19], a[232:235], v[224:227]\n"
"v_perm_b32 v26, v119, v115, s33\n"
"v_mfma_f32_16x16x32_bf16 v[228:231], v[16:19], a[236:239], v[228:231]\n"
"v_perm_b32 v27, v127, v123, s33\n"
"v_mfma_f32_16x16x32_bf16 v[168:171], v[20:23], a[224:227], v[168:171]\n"
"v_perm_b32 v28, v103, v99, s34\n"
"v_mfma_f32_16x16x32_bf16 v[172:175], v[20:23], a[228:231], v[172:175]\n"
"ds_read_b128 a[252:255], v1 offset:15360\n"
"v_perm_b32 v29, v111, v107, s34\n"
"v_mfma_f32_16x16x32_bf16 v[232:235], v[20:23], a[232:235], v[232:235]\n"
"v_perm_b32 v30, v119, v115, s34\n"
"v_mfma_f32_16x16x32_bf16 v[236:239], v[20:23], a[236:239], v[236:239]\n"
"v_perm_b32 v31, v127, v123, s34\n"
"s_add_u32 s35, s35, 0x40000\n"
"s_and_b32 s35, s35, 0x3fffff\n"
"v_mfma_f32_16x16x32_bf16 v[176:179], v[24:27], a[224:227], v[176:179]\n"
"buffer_load_dwordx4 v[96:99], v8, s[16:19], s35 offen sc1\n"
"s_waitcnt vmcnt(9)\n"
"v_perm_b32 v16, v36, v32, s33\n"
"v_mfma_f32_16x16x32_bf16 v[180:183], v[24:27], a[228:231], v[180:183]\n"
"v_perm_b32 v17, v44, v40, s33\n"
"v_mfma_f32_16x16x32_bf16 v[240:243], v[24:27], a[232:235], v[240:243]\n"
"v_perm_b32 v18, v52, v48, s33\n"
"v_mfma_f32_16x16x32_bf16 v[244:247], v[24:27], a[236:239], v[244:247]\n"
"buffer_load_dwordx4 v[100:103], v9, s[16:19], s35 offen sc1\n"
"v_perm_b32 v19, v60, v56, s33\n"
"v_mfma_f32_16x16x32_bf16 v[184:187], v[28:31], a[224:227], v[184:187]\n"
"v_perm_b32 v20, v36, v32, s34\n"
"v_mfma_f32_16x16x32_bf16 v[188:191], v[28:31], a[228:231], v[188:191]\n"
"v_perm_b32 v21, v44, v40, s34\n"
"v_mfma_f32_16x16x32_bf16 v[248:251], v[28:31], a[232:235], v[248:251]\n"
"buffer_load_dwordx4 v[104:107], v10, s[16:19], s35 offen sc1\n"
"v_perm_b32 v22, v52, v48, s34\n"
"v_mfma_f32_16x16x32_bf16 v[252:255], v[28:31], a[236:239], v[252:255]\n"
"v_perm_b32 v23, v60, v56, s34\n"
"; q ks=3\n"
"s_waitcnt lgkmcnt(0)\n"
"v_mfma_f32_16x16x32_bf16 v[128:131], v[16:19], a[240:243], v[128:131]\n"
"v_perm_b32 v24, v37, v33, s33\n"
"v_mfma_f32_16x16x32_bf16 v[132:135], v[16:19], a[244:247], v[132:135]\n"
"buffer_load_dwordx4 v[108:111], v11, s[16:19], s35 offen sc1\n"
"v_perm_b32 v25, v45, v41, s33\n"
"v_mfma_f32_16x16x32_bf16 v[192:195], v[16:19], a[248:251], v[192:195]\n"
"v_perm_b32 v26, v53, v49, s33\n"
"v_mfma_f32_16x16x32_bf16 v[196:199], v[16:19], a[252:255], v[196:199]\n"
"v_perm_b32 v27, v61, v57, s33\n"
"v_mfma_f32_16x16x32_bf16 v[136:139], v[20:23], a[240:243], v[136:139]\n"
"buffer_load_dwordx4 v[112:115], v12, s[16:19], s35 offen sc1\n"
"v_perm_b32 v28, v37, v33, s34\n"
"v_mfma_f32_16x16x32_bf16 v[140:143], v[20:23], a[244:247], v[140:143]\n"
"v_perm_b32 v29, v45, v41, s34\n"
"v_mfma_f32_16x16x32_bf16 v[200:203], v[20:23], a[248:251], v[200:203]\n"
"v_perm_b32 v30, v53, v49, s34\n"
"v_mfma_f32_16x16x32_bf16 v[204:207], v[20:23], a[252:255], v[204:207]\n"
"buffer_load_dwordx4 v[116:119], v13, s[16:19], s35 offen sc1\n"
"v_perm_b32 v31, v61, v57, s34\n"
"v_mfma_f32_16x16x32_bf16 v[144:147], v[24:27], a[240:243], v[144:147]\n"
"v_perm_b32 v16, v38, v34, s33\n"
"v_mfma_f32_16x16x32_bf16 v[148:151], v[24:27], a[244:247], v[148:151]\n"
"ds_read_b128 a[224:227], v1 offset:16384\n"
"v_perm_b32 v17, v46, v42, s33\n"
"v_mfma_f32_16x16x32_bf16 v[208:211], v[24:27], a[248:251], v[208:211]\n"
"buffer_load_dwordx4 v[120:123], v14, s[16:19], s35 offen sc1\n"
"v_perm_b32 v18, v54, v50, s33\n"
"v_mfma_f32_16x16x32_bf16 v[212:215], v[24:27], a[252:255], v[212:215]\n"
"v_perm_b32 v19, v62, v58, s33\n"
"v_mfma_f32_16x16x32_bf16 v[152:155], v[28:31], a[240:243], v[152:155]\n"
"v_perm_b32 v20, v38, v34, s34\n"
"v_mfma_f32_16x16x32_bf16 v[156:159], v[28:31], a[244:247], v[156:159]\n"
"buffer_load_dwordx4 v[124:127], v15, s[16:19], s35 offen sc1\n"
"v_perm_b32 v21, v46, v42, s34\n"
"v_mfma_f32_16x16x32_bf16 v[216:219], v[28:31], a[248:251], v[216:219]\n"
"ds_read_b128 a[228:231], v1 offset:17408\n"
"v_perm_b32 v22, v54, v50, s34\n"
"v_mfma_f32_16x16x32_bf16 v[220:223], v[28:31], a[252:255], v[220:223]\n"
"v_perm_b32 v23, v62, v58, s34\n"
"v_mfma_f32_16x16x32_bf16 v[160:163], v[16:19], a[240:243], v[160:163]\n"
"v_perm_b32 v24, v39, v35, s33\n"
"v_mfma_f32_16x16x32_bf16 v[164:167], v[16:19], a[244:247], v[164:167]\n"
"ds_read_b128 a[232:235], v1 offset:18432\n"
"v_perm_b32 v25, v47, v43, s33\n"
"v_mfma_f32_16x16x32_bf16 v[224:227], v[16:19], a[248:251], v[224:227]\n"
"v_perm_b32 v26, v55, v51, s33\n"
"v_mfma_f32_16x16x32_bf16 v[228:231], v[16:19], a[252:255], v[228:231]\n"
"v_perm_b32 v27, v63, v59, s33\n"
"v_mfma_f32_16x16x32_bf16 v[168:171], v[20:23], a[240:243], v[168:171]\n"
"v_perm_b32 v28, v39, v35, s34\n"
"v_mfma_f32_16x16x32_bf16 v[172:175], v[20:23], a[244:247], v[172:175]\n"
"ds_read_b128 a[236:239], v1 offset:19456\n"
"v_perm_b32 v29, v47, v43, s34\n"
"v_mfma_f32_16x16x32_bf16 v[232:235], v[20:23], a[248:251], v[232:235]\n"
"v_perm_b32 v30, v55, v51, s34\n"
"v_mfma_f32_16x16x32_bf16 v[236:239], v[20:23], a[252:255], v[236:239]\n"
"v_perm_b32 v31, v63, v59, s34\n"
"s_add_u32 s35, s35, 0x40000\n"
"s_and_b32 s35, s35, 0x3fffff\n"
"v_mfma_f32_16x16x32_bf16 v[176:179], v[24:27], a[240:243], v[176:179]\n"
"buffer_load_dwordx4 v[32:35], v8, s[16:19], s35 offen sc1\n"
"s_waitcnt vmcnt(9)\n"
"v_perm_b32 v16, v68, v64, s33\n"
"v_mfma_f32_16x16x32_bf16 v[180:183], v[24:27], a[244:247], v[180:183]\n"
"v_perm_b32 v17, v76, v72, s33\n"
"v_mfma_f32_16x16x32_bf16 v[240:243], v[24:27], a[248:251], v[240:243]\n"
"v_perm_b32 v18, v84, v80, s33\n"
"v_mfma_f32_16x16x32_bf16 v[244:247], v[24:27], a[252:255], v[244:247]\n"
"buffer_load_dwordx4 v[36:39], v9, s[16:19], s35 offen sc1\n"
"v_perm_b32 v19, v92, v88, s33\n"
"v_mfma_f32_16x16x32_bf16 v[184:187], v[28:31], a[240:243], v[184:187]\n"
"v_perm_b32 v20, v68, v64, s34\n"
"v_mfma_f32_16x16x32_bf16 v[188:191], v[28:31], a[244:247], v[188:191]\n"
"v_perm_b32 v21, v76, v72, s34\n"
"v_mfma_f32_16x16x32_bf16 v[248:251], v[28:31], a[248:251], v[248:251]\n"
"buffer_load_dwordx4 v[40:43], v10, s[16:19], s35 offen sc1\n"
"v_perm_b32 v22, v84, v80, s34\n"
"v_mfma_f32_16x16x32_bf16 v[252:255], v[28:31], a[252:255], v[252:255]\n"
"v_perm_b32 v23, v92, v88, s34\n"
"; q ks=4\n"
"s_waitcnt lgkmcnt(0)\n"
"v_mfma_f32_16x16x32_bf16 v[128:131], v[16:19], a[224:227], v[128:131]\n"
"v_perm_b32 v24, v69, v65, s33\n"
"s_lshl_b32 s79, s7, 11\n"
"v_mfma_f32_16x16x32_bf16 v[132:135], v[16:19], a[228:231], v[132:135]\n"
"buffer_load_dwordx4 v[44:47], v11, s[16:19], s35 offen sc1\n"
"v_perm_b32 v25, v77, v73, s33\n"
"v_mfma_f32_16x16x32_bf16 v[192:195], v[16:19], a[232:235], v[192:195]\n"
"v_perm_b32 v26, v85, v81, s33\n"
"s_lshl_b32 s74, s82, 15\n"
"v_mfma_f32_16x16x32_bf16 v[196:199], v[16:19], a[236:239], v[196:199]\n"
"v_perm_b32 v27, v93, v89, s33\n"
"s_lshl_b32 s11, s8, 19\n"
"v_mfma_f32_16x16x32_bf16 v[136:139], v[20:23], a[224:227], v[136:139]\n"
"buffer_load_dwordx4 v[48:51], v12, s[16:19], s35 offen sc1\n"
"v_perm_b32 v28, v69, v65, s34\n"
"v_mfma_f32_16x16x32_bf16 v[140:143], v[20:23], a[228:231], v[140:143]\n"
"v_perm_b32 v29, v77, v73, s34\n"
"s_add_u32 s68, s52, s11\n"
"s_addc_u32 s69, s53, 0\n"
"v_mfma_f32_16x16x32_bf16 v[200:203], v[20:23], a[232:235], v[200:203]\n"
"v_perm_b32 v30, v85, v81, s34\n"
"s_and_b32 s69, s69, 0xffff\n"
"v_mfma_f32_16x16x32_bf16 v[204:207], v[20:23], a[236:239], v[204:207]\n"
"buffer_load_dwordx4 v[52:55], v13, s[16:19], s35 offen sc1\n"
"v_perm_b32 v31, v93, v89, s34\n"
"v_mfma_f32_16x16x32_bf16 v[144:147], v[24:27], a[224:227], v[144:147]\n"
"v_perm_b32 v16, v70, v66, s33\n"
"s_mov_b32 s70, 0x80000\n"
"v_mfma_f32_16x16x32_bf16 v[148:151], v[24:27], a[228:231], v[148:151]\n"
"ds_read_b128 a[240:243], v1 offset:20480\n"
"v_perm_b32 v17, v78, v74, s33\n"
"v_mfma_f32_16x16x32_bf16 v[208:211], v[24:27], a[232:235], v[208:211]\n"
"buffer_load_dwordx4 v[56:59], v14, s[16:19], s35 offen sc1\n"
"v_perm_b32 v18, v86, v82, s33\n"
"v_mfma_f32_16x16x32_bf16 v[212:215], v[24:27], a[236:239], v[212:215]\n"
"v_perm_b32 v19, v94, v90, s33\n"
"s_mov_b32 s71, 0x20000\n"
"v_mfma_f32_16x16x32_bf16 v[152:155], v[28:31], a[224:227], v[152:155]\n"
"v_perm_b32 v20, v70, v66, s34\n"
"s_lshr_b32 s14, s6, 3\n"
"v_mfma_f32_16x16x32_bf16 v[156:159], v[28:31], a[228:231], v[156:159]\n"
"buffer_load_dwordx4 v[60:63], v15, s[16:19], s35 offen sc1\n"
"v_perm_b32 v21, v78, v74, s34\n"
"v_mfma_f32_16x16x32_bf16 v[216:219], v[28:31], a[232:235], v[216:219]\n"
"ds_read_b128 a[244:247], v1 offset:21504\n"
"v_perm_b32 v22, v86, v82, s34\n"
"v_mfma_f32_16x16x32_bf16 v[220:223], v[28:31], a[236:239], v[220:223]\n"
"v_perm_b32 v23, v94, v90, s34\n"
"s_lshl_b32 s14, s14, 2\n"
"v_mfma_f32_16x16x32_bf16 v[160:163], v[16:19], a[224:227], v[160:163]\n"
"v_perm_b32 v24, v71, v67, s33\n"
"s_lshl_b32 s15, s7, 0\n"
"v_mfma_f32_16x16x32_bf16 v[164:167], v[16:19], a[228:231], v[164:167]\n"
"ds_read_b128 a[248:251], v1 offset:22528\n"
"v_perm_b32 v25, v79, v75, s33\n"
"v_mfma_f32_16x16x32_bf16 v[224:227], v[16:19], a[232:235], v[224:227]\n"
"v_perm_b32 v26, v87, v83, s33\n"
"s_add_u32 s14, s14, s15\n"
"v_mfma_f32_16x16x32_bf16 v[228:231], v[16:19], a[236:239], v[228:231]\n"
"v_perm_b32 v27, v95, v91, s33\n"
"s_lshl_b32 s14, s14, 12\n"
"v_mfma_f32_16x16x32_bf16 v[168:171], v[20:23], a[224:227], v[168:171]\n"
"v_perm_b32 v28, v71, v67, s34\n"
"s_lshl_b32 s11, s87, 9\n"
"v_mfma_f32_16x16x32_bf16 v[172:175], v[20:23], a[228:231], v[172:175]\n"
"ds_read_b128 a[252:255], v1 offset:23552\n"
"v_perm_b32 v29, v79, v75, s34\n"
"v_mfma_f32_16x16x32_bf16 v[232:235], v[20:23], a[232:235], v[232:235]\n"
"v_perm_b32 v30, v87, v83, s34\n"
"s_add_u32 s88, s54, s11\n"
"s_addc_u32 s89, s55, 0\n"
"v_mfma_f32_16x16x32_bf16 v[236:239], v[20:23], a[236:239], v[236:239]\n"
"v_perm_b32 v31, v95, v91, s34\n"
"s_add_u32 s35, s35, 0x40000\n"
"s_and_b32 s35, s35, 0x3fffff\n"
"v_mfma_f32_16x16x32_bf16 v[176:179], v[24:27], a[224:227], v[176:179]\n"
"buffer_load_dwordx4 v[64:67], v8, s[16:19], s35 offen sc1\n"
"s_waitcnt vmcnt(9)\n"
"v_perm_b32 v16, v100, v96, s33\n"
"v_mfma_f32_16x16x32_bf16 v[180:183], v[24:27], a[228:231], v[180:183]\n"
"v_perm_b32 v17, v108, v104, s33\n"
"s_and_b32 s89, s89, 0xffff\n"
"v_mfma_f32_16x16x32_bf16 v[240:243], v[24:27], a[232:235], v[240:243]\n"
"v_perm_b32 v18, v116, v112, s33\n"
"s_mov_b32 s90, 0x1bf400\n"
"v_mfma_f32_16x16x32_bf16 v[244:247], v[24:27], a[236:239], v[244:247]\n"
"buffer_load_dwordx4 v[68:71], v9, s[16:19], s35 offen sc1\n"
"v_perm_b32 v19, v124, v120, s33\n"
"v_mfma_f32_16x16x32_bf16 v[184:187], v[28:31], a[224:227], v[184:187]\n"
"v_perm_b32 v20, v100, v96, s34\n"
"s_mov_b32 s91, 0x20000\n"
"v_mfma_f32_16x16x32_bf16 v[188:191], v[28:31], a[228:231], v[188:191]\n"
"v_perm_b32 v21, v108, v104, s34\n"
"s_add_u32 s92, s56, s11\n"
"s_addc_u32 s93, s57, 0\n"
"v_mfma_f32_16x16x32_bf16 v[248:251], v[28:31], a[232:235], v[248:251]\n"
"buffer_load_dwordx4 v[72:75], v10, s[16:19], s35 offen sc1\n"
"v_perm_b32 v22, v116, v112, s34\n"
"v_mfma_f32_16x16x32_bf16 v[252:255], v[28:31], a[236:239], v[252:255]\n"
"v_perm_b32 v23, v124, v120, s34\n"
"; q ks=5\n"
"s_waitcnt lgkmcnt(0)\n"
"v_mfma_f32_16x16x32_bf16 v[128:131], v[16:19], a[240:243], v[128:131]\n"
"v_perm_b32 v24, v101, v97, s33\n"
"s_and_b32 s93, s93, 0xffff\n"
"v_mfma_f32_16x16x32_bf16 v[132:135], v[16:19], a[244:247], v[132:135]\n"
"buffer_load_dwordx4 v[76:79], v11, s[16:19], s35 offen sc1\n"
"v_perm_b32 v25, v109, v105, s33\n"
"v_mfma_f32_16x16x32_bf16 v[192:195], v[16:19], a[248:251], v[192:195]\n"
"v_perm_b32 v26, v117, v113, s33\n"
"s_mov_b32 s94, 0x1bf400\n"
"v_mfma_f32_16x16x32_bf16 v[196:199], v[16:19], a[252:255], v[196:199]\n"
"v_perm_b32 v27, v125, v121, s33\n"
"s_mov_b32 s95, 0x20000\n"
"v_mfma_f32_16x16x32_bf16 v[136:139], v[20:23], a[240:243], v[136:139]\n"
"buffer_load_dwordx4 v[80:83], v12, s[16:19], s35 offen sc1\n"
"v_perm_b32 v28, v101, v97, s34\n"
"v_mfma_f32_16x16x32_bf16 v[140:143], v[20:23], a[244:247], v[140:143]\n"
"v_perm_b32 v29, v109, v105, s34\n"
"s_lshr_b32 s13, s6, 3\n"
"v_mfma_f32_16x16x32_bf16 v[200:203], v[20:23], a[248:251], v[200:203]\n"
"v_perm_b32 v30, v117, v113, s34\n"
"s_lshl_b32 s13, s13, 2\n"
"v_mfma_f32_16x16x32_bf16 v[204:207], v[20:23], a[252:255], v[204:207]\n"
"buffer_load_dwordx4 v[84:87], v13, s[16:19], s35 offen sc1\n"
"v_perm_b32 v31, v125, v121, s34\n"
"v_mfma_f32_16x16x32_bf16 v[144:147], v[24:27], a[240:243], v[144:147]\n"
"v_perm_b32 v16, v102, v98, s33\n"
"s_lshl_b32 s15, s7, 0\n"
"v_mfma_f32_16x16x32_bf16 v[148:151], v[24:27], a[244:247], v[148:151]\n"
"ds_read_b128 a[224:227], v1 offset:24576\n"
"v_perm_b32 v17, v110, v106, s33\n"
"v_mfma_f32_16x16x32_bf16 v[208:211], v[24:27], a[248:251], v[208:211]\n"
"buffer_load_dwordx4 v[88:91], v14, s[16:19], s35 offen sc1\n"
"v_perm_b32 v18, v118, v114, s33\n"
"v_mfma_f32_16x16x32_bf16 v[212:215], v[24:27], a[252:255], v[212:215]\n"
"v_perm_b32 v19, v126, v122, s33\n"
"s_add_u32 s13, s13, s15\n"
"v_mfma_f32_16x16x32_bf16 v[152:155], v[28:31], a[240:243], v[152:155]\n"
"v_perm_b32 v20, v102, v98, s34\n"
"s_lshl_b32 s12, s13, 11\n"
"v_mfma_f32_16x16x32_bf16 v[156:159], v[28:31], a[244:247], v[156:159]\n"
"buffer_load_dwordx4 v[92:95], v15, s[16:19], s35 offen sc1\n"
"v_perm_b32 v21, v110, v106, s34\n"
"v_mfma_f32_16x16x32_bf16 v[216:219], v[28:31], a[248:251], v[216:219]\n"
"ds_read_b128 a[228:231], v1 offset:25600\n"
"v_perm_b32 v22, v118, v114, s34\n"
"v_mfma_f32_16x16x32_bf16 v[220:223], v[28:31], a[252:255], v[220:223]\n"
"v_perm_b32 v23, v126, v122, s34\n"
"s_mul_i32 s13, s13, 0x3800\n"
"v_mfma_f32_16x16x32_bf16 v[160:163], v[16:19], a[240:243], v[160:163]\n"
"v_perm_b32 v24, v103, v99, s33\n"
"s_lshl_b32 s65, s87, 18\n"
"v_mfma_f32_16x16x32_bf16 v[164:167], v[16:19], a[244:247], v[164:167]\n"
"ds_read_b128 a[232:235], v1 offset:26624\n"
"v_perm_b32 v25, v111, v107, s33\n"
"v_mfma_f32_16x16x32_bf16 v[224:227], v[16:19], a[248:251], v[224:227]\n"
"v_perm_b32 v26, v119, v115, s33\n"
"v_mfma_f32_16x16x32_bf16 v[228:231], v[16:19], a[252:255], v[228:231]\n"
"v_perm_b32 v27, v127, v123, s33\n"
"v_mfma_f32_16x16x32_bf16 v[168:171], v[20:23], a[240:243], v[168:171]\n"
"v_perm_b32 v28, v103, v99, s34\n"
"v_mfma_f32_16x16x32_bf16 v[172:175], v[20:23], a[244:247], v[172:175]\n"
"ds_read_b128 a[236:239], v1 offset:27648\n"
"v_perm_b32 v29, v111, v107, s34\n"
"v_mfma_f32_16x16x32_bf16 v[232:235], v[20:23], a[248:251], v[232:235]\n"
"v_perm_b32 v30, v119, v115, s34\n"
"v_mfma_f32_16x16x32_bf16 v[236:239], v[20:23], a[252:255], v[236:239]\n"
"v_perm_b32 v31, v127, v123, s34\n"
"s_add_u32 s35, s35, 0x40000\n"
"s_and_b32 s35, s35, 0x3fffff\n"
"v_mfma_f32_16x16x32_bf16 v[176:179], v[24:27], a[240:243], v[176:179]\n"
"buffer_load_dwordx4 v[96:99], v8, s[16:19], s35 offen sc1\n"
"s_waitcnt vmcnt(9)\n"
"v_perm_b32 v16, v36, v32, s33\n"
"v_mfma_f32_16x16x32_bf16 v[180:183], v[24:27], a[244:247], v[180:183]\n"
"v_perm_b32 v17, v44, v40, s33\n"
"v_mfma_f32_16x16x32_bf16 v[240:243], v[24:27], a[248:251], v[240:243]\n"
"v_perm_b32 v18, v52, v48, s33\n"
"v_mfma_f32_16x16x32_bf16 v[244:247], v[24:27], a[252:255], v[244:247]\n"
"buffer_load_dwordx4 v[100:103], v9, s[16:19], s35 offen sc1\n"
"v_perm_b32 v19, v60, v56, s33\n"
"v_mfma_f32_16x16x32_bf16 v[184:187], v[28:31], a[240:243], v[184:187]\n"
"v_perm_b32 v20, v36, v32, s34\n"
"v_mfma_f32_16x16x32_bf16 v[188:191], v[28:31], a[244:247], v[188:191]\n"
"v_perm_b32 v21, v44, v40, s34\n"
"v_mfma_f32_16x16x32_bf16 v[248:251], v[28:31], a[248:251], v[248:251]\n"
"buffer_load_dwordx4 v[104:107], v10, s[16:19], s35 offen sc1\n"
"v_perm_b32 v22, v52, v48, s34\n"
"v_mfma_f32_16x16x32_bf16 v[252:255], v[28:31], a[252:255], v[252:255]\n"
"v_perm_b32 v23, v60, v56, s34\n"
"; q ks=6\n"
"s_waitcnt lgkmcnt(0)\n"
"v_mfma_f32_16x16x32_bf16 v[128:131], v[16:19], a[224:227], v[128:131]\n"
"v_perm_b32 v24, v37, v33, s33\n"
"v_mfma_f32_16x16x32_bf16 v[132:135], v[16:19], a[228:231], v[132:135]\n"
"buffer_load_dwordx4 v[108:111], v11, s[16:19], s35 offen sc1\n"
"v_perm_b32 v25, v45, v41, s33\n"
"v_mfma_f32_16x16x32_bf16 v[192:195], v[16:19], a[232:235], v[192:195]\n"
"v_perm_b32 v26, v53, v49, s33\n"
"v_mfma_f32_16x16x32_bf16 v[196:199], v[16:19], a[236:239], v[196:199]\n"
"v_perm_b32 v27, v61, v57, s33\n"
"v_mfma_f32_16x16x32_bf16 v[136:139], v[20:23], a[224:227], v[136:139]\n"
"buffer_load_dwordx4 v[112:115], v12, s[16:19], s35 offen sc1\n"
"v_perm_b32 v28, v37, v33, s34\n"
"v_mfma_f32_16x16x32_bf16 v[140:143], v[20:23], a[228:231], v[140:143]\n"
"v_perm_b32 v29, v45, v41, s34\n"
"v_mfma_f32_16x16x32_bf16 v[200:203], v[20:23], a[232:235], v[200:203]\n"
"v_perm_b32 v30, v53, v49, s34\n"
"v_mfma_f32_16x16x32_bf16 v[204:207], v[20:23], a[236:239], v[204:207]\n"
"buffer_load_dwordx4 v[116:119], v13, s[16:19], s35 offen sc1\n"
"v_perm_b32 v31, v61, v57, s34\n"
"v_mfma_f32_16x16x32_bf16 v[144:147], v[24:27], a[224:227], v[144:147]\n"
"v_perm_b32 v16, v38, v34, s33\n"
"v_mfma_f32_16x16x32_bf16 v[148:151], v[24:27], a[228:231], v[148:151]\n"
"ds_read_b128 a[240:243], v1 offset:28672\n"
"v_perm_b32 v17, v46, v42, s33\n"
"v_mfma_f32_16x16x32_bf16 v[208:211], v[24:27], a[232:235], v[208:211]\n"
"buffer_load_dwordx4 v[120:123], v14, s[16:19], s35 offen sc1\n"
"v_perm_b32 v18, v54, v50, s33\n"
"v_mfma_f32_16x16x32_bf16 v[212:215], v[24:27], a[236:239], v[212:215]\n"
"v_perm_b32 v19, v62, v58, s33\n"
"v_mfma_f32_16x16x32_bf16 v[152:155], v[28:31], a[224:227], v[152:155]\n"
"v_perm_b32 v20, v38, v34, s34\n"
"v_mfma_f32_16x16x32_bf16 v[156:159], v[28:31], a[228:231], v[156:159]\n"
"buffer_load_dwordx4 v[124:127], v15, s[16:19], s35 offen sc1\n"
"v_perm_b32 v21, v46, v42, s34\n"
"v_mfma_f32_16x16x32_bf16 v[216:219], v[28:31], a[232:235], v[216:219]\n"
"ds_read_b128 a[244:247], v1 offset:29696\n"
"v_perm_b32 v22, v54, v50, s34\n"
"v_mfma_f32_16x16x32_bf16 v[220:223], v[28:31], a[236:239], v[220:223]\n"
"v_perm_b32 v23, v62, v58, s34\n"
"v_mfma_f32_16x16x32_bf16 v[160:163], v[16:19], a[224:227], v[160:163]\n"
"v_perm_b32 v24, v39, v35, s33\n"
"v_mfma_f32_16x16x32_bf16 v[164:167], v[16:19], a[228:231], v[164:167]\n"
"ds_read_b128 a[248:251], v1 offset:30720\n"
"v_perm_b32 v25, v47, v43, s33\n"
"v_mfma_f32_16x16x32_bf16 v[224:227], v[16:19], a[232:235], v[224:227]\n"
"v_perm_b32 v26, v55, v51, s33\n"
"v_mfma_f32_16x16x32_bf16 v[228:231], v[16:19], a[236:239], v[228:231]\n"
"v_perm_b32 v27, v63, v59, s33\n"
"v_mfma_f32_16x16x32_bf16 v[168:171], v[20:23], a[224:227], v[168:171]\n"
"v_perm_b32 v28, v39, v35, s34\n"
"v_mfma_f32_16x16x32_bf16 v[172:175], v[20:23], a[228:231], v[172:175]\n"
"ds_read_b128 a[252:255], v1 offset:31744\n"
"v_perm_b32 v29, v47, v43, s34\n"
"v_mfma_f32_16x16x32_bf16 v[232:235], v[20:23], a[232:235], v[232:235]\n"
"v_perm_b32 v30, v55, v51, s34\n"
"v_mfma_f32_16x16x32_bf16 v[236:239], v[20:23], a[236:239], v[236:239]\n"
"v_perm_b32 v31, v63, v59, s34\n"
"s_add_u32 s35, s35, 0x40000\n"
"s_and_b32 s35, s35, 0x3fffff\n"
"v_mfma_f32_16x16x32_bf16 v[176:179], v[24:27], a[224:227], v[176:179]\n"
"buffer_load_dwordx4 v[32:35], v8, s[16:19], s35 offen sc1\n"
"s_waitcnt vmcnt(9)\n"
"v_perm_b32 v16, v68, v64, s33\n"
"v_mfma_f32_16x16x32_bf16 v[180:183], v[24:27], a[228:231], v[180:183]\n"
"v_perm_b32 v17, v76, v72, s33\n"
"v_mfma_f32_16x16x32_bf16 v[240:243], v[24:27], a[232:235], v[240:243]\n"
"v_perm_b32 v18, v84, v80, s33\n"
"v_mfma_f32_16x16x32_bf16 v[244:247], v[24:27], a[236:239], v[244:247]\n"
"buffer_load_dwordx4 v[36:39], v9, s[16:19], s35 offen sc1\n"
"v_perm_b32 v19, v92, v88, s33\n"
"v_mfma_f32_16x16x32_bf16 v[184:187], v[28:31], a[224:227], v[184:187]\n"
"v_perm_b32 v20, v68, v64, s34\n"
"v_mfma_f32_16x16x32_bf16 v[188:191], v[28:31], a[228:231], v[188:191]\n"
"v_perm_b32 v21, v76, v72, s34\n"
"v_mfma_f32_16x16x32_bf16 v[248:251], v[28:31], a[232:235], v[248:251]\n"
"buffer_load_dwordx4 v[40:43], v10, s[16:19], s35 offen sc1\n"
"v_perm_b32 v22, v84, v80, s34\n"
"v_mfma_f32_16x16x32_bf16 v[252:255], v[28:31], a[236:239], v[252:255]\n"
"v_perm_b32 v23, v92, v88, s34\n"
"; q ks=7\n"
"s_waitcnt lgkmcnt(0)\n"
"v_mfma_f32_16x16x32_bf16 v[128:131], v[16:19], a[240:243], v[128:131]\n"
"v_perm_b32 v24, v69, v65, s33\n"
"v_mfma_f32_16x16x32_bf16 v[132:135], v[16:19], a[244:247], v[132:135]\n"
"buffer_load_dwordx4 v[44:47], v11, s[16:19], s35 offen sc1\n"
"v_perm_b32 v25, v77, v73, s33\n"
"v_mfma_f32_16x16x32_bf16 v[192:195], v[16:19], a[248:251], v[192:195]\n"
"v_perm_b32 v26, v85, v81, s33\n"
"v_mfma_f32_16x16x32_bf16 v[196:199], v[16:19], a[252:255], v[196:199]\n"
"v_perm_b32 v27, v93, v89, s33\n"
"v_mfma_f32_16x16x32_bf16 v[136:139], v[20:23], a[240:243], v[136:139]\n"
"buffer_load_dwordx4 v[48:51], v12, s[16:19], s35 offen sc1\n"
"v_perm_b32 v28, v69, v65, s34\n"
"v_mfma_f32_16x16x32_bf16 v[140:143], v[20:23], a[244:247], v[140:143]\n"
"v_perm_b32 v29, v77, v73, s34\n"
"v_mfma_f32_16x16x32_bf16 v[200:203], v[20:23], a[248:251], v[200:203]\n"
"v_perm_b32 v30, v85, v81, s34\n"
"v_mfma_f32_16x16x32_bf16 v[204:207], v[20:23], a[252:255], v[204:207]\n"
"buffer_load_dwordx4 v[52:55], v13, s[16:19], s35 offen sc1\n"
"v_perm_b32 v31, v93, v89, s34\n"
"v_mfma_f32_16x16x32_bf16 v[144:147], v[24:27], a[240:243], v[144:147]\n"
"v_perm_b32 v16, v70, v66, s33\n"
"v_mfma_f32_16x16x32_bf16 v[148:151], v[24:27], a[244:247], v[148:151]\n"
"ds_read_b128 a[224:227], v1 offset:32768\n"
"v_perm_b32 v17, v78, v74, s33\n"
"v_mfma_f32_16x16x32_bf16 v[208:211], v[24:27], a[248:251], v[208:211]\n"
"buffer_load_dwordx4 v[56:59], v14, s[16:19], s35 offen sc1\n"
"v_perm_b32 v18, v86, v82, s33\n"
"v_mfma_f32_16x16x32_bf16 v[212:215], v[24:27], a[252:255], v[212:215]\n"
"v_perm_b32 v19, v94, v90, s33\n"
"v_mfma_f32_16x16x32_bf16 v[152:155], v[28:31], a[240:243], v[152:155]\n"
"v_perm_b32 v20, v70, v66, s34\n"
"v_mfma_f32_16x16x32_bf16 v[156:159], v[28:31], a[244:247], v[156:159]\n"
"buffer_load_dwordx4 v[60:63], v15, s[16:19], s35 offen sc1\n"
"v_perm_b32 v21, v78, v74, s34\n"
"v_mfma_f32_16x16x32_bf16 v[216:219], v[28:31], a[248:251], v[216:219]\n"
"ds_read_b128 a[228:231], v1 offset:33792\n"
"v_perm_b32 v22, v86, v82, s34\n"
"v_mfma_f32_16x16x32_bf16 v[220:223], v[28:31], a[252:255], v[220:223]\n"
"v_perm_b32 v23, v94, v90, s34\n"
"v_mfma_f32_16x16x32_bf16 v[160:163], v[16:19], a[240:243], v[160:163]\n"
"v_perm_b32 v24, v71, v67, s33\n"
"v_mfma_f32_16x16x32_bf16 v[164:167], v[16:19], a[244:247], v[164:167]\n"
"ds_read_b128 a[232:235], v1 offset:34816\n"
"v_perm_b32 v25, v79, v75, s33\n"
"v_mfma_f32_16x16x32_bf16 v[224:227], v[16:19], a[248:251], v[224:227]\n"
"v_perm_b32 v26, v87, v83, s33\n"
"v_mfma_f32_16x16x32_bf16 v[228:231], v[16:19], a[252:255], v[228:231]\n"
"v_perm_b32 v27, v95, v91, s33\n"
"v_mfma_f32_16x16x32_bf16 v[168:171], v[20:23], a[240:243], v[168:171]\n"
"v_perm_b32 v28, v71, v67, s34\n"
"v_mfma_f32_16x16x32_bf16 v[172:175], v[20:23], a[244:247], v[172:175]\n"
"ds_read_b128 a[236:239], v1 offset:35840\n"
"v_perm_b32 v29, v79, v75, s34\n"
"v_mfma_f32_16x16x32_bf16 v[232:235], v[20:23], a[248:251], v[232:235]\n"
"v_perm_b32 v30, v87, v83, s34\n"
"v_mfma_f32_16x16x32_bf16 v[236:239], v[20:23], a[252:255], v[236:239]\n"
"v_perm_b32 v31, v95, v91, s34\n"
"s_add_u32 s35, s35, 0x40000\n"
"s_and_b32 s35, s35, 0x3fffff\n"
"v_mfma_f32_16x16x32_bf16 v[176:179], v[24:27], a[240:243], v[176:179]\n"
"buffer_load_dwordx4 v[64:67], v8, s[16:19], s35 offen sc1\n"
"s_waitcnt vmcnt(9)\n"
"v_perm_b32 v16, v100, v96, s33\n"
"v_mfma_f32_16x16x32_bf16 v[180:183], v[24:27], a[244:247], v[180:183]\n"
"v_perm_b32 v17, v108, v104, s33\n"
"v_mfma_f32_16x16x32_bf16 v[240:243], v[24:27], a[248:251], v[240:243]\n"
"v_perm_b32 v18, v116, v112, s33\n"
"v_mfma_f32_16x16x32_bf16 v[244:247], v[24:27], a[252:255], v[244:247]\n"
"buffer_load_dwordx4 v[68:71], v9, s[16:19], s35 offen sc1\n"
"v_perm_b32 v19, v124, v120, s33\n"
"v_mfma_f32_16x16x32_bf16 v[184:187], v[28:31], a[240:243], v[184:187]\n"
"v_perm_b32 v20, v100, v96, s34\n"
"v_mfma_f32_16x16x32_bf16 v[188:191], v[28:31], a[244:247], v[188:191]\n"
"v_perm_b32 v21, v108, v104, s34\n"
"v_mfma_f32_16x16x32_bf16 v[248:251], v[28:31], a[248:251], v[248:251]\n"
"buffer_load_dwordx4 v[72:75], v10, s[16:19], s35 offen sc1\n"
"v_perm_b32 v22, v116, v112, s34\n"
"v_mfma_f32_16x16x32_bf16 v[252:255], v[28:31], a[252:255], v[252:255]\n"
"v_perm_b32 v23, v124, v120, s34\n"
"; q ks=8\n"
"s_waitcnt lgkmcnt(0)\n"
"v_mfma_f32_16x16x32_bf16 v[128:131], v[16:19], a[224:227], v[128:131]\n"
"v_perm_b32 v24, v101, v97, s33\n"
"v_mfma_f32_16x16x32_bf16 v[132:135], v[16:19], a[228:231], v[132:135]\n"
"buffer_load_dwordx4 v[76:79], v11, s[16:19], s35 offen sc1\n"
"v_perm_b32 v25, v109, v105, s33\n"
"v_mfma_f32_16x16x32_bf16 v[192:195], v[16:19], a[232:235], v[192:195]\n"
"v_perm_b32 v26, v117, v113, s33\n"
"v_mfma_f32_16x16x32_bf16 v[196:199], v[16:19], a[236:239], v[196:199]\n"
"v_perm_b32 v27, v125, v121, s33\n"
"v_mfma_f32_16x16x32_bf16 v[136:139], v[20:23], a[224:227], v[136:139]\n"
"buffer_load_dwordx4 v[80:83], v12, s[16:19], s35 offen sc1\n"
"v_perm_b32 v28, v101, v97, s34\n"
"v_mfma_f32_16x16x32_bf16 v[140:143], v[20:23], a[228:231], v[140:143]\n"
"v_perm_b32 v29, v109, v105, s34\n"
"v_mfma_f32_16x16x32_bf16 v[200:203], v[20:23], a[232:235], v[200:203]\n"
"v_perm_b32 v30, v117, v113, s34\n"
"v_mfma_f32_16x16x32_bf16 v[204:207], v[20:23], a[236:239], v[204:207]\n"
"buffer_load_dwordx4 v[84:87], v13, s[16:19], s35 offen sc1\n"
"v_perm_b32 v31, v125, v121, s34\n"
"v_mfma_f32_16x16x32_bf16 v[144:147], v[24:27], a[224:227], v[144:147]\n"
"v_perm_b32 v16, v102, v98, s33\n"
"v_mfma_f32_16x16x32_bf16 v[148:151], v[24:27], a[228:231], v[148:151]\n"
"ds_read_b128 a[240:243], v1 offset:36864\n"
"v_perm_b32 v17, v110, v106, s33\n"
"v_mfma_f32_16x16x32_bf16 v[208:211], v[24:27], a[232:235], v[208:211]\n"
"buffer_load_dwordx4 v[88:91], v14, s[16:19], s35 offen sc1\n"
"v_perm_b32 v18, v118, v114, s33\n"
"v_mfma_f32_16x16x32_bf16 v[212:215], v[24:27], a[236:239], v[212:215]\n"
"v_perm_b32 v19, v126, v122, s33\n"
"v_mfma_f32_16x16x32_bf16 v[152:155], v[28:31], a[224:227], v[152:155]\n"
"v_perm_b32 v20, v102, v98, s34\n"
"v_mfma_f32_16x16x32_bf16 v[156:159], v[28:31], a[228:231], v[156:159]\n"
"buffer_load_dwordx4 v[92:95], v15, s[16:19], s35 offen sc1\n"
"v_perm_b32 v21, v110, v106, s34\n"
"v_mfma_f32_16x16x32_bf16 v[216:219], v[28:31], a[232:235], v[216:219]\n"
"ds_read_b128 a[244:247], v1 offset:37888\n"
"v_perm_b32 v22, v118, v114, s34\n"
"v_mfma_f32_16x16x32_bf16 v[220:223], v[28:31], a[236:239], v[220:223]\n"
"v_perm_b32 v23, v126, v122, s34\n"
"v_mfma_f32_16x16x32_bf16 v[160:163], v[16:19], a[224:227], v[160:163]\n"
"v_perm_b32 v24, v103, v99, s33\n"
"v_mfma_f32_16x16x32_bf16 v[164:167], v[16:19], a[228:231], v[164:167]\n"
"ds_read_b128 a[248:251], v1 offset:38912\n"
"v_perm_b32 v25, v111, v107, s33\n"
"v_mfma_f32_16x16x32_bf16 v[224:227], v[16:19], a[232:235], v[224:227]\n"
"v_perm_b32 v26, v119, v115, s33\n"
"v_mfma_f32_16x16x32_bf16 v[228:231], v[16:19], a[236:239], v[228:231]\n"
"v_perm_b32 v27, v127, v123, s33\n"
"v_mfma_f32_16x16x32_bf16 v[168:171], v[20:23], a[224:227], v[168:171]\n"
"v_perm_b32 v28, v103, v99, s34\n"
"v_mfma_f32_16x16x32_bf16 v[172:175], v[20:23], a[228:231], v[172:175]\n"
"ds_read_b128 a[252:255], v1 offset:39936\n"
"v_perm_b32 v29, v111, v107, s34\n"
"v_mfma_f32_16x16x32_bf16 v[232:235], v[20:23], a[232:235], v[232:235]\n"
"v_perm_b32 v30, v119, v115, s34\n"
"v_mfma_f32_16x16x32_bf16 v[236:239], v[20:23], a[236:239], v[236:239]\n"
"v_perm_b32 v31, v127, v123, s34\n"
"s_add_u32 s35, s35, 0x40000\n"
"s_and_b32 s35, s35, 0x3fffff\n"
"v_mfma_f32_16x16x32_bf16 v[176:179], v[24:27], a[224:227], v[176:179]\n"
"buffer_load_dwordx4 v[96:99], v8, s[16:19], s35 offen sc1\n"
"s_waitcnt vmcnt(9)\n"
"v_perm_b32 v16, v36, v32, s33\n"
"v_mfma_f32_16x16x32_bf16 v[180:183], v[24:27], a[228:231], v[180:183]\n"
"v_perm_b32 v17, v44, v40, s33\n"
"v_mfma_f32_16x16x32_bf16 v[240:243], v[24:27], a[232:235], v[240:243]\n"
"v_perm_b32 v18, v52, v48, s33\n"
"v_mfma_f32_16x16x32_bf16 v[244:247], v[24:27], a[236:239], v[244:247]\n"
"buffer_load_dwordx4 v[100:103], v9, s[16:19], s35 offen sc1\n"
"v_perm_b32 v19, v60, v56, s33\n"
"v_mfma_f32_16x16x32_bf16 v[184:187], v[28:31], a[224:227], v[184:187]\n"
"v_perm_b32 v20, v36, v32, s34\n"
"v_mfma_f32_16x16x32_bf16 v[188:191], v[28:31], a[228:231], v[188:191]\n"
"v_perm_b32 v21, v44, v40, s34\n"
"v_mfma_f32_16x16x32_bf16 v[248:251], v[28:31], a[232:235], v[248:251]\n"
"buffer_load_dwordx4 v[104:107], v10, s[16:19], s35 offen sc1\n"
"v_perm_b32 v22, v52, v48, s34\n"
"v_mfma_f32_16x16x32_bf16 v[252:255], v[28:31], a[236:239], v[252:255]\n"
"v_perm_b32 v23, v60, v56, s34\n"
"; q ks=9\n"
"s_waitcnt lgkmcnt(0)\n"
"v_mfma_f32_16x16x32_bf16 v[128:131], v[16:19], a[240:243], v[128:131]\n"
"v_perm_b32 v24, v37, v33, s33\n"
"v_mfma_f32_16x16x32_bf16 v[132:135], v[16:19], a[244:247], v[132:135]\n"
"buffer_load_dwordx4 v[108:111], v11, s[16:19], s35 offen sc1\n"
"v_perm_b32 v25, v45, v41, s33\n"
"v_mfma_f32_16x16x32_bf16 v[192:195], v[16:19], a[248:251], v[192:195]\n"
"v_perm_b32 v26, v53, v49, s33\n"
"v_mfma_f32_16x16x32_bf16 v[196:199], v[16:19], a[252:255], v[196:199]\n"
"v_perm_b32 v27, v61, v57, s33\n"
"v_mfma_f32_16x16x32_bf16 v[136:139], v[20:23], a[240:243], v[136:139]\n"
"buffer_load_dwordx4 v[112:115], v12, s[16:19], s35 offen sc1\n"
"v_perm_b32 v28, v37, v33, s34\n"
"v_mfma_f32_16x16x32_bf16 v[140:143], v[20:23], a[244:247], v[140:143]\n"
"v_perm_b32 v29, v45, v41, s34\n"
"v_mfma_f32_16x16x32_bf16 v[200:203], v[20:23], a[248:251], v[200:203]\n"
"v_perm_b32 v30, v53, v49, s34\n"
"v_mfma_f32_16x16x32_bf16 v[204:207], v[20:23], a[252:255], v[204:207]\n"
"buffer_load_dwordx4 v[116:119], v13, s[16:19], s35 offen sc1\n"
"v_perm_b32 v31, v61, v57, s34\n"
"v_mfma_f32_16x16x32_bf16 v[144:147], v[24:27], a[240:243], v[144:147]\n"
"v_perm_b32 v16, v38, v34, s33\n"
"v_mfma_f32_16x16x32_bf16 v[148:151], v[24:27], a[244:247], v[148:151]\n"
"ds_read_b128 a[224:227], v1 offset:40960\n"
"v_perm_b32 v17, v46, v42, s33\n"
"v_mfma_f32_16x16x32_bf16 v[208:211], v[24:27], a[248:251], v[208:211]\n"
"buffer_load_dwordx4 v[120:123], v14, s[16:19], s35 offen sc1\n"
"v_perm_b32 v18, v54, v50, s33\n"
"v_mfma_f32_16x16x32_bf16 v[212:215], v[24:27], a[252:255], v[212:215]\n"
"v_perm_b32 v19, v62, v58, s33\n"
"v_mfma_f32_16x16x32_bf16 v[152:155], v[28:31], a[240:243], v[152:155]\n"
"v_perm_b32 v20, v38, v34, s34\n"
"v_mfma_f32_16x16x32_bf16 v[156:159], v[28:31], a[244:247], v[156:159]\n"
"buffer_load_dwordx4 v[124:127], v15, s[16:19], s35 offen sc1\n"
"v_perm_b32 v21, v46, v42, s34\n"
"v_mfma_f32_16x16x32_bf16 v[216:219], v[28:31], a[248:251], v[216:219]\n"
"ds_read_b128 a[228:231], v1 offset:41984\n"
"v_perm_b32 v22, v54, v50, s34\n"
"v_mfma_f32_16x16x32_bf16 v[220:223], v[28:31], a[252:255], v[220:223]\n"
"v_perm_b32 v23, v62, v58, s34\n"
"v_mfma_f32_16x16x32_bf16 v[160:163], v[16:19], a[240:243], v[160:163]\n"
"v_perm_b32 v24, v39, v35, s33\n"
"v_mfma_f32_16x16x32_bf16 v[164:167], v[16:19], a[244:247], v[164:167]\n"
"ds_read_b128 a[232:235], v1 offset:43008\n"
"v_perm_b32 v25, v47, v43, s33\n"
"v_mfma_f32_16x16x32_bf16 v[224:227], v[16:19], a[248:251], v[224:227]\n"
"v_perm_b32 v26, v55, v51, s33\n"
"v_mfma_f32_16x16x32_bf16 v[228:231], v[16:19], a[252:255], v[228:231]\n"
"v_perm_b32 v27, v63, v59, s33\n"
"v_mfma_f32_16x16x32_bf16 v[168:171], v[20:23], a[240:243], v[168:171]\n"
"v_perm_b32 v28, v39, v35, s34\n"
"v_mfma_f32_16x16x32_bf16 v[172:175], v[20:23], a[244:247], v[172:175]\n"
"ds_read_b128 a[236:239], v1 offset:44032\n"
"v_perm_b32 v29, v47, v43, s34\n"
"v_mfma_f32_16x16x32_bf16 v[232:235], v[20:23], a[248:251], v[232:235]\n"
"v_perm_b32 v30, v55, v51, s34\n"
"v_mfma_f32_16x16x32_bf16 v[236:239], v[20:23], a[252:255], v[236:239]\n"
"v_perm_b32 v31, v63, v59, s34\n"
"s_add_u32 s35, s35, 0x40000\n"
"s_and_b32 s35, s35, 0x3fffff\n"
"v_mfma_f32_16x16x32_bf16 v[176:179], v[24:27], a[240:243], v[176:179]\n"
"buffer_load_dwordx4 v[32:35], v8, s[16:19], s35 offen sc1\n"
"s_waitcnt vmcnt(9)\n"
"v_perm_b32 v16, v68, v64, s33\n"
"v_mfma_f32_16x16x32_bf16 v[180:183], v[24:27], a[244:247], v[180:183]\n"
"v_perm_b32 v17, v76, v72, s33\n"
"v_mfma_f32_16x16x32_bf16 v[240:243], v[24:27], a[248:251], v[240:243]\n"
"v_perm_b32 v18, v84, v80, s33\n"
"v_mfma_f32_16x16x32_bf16 v[244:247], v[24:27], a[252:255], v[244:247]\n"
"buffer_load_dwordx4 v[36:39], v9, s[16:19], s35 offen sc1\n"
"v_perm_b32 v19, v92, v88, s33\n"
"v_mfma_f32_16x16x32_bf16 v[184:187], v[28:31], a[240:243], v[184:187]\n"
"v_perm_b32 v20, v68, v64, s34\n"
"v_mfma_f32_16x16x32_bf16 v[188:191], v[28:31], a[244:247], v[188:191]\n"
"v_perm_b32 v21, v76, v72, s34\n"
"v_mfma_f32_16x16x32_bf16 v[248:251], v[28:31], a[248:251], v[248:251]\n"
"buffer_load_dwordx4 v[40:43], v10, s[16:19], s35 offen sc1\n"
"v_perm_b32 v22, v84, v80, s34\n"
"v_mfma_f32_16x16x32_bf16 v[252:255], v[28:31], a[252:255], v[252:255]\n"
"v_perm_b32 v23, v92, v88, s34\n"
"; q ks=10\n"
"s_waitcnt lgkmcnt(0)\n"
"v_mfma_f32_16x16x32_bf16 v[128:131], v[16:19], a[224:227], v[128:131]\n"
"v_perm_b32 v24, v69, v65, s33\n"
"v_mfma_f32_16x16x32_bf16 v[132:135], v[16:19], a[228:231], v[132:135]\n"
"buffer_load_dwordx4 v[44:47], v11, s[16:19], s35 offen sc1\n"
"v_perm_b32 v25, v77, v73, s33\n"
"v_mfma_f32_16x16x32_bf16 v[192:195], v[16:19], a[232:235], v[192:195]\n"
"v_perm_b32 v26, v85, v81, s33\n"
"v_mfma_f32_16x16x32_bf16 v[196:199], v[16:19], a[236:239], v[196:199]\n"
"v_perm_b32 v27, v93, v89, s33\n"
"v_mfma_f32_16x16x32_bf16 v[136:139], v[20:23], a[224:227], v[136:139]\n"
"buffer_load_dwordx4 v[48:51], v12, s[16:19], s35 offen sc1\n"
"v_perm_b32 v28, v69, v65, s34\n"
"v_mfma_f32_16x16x32_bf16 v[140:143], v[20:23], a[228:231], v[140:143]\n"
"v_perm_b32 v29, v77, v73, s34\n"
"v_mfma_f32_16x16x32_bf16 v[200:203], v[20:23], a[232:235], v[200:203]\n"
"v_perm_b32 v30, v85, v81, s34\n"
"v_mfma_f32_16x16x32_bf16 v[204:207], v[20:23], a[236:239], v[204:207]\n"
"buffer_load_dwordx4 v[52:55], v13, s[16:19], s35 offen sc1\n"
"v_perm_b32 v31, v93, v89, s34\n"
"v_mfma_f32_16x16x32_bf16 v[144:147], v[24:27], a[224:227], v[144:147]\n"
"v_perm_b32 v16, v70, v66, s33\n"
"v_mfma_f32_16x16x32_bf16 v[148:151], v[24:27], a[228:231], v[148:151]\n"
"ds_read_b128 a[240:243], v1 offset:45056\n"
"v_perm_b32 v17, v78, v74, s33\n"
"v_mfma_f32_16x16x32_bf16 v[208:211], v[24:27], a[232:235], v[208:211]\n"
"buffer_load_dwordx4 v[56:59], v14, s[16:19], s35 offen sc1\n"
"v_perm_b32 v18, v86, v82, s33\n"
"v_mfma_f32_16x16x32_bf16 v[212:215], v[24:27], a[236:239], v[212:215]\n"
"v_perm_b32 v19, v94, v90, s33\n"
"v_mfma_f32_16x16x32_bf16 v[152:155], v[28:31], a[224:227], v[152:155]\n"
"v_perm_b32 v20, v70, v66, s34\n"
"v_mfma_f32_16x16x32_bf16 v[156:159], v[28:31], a[228:231], v[156:159]\n"
"buffer_load_dwordx4 v[60:63], v15, s[16:19], s35 offen sc1\n"
"v_perm_b32 v21, v78, v74, s34\n"
"v_mfma_f32_16x16x32_bf16 v[216:219], v[28:31], a[232:235], v[216:219]\n"
"ds_read_b128 a[244:247], v1 offset:46080\n"
"v_perm_b32 v22, v86, v82, s34\n"
"v_mfma_f32_16x16x32_bf16 v[220:223], v[28:31], a[236:239], v[220:223]\n"
"v_perm_b32 v23, v94, v90, s34\n"
"v_mfma_f32_16x16x32_bf16 v[160:163], v[16:19], a[224:227], v[160:163]\n"
"v_perm_b32 v24, v71, v67, s33\n"
"v_mfma_f32_16x16x32_bf16 v[164:167], v[16:19], a[228:231], v[164:167]\n"
"ds_read_b128 a[248:251], v1 offset:47104\n"
"v_perm_b32 v25, v79, v75, s33\n"
"v_mfma_f32_16x16x32_bf16 v[224:227], v[16:19], a[232:235], v[224:227]\n"
"v_perm_b32 v26, v87, v83, s33\n"
"v_mfma_f32_16x16x32_bf16 v[228:231], v[16:19], a[236:239], v[228:231]\n"
"v_perm_b32 v27, v95, v91, s33\n"
"v_mfma_f32_16x16x32_bf16 v[168:171], v[20:23], a[224:227], v[168:171]\n"
"v_perm_b32 v28, v71, v67, s34\n"
"v_mfma_f32_16x16x32_bf16 v[172:175], v[20:23], a[228:231], v[172:175]\n"
"ds_read_b128 a[252:255], v1 offset:48128\n"
"v_perm_b32 v29, v79, v75, s34\n"
"v_mfma_f32_16x16x32_bf16 v[232:235], v[20:23], a[232:235], v[232:235]\n"
"v_perm_b32 v30, v87, v83, s34\n"
"v_mfma_f32_16x16x32_bf16 v[236:239], v[20:23], a[236:239], v[236:239]\n"
"v_perm_b32 v31, v95, v91, s34\n"
"s_add_u32 s35, s35, 0x40000\n"
"s_and_b32 s35, s35, 0x3fffff\n"
"v_mfma_f32_16x16x32_bf16 v[176:179], v[24:27], a[224:227], v[176:179]\n"
"buffer_load_dwordx4 v[64:67], v8, s[16:19], s35 offen sc1\n"
"s_waitcnt vmcnt(9)\n"
"v_perm_b32 v16, v100, v96, s33\n"
"v_mfma_f32_16x16x32_bf16 v[180:183], v[24:27], a[228:231], v[180:183]\n"
"v_perm_b32 v17, v108, v104, s33\n"
"v_mfma_f32_16x16x32_bf16 v[240:243], v[24:27], a[232:235], v[240:243]\n"
"v_perm_b32 v18, v116, v112, s33\n"
"v_mfma_f32_16x16x32_bf16 v[244:247], v[24:27], a[236:239], v[244:247]\n"
"buffer_load_dwordx4 v[68:71], v9, s[16:19], s35 offen sc1\n"
"v_perm_b32 v19, v124, v120, s33\n"
"v_mfma_f32_16x16x32_bf16 v[184:187], v[28:31], a[224:227], v[184:187]\n"
"v_perm_b32 v20, v100, v96, s34\n"
"v_mfma_f32_16x16x32_bf16 v[188:191], v[28:31], a[228:231], v[188:191]\n"
"v_perm_b32 v21, v108, v104, s34\n"
"v_mfma_f32_16x16x32_bf16 v[248:251], v[28:31], a[232:235], v[248:251]\n"
"buffer_load_dwordx4 v[72:75], v10, s[16:19], s35 offen sc1\n"
"v_perm_b32 v22, v116, v112, s34\n"
"v_mfma_f32_16x16x32_bf16 v[252:255], v[28:31], a[236:239], v[252:255]\n"
"v_perm_b32 v23, v124, v120, s34\n"
"; q ks=11\n"
"s_waitcnt lgkmcnt(0)\n"
"v_mfma_f32_16x16x32_bf16 v[128:131], v[16:19], a[240:243], v[128:131]\n"
"v_perm_b32 v24, v101, v97, s33\n"
"v_mfma_f32_16x16x32_bf16 v[132:135], v[16:19], a[244:247], v[132:135]\n"
"buffer_load_dwordx4 v[76:79], v11, s[16:19], s35 offen sc1\n"
"v_perm_b32 v25, v109, v105, s33\n"
"v_mfma_f32_16x16x32_bf16 v[192:195], v[16:19], a[248:251], v[192:195]\n"
"v_perm_b32 v26, v117, v113, s33\n"
"v_mfma_f32_16x16x32_bf16 v[196:199], v[16:19], a[252:255], v[196:199]\n"
"v_perm_b32 v27, v125, v121, s33\n"
"v_mfma_f32_16x16x32_bf16 v[136:139], v[20:23], a[240:243], v[136:139]\n"
"buffer_load_dwordx4 v[80:83], v12, s[16:19], s35 offen sc1\n"
"v_perm_b32 v28, v101, v97, s34\n"
"v_mfma_f32_16x16x32_bf16 v[140:143], v[20:23], a[244:247], v[140:143]\n"
"v_perm_b32 v29, v109, v105, s34\n"
"v_mfma_f32_16x16x32_bf16 v[200:203], v[20:23], a[248:251], v[200:203]\n"
"v_perm_b32 v30, v117, v113, s34\n"
"v_mfma_f32_16x16x32_bf16 v[204:207], v[20:23], a[252:255], v[204:207]\n"
"buffer_load_dwordx4 v[84:87], v13, s[16:19], s35 offen sc1\n"
"v_perm_b32 v31, v125, v121, s34\n"
"v_mfma_f32_16x16x32_bf16 v[144:147], v[24:27], a[240:243], v[144:147]\n"
"v_perm_b32 v16, v102, v98, s33\n"
"v_mfma_f32_16x16x32_bf16 v[148:151], v[24:27], a[244:247], v[148:151]\n"
"ds_read_b128 a[224:227], v1 offset:49152\n"
"v_perm_b32 v17, v110, v106, s33\n"
"v_mfma_f32_16x16x32_bf16 v[208:211], v[24:27], a[248:251], v[208:211]\n"
"buffer_load_dwordx4 v[88:91], v14, s[16:19], s35 offen sc1\n"
"v_perm_b32 v18, v118, v114, s33\n"
"v_mfma_f32_16x16x32_bf16 v[212:215], v[24:27], a[252:255], v[212:215]\n"
"v_perm_b32 v19, v126, v122, s33\n"
"v_mfma_f32_16x16x32_bf16 v[152:155], v[28:31], a[240:243], v[152:155]\n"
"v_perm_b32 v20, v102, v98, s34\n"
"v_mfma_f32_16x16x32_bf16 v[156:159], v[28:31], a[244:247], v[156:159]\n"
"buffer_load_dwordx4 v[92:95], v15, s[16:19], s35 offen sc1\n"
"v_perm_b32 v21, v110, v106, s34\n"
"v_mfma_f32_16x16x32_bf16 v[216:219], v[28:31], a[248:251], v[216:219]\n"
"ds_read_b128 a[228:231], v1 offset:50176\n"
"v_perm_b32 v22, v118, v114, s34\n"
"v_mfma_f32_16x16x32_bf16 v[220:223], v[28:31], a[252:255], v[220:223]\n"
"v_perm_b32 v23, v126, v122, s34\n"
"v_mfma_f32_16x16x32_bf16 v[160:163], v[16:19], a[240:243], v[160:163]\n"
"v_perm_b32 v24, v103, v99, s33\n"
"v_mfma_f32_16x16x32_bf16 v[164:167], v[16:19], a[244:247], v[164:167]\n"
"ds_read_b128 a[232:235], v1 offset:51200\n"
"v_perm_b32 v25, v111, v107, s33\n"
"v_mfma_f32_16x16x32_bf16 v[224:227], v[16:19], a[248:251], v[224:227]\n"
"v_perm_b32 v26, v119, v115, s33\n"
"v_mfma_f32_16x16x32_bf16 v[228:231], v[16:19], a[252:255], v[228:231]\n"
"v_perm_b32 v27, v127, v123, s33\n"
"v_mfma_f32_16x16x32_bf16 v[168:171], v[20:23], a[240:243], v[168:171]\n"
"v_perm_b32 v28, v103, v99, s34\n"
"v_mfma_f32_16x16x32_bf16 v[172:175], v[20:23], a[244:247], v[172:175]\n"
"ds_read_b128 a[236:239], v1 offset:52224\n"
"v_perm_b32 v29, v111, v107, s34\n"
"v_mfma_f32_16x16x32_bf16 v[232:235], v[20:23], a[248:251], v[232:235]\n"
"v_perm_b32 v30, v119, v115, s34\n"
"v_mfma_f32_16x16x32_bf16 v[236:239], v[20:23], a[252:255], v[236:239]\n"
"v_perm_b32 v31, v127, v123, s34\n"
"s_add_u32 s35, s35, 0x40000\n"
"s_and_b32 s35, s35, 0x3fffff\n"
"v_mfma_f32_16x16x32_bf16 v[176:179], v[24:27], a[240:243], v[176:179]\n"
"buffer_load_dwordx4 v[96:99], v8, s[16:19], s35 offen sc1\n"
"s_waitcnt vmcnt(9)\n"
"v_perm_b32 v16, v36, v32, s33\n"
"v_mfma_f32_16x16x32_bf16 v[180:183], v[24:27], a[244:247], v[180:183]\n"
"v_perm_b32 v17, v44, v40, s33\n"
"v_mfma_f32_16x16x32_bf16 v[240:243], v[24:27], a[248:251], v[240:243]\n"
"v_perm_b32 v18, v52, v48, s33\n"
"v_mfma_f32_16x16x32_bf16 v[244:247], v[24:27], a[252:255], v[244:247]\n"
"buffer_load_dwordx4 v[100:103], v9, s[16:19], s35 offen sc1\n"
"v_perm_b32 v19, v60, v56, s33\n"
"v_mfma_f32_16x16x32_bf16 v[184:187], v[28:31], a[240:243], v[184:187]\n"
"v_perm_b32 v20, v36, v32, s34\n"
"v_mfma_f32_16x16x32_bf16 v[188:191], v[28:31], a[244:247], v[188:191]\n"
"v_perm_b32 v21, v44, v40, s34\n"
"v_mfma_f32_16x16x32_bf16 v[248:251], v[28:31], a[248:251], v[248:251]\n"
"buffer_load_dwordx4 v[104:107], v10, s[16:19], s35 offen sc1\n"
"v_perm_b32 v22, v52, v48, s34\n"
"v_mfma_f32_16x16x32_bf16 v[252:255], v[28:31], a[252:255], v[252:255]\n"
"v_perm_b32 v23, v60, v56, s34\n"
"; q ks=12\n"
"s_waitcnt lgkmcnt(0)\n"
"v_mfma_f32_16x16x32_bf16 v[128:131], v[16:19], a[224:227], v[128:131]\n"
"v_perm_b32 v24, v37, v33, s33\n"
"v_mfma_f32_16x16x32_bf16 v[132:135], v[16:19], a[228:231], v[132:135]\n"
"buffer_load_dwordx4 v[108:111], v11, s[16:19], s35 offen sc1\n"
"v_perm_b32 v25, v45, v41, s33\n"
"v_mfma_f32_16x16x32_bf16 v[192:195], v[16:19], a[232:235], v[192:195]\n"
"v_perm_b32 v26, v53, v49, s33\n"
"v_mfma_f32_16x16x32_bf16 v[196:199], v[16:19], a[236:239], v[196:199]\n"
"v_perm_b32 v27, v61, v57, s33\n"
"v_mfma_f32_16x16x32_bf16 v[136:139], v[20:23], a[224:227], v[136:139]\n"
"buffer_load_dwordx4 v[112:115], v12, s[16:19], s35 offen sc1\n"
"v_perm_b32 v28, v37, v33, s34\n"
"v_mfma_f32_16x16x32_bf16 v[140:143], v[20:23], a[228:231], v[140:143]\n"
"v_perm_b32 v29, v45, v41, s34\n"
"v_mfma_f32_16x16x32_bf16 v[200:203], v[20:23], a[232:235], v[200:203]\n"
"v_perm_b32 v30, v53, v49, s34\n"
"v_mfma_f32_16x16x32_bf16 v[204:207], v[20:23], a[236:239], v[204:207]\n"
"buffer_load_dwordx4 v[116:119], v13, s[16:19], s35 offen sc1\n"
"v_perm_b32 v31, v61, v57, s34\n"
"v_mfma_f32_16x16x32_bf16 v[144:147], v[24:27], a[224:227], v[144:147]\n"
"v_perm_b32 v16, v38, v34, s33\n"
"v_mfma_f32_16x16x32_bf16 v[148:151], v[24:27], a[228:231], v[148:151]\n"
"ds_read_b128 a[240:243], v1 offset:53248\n"
"v_perm_b32 v17, v46, v42, s33\n"
"v_mfma_f32_16x16x32_bf16 v[208:211], v[24:27], a[232:235], v[208:211]\n"
"buffer_load_dwordx4 v[120:123], v14, s[16:19], s35 offen sc1\n"
"v_perm_b32 v18, v54, v50, s33\n"
"v_mfma_f32_16x16x32_bf16 v[212:215], v[24:27], a[236:239], v[212:215]\n"
"v_perm_b32 v19, v62, v58, s33\n"
"v_mfma_f32_16x16x32_bf16 v[152:155], v[28:31], a[224:227], v[152:155]\n"
"v_perm_b32 v20, v38, v34, s34\n"
"v_mfma_f32_16x16x32_bf16 v[156:159], v[28:31], a[228:231], v[156:159]\n"
"buffer_load_dwordx4 v[124:127], v15, s[16:19], s35 offen sc1\n"
"v_perm_b32 v21, v46, v42, s34\n"
"v_mfma_f32_16x16x32_bf16 v[216:219], v[28:31], a[232:235], v[216:219]\n"
"ds_read_b128 a[244:247], v1 offset:54272\n"
"v_perm_b32 v22, v54, v50, s34\n"
"v_mfma_f32_16x16x32_bf16 v[220:223], v[28:31], a[236:239], v[220:223]\n"
"v_perm_b32 v23, v62, v58, s34\n"
"v_mfma_f32_16x16x32_bf16 v[160:163], v[16:19], a[224:227], v[160:163]\n"
"v_perm_b32 v24, v39, v35, s33\n"
"v_mfma_f32_16x16x32_bf16 v[164:167], v[16:19], a[228:231], v[164:167]\n"
"ds_read_b128 a[248:251], v1 offset:55296\n"
"v_perm_b32 v25, v47, v43, s33\n"
"v_mfma_f32_16x16x32_bf16 v[224:227], v[16:19], a[232:235], v[224:227]\n"
"v_perm_b32 v26, v55, v51, s33\n"
"v_mfma_f32_16x16x32_bf16 v[228:231], v[16:19], a[236:239], v[228:231]\n"
"v_perm_b32 v27, v63, v59, s33\n"
"v_mfma_f32_16x16x32_bf16 v[168:171], v[20:23], a[224:227], v[168:171]\n"
"v_perm_b32 v28, v39, v35, s34\n"
"v_mfma_f32_16x16x32_bf16 v[172:175], v[20:23], a[228:231], v[172:175]\n"
"ds_read_b128 a[252:255], v1 offset:56320\n"
"v_perm_b32 v29, v47, v43, s34\n"
"v_mfma_f32_16x16x32_bf16 v[232:235], v[20:23], a[232:235], v[232:235]\n"
"v_perm_b32 v30, v55, v51, s34\n"
"v_mfma_f32_16x16x32_bf16 v[236:239], v[20:23], a[236:239], v[236:239]\n"
"v_perm_b32 v31, v63, v59, s34\n"
"s_add_u32 s35, s35, 0x40000\n"
"s_and_b32 s35, s35, 0x3fffff\n"
"v_mfma_f32_16x16x32_bf16 v[176:179], v[24:27], a[224:227], v[176:179]\n"
"buffer_load_dwordx4 v[32:35], v8, s[16:19], s35 offen sc1\n"
"s_waitcnt vmcnt(9)\n"
"v_perm_b32 v16, v68, v64, s33\n"
"v_mfma_f32_16x16x32_bf16 v[180:183], v[24:27], a[228:231], v[180:183]\n"
"v_perm_b32 v17, v76, v72, s33\n"
"v_mfma_f32_16x16x32_bf16 v[240:243], v[24:27], a[232:235], v[240:243]\n"
"v_perm_b32 v18, v84, v80, s33\n"
"v_mfma_f32_16x16x32_bf16 v[244:247], v[24:27], a[236:239], v[244:247]\n"
"buffer_load_dwordx4 v[36:39], v9, s[16:19], s35 offen sc1\n"
"v_perm_b32 v19, v92, v88, s33\n"
"v_mfma_f32_16x16x32_bf16 v[184:187], v[28:31], a[224:227], v[184:187]\n"
"v_perm_b32 v20, v68, v64, s34\n"
"v_mfma_f32_16x16x32_bf16 v[188:191], v[28:31], a[228:231], v[188:191]\n"
"v_perm_b32 v21, v76, v72, s34\n"
"v_mfma_f32_16x16x32_bf16 v[248:251], v[28:31], a[232:235], v[248:251]\n"
"buffer_load_dwordx4 v[40:43], v10, s[16:19], s35 offen sc1\n"
"v_perm_b32 v22, v84, v80, s34\n"
"v_mfma_f32_16x16x32_bf16 v[252:255], v[28:31], a[236:239], v[252:255]\n"
"v_perm_b32 v23, v92, v88, s34\n"
"; q ks=13\n"
"s_waitcnt lgkmcnt(0)\n"
"v_mfma_f32_16x16x32_bf16 v[128:131], v[16:19], a[240:243], v[128:131]\n"
"v_perm_b32 v24, v69, v65, s33\n"
"v_mfma_f32_16x16x32_bf16 v[132:135], v[16:19], a[244:247], v[132:135]\n"
"buffer_load_dwordx4 v[44:47], v11, s[16:19], s35 offen sc1\n"
"v_perm_b32 v25, v77, v73, s33\n"
"v_mfma_f32_16x16x32_bf16 v[192:195], v[16:19], a[248:251], v[192:195]\n"
"v_perm_b32 v26, v85, v81, s33\n"
"v_mfma_f32_16x16x32_bf16 v[196:199], v[16:19], a[252:255], v[196:199]\n"
"v_perm_b32 v27, v93, v89, s33\n"
"v_mfma_f32_16x16x32_bf16 v[136:139], v[20:23], a[240:243], v[136:139]\n"
"buffer_load_dwordx4 v[48:51], v12, s[16:19], s35 offen sc1\n"
"v_perm_b32 v28, v69, v65, s34\n"
"v_mfma_f32_16x16x32_bf16 v[140:143], v[20:23], a[244:247], v[140:143]\n"
"v_perm_b32 v29, v77, v73, s34\n"
"v_mfma_f32_16x16x32_bf16 v[200:203], v[20:23], a[248:251], v[200:203]\n"
"v_perm_b32 v30, v85, v81, s34\n"
"v_mfma_f32_16x16x32_bf16 v[204:207], v[20:23], a[252:255], v[204:207]\n"
"buffer_load_dwordx4 v[52:55], v13, s[16:19], s35 offen sc1\n"
"v_perm_b32 v31, v93, v89, s34\n"
"v_mfma_f32_16x16x32_bf16 v[144:147], v[24:27], a[240:243], v[144:147]\n"
"v_perm_b32 v16, v70, v66, s33\n"
"v_mfma_f32_16x16x32_bf16 v[148:151], v[24:27], a[244:247], v[148:151]\n"
"ds_read_b128 a[224:227], v1 offset:57344\n"
"v_perm_b32 v17, v78, v74, s33\n"
"v_mfma_f32_16x16x32_bf16 v[208:211], v[24:27], a[248:251], v[208:211]\n"
"buffer_load_dwordx4 v[56:59], v14, s[16:19], s35 offen sc1\n"
"v_perm_b32 v18, v86, v82, s33\n"
"v_mfma_f32_16x16x32_bf16 v[212:215], v[24:27], a[252:255], v[212:215]\n"
"v_perm_b32 v19, v94, v90, s33\n"
"v_mfma_f32_16x16x32_bf16 v[152:155], v[28:31], a[240:243], v[152:155]\n"
"v_perm_b32 v20, v70, v66, s34\n"
"v_mfma_f32_16x16x32_bf16 v[156:159], v[28:31], a[244:247], v[156:159]\n"
"buffer_load_dwordx4 v[60:63], v15, s[16:19], s35 offen sc1\n"
"v_perm_b32 v21, v78, v74, s34\n"
"v_mfma_f32_16x16x32_bf16 v[216:219], v[28:31], a[248:251], v[216:219]\n"
"ds_read_b128 a[228:231], v1 offset:58368\n"
"v_perm_b32 v22, v86, v82, s34\n"
"s_add_u32 m0, s40, 0x10000\n"
"v_mfma_f32_16x16x32_bf16 v[220:223], v[28:31], a[252:255], v[220:223]\n"
"global_load_dwordx4 a[28:31], v1, s[44:45]\n"
"v_perm_b32 v23, v94, v90, s34\n"
"s_and_b32 s75, s82, 7\n"
"s_lshl_b32 s75, s75, 14\n"
"v_mfma_f32_16x16x32_bf16 v[160:163], v[16:19], a[240:243], v[160:163]\n"
"buffer_load_dwordx4 v2, s[20:23], s75 offen lds\n"
"v_perm_b32 v24, v71, v67, s33\n"
"v_mfma_f32_16x16x32_bf16 v[164:167], v[16:19], a[244:247], v[164:167]\n"
"ds_read_b128 a[232:235], v1 offset:59392\n"
"v_perm_b32 v25, v79, v75, s33\n"
"v_mfma_f32_16x16x32_bf16 v[224:227], v[16:19], a[248:251], v[224:227]\n"
"v_perm_b32 v26, v87, v83, s33\n"
"v_mfma_f32_16x16x32_bf16 v[228:231], v[16:19], a[252:255], v[228:231]\n"
"buffer_load_dwordx4 v3, s[20:23], s75 offen offset:1024 lds\n"
"v_perm_b32 v27, v95, v91, s33\n"
"v_mfma_f32_16x16x32_bf16 v[168:171], v[20:23], a[240:243], v[168:171]\n"
"v_perm_b32 v28, v71, v67, s34\n"
"v_mfma_f32_16x16x32_bf16 v[172:175], v[20:23], a[244:247], v[172:175]\n"
"ds_read_b128 a[236:239], v1 offset:60416\n"
"v_perm_b32 v29, v79, v75, s34\n"
"v_mfma_f32_16x16x32_bf16 v[232:235], v[20:23], a[248:251], v[232:235]\n"
"buffer_load_dwordx4 v4, s[20:23], s75 offen offset:2048 lds\n"
"v_perm_b32 v30, v87, v83, s34\n"
"v_mfma_f32_16x16x32_bf16 v[236:239], v[20:23], a[252:255], v[236:239]\n"
"v_perm_b32 v31, v95, v91, s34\n"
"v_mfma_f32_16x16x32_bf16 v[176:179], v[24:27], a[240:243], v[176:179]\n"
"s_waitcnt vmcnt(12)\n"
"v_perm_b32 v16, v100, v96, s33\n"
"v_mfma_f32_16x16x32_bf16 v[180:183], v[24:27], a[244:247], v[180:183]\n"
"buffer_load_dwordx4 v5, s[20:23], s75 offen offset:3072 lds\n"
"v_perm_b32 v17, v108, v104, s33\n"
"v_mfma_f32_16x16x32_bf16 v[240:243], v[24:27], a[248:251], v[240:243]\n"
"v_perm_b32 v18, v116, v112, s33\n"
"v_mfma_f32_16x16x32_bf16 v[244:247], v[24:27], a[252:255], v[244:247]\n"
"v_perm_b32 v19, v124, v120, s33\n"
"v_mfma_f32_16x16x32_bf16 v[184:187], v[28:31], a[240:243], v[184:187]\n"
"v_perm_b32 v20, v100, v96, s34\n"
"v_mfma_f32_16x16x32_bf16 v[188:191], v[28:31], a[244:247], v[188:191]\n"
"v_perm_b32 v21, v108, v104, s34\n"
"v_mfma_f32_16x16x32_bf16 v[248:251], v[28:31], a[248:251], v[248:251]\n"
"v_perm_b32 v22, v116, v112, s34\n"
"s_add_u32 m0, s40, 0x14000\n"
"v_mfma_f32_16x16x32_bf16 v[252:255], v[28:31], a[252:255], v[252:255]\n"
"v_perm_b32 v23, v124, v120, s34\n"
"s_add_u32 s41, s75, 0x4000\n"
"s_and_b32 s41, s41, 0x1ffff\n"
"; q ks=14\n"
"s_waitcnt lgkmcnt(0)\n"
"v_mfma_f32_16x16x32_bf16 v[128:131], v[16:19], a[224:227], v[128:131]\n"
"buffer_load_dwordx4 v2, s[20:23], s41 offen lds\n"
"v_perm_b32 v24, v101, v97, s33\n"
"v_mfma_f32_16x16x32_bf16 v[132:135], v[16:19], a[228:231], v[132:135]\n"
"v_perm_b32 v25, v109, v105, s33\n"
"v_lshrrev_b32 v67, 4, v1\n"
"v_mfma_f32_16x16x32_bf16 v[192:195], v[16:19], a[232:235], v[192:195]\n"
"v_perm_b32 v26, v117, v113, s33\n"
"v_and_b32 v68, 15, v67\n"
"v_mfma_f32_16x16x32_bf16 v[196:199], v[16:19], a[236:239], v[196:199]\n"
"buffer_load_dwordx4 v3, s[20:23], s41 offen offset:1024 lds\n"
"v_perm_b32 v27, v125, v121, s33\n"
"v_mfma_f32_16x16x32_bf16 v[136:139], v[20:23], a[224:227], v[136:139]\n"
"v_perm_b32 v28, v101, v97, s34\n"
"v_lshrrev_b32 v69, 4, v67\n"
"v_mfma_f32_16x16x32_bf16 v[140:143], v[20:23], a[228:231], v[140:143]\n"
"v_perm_b32 v29, v109, v105, s34\n"
"v_add_u32 v64, 4, v68\n"
"v_mfma_f32_16x16x32_bf16 v[200:203], v[20:23], a[232:235], v[200:203]\n"
"buffer_load_dwordx4 v4, s[20:23], s41 offen offset:2048 lds\n"
"v_perm_b32 v30, v117, v113, s34\n"
"v_mfma_f32_16x16x32_bf16 v[204:207], v[20:23], a[236:239], v[204:207]\n"
"v_perm_b32 v31, v125, v121, s34\n"
"v_and_b32 v64, 15, v64\n"
"v_mfma_f32_16x16x32_bf16 v[144:147], v[24:27], a[224:227], v[144:147]\n"
"v_perm_b32 v16, v102, v98, s33\n"
"v_lshrrev_b32 v65, 5, v67\n"
"v_mfma_f32_16x16x32_bf16 v[148:151], v[24:27], a[228:231], v[148:151]\n"
"ds_read_b128 a[240:243], v1 offset:61440\n"
"v_perm_b32 v17, v110, v106, s33\n"
"v_mfma_f32_16x16x32_bf16 v[208:211], v[24:27], a[232:235], v[208:211]\n"
"buffer_load_dwordx4 v5, s[20:23], s41 offen offset:3072 lds\n"
"v_perm_b32 v18, v118, v114, s33\n"
"v_mfma_f32_16x16x32_bf16 v[212:215], v[24:27], a[236:239], v[212:215]\n"
"v_perm_b32 v19, v126, v122, s33\n"
"v_lshlrev_b32 v65, 3, v65\n"
"v_mfma_f32_16x16x32_bf16 v[152:155], v[28:31], a[224:227], v[152:155]\n"
"v_perm_b32 v20, v102, v98, s34\n"
"v_xor_b32 v64, v64, v65\n"
"v_mfma_f32_16x16x32_bf16 v[156:159], v[28:31], a[228:231], v[156:159]\n"
"ds_read_b128 a[244:247], v1 offset:62464\n"
"v_perm_b32 v21, v110, v106, s34\n"
"v_mfma_f32_16x16x32_bf16 v[216:219], v[28:31], a[232:235], v[216:219]\n"
"v_perm_b32 v22, v118, v114, s34\n"
"s_add_u32 m0, s40, 0x18000\n"
"v_mfma_f32_16x16x32_bf16 v[220:223], v[28:31], a[236:239], v[220:223]\n"
"v_perm_b32 v23, v126, v122, s34\n"
"v_and_b32 v66, 31, v67\n"
"v_mfma_f32_16x16x32_bf16 v[160:163], v[16:19], a[224:227], v[160:163]\n"
"buffer_load_dwordx4 v6, s[24:27], s75 offen lds\n"
"v_perm_b32 v24, v103, v99, s33\n"
"v_mfma_f32_16x16x32_bf16 v[164:167], v[16:19], a[228:231], v[164:167]\n"
"ds_read_b128 a[248:251], v1 offset:63488\n"
"v_perm_b32 v25, v111, v107, s33\n"
"v_mfma_f32_16x16x32_bf16 v[224:227], v[16:19], a[232:235], v[224:227]\n"
"v_perm_b32 v26, v119, v115, s33\n"
"v_lshlrev_b32 v66, 8, v66\n"
"v_mfma_f32_16x16x32_bf16 v[228:231], v[16:19], a[236:239], v[228:231]\n"
"buffer_load_dwordx4 v7, s[24:27], s75 offen offset:1024 lds\n"
"v_perm_b32 v27, v127, v123, s33\n"
"v_mfma_f32_16x16x32_bf16 v[168:171], v[20:23], a[224:227], v[168:171]\n"
"v_perm_b32 v28, v103, v99, s34\n"
"v_add_u32 v66, 0x10000, v66\n"
"v_mfma_f32_16x16x32_bf16 v[172:175], v[20:23], a[228:231], v[172:175]\n"
"ds_read_b128 a[252:255], v1 offset:64512\n"
"v_perm_b32 v29, v111, v107, s34\n"
"v_mfma_f32_16x16x32_bf16 v[232:235], v[20:23], a[232:235], v[232:235]\n"
"buffer_load_dwordx4 v6, s[24:27], s75 offen offset:2048 lds\n"
"v_perm_b32 v30, v119, v115, s34\n"
"v_mfma_f32_16x16x32_bf16 v[236:239], v[20:23], a[236:239], v[236:239]\n"
"v_perm_b32 v31, v127, v123, s34\n"
"v_xor_b32 v65, 0, v64\n"
"v_mfma_f32_16x16x32_bf16 v[176:179], v[24:27], a[224:227], v[176:179]\n"
"s_waitcnt vmcnt(12)\n"
"v_perm_b32 v16, v36, v32, s33\n"
"v_mfma_f32_16x16x32_bf16 v[180:183], v[24:27], a[228:231], v[180:183]\n"
"buffer_load_dwordx4 v7, s[24:27], s75 offen offset:3072 lds\n"
"v_perm_b32 v17, v44, v40, s33\n"
"v_mfma_f32_16x16x32_bf16 v[240:243], v[24:27], a[232:235], v[240:243]\n"
"v_perm_b32 v18, v52, v48, s33\n"
"v_lshl_add_u32 v8, v65, 4, v66\n"
"v_mfma_f32_16x16x32_bf16 v[244:247], v[24:27], a[236:239], v[244:247]\n"
"v_perm_b32 v19, v60, v56, s33\n"
"v_xor_b32 v65, 1, v64\n"
"v_mfma_f32_16x16x32_bf16 v[184:187], v[28:31], a[224:227], v[184:187]\n"
"v_perm_b32 v20, v36, v32, s34\n"
"v_lshl_add_u32 v9, v65, 4, v66\n"
"v_mfma_f32_16x16x32_bf16 v[188:191], v[28:31], a[228:231], v[188:191]\n"
"v_perm_b32 v21, v44, v40, s34\n"
"v_xor_b32 v65, 2, v64\n"
"v_mfma_f32_16x16x32_bf16 v[248:251], v[28:31], a[232:235], v[248:251]\n"
"v_perm_b32 v22, v52, v48, s34\n"
"v_lshl_add_u32 v10, v65, 4, v66\n"
"v_mfma_f32_16x16x32_bf16 v[252:255], v[28:31], a[236:239], v[252:255]\n"
"v_perm_b32 v23, v60, v56, s34\n"
"; q ks=15\n"
"s_waitcnt lgkmcnt(0)\n"
"v_mfma_f32_16x16x32_bf16 v[128:131], v[16:19], a[240:243], v[128:131]\n"
"v_perm_b32 v24, v37, v33, s33\n"
"v_xor_b32 v65, 3, v64\n"
"v_mfma_f32_16x16x32_bf16 v[132:135], v[16:19], a[244:247], v[132:135]\n"
"v_perm_b32 v25, v45, v41, s33\n"
"v_lshl_add_u32 v11, v65, 4, v66\n"
"v_mfma_f32_16x16x32_bf16 v[192:195], v[16:19], a[248:251], v[192:195]\n"
"v_perm_b32 v26, v53, v49, s33\n"
"v_xor_b32 v65, 4, v64\n"
"v_mfma_f32_16x16x32_bf16 v[196:199], v[16:19], a[252:255], v[196:199]\n"
"v_perm_b32 v27, v61, v57, s33\n"
"v_lshl_add_u32 v12, v65, 4, v66\n"
"v_mfma_f32_16x16x32_bf16 v[136:139], v[20:23], a[240:243], v[136:139]\n"
"v_perm_b32 v28, v37, v33, s34\n"
"v_xor_b32 v65, 5, v64\n"
"v_mfma_f32_16x16x32_bf16 v[140:143], v[20:23], a[244:247], v[140:143]\n"
"v_perm_b32 v29, v45, v41, s34\n"
"v_lshl_add_u32 v13, v65, 4, v66\n"
"v_mfma_f32_16x16x32_bf16 v[200:203], v[20:23], a[248:251], v[200:203]\n"
"v_perm_b32 v30, v53, v49, s34\n"
"v_xor_b32 v65, 6, v64\n"
"v_mfma_f32_16x16x32_bf16 v[204:207], v[20:23], a[252:255], v[204:207]\n"
"v_perm_b32 v31, v61, v57, s34\n"
"v_lshl_add_u32 v14, v65, 4, v66\n"
"v_mfma_f32_16x16x32_bf16 v[144:147], v[24:27], a[240:243], v[144:147]\n"
"v_perm_b32 v16, v38, v34, s33\n"
"v_xor_b32 v65, 7, v64\n"
"v_mfma_f32_16x16x32_bf16 v[148:151], v[24:27], a[244:247], v[148:151]\n"
"v_perm_b32 v17, v46, v42, s33\n"
"v_lshl_add_u32 v15, v65, 4, v66\n"
"v_mfma_f32_16x16x32_bf16 v[208:211], v[24:27], a[248:251], v[208:211]\n"
"v_perm_b32 v18, v54, v50, s33\n"
"v_and_b32 v64, 3, v67\n"
"v_mfma_f32_16x16x32_bf16 v[212:215], v[24:27], a[252:255], v[212:215]\n"
"v_perm_b32 v19, v62, v58, s33\n"
"v_lshlrev_b32 v64, 3, v64\n"
"v_mfma_f32_16x16x32_bf16 v[152:155], v[28:31], a[240:243], v[152:155]\n"
"v_perm_b32 v20, v38, v34, s34\n"
"v_bfe_u32 v65, v67, 2, 2\n"
"v_mfma_f32_16x16x32_bf16 v[156:159], v[28:31], a[244:247], v[156:159]\n"
"v_perm_b32 v21, v46, v42, s34\n"
"v_lshl_add_u32 v64, v65, 6, v64\n"
"v_mfma_f32_16x16x32_bf16 v[216:219], v[28:31], a[248:251], v[216:219]\n"
"v_perm_b32 v22, v54, v50, s34\n"
"v_bfe_u32 v65, v67, 4, 1\n"
"v_mfma_f32_16x16x32_bf16 v[220:223], v[28:31], a[252:255], v[220:223]\n"
"v_perm_b32 v23, v62, v58, s34\n"
"v_lshl_add_u32 v64, v65, 5, v64\n"
"v_mfma_f32_16x16x32_bf16 v[160:163], v[16:19], a[240:243], v[160:163]\n"
"v_perm_b32 v24, v39, v35, s33\n"
"v_lshrrev_b32 v65, 5, v67\n"
"v_mfma_f32_16x16x32_bf16 v[164:167], v[16:19], a[244:247], v[164:167]\n"
"v_perm_b32 v25, v47, v43, s33\n"
"v_lshl_add_u32 v64, v65, 8, v64\n"
"v_mfma_f32_16x16x32_bf16 v[224:227], v[16:19], a[248:251], v[224:227]\n"
"v_perm_b32 v26, v55, v51, s33\n"
"v_add_u32 v112, 0x18000, v64\n"
"v_mfma_f32_16x16x32_bf16 v[228:231], v[16:19], a[252:255], v[228:231]\n"
"v_perm_b32 v27, v63, v59, s33\n"
"v_lshrrev_b32 v64, 3, v68\n"
"v_mfma_f32_16x16x32_bf16 v[168:171], v[20:23], a[240:243], v[168:171]\n"
"v_perm_b32 v28, v39, v35, s34\n"
"v_lshlrev_b32 v64, 16, v64\n"
"v_mfma_f32_16x16x32_bf16 v[172:175], v[20:23], a[244:247], v[172:175]\n"
"v_perm_b32 v29, v47, v43, s34\n"
"v_lshl_add_u32 v64, v69, 7, v64\n"
"v_mfma_f32_16x16x32_bf16 v[232:235], v[20:23], a[248:251], v[232:235]\n"
"v_perm_b32 v30, v55, v51, s34\n"
"v_and_b32 v65, 7, v68\n"
"v_mfma_f32_16x16x32_bf16 v[236:239], v[20:23], a[252:255], v[236:239]\n"
"v_perm_b32 v31, v63, v59, s34\n"
"v_lshl_add_u32 v113, v65, 4, v64\n"
"v_mfma_f32_16x16x32_bf16 v[176:179], v[24:27], a[240:243], v[176:179]\n"
"v_mfma_f32_16x16x32_bf16 v[180:183], v[24:27], a[244:247], v[180:183]\n"
"v_lshrrev_b32 v64, 5, v67\n"
"v_mfma_f32_16x16x32_bf16 v[240:243], v[24:27], a[248:251], v[240:243]\n"
"v_and_b32 v65, 0x1ff, v1\n"
"v_mfma_f32_16x16x32_bf16 v[244:247], v[24:27], a[252:255], v[244:247]\n"
"v_lshl_add_u32 v114, v64, 12, v65\n"
"v_mfma_f32_16x16x32_bf16 v[184:187], v[28:31], a[240:243], v[184:187]\n"
"s_mov_b32 s11, 0xe00\n"
"v_mfma_f32_16x16x32_bf16 v[188:191], v[28:31], a[244:247], v[188:191]\n"
"v_mad_u32_u24 v115, v64, s11, v65\n"
"v_mfma_f32_16x16x32_bf16 v[248:251], v[28:31], a[248:251], v[248:251]\n"
"v_mfma_f32_16x16x32_bf16 v[252:255], v[28:31], a[252:255], v[252:255]\n"
"; q end\n"
"s_waitcnt vmcnt(0)\n"
"s_barrier\n"
"; A(0)\n"
"ds_read_b128 a[0:3], v8\n"
"ds_read_b128 a[32:35], v8 offset:8192\n"
"ds_read_b128 a[4:7], v9\n"
"ds_read_b128 a[36:39], v9 offset:8192\n"
"ds_read_b128 a[8:11], v10\n"
"ds_read_b128 a[40:43], v10 offset:8192\n"
"ds_read_b128 a[12:15], v11\n"
"ds_read_b128 a[44:47], v11 offset:8192\n"
"ds_read_b128 a[16:19], v12\n"
"ds_read_b128 a[48:51], v12 offset:8192\n"
"ds_read_b128 a[20:23], v13\n"
"ds_read_b128 a[52:55], v13 offset:8192\n"
"ds_read_b128 a[24:27], v14\n"
"ds_read_b128 a[56:59], v14 offset:8192\n"
"ds_read_b128 a[28:31], v15\n"
"ds_read_b128 a[60:63], v15 offset:8192\n"
"v_cvt_pk_bf16_f32 v122, v128, v136\n"
"v_cvt_pk_bf16_f32 v123, v132, v140\n"
"v_cvt_pk_bf16_f32 v124, v144, v152\n"
"v_cvt_pk_bf16_f32 v125, v148, v156\n"
"v_permlane16_swap_b32 v122, v123\n"
"v_cvt_pk_bf16_f32 v126, v160, v168\n"
"v_cvt_pk_bf16_f32 v127, v164, v172\n"
"v_permlane16_swap_b32 v124, v125\n"
"v_accvgpr_write_b32 a128, v122\n"
"v_accvgpr_write_b32 a144, v123\n"
"v_cvt_pk_bf16_f32 v122, v176, v184\n"
"v_cvt_pk_bf16_f32 v123, v180, v188\n"
"v_permlane16_swap_b32 v126, v127\n"
"v_accvgpr_write_b32 a129, v124\n"
"v_accvgpr_write_b32 a145, v125\n"
"v_cvt_pk_bf16_f32 v124, v129, v137\n"
"v_cvt_pk_bf16_f32 v125, v133, v141\n"
"v_permlane16_swap_b32 v122, v123\n"
"v_accvgpr_write_b32 a130, v126\n"
"v_accvgpr_write_b32 a146, v127\n"
"v_cvt_pk_bf16_f32 v126, v145, v153\n"
"v_cvt_pk_bf16_f32 v127, v149, v157\n"
"v_permlane16_swap_b32 v124, v125\n"
"v_accvgpr_write_b32 a131, v122\n"
"v_accvgpr_write_b32 a147, v123\n"
"v_cvt_pk_bf16_f32 v122, v161, v169\n"
"v_cvt_pk_bf16_f32 v123, v165, v173\n"
"v_permlane16_swap_b32 v126, v127\n"
"v_accvgpr_write_b32 a132, v124\n"
"v_accvgpr_write_b32 a148, v125\n"
"v_cvt_pk_bf16_f32 v124, v177, v185\n"
"v_cvt_pk_bf16_f32 v125, v181, v189\n"
"v_permlane16_swap_b32 v122, v123\n"
"v_accvgpr_write_b32 a133, v126\n"
"v_accvgpr_write_b32 a149, v127\n"
"v_cvt_pk_bf16_f32 v126, v130, v138\n"
"v_cvt_pk_bf16_f32 v127, v134, v142\n"
"v_permlane16_swap_b32 v124, v125\n"
"v_accvgpr_write_b32 a134, v122\n"
"v_accvgpr_write_b32 a150, v123\n"
"v_cvt_pk_bf16_f32 v122, v146, v154\n"
"v_cvt_pk_bf16_f32 v123, v150, v158\n"
"v_permlane16_swap_b32 v126, v127\n"
"v_accvgpr_write_b32 a135, v124\n"
"v_accvgpr_write_b32 a151, v125\n"
"v_cvt_pk_bf16_f32 v124, v162, v170\n"
"v_cvt_pk_bf16_f32 v125, v166, v174\n"
"v_permlane16_swap_b32 v122, v123\n"
"v_accvgpr_write_b32 a136, v126\n"
"v_accvgpr_write_b32 a152, v127\n"
"v_cvt_pk_bf16_f32 v126, v178, v186\n"
"v_cvt_pk_bf16_f32 v127, v182, v190\n"
"v_permlane16_swap_b32 v124, v125\n"
"v_accvgpr_write_b32 a137, v122\n"
"v_accvgpr_write_b32 a153, v123\n"
"v_cvt_pk_bf16_f32 v122, v131, v139\n"
"v_cvt_pk_bf16_f32 v123, v135, v143\n"
"v_permlane16_swap_b32 v126, v127\n"
"v_accvgpr_write_b32 a138, v124\n"
"v_accvgpr_write_b32 a154, v125\n"
"v_cvt_pk_bf16_f32 v124, v147, v155\n"
"v_cvt_pk_bf16_f32 v125, v151, v159\n"
"v_permlane16_swap_b32 v122, v123\n"
"v_accvgpr_write_b32 a139, v126\n"
"v_accvgpr_write_b32 a155, v127\n"
"v_cvt_pk_bf16_f32 v126, v163, v171\n"
"v_cvt_pk_bf16_f32 v127, v167, v175\n"
"v_permlane16_swap_b32 v124, v125\n"
"v_accvgpr_write_b32 a140, v122\n"
"v_accvgpr_write_b32 a156, v123\n"
"v_cvt_pk_bf16_f32 v122, v179, v187\n"
"v_cvt_pk_bf16_f32 v123, v183, v191\n"
"v_permlane16_swap_b32 v126, v127\n"
"v_accvgpr_write_b32 a141, v124\n"
"v_accvgpr_write_b32 a157, v125\n"
"v_permlane16_swap_b32 v122, v123\n"
"v_accvgpr_write_b32 a142, v126\n"
"v_accvgpr_write_b32 a158, v127\n"
"v_accvgpr_write_b32 a143, v122\n"
"v_accvgpr_write_b32 a159, v123\n"
"s_waitcnt lgkmcnt(0)\n"
"v_mfma_f32_32x32x16_bf16 v[128:143], a[0:3], a[128:131], 0\n"
"v_cvt_pk_bf16_f32 v122, v192, v200\n"
"v_cvt_pk_bf16_f32 v123, v196, v204\n"
"v_cvt_pk_bf16_f32 v124, v208, v216\n"
"v_cvt_pk_bf16_f32 v125, v212, v220\n"
"v_permlane16_swap_b32 v122, v123\n"
"v_mfma_f32_32x32x16_bf16 v[160:175], a[32:35], a[128:131], 0\n"
"v_cvt_pk_bf16_f32 v126, v224, v232\n"
"v_cvt_pk_bf16_f32 v127, v228, v236\n"
"v_permlane16_swap_b32 v124, v125\n"
"v_accvgpr_write_b32 a160, v122\n"
"v_accvgpr_write_b32 a176, v123\n"
"v_mfma_f32_32x32x16_bf16 v[128:143], a[4:7], a[132:135], v[128:143]\n"
"v_cvt_pk_bf16_f32 v122, v240, v248\n"
"v_cvt_pk_bf16_f32 v123, v244, v252\n"
"v_permlane16_swap_b32 v126, v127\n"
"v_accvgpr_write_b32 a161, v124\n"
"v_accvgpr_write_b32 a177, v125\n"
"v_mfma_f32_32x32x16_bf16 v[160:175], a[36:39], a[132:135], v[160:175]\n"
"v_cvt_pk_bf16_f32 v124, v193, v201\n"
"v_cvt_pk_bf16_f32 v125, v197, v205\n"
"v_permlane16_swap_b32 v122, v123\n"
"v_accvgpr_write_b32 a162, v126\n"
"v_accvgpr_write_b32 a178, v127\n"
"v_mfma_f32_32x32x16_bf16 v[128:143], a[8:11], a[136:139], v[128:143]\n"
"v_cvt_pk_bf16_f32 v126, v209, v217\n"
"v_cvt_pk_bf16_f32 v127, v213, v221\n"
"v_permlane16_swap_b32 v124, v125\n"
"v_accvgpr_write_b32 a163, v122\n"
"v_accvgpr_write_b32 a179, v123\n"
"v_mfma_f32_32x32x16_bf16 v[160:175], a[40:43], a[136:139], v[160:175]\n"
"v_cvt_pk_bf16_f32 v122, v225, v233\n"
"v_cvt_pk_bf16_f32 v123, v229, v237\n"
"v_permlane16_swap_b32 v126, v127\n"
"v_accvgpr_write_b32 a164, v124\n"
"v_accvgpr_write_b32 a180, v125\n"
"v_mfma_f32_32x32x16_bf16 v[128:143], a[12:15], a[140:143], v[128:143]\n"
"v_cvt_pk_bf16_f32 v124, v241, v249\n"
"v_cvt_pk_bf16_f32 v125, v245, v253\n"
"v_permlane16_swap_b32 v122, v123\n"
"v_accvgpr_write_b32 a165, v126\n"
"v_accvgpr_write_b32 a181, v127\n"
"v_mfma_f32_32x32x16_bf16 v[160:175], a[44:47], a[140:143], v[160:175]\n"
"v_cvt_pk_bf16_f32 v126, v194, v202\n"
"v_cvt_pk_bf16_f32 v127, v198, v206\n"
"v_permlane16_swap_b32 v124, v125\n"
"v_accvgpr_write_b32 a166, v122\n"
"v_accvgpr_write_b32 a182, v123\n"
"v_mfma_f32_32x32x16_bf16 v[128:143], a[16:19], a[144:147], v[128:143]\n"
"v_cvt_pk_bf16_f32 v122, v210, v218\n"
"v_cvt_pk_bf16_f32 v123, v214, v222\n"
"v_permlane16_swap_b32 v126, v127\n"
"v_accvgpr_write_b32 a167, v124\n"
"v_accvgpr_write_b32 a183, v125\n"
"v_mfma_f32_32x32x16_bf16 v[160:175], a[48:51], a[144:147], v[160:175]\n"
"v_cvt_pk_bf16_f32 v124, v226, v234\n"
"v_cvt_pk_bf16_f32 v125, v230, v238\n"
"v_permlane16_swap_b32 v122, v123\n"
"v_accvgpr_write_b32 a168, v126\n"
"v_accvgpr_write_b32 a184, v127\n"
"v_mfma_f32_32x32x16_bf16 v[128:143], a[20:23], a[148:151], v[128:143]\n"
"v_cvt_pk_bf16_f32 v126, v242, v250\n"
"v_cvt_pk_bf16_f32 v127, v246, v254\n"
"v_permlane16_swap_b32 v124, v125\n"
"v_accvgpr_write_b32 a169, v122\n"
"v_accvgpr_write_b32 a185, v123\n"
"v_mfma_f32_32x32x16_bf16 v[160:175], a[52:55], a[148:151], v[160:175]\n"
"v_cvt_pk_bf16_f32 v122, v195, v203\n"
"v_cvt_pk_bf16_f32 v123, v199, v207\n"
"v_permlane16_swap_b32 v126, v127\n"
"v_accvgpr_write_b32 a170, v124\n"
"v_accvgpr_write_b32 a186, v125\n"
"v_mfma_f32_32x32x16_bf16 v[128:143], a[24:27], a[152:155], v[128:143]\n"
"v_cvt_pk_bf16_f32 v124, v211, v219\n"
"v_cvt_pk_bf16_f32 v125, v215, v223\n"
"v_permlane16_swap_b32 v122, v123\n"
"v_accvgpr_write_b32 a171, v126\n"
"v_accvgpr_write_b32 a187, v127\n"
"v_mfma_f32_32x32x16_bf16 v[160:175], a[56:59], a[152:155], v[160:175]\n"
"v_cvt_pk_bf16_f32 v126, v227, v235\n"
"v_cvt_pk_bf16_f32 v127, v231, v239\n"
"v_permlane16_swap_b32 v124, v125\n"
"v_accvgpr_write_b32 a172, v122\n"
"v_accvgpr_write_b32 a188, v123\n"
"v_mfma_f32_32x32x16_bf16 v[128:143], a[28:31], a[156:159], v[128:143]\n"
"v_cvt_pk_bf16_f32 v122, v243, v251\n"
"v_cvt_pk_bf16_f32 v123, v247, v255\n"
"v_permlane16_swap_b32 v126, v127\n"
"v_accvgpr_write_b32 a173, v124\n"
"v_accvgpr_write_b32 a189, v125\n"
"v_mfma_f32_32x32x16_bf16 v[160:175], a[60:63], a[156:159], v[160:175]\n"
"v_permlane16_swap_b32 v122, v123\n"
"v_accvgpr_write_b32 a174, v126\n"
"v_accvgpr_write_b32 a190, v127\n"
"v_accvgpr_write_b32 a175, v122\n"
"v_accvgpr_write_b32 a191, v123\n"
"v_mfma_f32_32x32x16_bf16 v[144:159], a[0:3], a[160:163], 0\n"
"v_mfma_f32_32x32x16_bf16 v[176:191], a[32:35], a[160:163], 0\n"
"v_mfma_f32_32x32x16_bf16 v[144:159], a[4:7], a[164:167], v[144:159]\n"
"v_mfma_f32_32x32x16_bf16 v[176:191], a[36:39], a[164:167], v[176:191]\n"
"v_max3_f32 v20, v128, v129, v130\n"
"s_nop 1\n"
"v_max3_f32 v16, v143, v160, v161\n"
"v_max3_f32 v20, v20, v131, v132\n"
"v_max3_f32 v16, v16, v162, v163\n"
"v_max3_f32 v20, v20, v133, v134\n"
"v_max3_f32 v16, v16, v164, v165\n"
"v_mfma_f32_32x32x16_bf16 v[144:159], a[8:11], a[168:171], v[144:159]\n"
"ds_read_b128 a[192:195], v8 offset:16384\n"
"v_max3_f32 v20, v20, v135, v136\n"
"v_max3_f32 v16, v16, v166, v167\n"
"v_max3_f32 v20, v20, v137, v138\n"
"v_max3_f32 v16, v16, v168, v169\n"
"v_max3_f32 v20, v20, v139, v140\n"
"v_mfma_f32_32x32x16_bf16 v[176:191], a[40:43], a[168:171], v[176:191]\n"
"ds_read_b128 a[196:199], v9 offset:16384\n"
"v_max3_f32 v16, v16, v170, v171\n"
"v_max3_f32 v20, v20, v141, v142\n"
"v_max3_f32 v16, v16, v172, v173\n"
"v_max3_f32 v16, v16, v174, v175\n"
"v_max_f32 v20, v20, v16\n"
"v_mfma_f32_32x32x16_bf16 v[144:159], a[12:15], a[172:175], v[144:159]\n"
"ds_read_b128 a[200:203], v10 offset:16384\n"
"v_mov_b32 v16, v20\n"
"s_nop 1\n"
"v_permlane32_swap_b32 v20, v16\n"
"v_max_f32 v20, v20, v16\n"
"v_sub_f32 v32, 0xc2a00000, v20\n"
"v_mfma_f32_32x32x16_bf16 v[176:191], a[44:47], a[172:175], v[176:191]\n"
"ds_read_b128 a[204:207], v11 offset:16384\n"
"v_sub_f32 v33, 0xc2a00000, v20\n"
"v_sub_f32 v34, 0xc2a00000, v20\n"
"v_sub_f32 v35, 0xc2a00000, v20\n"
"v_sub_f32 v36, 0xc2a00000, v20\n"
"v_sub_f32 v37, 0xc2a00000, v20\n"
"v_mfma_f32_32x32x16_bf16 v[144:159], a[16:19], a[176:179], v[144:159]\n"
"ds_read_b128 a[208:211], v12 offset:16384\n"
"v_sub_f32 v38, 0xc2a00000, v20\n"
"v_sub_f32 v39, 0xc2a00000, v20\n"
"v_sub_f32 v40, 0xc2a00000, v20\n"
"v_sub_f32 v41, 0xc2a00000, v20\n"
"v_sub_f32 v42, 0xc2a00000, v20\n"
"v_mfma_f32_32x32x16_bf16 v[176:191], a[48:51], a[176:179], v[176:191]\n"
"ds_read_b128 a[212:215], v13 offset:16384\n"
"v_sub_f32 v43, 0xc2a00000, v20\n"
"v_sub_f32 v44, 0xc2a00000, v20\n"
"v_sub_f32 v45, 0xc2a00000, v20\n"
"v_sub_f32 v46, 0xc2a00000, v20\n"
"v_sub_f32 v47, 0xc2a00000, v20\n"
"v_mfma_f32_32x32x16_bf16 v[144:159], a[20:23], a[180:183], v[144:159]\n"
"v_add_f32 v128, v128, v32\n"
"v_add_f32 v129, v129, v32\n"
"v_add_f32 v130, v130, v32\n"
"v_add_f32 v131, v131, v32\n"
"v_add_f32 v132, v132, v32\n"
"v_add_f32 v133, v133, v32\n"
"v_mfma_f32_32x32x16_bf16 v[176:191], a[52:55], a[180:183], v[176:191]\n"
"v_add_f32 v134, v134, v32\n"
"v_add_f32 v135, v135, v32\n"
"v_add_f32 v136, v136, v32\n"
"v_add_f32 v137, v137, v32\n"
"v_add_f32 v138, v138, v32\n"
"v_add_f32 v139, v139, v32\n"
"v_mfma_f32_32x32x16_bf16 v[144:159], a[24:27], a[184:187], v[144:159]\n"
"v_add_f32 v140, v140, v32\n"
"v_add_f32 v141, v141, v32\n"
"v_add_f32 v142, v142, v32\n"
"v_add_f32 v143, v143, v32\n"
"v_add_f32 v160, v160, v32\n"
"v_add_f32 v161, v161, v32\n"
"v_mfma_f32_32x32x16_bf16 v[176:191], a[56:59], a[184:187], v[176:191]\n"
"v_add_f32 v162, v162, v32\n"
"v_add_f32 v163, v163, v32\n"
"v_add_f32 v164, v164, v32\n"
"v_add_f32 v165, v165, v32\n"
"v_add_f32 v166, v166, v32\n"
"v_add_f32 v167, v167, v32\n"
"v_mfma_f32_32x32x16_bf16 v[144:159], a[28:31], a[188:191], v[144:159]\n"
"v_add_f32 v168, v168, v32\n"
"v_add_f32 v169, v169, v32\n"
"v_add_f32 v170, v170, v32\n"
"v_add_f32 v171, v171, v32\n"
"v_add_f32 v172, v172, v32\n"
"v_add_f32 v173, v173, v32\n"
"v_mfma_f32_32x32x16_bf16 v[176:191], a[60:63], a[188:191], v[176:191]\n"
"v_add_f32 v174, v174, v32\n"
"v_add_f32 v175, v175, v32\n"
"s_nop 2\n"
"v_max3_f32 v21, v144, v145, v146\n"
"s_nop 5\n"
"v_max3_f32 v17, v159, v176, v177\n"
"v_max3_f32 v21, v21, v147, v148\n"
"v_max3_f32 v17, v17, v178, v179\n"
"v_max3_f32 v21, v21, v149, v150\n"
"v_max3_f32 v17, v17, v180, v181\n"
"v_max3_f32 v21, v21, v151, v152\n"
"v_max3_f32 v17, v17, v182, v183\n"
"v_max3_f32 v21, v21, v153, v154\n"
"v_max3_f32 v17, v17, v184, v185\n"
"v_max3_f32 v21, v21, v155, v156\n"
"v_max3_f32 v17, v17, v186, v187\n"
"v_max3_f32 v21, v21, v157, v158\n"
"v_max3_f32 v17, v17, v188, v189\n"
"v_max3_f32 v17, v17, v190, v191\n"
"v_max_f32 v21, v21, v17\n"
"v_mov_b32 v17, v21\n"
"s_nop 1\n"
"v_permlane32_swap_b32 v21, v17\n"
"v_max_f32 v21, v21, v17\n"
"v_sub_f32 v48, 0xc2a00000, v21\n"
"v_sub_f32 v49, 0xc2a00000, v21\n"
"v_sub_f32 v50, 0xc2a00000, v21\n"
"v_sub_f32 v51, 0xc2a00000, v21\n"
"v_sub_f32 v52, 0xc2a00000, v21\n"
"v_sub_f32 v53, 0xc2a00000, v21\n"
"v_sub_f32 v54, 0xc2a00000, v21\n"
"v_sub_f32 v55, 0xc2a00000, v21\n"
"v_sub_f32 v56, 0xc2a00000, v21\n"
"v_sub_f32 v57, 0xc2a00000, v21\n"
"v_sub_f32 v58, 0xc2a00000, v21\n"
"v_sub_f32 v59, 0xc2a00000, v21\n"
"v_sub_f32 v60, 0xc2a00000, v21\n"
"v_sub_f32 v61, 0xc2a00000, v21\n"
"v_sub_f32 v62, 0xc2a00000, v21\n"
"v_sub_f32 v63, 0xc2a00000, v21\n"
"s_barrier\n"
"v_add_f32 v144, v144, v48\n"
"v_add_f32 v145, v145, v48\n"
"v_add_f32 v146, v146, v48\n"
"v_add_f32 v147, v147, v48\n"
"v_add_f32 v148, v148, v48\n"
"v_add_f32 v149, v149, v48\n"
"v_add_f32 v150, v150, v48\n"
"v_add_f32 v151, v151, v48\n"
"v_add_f32 v152, v152, v48\n"
"v_add_f32 v153, v153, v48\n"
"v_add_f32 v154, v154, v48\n"
"v_add_f32 v155, v155, v48\n"
"v_add_f32 v156, v156, v48\n"
"v_add_f32 v157, v157, v48\n"
"v_add_f32 v158, v158, v48\n"
"v_add_f32 v159, v159, v48\n"
"v_add_f32 v176, v176, v48\n"
"v_add_f32 v177, v177, v48\n"
"v_add_f32 v178, v178, v48\n"
"v_add_f32 v179, v179, v48\n"
"v_add_f32 v180, v180, v48\n"
"v_add_f32 v181, v181, v48\n"
"v_add_f32 v182, v182, v48\n"
"v_add_f32 v183, v183, v48\n"
"v_add_f32 v184, v184, v48\n"
"v_add_f32 v185, v185, v48\n"
"v_add_f32 v186, v186, v48\n"
"v_add_f32 v187, v187, v48\n"
"v_add_f32 v188, v188, v48\n"
"v_add_f32 v189, v189, v48\n"
"v_add_f32 v190, v190, v48\n"
"v_add_f32 v191, v191, v48\n"
"v_exp_f32 v16, v128\n"
"v_exp_f32 v17, v129\n"
"v_exp_f32 v18, v130\n"
"v_add_f32 v20, v16, v17\n"
"v_cvt_pk_bf16_f32 v64, v16, v17\n"
"v_exp_f32 v19, v131\n"
"v_add_f32 v20, v20, v18\n"
"v_exp_f32 v16, v132\n"
"v_add_f32 v20, v20, v19\n"
"v_cvt_pk_bf16_f32 v65, v18, v19\n"
"v_exp_f32 v17, v133\n"
"v_add_f32 v20, v20, v16\n"
"v_exp_f32 v18, v134\n"
"v_add_f32 v20, v20, v17\n"
"v_cvt_pk_bf16_f32 v66, v16, v17\n"
"v_exp_f32 v19, v135\n"
"v_add_f32 v20, v20, v18\n"
"v_exp_f32 v16, v136\n"
"v_add_f32 v20, v20, v19\n"
"v_cvt_pk_bf16_f32 v67, v18, v19\n"
"v_exp_f32 v17, v137\n"
"v_add_f32 v20, v20, v16\n"
"v_exp_f32 v18, v138\n"
"v_add_f32 v20, v20, v17\n"
"v_cvt_pk_bf16_f32 v68, v16, v17\n"
"v_exp_f32 v19, v139\n"
"v_add_f32 v20, v20, v18\n"
"v_exp_f32 v16, v140\n"
"v_add_f32 v20, v20, v19\n"
"v_cvt_pk_bf16_f32 v69, v18, v19\n"
"v_exp_f32 v17, v141\n"
"v_add_f32 v20, v20, v16\n"
"v_exp_f32 v18, v142\n"
"v_add_f32 v20, v20, v17\n"
"v_cvt_pk_bf16_f32 v70, v16, v17\n"
"v_exp_f32 v19, v143\n"
"v_add_f32 v20, v20, v18\n"
"v_exp_f32 v16, v144\n"
"v_add_f32 v20, v20, v19\n"
"v_cvt_pk_bf16_f32 v71, v18, v19\n"
"v_exp_f32 v17, v145\n"
"v_exp_f32 v18, v146\n"
"v_add_f32 v21, v16, v17\n"
"v_cvt_pk_bf16_f32 v72, v16, v17\n"
"v_exp_f32 v19, v147\n"
"v_add_f32 v21, v21, v18\n"
"; A(1)\n"
"s_waitcnt lgkmcnt(2)\n"
"v_mfma_f32_32x32x16_bf16 v[192:207], a[192:195], a[128:131], v[32:47]\n"
"ds_read_b128 a[216:219], v14 offset:16384\n"
"v_accvgpr_write_b32 a252, -1\n"
"v_accvgpr_write_b32 a253, -1\n"
"s_add_u32 m0, s40, 0x10000\n"
"s_mov_b32 s75, s41\n"
"s_add_u32 s41, s41, 0x4000\n"
"s_and_b32 s41, s41, 0x1ffff\n"
"v_mfma_f32_32x32x16_bf16 v[208:223], a[192:195], a[160:163], v[48:63]\n"
"buffer_load_dwordx4 v2, s[20:23], s41 offen lds\n"
"v_accvgpr_write_b32 a254, -1\n"
"v_accvgpr_write_b32 a255, -1\n"
"v_exp_f32 v16, v148\n"
"v_add_f32 v21, v21, v19\n"
"v_mfma_f32_32x32x16_bf16 v[192:207], a[196:199], a[132:135], v[192:207]\n"
"ds_read_b128 a[192:195], v15 offset:16384\n"
"v_cvt_pk_bf16_f32 v73, v18, v19\n"
"v_exp_f32 v17, v149\n"
"v_add_f32 v21, v21, v16\n"
"v_exp_f32 v18, v150\n"
"v_mfma_f32_32x32x16_bf16 v[208:223], a[196:199], a[164:167], v[208:223]\n"
"buffer_load_dwordx4 v3, s[20:23], s41 offen offset:1024 lds\n"
"v_add_f32 v21, v21, v17\n"
"v_cvt_pk_bf16_f32 v74, v16, v17\n"
"v_exp_f32 v19, v151\n"
"v_add_f32 v21, v21, v18\n"
"v_mfma_f32_32x32x16_bf16 v[192:207], a[200:203], a[136:139], v[192:207]\n"
"ds_read_b128 a[196:199], v8 offset:24576\n"
"v_exp_f32 v16, v152\n"
"v_add_f32 v21, v21, v19\n"
"v_cvt_pk_bf16_f32 v75, v18, v19\n"
"v_exp_f32 v17, v153\n"
"v_mfma_f32_32x32x16_bf16 v[208:223], a[200:203], a[168:171], v[208:223]\n"
"buffer_load_dwordx4 v4, s[20:23], s41 offen offset:2048 lds\n"
"v_add_f32 v21, v21, v16\n"
"v_exp_f32 v18, v154\n"
"v_add_f32 v21, v21, v17\n"
"v_cvt_pk_bf16_f32 v76, v16, v17\n"
"v_mfma_f32_32x32x16_bf16 v[192:207], a[204:207], a[140:143], v[192:207]\n"
"ds_read_b128 a[200:203], v9 offset:24576\n"
"v_exp_f32 v19, v155\n"
"v_add_f32 v21, v21, v18\n"
"v_exp_f32 v16, v156\n"
"v_add_f32 v21, v21, v19\n"
"v_mfma_f32_32x32x16_bf16 v[208:223], a[204:207], a[172:175], v[208:223]\n"
"buffer_load_dwordx4 v5, s[20:23], s41 offen offset:3072 lds\n"
"v_cvt_pk_bf16_f32 v77, v18, v19\n"
"v_exp_f32 v17, v157\n"
"v_add_f32 v21, v21, v16\n"
"s_waitcnt lgkmcnt(2)\n"
"v_mfma_f32_32x32x16_bf16 v[192:207], a[208:211], a[144:147], v[192:207]\n"
"ds_read_b128 a[204:207], v10 offset:24576\n"
"v_exp_f32 v18, v158\n"
"v_add_f32 v21, v21, v17\n"
"v_cvt_pk_bf16_f32 v78, v16, v17\n"
"v_exp_f32 v19, v159\n"
"s_add_u32 m0, s40, 0x1c000\n"
"v_mfma_f32_32x32x16_bf16 v[208:223], a[208:211], a[176:179], v[208:223]\n"
"buffer_load_dwordx4 v6, s[24:27], s75 offen lds\n"
"v_add_f32 v21, v21, v18\n"
"v_add_f32 v21, v21, v19\n"
"v_cvt_pk_bf16_f32 v79, v18, v19\n"
"v_exp_f32 v16, v160\n"
"v_mfma_f32_32x32x16_bf16 v[192:207], a[212:215], a[148:151], v[192:207]\n"
"ds_read_b128 a[208:211], v11 offset:24576\n"
"v_exp_f32 v17, v161\n"
"v_add_f32 v20, v20, v16\n"
"v_exp_f32 v18, v162\n"
"v_add_f32 v20, v20, v17\n"
"v_mfma_f32_32x32x16_bf16 v[208:223], a[212:215], a[180:183], v[208:223]\n"
"buffer_load_dwordx4 v7, s[24:27], s75 offen offset:1024 lds\n"
"v_cvt_pk_bf16_f32 v96, v16, v17\n"
"v_exp_f32 v19, v163\n"
"v_add_f32 v20, v20, v18\n"
"v_mfma_f32_32x32x16_bf16 v[192:207], a[216:219], a[152:155], v[192:207]\n"
"ds_read_b128 a[212:215], v12 offset:24576\n"
"v_exp_f32 v16, v164\n"
"v_add_f32 v20, v20, v19\n"
"v_cvt_pk_bf16_f32 v97, v18, v19\n"
"v_exp_f32 v17, v165\n"
"v_add_f32 v20, v20, v16\n"
"v_mfma_f32_32x32x16_bf16 v[208:223], a[216:219], a[184:187], v[208:223]\n"
"buffer_load_dwordx4 v6, s[24:27], s75 offen offset:2048 lds\n"
"v_exp_f32 v18, v166\n"
"v_add_f32 v20, v20, v17\n"
"v_cvt_pk_bf16_f32 v98, v16, v17\n"
"v_exp_f32 v19, v167\n"
"v_mfma_f32_32x32x16_bf16 v[192:207], a[192:195], a[156:159], v[192:207]\n"
"ds_read_b128 a[216:219], v13 offset:24576\n"
"v_add_f32 v20, v20, v18\n"
"v_exp_f32 v16, v168\n"
"v_add_f32 v20, v20, v19\n"
"v_cvt_pk_bf16_f32 v99, v18, v19\n"
"v_mfma_f32_32x32x16_bf16 v[208:223], a[192:195], a[188:191], v[208:223]\n"
"buffer_load_dwordx4 v7, s[24:27], s75 offen offset:3072 lds\n"
"v_exp_f32 v17, v169\n"
"v_add_f32 v20, v20, v16\n"
"v_exp_f32 v18, v170\n"
"s_waitcnt lgkmcnt(2)\n"
"v_mfma_f32_32x32x16_bf16 v[224:239], a[196:199], a[128:131], v[32:47]\n"
"ds_read_b128 a[192:195], v14 offset:24576\n"
"v_add_f32 v20, v20, v17\n"
"v_cvt_pk_bf16_f32 v100, v16, v17\n"
"v_exp_f32 v19, v171\n"
"v_add_f32 v20, v20, v18\n"
"v_mfma_f32_32x32x16_bf16 v[240:255], a[196:199], a[160:163], v[48:63]\n"
"v_exp_f32 v16, v172\n"
"v_add_f32 v20, v20, v19\n"
"v_cvt_pk_bf16_f32 v101, v18, v19\n"
"v_exp_f32 v17, v173\n"
"s_add_u32 s64, s84, 0x8000\n"
"v_mfma_f32_32x32x16_bf16 v[224:239], a[200:203], a[132:135], v[224:239]\n"
"ds_read_b128 a[196:199], v15 offset:24576\n"
"buffer_store_dwordx4 a[252:255], v113, s[28:31], s64 offen sc1\n"
"v_add_f32 v20, v20, v16\n"
"v_exp_f32 v18, v174\n"
"v_add_f32 v20, v20, v17\n"
"v_cvt_pk_bf16_f32 v102, v16, v17\n"
"v_mfma_f32_32x32x16_bf16 v[240:255], a[200:203], a[164:167], v[240:255]\n"
"v_exp_f32 v19, v175\n"
"v_add_f32 v20, v20, v18\n"
"v_exp_f32 v16, v176\n"
"v_add_f32 v20, v20, v19\n"
"v_mfma_f32_32x32x16_bf16 v[224:239], a[204:207], a[136:139], v[224:239]\n"
"ds_read_b64_tr_b16 a[200:201], v112\n"
"v_cvt_pk_bf16_f32 v103, v18, v19\n"
"v_exp_f32 v17, v177\n"
"v_add_f32 v21, v21, v16\n"
"v_exp_f32 v18, v178\n"
"v_mfma_f32_32x32x16_bf16 v[240:255], a[204:207], a[168:171], v[240:255]\n"
"ds_read_b64_tr_b16 a[202:203], v112 offset:2048\n"
"v_add_f32 v21, v21, v17\n"
"v_cvt_pk_bf16_f32 v104, v16, v17\n"
"v_exp_f32 v19, v179\n"
"v_add_f32 v21, v21, v18\n"
"v_mfma_f32_32x32x16_bf16 v[224:239], a[208:211], a[140:143], v[224:239]\n"
"ds_read_b64_tr_b16 a[204:205], v112 offset:512\n"
"v_exp_f32 v16, v180\n"
"v_add_f32 v21, v21, v19\n"
"v_cvt_pk_bf16_f32 v105, v18, v19\n"
"s_add_u32 s64, s84, 0x28000\n"
"v_mfma_f32_32x32x16_bf16 v[240:255], a[208:211], a[172:175], v[240:255]\n"
"ds_read_b64_tr_b16 a[206:207], v112 offset:2560\n"
"buffer_store_dwordx4 a[252:255], v113, s[28:31], s64 offen sc1\n"
"v_exp_f32 v17, v181\n"
"v_add_f32 v21, v21, v16\n"
"v_exp_f32 v18, v182\n"
"s_waitcnt lgkmcnt(4)\n"
"v_mfma_f32_32x32x16_bf16 v[224:239], a[212:215], a[144:147], v[224:239]\n"
"ds_read_b64_tr_b16 a[208:209], v112 offset:1024\n"
"v_add_f32 v21, v21, v17\n"
"v_cvt_pk_bf16_f32 v106, v16, v17\n"
"v_exp_f32 v19, v183\n"
"v_add_f32 v21, v21, v18\n"
"v_mfma_f32_32x32x16_bf16 v[240:255], a[212:215], a[176:179], v[240:255]\n"
"ds_read_b64_tr_b16 a[210:211], v112 offset:3072\n"
"v_exp_f32 v16, v184\n"
"v_add_f32 v21, v21, v19\n"
"v_cvt_pk_bf16_f32 v107, v18, v19\n"
"v_exp_f32 v17, v185\n"
"v_mfma_f32_32x32x16_bf16 v[224:239], a[216:219], a[148:151], v[224:239]\n"
"ds_read_b64_tr_b16 a[212:213], v112 offset:1536\n"
"v_add_f32 v21, v21, v16\n"
"v_exp_f32 v18, v186\n"
"v_add_f32 v21, v21, v17\n"
"v_cvt_pk_bf16_f32 v108, v16, v17\n"
"v_mfma_f32_32x32x16_bf16 v[240:255], a[216:219], a[180:183], v[240:255]\n"
"ds_read_b64_tr_b16 a[214:215], v112 offset:3584\n"
"v_exp_f32 v19, v187\n"
"v_add_f32 v21, v21, v18\n"
"v_exp_f32 v16, v188\n"
"v_add_f32 v21, v21, v19\n"
"v_mfma_f32_32x32x16_bf16 v[224:239], a[192:195], a[152:155], v[224:239]\n"
"ds_read_b64_tr_b16 a[216:217], v112 offset:4096\n"
"v_cvt_pk_bf16_f32 v109, v18, v19\n"
"v_exp_f32 v17, v189\n"
"v_add_f32 v21, v21, v16\n"
"s_add_u32 s64, s84, 0x48000\n"
"v_mfma_f32_32x32x16_bf16 v[240:255], a[192:195], a[184:187], v[240:255]\n"
"ds_read_b64_tr_b16 a[218:219], v112 offset:6144\n"
"buffer_store_dwordx4 a[252:255], v113, s[28:31], s64 offen sc1\n"
"v_exp_f32 v18, v190\n"
"v_add_f32 v21, v21, v17\n"
"v_cvt_pk_bf16_f32 v110, v16, v17\n"
"v_mfma_f32_32x32x16_bf16 v[224:239], a[196:199], a[156:159], v[224:239]\n"
"ds_read_b64_tr_b16 a[192:193], v112 offset:4608\n"
"v_exp_f32 v19, v191\n"
"v_add_f32 v21, v21, v18\n"
"v_add_f32 v21, v21, v19\n"
"v_cvt_pk_bf16_f32 v111, v18, v19\n"
"v_max_f32 v24, v20, v21\n"
"v_cmp_nge_f32 vcc, s43, v24\n"
"v_mfma_f32_32x32x16_bf16 v[240:255], a[196:199], a[188:191], v[240:255]\n"
"ds_read_b64_tr_b16 a[194:195], v112 offset:6656\n"
"s_cbranch_vccnz L_rare0_%=\n"
"L_back0_%=:\n"
"; B(1)\n"
"s_waitcnt lgkmcnt(4)\n"
"v_mfma_f32_32x32x16_bf16 a[0:15], a[200:203], v[64:67], 0\n"
"ds_read_b64_tr_b16 a[196:197], v112 offset:5120\n"
"v_mov_b32 v22, v20\n"
"v_mov_b32 v23, v21\n"
"v_lshrrev_b32 v28, 4, v1\n"
"v_and_b32 v26, 3, v28\n"
"v_mfma_f32_32x32x16_bf16 a[16:31], a[200:203], v[72:75], 0\n"
"ds_read_b64_tr_b16 a[198:199], v112 offset:7168\n"
"v_bfe_u32 v27, v28, 4, 1\n"
"v_lshlrev_b32 v27, 1, v27\n"
"v_xor_b32 v26, v26, v27\n"
"v_lshlrev_b32 v116, 4, v26\n"
"v_mfma_f32_32x32x16_bf16 a[32:47], a[204:207], v[64:67], 0\n"
"ds_read_b64_tr_b16 a[200:201], v112 offset:5632\n"
"v_lshrrev_b32 v26, 5, v28\n"
"v_lshlrev_b32 v26, 4, v26\n"
"v_bfe_u32 v27, v28, 4, 1\n"
"v_lshl_add_u32 v26, v27, 2, v26\n"
"v_mfma_f32_32x32x16_bf16 a[48:63], a[204:207], v[72:75], 0\n"
"ds_read_b64_tr_b16 a[202:203], v112 offset:7680\n"
"v_bfe_u32 v27, v28, 2, 2\n"
"v_add_u32 v26, v26, v27\n"
"v_lshl_add_u32 v116, v26, 10, v116\n"
"v_add_u32 v117, 0x1c00, v116\n"
"v_mfma_f32_32x32x16_bf16 a[64:79], a[208:211], v[64:67], 0\n"
"ds_read_b64_tr_b16 a[204:205], v112 offset:8192\n"
"v_exp_f32 v16, v192\n"
"v_exp_f32 v17, v193\n"
"v_mfma_f32_32x32x16_bf16 a[80:95], a[208:211], v[72:75], 0\n"
"ds_read_b64_tr_b16 a[206:207], v112 offset:10240\n"
"buffer_load_dwordx4 a[224:227], v1, s[68:71], s14 offen\n"
"v_exp_f32 v18, v194\n"
"v_add_f32 v20, v16, v17\n"
"v_mfma_f32_32x32x16_bf16 a[96:111], a[212:215], v[64:67], 0\n"
"ds_read_b64_tr_b16 a[208:209], v112 offset:8704\n"
"v_cvt_pk_bf16_f32 v80, v16, v17\n"
"v_exp_f32 v19, v195\n"
"v_add_f32 v20, v20, v18\n"
"v_mfma_f32_32x32x16_bf16 a[112:127], a[212:215], v[72:75], 0\n"
"ds_read_b64_tr_b16 a[210:211], v112 offset:10752\n"
"v_exp_f32 v16, v196\n"
"v_add_f32 v20, v20, v19\n"
"s_waitcnt lgkmcnt(4)\n"
"v_mfma_f32_32x32x16_bf16 a[0:15], a[216:219], v[68:71], a[0:15]\n"
"ds_read_b64_tr_b16 a[212:213], v112 offset:9216\n"
"v_cvt_pk_bf16_f32 v81, v18, v19\n"
"v_exp_f32 v17, v197\n"
"v_add_f32 v20, v20, v16\n"
"v_mfma_f32_32x32x16_bf16 a[16:31], a[216:219], v[76:79], a[16:31]\n"
"ds_read_b64_tr_b16 a[214:215], v112 offset:11264\n"
"v_exp_f32 v18, v198\n"
"v_add_f32 v20, v20, v17\n"
"v_cvt_pk_bf16_f32 v82, v16, v17\n"
"v_mfma_f32_32x32x16_bf16 a[32:47], a[192:195], v[68:71], a[32:47]\n"
"ds_read_b64_tr_b16 a[216:217], v112 offset:9728\n"
"v_exp_f32 v19, v199\n"
"v_add_f32 v20, v20, v18\n"
"v_mfma_f32_32x32x16_bf16 a[48:63], a[192:195], v[76:79], a[48:63]\n"
"ds_read_b64_tr_b16 a[218:219], v112 offset:11776\n"
"buffer_load_dwordx4 a[228:231], v1, s[68:71], s14 offen offset:1024\n"
"v_exp_f32 v16, v200\n"
"v_add_f32 v20, v20, v19\n"
"v_cvt_pk_bf16_f32 v83, v18, v19\n"
"v_mfma_f32_32x32x16_bf16 a[64:79], a[196:199], v[68:71], a[64:79]\n"
"ds_read_b64_tr_b16 a[192:193], v112 offset:12288\n"
"v_exp_f32 v17, v201\n"
"v_add_f32 v20, v20, v16\n"
"v_mfma_f32_32x32x16_bf16 a[80:95], a[196:199], v[76:79], a[80:95]\n"
"ds_read_b64_tr_b16 a[194:195], v112 offset:14336\n"
"v_exp_f32 v18, v202\n"
"v_add_f32 v20, v20, v17\n"
"v_cvt_pk_bf16_f32 v84, v16, v17\n"
"v_mfma_f32_32x32x16_bf16 a[96:111], a[200:203], v[68:71], a[96:111]\n"
"ds_read_b64_tr_b16 a[196:197], v112 offset:12800\n"
"v_exp_f32 v19, v203\n"
"v_add_f32 v20, v20, v18\n"
"v_exp_f32 v16, v204\n"
"v_mfma_f32_32x32x16_bf16 a[112:127], a[200:203], v[76:79], a[112:127]\n"
"ds_read_b64_tr_b16 a[198:199], v112 offset:14848\n"
"v_add_f32 v20, v20, v19\n"
"v_cvt_pk_bf16_f32 v85, v18, v19\n"
"s_waitcnt lgkmcnt(4)\n"
"v_mfma_f32_32x32x16_bf16 a[0:15], a[204:207], v[96:99], a[0:15]\n"
"ds_read_b64_tr_b16 a[200:201], v112 offset:13312\n"
"v_exp_f32 v17, v205\n"
"v_add_f32 v20, v20, v16\n"
"v_mfma_f32_32x32x16_bf16 a[16:31], a[204:207], v[104:107], a[16:31]\n"
"ds_read_b64_tr_b16 a[202:203], v112 offset:15360\n"
"v_exp_f32 v18, v206\n"
"v_add_f32 v20, v20, v17\n"
"v_cvt_pk_bf16_f32 v86, v16, v17\n"
"v_mfma_f32_32x32x16_bf16 a[32:47], a[208:211], v[96:99], a[32:47]\n"
"ds_read_b64_tr_b16 a[204:205], v112 offset:13824\n"
"v_exp_f32 v19, v207\n"
"v_add_f32 v20, v20, v18\n"
"v_exp_f32 v16, v208\n"
"v_mfma_f32_32x32x16_bf16 a[48:63], a[208:211], v[104:107], a[48:63]\n"
"ds_read_b64_tr_b16 a[206:207], v112 offset:15872\n"
"v_add_f32 v20, v20, v19\n"
"v_cvt_pk_bf16_f32 v87, v18, v19\n"
"v_exp_f32 v17, v209\n"
"v_mfma_f32_32x32x16_bf16 a[64:79], a[212:215], v[96:99], a[64:79]\n"
"v_exp_f32 v18, v210\n"
"v_add_f32 v21, v16, v17\n"
"v_cvt_pk_bf16_f32 v88, v16, v17\n"
"v_mfma_f32_32x32x16_bf16 a[80:95], a[212:215], v[104:107], a[80:95]\n"
"v_exp_f32 v19, v211\n"
"s_waitcnt vmcnt(5)\n"
"s_waitcnt lgkmcnt(0)\n"
"s_barrier\n"
"v_mfma_f32_32x32x16_bf16 a[96:111], a[216:219], v[96:99], a[96:111]\n"
"ds_read_b128 a[208:211], v8\n"
"v_add_f32 v21, v21, v18\n"
"v_exp_f32 v16, v212\n"
"v_add_f32 v21, v21, v19\n"
"v_mfma_f32_32x32x16_bf16 a[112:127], a[216:219], v[104:107], a[112:127]\n"
"ds_read_b128 a[212:215], v9\n"
"v_cvt_pk_bf16_f32 v89, v18, v19\n"
"v_exp_f32 v17, v213\n"
"v_mfma_f32_32x32x16_bf16 a[0:15], a[192:195], v[100:103], a[0:15]\n"
"ds_read_b128 a[216:219], v10\n"
"v_add_f32 v21, v21, v16\n"
"v_exp_f32 v18, v214\n"
"v_add_f32 v21, v21, v17\n"
"v_mfma_f32_32x32x16_bf16 a[16:31], a[192:195], v[108:111], a[16:31]\n"
"v_cvt_pk_bf16_f32 v90, v16, v17\n"
"v_exp_f32 v19, v215\n"
"v_add_f32 v21, v21, v18\n"
"v_mfma_f32_32x32x16_bf16 a[32:47], a[196:199], v[100:103], a[32:47]\n"
"ds_read_b128 a[192:195], v11\n"
"v_exp_f32 v16, v216\n"
"v_add_f32 v21, v21, v19\n"
"v_cvt_pk_bf16_f32 v91, v18, v19\n"
"v_mfma_f32_32x32x16_bf16 a[48:63], a[196:199], v[108:111], a[48:63]\n"
"v_exp_f32 v17, v217\n"
"v_add_f32 v21, v21, v16\n"
"v_exp_f32 v18, v218\n"
"v_add_f32 v21, v21, v17\n"
"v_mfma_f32_32x32x16_bf16 a[64:79], a[200:203], v[100:103], a[64:79]\n"
"ds_read_b128 a[196:199], v12\n"
"v_cvt_pk_bf16_f32 v92, v16, v17\n"
"v_exp_f32 v19, v219\n"
"v_add_f32 v21, v21, v18\n"
"v_mfma_f32_32x32x16_bf16 a[80:95], a[200:203], v[108:111], a[80:95]\n"
"v_exp_f32 v16, v220\n"
"v_add_f32 v21, v21, v19\n"
"v_cvt_pk_bf16_f32 v93, v18, v19\n"
"v_mfma_f32_32x32x16_bf16 a[96:111], a[204:207], v[100:103], a[96:111]\n"
"ds_read_b128 a[200:203], v13\n"
"v_exp_f32 v17, v221\n"
"v_add_f32 v21, v21, v16\n"
"v_exp_f32 v18, v222\n"
"v_mfma_f32_32x32x16_bf16 a[112:127], a[204:207], v[108:111], a[112:127]\n"
"v_add_f32 v21, v21, v17\n"
"v_cvt_pk_bf16_f32 v94, v16, v17\n"
"v_exp_f32 v19, v223\n"
"v_add_f32 v21, v21, v18\n"
"; A(2)\n"
"s_waitcnt lgkmcnt(2)\n"
"v_mfma_f32_32x32x16_bf16 v[128:143], a[208:211], a[128:131], v[32:47]\n"
"ds_read_b128 a[204:207], v14\n"
"s_add_u32 m0, s40, 0x14000\n"
"s_mov_b32 s75, s41\n"
"s_add_u32 s41, s41, 0x4000\n"
"s_and_b32 s41, s41, 0x1ffff\n"
"v_mfma_f32_32x32x16_bf16 v[144:159], a[208:211], a[160:163], v[48:63]\n"
"buffer_load_dwordx4 v2, s[20:23], s41 offen lds\n"
"v_add_f32 v21, v21, v19\n"
"v_cvt_pk_bf16_f32 v95, v18, v19\n"
"v_exp_f32 v16, v224\n"
"v_mfma_f32_32x32x16_bf16 v[128:143], a[212:215], a[132:135], v[128:143]\n"
"ds_read_b128 a[208:211], v15\n"
"v_exp_f32 v17, v225\n"
"v_add_f32 v20, v20, v16\n"
"v_mfma_f32_32x32x16_bf16 v[144:159], a[212:215], a[164:167], v[144:159]\n"
"buffer_load_dwordx4 v3, s[20:23], s41 offen offset:1024 lds\n"
"v_exp_f32 v18, v226\n"
"v_add_f32 v20, v20, v17\n"
"v_cvt_pk_bf16_f32 v96, v16, v17\n"
"v_exp_f32 v19, v227\n"
"v_mfma_f32_32x32x16_bf16 v[128:143], a[216:219], a[136:139], v[128:143]\n"
"ds_read_b128 a[212:215], v8 offset:8192\n"
"v_add_f32 v20, v20, v18\n"
"v_exp_f32 v16, v228\n"
"v_add_f32 v20, v20, v19\n"
"v_mfma_f32_32x32x16_bf16 v[144:159], a[216:219], a[168:171], v[144:159]\n"
"buffer_load_dwordx4 v4, s[20:23], s41 offen offset:2048 lds\n"
"v_cvt_pk_bf16_f32 v97, v18, v19\n"
"v_exp_f32 v17, v229\n"
"v_add_f32 v20, v20, v16\n"
"v_mfma_f32_32x32x16_bf16 v[128:143], a[192:195], a[140:143], v[128:143]\n"
"ds_read_b128 a[216:219], v9 offset:8192\n"
"v_exp_f32 v18, v230\n"
"v_add_f32 v20, v20, v17\n"
"v_cvt_pk_bf16_f32 v98, v16, v17\n"
"v_mfma_f32_32x32x16_bf16 v[144:159], a[192:195], a[172:175], v[144:159]\n"
"buffer_load_dwordx4 v5, s[20:23], s41 offen offset:3072 lds\n"
"v_exp_f32 v19, v231\n"
"v_add_f32 v20, v20, v18\n"
"s_waitcnt lgkmcnt(2)\n"
"v_mfma_f32_32x32x16_bf16 v[128:143], a[196:199], a[144:147], v[128:143]\n"
"ds_read_b128 a[192:195], v10 offset:8192\n"
"v_exp_f32 v16, v232\n"
"v_add_f32 v20, v20, v19\n"
"s_add_u32 m0, s40, 0x18000\n"
"v_mfma_f32_32x32x16_bf16 v[144:159], a[196:199], a[176:179], v[144:159]\n"
"buffer_load_dwordx4 v6, s[24:27], s75 offen lds\n"
"v_cvt_pk_bf16_f32 v99, v18, v19\n"
"v_exp_f32 v17, v233\n"
"v_add_f32 v20, v20, v16\n"
"v_mfma_f32_32x32x16_bf16 v[128:143], a[200:203], a[148:151], v[128:143]\n"
"ds_read_b128 a[196:199], v11 offset:8192\n"
"v_exp_f32 v18, v234\n"
"v_add_f32 v20, v20, v17\n"
"v_cvt_pk_bf16_f32 v100, v16, v17\n"
"v_mfma_f32_32x32x16_bf16 v[144:159], a[200:203], a[180:183], v[144:159]\n"
"buffer_load_dwordx4 v7, s[24:27], s75 offen offset:1024 lds\n"
"v_exp_f32 v19, v235\n"
"v_add_f32 v20, v20, v18\n"
"v_exp_f32 v16, v236\n"
"v_mfma_f32_32x32x16_bf16 v[128:143], a[204:207], a[152:155], v[128:143]\n"
"ds_read_b128 a[200:203], v12 offset:8192\n"
"v_add_f32 v20, v20, v19\n"
"v_cvt_pk_bf16_f32 v101, v18, v19\n"
"v_exp_f32 v17, v237\n"
"v_mfma_f32_32x32x16_bf16 v[144:159], a[204:207], a[184:187], v[144:159]\n"
"buffer_load_dwordx4 v6, s[24:27], s75 offen offset:2048 lds\n"
"v_add_f32 v20, v20, v16\n"
"v_exp_f32 v18, v238\n"
"v_add_f32 v20, v20, v17\n"
"v_mfma_f32_32x32x16_bf16 v[128:143], a[208:211], a[156:159], v[128:143]\n"
"ds_read_b128 a[204:207], v13 offset:8192\n"
"v_cvt_pk_bf16_f32 v102, v16, v17\n"
"v_exp_f32 v19, v239\n"
"v_add_f32 v20, v20, v18\n"
"v_mfma_f32_32x32x16_bf16 v[144:159], a[208:211], a[188:191], v[144:159]\n"
"buffer_load_dwordx4 v7, s[24:27], s75 offen offset:3072 lds\n"
"v_exp_f32 v16, v240\n"
"v_add_f32 v20, v20, v19\n"
"s_waitcnt lgkmcnt(2)\n"
"v_mfma_f32_32x32x16_bf16 v[160:175], a[212:215], a[128:131], v[32:47]\n"
"ds_read_b128 a[208:211], v14 offset:8192\n"
"v_cvt_pk_bf16_f32 v103, v18, v19\n"
"v_exp_f32 v17, v241\n"
"v_add_f32 v21, v21, v16\n"
"v_mfma_f32_32x32x16_bf16 v[176:191], a[212:215], a[160:163], v[48:63]\n"
"v_exp_f32 v18, v242\n"
"v_add_f32 v21, v21, v17\n"
"v_cvt_pk_bf16_f32 v104, v16, v17\n"
"s_add_u32 s64, s84, 0x68000\n"
"v_mfma_f32_32x32x16_bf16 v[160:175], a[216:219], a[132:135], v[160:175]\n"
"ds_read_b128 a[212:215], v15 offset:8192\n"
"buffer_store_dwordx4 a[252:255], v113, s[28:31], s64 offen sc1\n"
"v_exp_f32 v19, v243\n"
"v_add_f32 v21, v21, v18\n"
"v_mfma_f32_32x32x16_bf16 v[176:191], a[216:219], a[164:167], v[176:191]\n"
"v_exp_f32 v16, v244\n"
"v_add_f32 v21, v21, v19\n"
"v_cvt_pk_bf16_f32 v105, v18, v19\n"
"v_exp_f32 v17, v245\n"
"v_mfma_f32_32x32x16_bf16 v[160:175], a[192:195], a[136:139], v[160:175]\n"
"ds_read_b64_tr_b16 a[216:217], v112 offset:16384\n"
"v_add_f32 v21, v21, v16\n"
"v_exp_f32 v18, v246\n"
"v_add_f32 v21, v21, v17\n"
"v_mfma_f32_32x32x16_bf16 v[176:191], a[192:195], a[168:171], v[176:191]\n"
"ds_read_b64_tr_b16 a[218:219], v112 offset:18432\n"
"v_cvt_pk_bf16_f32 v106, v16, v17\n"
"v_exp_f32 v19, v247\n"
"v_add_f32 v21, v21, v18\n"
"v_mfma_f32_32x32x16_bf16 v[160:175], a[196:199], a[140:143], v[160:175]\n"
"ds_read_b64_tr_b16 a[192:193], v112 offset:16896\n"
"v_exp_f32 v16, v248\n"
"v_add_f32 v21, v21, v19\n"
"s_add_u32 s64, s84, 0xc000\n"
"v_mfma_f32_32x32x16_bf16 v[176:191], a[196:199], a[172:175], v[176:191]\n"
"ds_read_b64_tr_b16 a[194:195], v112 offset:18944\n"
"buffer_store_dwordx4 a[252:255], v113, s[28:31], s64 offen sc1\n"
"v_cvt_pk_bf16_f32 v107, v18, v19\n"
"s_waitcnt lgkmcnt(4)\n"
"v_mfma_f32_32x32x16_bf16 v[160:175], a[200:203], a[144:147], v[160:175]\n"
"ds_read_b64_tr_b16 a[196:197], v112 offset:17408\n"
"v_exp_f32 v17, v249\n"
"v_add_f32 v21, v21, v16\n"
"v_exp_f32 v18, v250\n"
"v_mfma_f32_32x32x16_bf16 v[176:191], a[200:203], a[176:179], v[176:191]\n"
"ds_read_b64_tr_b16 a[198:199], v112 offset:19456\n"
"v_add_f32 v21, v21, v17\n"
"v_cvt_pk_bf16_f32 v108, v16, v17\n"
"v_exp_f32 v19, v251\n"
"v_mfma_f32_32x32x16_bf16 v[160:175], a[204:207], a[148:151], v[160:175]\n"
"ds_read_b64_tr_b16 a[200:201], v112 offset:17920\n"
"v_add_f32 v21, v21, v18\n"
"v_exp_f32 v16, v252\n"
"v_add_f32 v21, v21, v19\n"
"v_mfma_f32_32x32x16_bf16 v[176:191], a[204:207], a[180:183], v[176:191]\n"
"ds_read_b64_tr_b16 a[202:203], v112 offset:19968\n"
"v_cvt_pk_bf16_f32 v109, v18, v19\n"
"v_exp_f32 v17, v253\n"
"v_add_f32 v21, v21, v16\n"
"v_mfma_f32_32x32x16_bf16 v[160:175], a[208:211], a[152:155], v[160:175]\n"
"ds_read_b64_tr_b16 a[204:205], v112 offset:20480\n"
"v_exp_f32 v18, v254\n"
"v_add_f32 v21, v21, v17\n"
"v_cvt_pk_bf16_f32 v110, v16, v17\n"
"s_add_u32 s64, s84, 0x2c000\n"
"v_mfma_f32_32x32x16_bf16 v[176:191], a[208:211], a[184:187], v[176:191]\n"
"ds_read_b64_tr_b16 a[206:207], v112 offset:22528\n"
"buffer_store_dwordx4 a[252:255], v113, s[28:31], s64 offen sc1\n"
"v_exp_f32 v19, v255\n"
"v_add_f32 v21, v21, v18\n"
"v_mfma_f32_32x32x16_bf16 v[160:175], a[212:215], a[156:159], v[160:175]\n"
"ds_read_b64_tr_b16 a[208:209], v112 offset:20992\n"
"v_add_f32 v21, v21, v19\n"
"v_cvt_pk_bf16_f32 v111, v18, v19\n"
"v_max_f32 v24, v20, v21\n"
"v_cmp_nge_f32 vcc, s43, v24\n"
"v_mfma_f32_32x32x16_bf16 v[176:191], a[212:215], a[188:191], v[176:191]\n"
"ds_read_b64_tr_b16 a[210:211], v112 offset:23040\n"
"s_cbranch_vccnz L_rare1_%=\n"
"L_back1_%=:\n"
"; B(2)\n"
"s_waitcnt lgkmcnt(4)\n"
"v_mfma_f32_32x32x16_bf16 a[0:15], a[216:219], v[80:83], a[0:15]\n"
"ds_read_b64_tr_b16 a[212:213], v112 offset:21504\n"
"v_add_f32 v22, v22, v20\n"
"v_add_f32 v23, v23, v21\n"
"v_and_b32 v26, 15, v28\n"
"v_lshlrev_b32 v26, 4, v26\n"
"v_mfma_f32_32x32x16_bf16 a[16:31], a[216:219], v[88:91], a[16:31]\n"
"ds_read_b64_tr_b16 a[214:215], v112 offset:23552\n"
"v_lshrrev_b32 v27, 5, v28\n"
"v_lshl_add_u32 v26, v27, 8, v26\n"
"v_bfe_u32 v27, v28, 4, 1\n"
"v_lshl_add_u32 v29, v27, 10, v26\n"
"s_lshl_b32 s11, s7, 2\n"
"v_mfma_f32_32x32x16_bf16 a[32:47], a[192:195], v[80:83], a[32:47]\n"
"ds_read_b64_tr_b16 a[216:217], v112 offset:22016\n"
"s_sub_u32 s11, s11, s82\n"
"s_add_u32 s15, s11, 0\n"
"s_and_b32 s15, s15, 15\n"
"s_lshl_b32 s15, s15, 12\n"
"v_mfma_f32_32x32x16_bf16 a[48:63], a[192:195], v[88:91], a[48:63]\n"
"ds_read_b64_tr_b16 a[218:219], v112 offset:24064\n"
"v_add_u32 v118, s15, v29\n"
"s_add_u32 s15, s11, 1\n"
"s_and_b32 s15, s15, 15\n"
"s_lshl_b32 s15, s15, 12\n"
"v_mfma_f32_32x32x16_bf16 a[64:79], a[196:199], v[80:83], a[64:79]\n"
"ds_read_b64_tr_b16 a[192:193], v112 offset:24576\n"
"v_add_u32 v119, s15, v29\n"
"s_add_u32 s15, s11, 2\n"
"s_and_b32 s15, s15, 15\n"
"s_lshl_b32 s15, s15, 12\n"
"v_add_u32 v120, s15, v29\n"
"v_mfma_f32_32x32x16_bf16 a[80:95], a[196:199], v[88:91], a[80:95]\n"
"ds_read_b64_tr_b16 a[194:195], v112 offset:26624\n"
"buffer_load_dwordx4 a[232:235], v1, s[68:71], s14 offen offset:2048\n"
"s_add_u32 s15, s11, 3\n"
"s_and_b32 s15, s15, 15\n"
"s_lshl_b32 s15, s15, 12\n"
"v_mfma_f32_32x32x16_bf16 a[96:111], a[200:203], v[80:83], a[96:111]\n"
"ds_read_b64_tr_b16 a[196:197], v112 offset:25088\n"
"v_add_u32 v121, s15, v29\n"
"v_exp_f32 v16, v128\n"
"v_mfma_f32_32x32x16_bf16 a[112:127], a[200:203], v[88:91], a[112:127]\n"
"ds_read_b64_tr_b16 a[198:199], v112 offset:27136\n"
"v_exp_f32 v17, v129\n"
"v_exp_f32 v18, v130\n"
"v_add_f32 v20, v16, v17\n"
"s_waitcnt lgkmcnt(4)\n"
"v_mfma_f32_32x32x16_bf16 a[0:15], a[204:207], v[84:87], a[0:15]\n"
"ds_read_b64_tr_b16 a[200:201], v112 offset:25600\n"
"v_cvt_pk_bf16_f32 v64, v16, v17\n"
"v_exp_f32 v19, v131\n"
"v_add_f32 v20, v20, v18\n"
"v_mfma_f32_32x32x16_bf16 a[16:31], a[204:207], v[92:95], a[16:31]\n"
"ds_read_b64_tr_b16 a[202:203], v112 offset:27648\n"
"v_exp_f32 v16, v132\n"
"v_add_f32 v20, v20, v19\n"
"v_cvt_pk_bf16_f32 v65, v18, v19\n"
"v_mfma_f32_32x32x16_bf16 a[32:47], a[208:211], v[84:87], a[32:47]\n"
"ds_read_b64_tr_b16 a[204:205], v112 offset:26112\n"
"v_exp_f32 v17, v133\n"
"v_add_f32 v20, v20, v16\n"
"v_mfma_f32_32x32x16_bf16 a[48:63], a[208:211], v[92:95], a[48:63]\n"
"ds_read_b64_tr_b16 a[206:207], v112 offset:28160\n"
"buffer_load_dwordx4 a[236:239], v1, s[68:71], s14 offen offset:3072\n"
"v_exp_f32 v18, v134\n"
"v_add_f32 v20, v20, v17\n"
"v_cvt_pk_bf16_f32 v66, v16, v17\n"
"s_add_u32 s14, s14, 0x1000\n"
"v_mfma_f32_32x32x16_bf16 a[64:79], a[212:215], v[84:87], a[64:79]\n"
"ds_read_b64_tr_b16 a[208:209], v112 offset:28672\n"
"v_exp_f32 v19, v135\n"
"v_add_f32 v20, v20, v18\n"
"v_mfma_f32_32x32x16_bf16 a[80:95], a[212:215], v[92:95], a[80:95]\n"
"ds_read_b64_tr_b16 a[210:211], v112 offset:30720\n"
"v_exp_f32 v16, v136\n"
"v_add_f32 v20, v20, v19\n"
"v_cvt_pk_bf16_f32 v67, v18, v19\n"
"v_mfma_f32_32x32x16_bf16 a[96:111], a[216:219], v[84:87], a[96:111]\n"
"ds_read_b64_tr_b16 a[212:213], v112 offset:29184\n"
"v_exp_f32 v17, v137\n"
"v_add_f32 v20, v20, v16\n"
"v_exp_f32 v18, v138\n"
"v_add_f32 v20, v20, v17\n"
"v_mfma_f32_32x32x16_bf16 a[112:127], a[216:219], v[92:95], a[112:127]\n"
"ds_read_b64_tr_b16 a[214:215], v112 offset:31232\n"
"v_cvt_pk_bf16_f32 v68, v16, v17\n"
"v_exp_f32 v19, v139\n"
"s_waitcnt lgkmcnt(4)\n"
"v_mfma_f32_32x32x16_bf16 a[0:15], a[192:195], v[96:99], a[0:15]\n"
"ds_read_b64_tr_b16 a[216:217], v112 offset:29696\n"
"v_add_f32 v20, v20, v18\n"
"v_exp_f32 v16, v140\n"
"v_add_f32 v20, v20, v19\n"
"v_mfma_f32_32x32x16_bf16 a[16:31], a[192:195], v[104:107], a[16:31]\n"
"ds_read_b64_tr_b16 a[218:219], v112 offset:31744\n"
"v_cvt_pk_bf16_f32 v69, v18, v19\n"
"v_exp_f32 v17, v141\n"
"v_add_f32 v20, v20, v16\n"
"v_mfma_f32_32x32x16_bf16 a[32:47], a[196:199], v[96:99], a[32:47]\n"
"ds_read_b64_tr_b16 a[192:193], v112 offset:30208\n"
"v_exp_f32 v18, v142\n"
"v_add_f32 v20, v20, v17\n"
"v_cvt_pk_bf16_f32 v70, v16, v17\n"
"v_mfma_f32_32x32x16_bf16 a[48:63], a[196:199], v[104:107], a[48:63]\n"
"ds_read_b64_tr_b16 a[194:195], v112 offset:32256\n"
"v_exp_f32 v19, v143\n"
"v_add_f32 v20, v20, v18\n"
"v_exp_f32 v16, v144\n"
"v_mfma_f32_32x32x16_bf16 a[64:79], a[200:203], v[96:99], a[64:79]\n"
"v_add_f32 v20, v20, v19\n"
"v_cvt_pk_bf16_f32 v71, v18, v19\n"
"v_exp_f32 v17, v145\n"
"v_exp_f32 v18, v146\n"
"v_mfma_f32_32x32x16_bf16 a[80:95], a[200:203], v[104:107], a[80:95]\n"
"v_add_f32 v21, v16, v17\n"
"s_waitcnt vmcnt(5)\n"
"s_waitcnt lgkmcnt(0)\n"
"s_barrier\n"
"v_mfma_f32_32x32x16_bf16 a[96:111], a[204:207], v[96:99], a[96:111]\n"
"ds_read_b128 a[196:199], v8 offset:16384\n"
"v_cvt_pk_bf16_f32 v72, v16, v17\n"
"v_exp_f32 v19, v147\n"
"v_add_f32 v21, v21, v18\n"
"v_mfma_f32_32x32x16_bf16 a[112:127], a[204:207], v[104:107], a[112:127]\n"
"ds_read_b128 a[200:203], v9 offset:16384\n"
"v_exp_f32 v16, v148\n"
"v_add_f32 v21, v21, v19\n"
"v_cvt_pk_bf16_f32 v73, v18, v19\n"
"v_mfma_f32_32x32x16_bf16 a[0:15], a[208:211], v[100:103], a[0:15]\n"
"ds_read_b128 a[204:207], v10 offset:16384\n"
"v_exp_f32 v17, v149\n"
"v_add_f32 v21, v21, v16\n"
"v_mfma_f32_32x32x16_bf16 a[16:31], a[208:211], v[108:111], a[16:31]\n"
"v_exp_f32 v18, v150\n"
"v_add_f32 v21, v21, v17\n"
"v_cvt_pk_bf16_f32 v74, v16, v17\n"
"v_exp_f32 v19, v151\n"
"v_mfma_f32_32x32x16_bf16 a[32:47], a[212:215], v[100:103], a[32:47]\n"
"ds_read_b128 a[208:211], v11 offset:16384\n"
"v_add_f32 v21, v21, v18\n"
"v_exp_f32 v16, v152\n"
"v_add_f32 v21, v21, v19\n"
"v_mfma_f32_32x32x16_bf16 a[48:63], a[212:215], v[108:111], a[48:63]\n"
"v_cvt_pk_bf16_f32 v75, v18, v19\n"
"v_exp_f32 v17, v153\n"
"v_add_f32 v21, v21, v16\n"
"v_exp_f32 v18, v154\n"
"v_mfma_f32_32x32x16_bf16 a[64:79], a[216:219], v[100:103], a[64:79]\n"
"ds_read_b128 a[212:215], v12 offset:16384\n"
"v_add_f32 v21, v21, v17\n"
"v_cvt_pk_bf16_f32 v76, v16, v17\n"
"v_exp_f32 v19, v155\n"
"v_mfma_f32_32x32x16_bf16 a[80:95], a[216:219], v[108:111], a[80:95]\n"
"v_add_f32 v21, v21, v18\n"
"v_exp_f32 v16, v156\n"
"v_add_f32 v21, v21, v19\n"
"v_cvt_pk_bf16_f32 v77, v18, v19\n"
"v_mfma_f32_32x32x16_bf16 a[96:111], a[192:195], v[100:103], a[96:111]\n"
"ds_read_b128 a[216:219], v13 offset:16384\n"
"v_exp_f32 v17, v157\n"
"v_add_f32 v21, v21, v16\n"
"v_exp_f32 v18, v158\n"
"v_mfma_f32_32x32x16_bf16 a[112:127], a[192:195], v[108:111], a[112:127]\n"
"v_add_f32 v21, v21, v17\n"
"v_cvt_pk_bf16_f32 v78, v16, v17\n"
"v_exp_f32 v19, v159\n"
"v_add_f32 v21, v21, v18\n"
"; A(3)\n"
"s_waitcnt lgkmcnt(2)\n"
"v_mfma_f32_32x32x16_bf16 v[192:207], a[196:199], a[128:131], v[32:47]\n"
"ds_read_b128 a[192:195], v14 offset:16384\n"
"s_add_u32 m0, s40, 0x10000\n"
"s_mov_b32 s75, s41\n"
"s_add_u32 s41, s41, 0x4000\n"
"s_and_b32 s41, s41, 0x1ffff\n"
"v_mfma_f32_32x32x16_bf16 v[208:223], a[196:199], a[160:163], v[48:63]\n"
"buffer_load_dwordx4 v2, s[20:23], s41 offen lds\n"
"v_add_f32 v21, v21, v19\n"
"v_cvt_pk_bf16_f32 v79, v18, v19\n"
"v_exp_f32 v16, v160\n"
"v_mfma_f32_32x32x16_bf16 v[192:207], a[200:203], a[132:135], v[192:207]\n"
"ds_read_b128 a[196:199], v15 offset:16384\n"
"v_exp_f32 v17, v161\n"
"v_add_f32 v20, v20, v16\n"
"v_mfma_f32_32x32x16_bf16 v[208:223], a[200:203], a[164:167], v[208:223]\n"
"buffer_load_dwordx4 v3, s[20:23], s41 offen offset:1024 lds\n"
"v_exp_f32 v18, v162\n"
"v_add_f32 v20, v20, v17\n"
"v_cvt_pk_bf16_f32 v96, v16, v17\n"
"v_mfma_f32_32x32x16_bf16 v[192:207], a[204:207], a[136:139], v[192:207]\n"
"ds_read_b128 a[200:203], v8 offset:24576\n"
"v_exp_f32 v19, v163\n"
"v_add_f32 v20, v20, v18\n"
"v_exp_f32 v16, v164\n"
"v_mfma_f32_32x32x16_bf16 v[208:223], a[204:207], a[168:171], v[208:223]\n"
"buffer_load_dwordx4 v4, s[20:23], s41 offen offset:2048 lds\n"
"v_add_f32 v20, v20, v19\n"
"v_cvt_pk_bf16_f32 v97, v18, v19\n"
"v_exp_f32 v17, v165\n"
"v_mfma_f32_32x32x16_bf16 v[192:207], a[208:211], a[140:143], v[192:207]\n"
"ds_read_b128 a[204:207], v9 offset:24576\n"
"v_add_f32 v20, v20, v16\n"
"v_exp_f32 v18, v166\n"
"v_add_f32 v20, v20, v17\n"
"v_mfma_f32_32x32x16_bf16 v[208:223], a[208:211], a[172:175], v[208:223]\n"
"buffer_load_dwordx4 v5, s[20:23], s41 offen offset:3072 lds\n"
"v_cvt_pk_bf16_f32 v98, v16, v17\n"
"v_exp_f32 v19, v167\n"
"v_add_f32 v20, v20, v18\n"
"s_waitcnt lgkmcnt(2)\n"
"v_mfma_f32_32x32x16_bf16 v[192:207], a[212:215], a[144:147], v[192:207]\n"
"ds_read_b128 a[208:211], v10 offset:24576\n"
"v_exp_f32 v16, v168\n"
"v_add_f32 v20, v20, v19\n"
"s_add_u32 m0, s40, 0x1c000\n"
"v_mfma_f32_32x32x16_bf16 v[208:223], a[212:215], a[176:179], v[208:223]\n"
"buffer_load_dwordx4 v6, s[24:27], s75 offen lds\n"
"v_cvt_pk_bf16_f32 v99, v18, v19\n"
"v_exp_f32 v17, v169\n"
"v_add_f32 v20, v20, v16\n"
"v_mfma_f32_32x32x16_bf16 v[192:207], a[216:219], a[148:151], v[192:207]\n"
"ds_read_b128 a[212:215], v11 offset:24576\n"
"v_exp_f32 v18, v170\n"
"v_add_f32 v20, v20, v17\n"
"v_cvt_pk_bf16_f32 v100, v16, v17\n"
"v_mfma_f32_32x32x16_bf16 v[208:223], a[216:219], a[180:183], v[208:223]\n"
"buffer_load_dwordx4 v7, s[24:27], s75 offen offset:1024 lds\n"
"v_exp_f32 v19, v171\n"
"v_add_f32 v20, v20, v18\n"
"v_mfma_f32_32x32x16_bf16 v[192:207], a[192:195], a[152:155], v[192:207]\n"
"ds_read_b128 a[216:219], v12 offset:24576\n"
"v_exp_f32 v16, v172\n"
"v_add_f32 v20, v20, v19\n"
"v_cvt_pk_bf16_f32 v101, v18, v19\n"
"v_mfma_f32_32x32x16_bf16 v[208:223], a[192:195], a[184:187], v[208:223]\n"
"buffer_load_dwordx4 v6, s[24:27], s75 offen offset:2048 lds\n"
"v_exp_f32 v17, v173\n"
"v_add_f32 v20, v20, v16\n"
"v_exp_f32 v18, v174\n"
"v_mfma_f32_32x32x16_bf16 v[192:207], a[196:199], a[156:159], v[192:207]\n"
"ds_read_b128 a[192:195], v13 offset:24576\n"
"v_add_f32 v20, v20, v17\n"
"v_cvt_pk_bf16_f32 v102, v16, v17\n"
"v_exp_f32 v19, v175\n"
"v_mfma_f32_32x32x16_bf16 v[208:223], a[196:199], a[188:191], v[208:223]\n"
"buffer_load_dwordx4 v7, s[24:27], s75 offen offset:3072 lds\n"
"v_add_f32 v20, v20, v18\n"
"v_exp_f32 v16, v176\n"
"s_waitcnt lgkmcnt(2)\n"
"v_mfma_f32_32x32x16_bf16 v[224:239], a[200:203], a[128:131], v[32:47]\n"
"ds_read_b128 a[196:199], v14 offset:24576\n"
"v_add_f32 v20, v20, v19\n"
"v_cvt_pk_bf16_f32 v103, v18, v19\n"
"v_exp_f32 v17, v177\n"
"v_mfma_f32_32x32x16_bf16 v[240:255], a[200:203], a[160:163], v[48:63]\n"
"v_add_f32 v21, v21, v16\n"
"v_exp_f32 v18, v178\n"
"v_add_f32 v21, v21, v17\n"
"s_add_u32 s64, s84, 0x4c000\n"
"v_mfma_f32_32x32x16_bf16 v[224:239], a[204:207], a[132:135], v[224:239]\n"
"ds_read_b128 a[200:203], v15 offset:24576\n"
"buffer_store_dwordx4 a[252:255], v113, s[28:31], s64 offen sc1\n"
"v_cvt_pk_bf16_f32 v104, v16, v17\n"
"v_exp_f32 v19, v179\n"
"v_mfma_f32_32x32x16_bf16 v[240:255], a[204:207], a[164:167], v[240:255]\n"
"v_add_f32 v21, v21, v18\n"
"v_exp_f32 v16, v180\n"
"v_add_f32 v21, v21, v19\n"
"v_cvt_pk_bf16_f32 v105, v18, v19\n"
"v_mfma_f32_32x32x16_bf16 v[224:239], a[208:211], a[136:139], v[224:239]\n"
"ds_read_b64_tr_b16 a[204:205], v112\n"
"v_exp_f32 v17, v181\n"
"v_add_f32 v21, v21, v16\n"
"v_mfma_f32_32x32x16_bf16 v[240:255], a[208:211], a[168:171], v[240:255]\n"
"ds_read_b64_tr_b16 a[206:207], v112 offset:2048\n"
"v_exp_f32 v18, v182\n"
"v_add_f32 v21, v21, v17\n"
"v_cvt_pk_bf16_f32 v106, v16, v17\n"
"v_mfma_f32_32x32x16_bf16 v[224:239], a[212:215], a[140:143], v[224:239]\n"
"ds_read_b64_tr_b16 a[208:209], v112 offset:512\n"
"v_exp_f32 v19, v183\n"
"v_add_f32 v21, v21, v18\n"
"v_exp_f32 v16, v184\n"
"v_mfma_f32_32x32x16_bf16 v[240:255], a[212:215], a[172:175], v[240:255]\n"
"ds_read_b64_tr_b16 a[210:211], v112 offset:2560\n"
"v_add_f32 v21, v21, v19\n"
"v_cvt_pk_bf16_f32 v107, v18, v19\n"
"v_exp_f32 v17, v185\n"
"s_waitcnt lgkmcnt(4)\n"
"v_mfma_f32_32x32x16_bf16 v[224:239], a[216:219], a[144:147], v[224:239]\n"
"ds_read_b64_tr_b16 a[212:213], v112 offset:1024\n"
"v_add_f32 v21, v21, v16\n"
"v_exp_f32 v18, v186\n"
"s_add_u32 s64, s84, 0x6c000\n"
"v_mfma_f32_32x32x16_bf16 v[240:255], a[216:219], a[176:179], v[240:255]\n"
"ds_read_b64_tr_b16 a[214:215], v112 offset:3072\n"
"buffer_store_dwordx4 a[252:255], v113, s[28:31], s64 offen sc1\n"
"v_add_f32 v21, v21, v17\n"
"v_cvt_pk_bf16_f32 v108, v16, v17\n"
"v_mfma_f32_32x32x16_bf16 v[224:239], a[192:195], a[148:151], v[224:239]\n"
"ds_read_b64_tr_b16 a[216:217], v112 offset:1536\n"
"v_exp_f32 v19, v187\n"
"v_add_f32 v21, v21, v18\n"
"v_exp_f32 v16, v188\n"
"v_mfma_f32_32x32x16_bf16 v[240:255], a[192:195], a[180:183], v[240:255]\n"
"ds_read_b64_tr_b16 a[218:219], v112 offset:3584\n"
"v_add_f32 v21, v21, v19\n"
"v_cvt_pk_bf16_f32 v109, v18, v19\n"
"v_exp_f32 v17, v189\n"
"v_mfma_f32_32x32x16_bf16 v[224:239], a[196:199], a[152:155], v[224:239]\n"
"ds_read_b64_tr_b16 a[192:193], v112 offset:4096\n"
"v_add_f32 v21, v21, v16\n"
"v_exp_f32 v18, v190\n"
"v_add_f32 v21, v21, v17\n"
"v_mfma_f32_32x32x16_bf16 v[240:255], a[196:199], a[184:187], v[240:255]\n"
"ds_read_b64_tr_b16 a[194:195], v112 offset:6144\n"
"v_cvt_pk_bf16_f32 v110, v16, v17\n"
"v_exp_f32 v19, v191\n"
"v_add_f32 v21, v21, v18\n"
"v_mfma_f32_32x32x16_bf16 v[224:239], a[200:203], a[156:159], v[224:239]\n"
"ds_read_b64_tr_b16 a[196:197], v112 offset:4608\n"
"v_add_f32 v21, v21, v19\n"
"v_cvt_pk_bf16_f32 v111, v18, v19\n"
"v_max_f32 v24, v20, v21\n"
"v_cmp_nge_f32 vcc, s43, v24\n"
"v_mfma_f32_32x32x16_bf16 v[240:255], a[200:203], a[188:191], v[240:255]\n"
"ds_read_b64_tr_b16 a[198:199], v112 offset:6656\n"
"s_cbranch_vccnz L_rare2_%=\n"
"L_back2_%=:\n"
"; B(3)\n"
"s_waitcnt lgkmcnt(4)\n"
"v_mfma_f32_32x32x16_bf16 a[0:15], a[204:207], v[64:67], a[0:15]\n"
"ds_read_b64_tr_b16 a[200:201], v112 offset:5120\n"
"v_add_f32 v22, v22, v20\n"
"v_add_f32 v23, v23, v21\n"
"s_lshl_b32 s11, s8, 19\n"
"s_lshl_b32 s15, s7, 6\n"
"v_mfma_f32_32x32x16_bf16 a[16:31], a[204:207], v[72:75], a[16:31]\n"
"ds_read_b64_tr_b16 a[202:203], v112 offset:7168\n"
"s_add_u32 s11, s11, s15\n"
"s_add_u32 s16, s52, s11\n"
"s_addc_u32 s17, s53, 0\n"
"s_and_b32 s17, s17, 0xffff\n"
"v_mfma_f32_32x32x16_bf16 a[32:47], a[208:211], v[64:67], a[32:47]\n"
"ds_read_b64_tr_b16 a[204:205], v112 offset:5632\n"
"s_mov_b32 s18, 0x7ff40\n"
"s_mov_b32 s19, 0x20000\n"
"v_exp_f32 v16, v192\n"
"v_mfma_f32_32x32x16_bf16 a[48:63], a[208:211], v[72:75], a[48:63]\n"
"ds_read_b64_tr_b16 a[206:207], v112 offset:7680\n"
"buffer_load_dwordx4 a[240:243], v115, s[88:91], s13 offen\n"
"v_exp_f32 v17, v193\n"
"v_mfma_f32_32x32x16_bf16 a[64:79], a[212:215], v[64:67], a[64:79]\n"
"ds_read_b64_tr_b16 a[208:209], v112 offset:8192\n"
"v_exp_f32 v18, v194\n"
"v_add_f32 v20, v16, v17\n"
"v_cvt_pk_bf16_f32 v80, v16, v17\n"
"v_mfma_f32_32x32x16_bf16 a[80:95], a[212:215], v[72:75], a[80:95]\n"
"ds_read_b64_tr_b16 a[210:211], v112 offset:10240\n"
"v_exp_f32 v19, v195\n"
"v_add_f32 v20, v20, v18\n"
"v_mfma_f32_32x32x16_bf16 a[96:111], a[216:219], v[64:67], a[96:111]\n"
"ds_read_b64_tr_b16 a[212:213], v112 offset:8704\n"
"v_exp_f32 v16, v196\n"
"v_add_f32 v20, v20, v19\n"
"v_cvt_pk_bf16_f32 v81, v18, v19\n"
"v_mfma_f32_32x32x16_bf16 a[112:127], a[216:219], v[72:75], a[112:127]\n"
"ds_read_b64_tr_b16 a[214:215], v112 offset:10752\n"
"v_exp_f32 v17, v197\n"
"v_add_f32 v20, v20, v16\n"
"s_waitcnt lgkmcnt(4)\n"
"v_mfma_f32_32x32x16_bf16 a[0:15], a[192:195], v[68:71], a[0:15]\n"
"ds_read_b64_tr_b16 a[216:217], v112 offset:9216\n"
"v_exp_f32 v18, v198\n"
"v_add_f32 v20, v20, v17\n"
"v_cvt_pk_bf16_f32 v82, v16, v17\n"
"v_mfma_f32_32x32x16_bf16 a[16:31], a[192:195], v[76:79], a[16:31]\n"
"ds_read_b64_tr_b16 a[218:219], v112 offset:11264\n"
"v_exp_f32 v19, v199\n"
"v_add_f32 v20, v20, v18\n"
"v_mfma_f32_32x32x16_bf16 a[32:47], a[196:199], v[68:71], a[32:47]\n"
"ds_read_b64_tr_b16 a[192:193], v112 offset:9728\n"
"buffer_load_dwordx4 a[244:247], v115, s[92:95], s13 offen\n"
"v_exp_f32 v16, v200\n"
"v_add_f32 v20, v20, v19\n"
"s_add_u32 s13, s13, 0x1c00\n"
"v_mfma_f32_32x32x16_bf16 a[48:63], a[196:199], v[76:79], a[48:63]\n"
"ds_read_b64_tr_b16 a[194:195], v112 offset:11776\n"
"v_cvt_pk_bf16_f32 v83, v18, v19\n"
"v_exp_f32 v17, v201\n"
"v_add_f32 v20, v20, v16\n"
"v_mfma_f32_32x32x16_bf16 a[64:79], a[200:203], v[68:71], a[64:79]\n"
"ds_read_b64_tr_b16 a[196:197], v112 offset:12288\n"
"v_exp_f32 v18, v202\n"
"v_add_f32 v20, v20, v17\n"
"v_cvt_pk_bf16_f32 v84, v16, v17\n"
"v_mfma_f32_32x32x16_bf16 a[80:95], a[200:203], v[76:79], a[80:95]\n"
"ds_read_b64_tr_b16 a[198:199], v112 offset:14336\n"
"v_exp_f32 v19, v203\n"
"v_add_f32 v20, v20, v18\n"
"v_mfma_f32_32x32x16_bf16 a[96:111], a[204:207], v[68:71], a[96:111]\n"
"ds_read_b64_tr_b16 a[200:201], v112 offset:12800\n"
"v_exp_f32 v16, v204\n"
"v_add_f32 v20, v20, v19\n"
"v_cvt_pk_bf16_f32 v85, v18, v19\n"
"v_mfma_f32_32x32x16_bf16 a[112:127], a[204:207], v[76:79], a[112:127]\n"
"ds_read_b64_tr_b16 a[202:203], v112 offset:14848\n"
"v_exp_f32 v17, v205\n"
"v_add_f32 v20, v20, v16\n"
"s_waitcnt lgkmcnt(4)\n"
"v_mfma_f32_32x32x16_bf16 a[0:15], a[208:211], v[96:99], a[0:15]\n"
"ds_read_b64_tr_b16 a[204:205], v112 offset:13312\n"
"v_exp_f32 v18, v206\n"
"v_add_f32 v20, v20, v17\n"
"v_cvt_pk_bf16_f32 v86, v16, v17\n"
"v_mfma_f32_32x32x16_bf16 a[16:31], a[208:211], v[104:107], a[16:31]\n"
"ds_read_b64_tr_b16 a[206:207], v112 offset:15360\n"
"v_exp_f32 v19, v207\n"
"v_add_f32 v20, v20, v18\n"
"v_mfma_f32_32x32x16_bf16 a[32:47], a[212:215], v[96:99], a[32:47]\n"
"ds_read_b64_tr_b16 a[208:209], v112 offset:13824\n"
"v_exp_f32 v16, v208\n"
"v_add_f32 v20, v20, v19\n"
"v_cvt_pk_bf16_f32 v87, v18, v19\n"
"v_mfma_f32_32x32x16_bf16 a[48:63], a[212:215], v[104:107], a[48:63]\n"
"ds_read_b64_tr_b16 a[210:211], v112 offset:15872\n"
"v_exp_f32 v17, v209\n"
"v_exp_f32 v18, v210\n"
"v_add_f32 v21, v16, v17\n"
"v_mfma_f32_32x32x16_bf16 a[64:79], a[216:219], v[96:99], a[64:79]\n"
"global_load_dwordx4 a[248:251], v1, s[44:45]\n"
"v_cvt_pk_bf16_f32 v88, v16, v17\n"
"v_exp_f32 v19, v211\n"
"v_add_f32 v21, v21, v18\n"
"v_mfma_f32_32x32x16_bf16 a[80:95], a[216:219], v[104:107], a[80:95]\n"
"s_waitcnt vmcnt(5)\n"
"s_waitcnt lgkmcnt(0)\n"
"s_barrier\n"
"v_mfma_f32_32x32x16_bf16 a[96:111], a[192:195], v[96:99], a[96:111]\n"
"ds_read_b128 a[212:215], v8\n"
"v_exp_f32 v16, v212\n"
"v_add_f32 v21, v21, v19\n"
"v_cvt_pk_bf16_f32 v89, v18, v19\n"
"v_mfma_f32_32x32x16_bf16 a[112:127], a[192:195], v[104:107], a[112:127]\n"
"ds_read_b128 a[216:219], v9\n"
"v_exp_f32 v17, v213\n"
"v_add_f32 v21, v21, v16\n"
"v_mfma_f32_32x32x16_bf16 a[0:15], a[196:199], v[100:103], a[0:15]\n"
"ds_read_b128 a[192:195], v10\n"
"v_exp_f32 v18, v214\n"
"v_add_f32 v21, v21, v17\n"
"v_cvt_pk_bf16_f32 v90, v16, v17\n"
"v_mfma_f32_32x32x16_bf16 a[16:31], a[196:199], v[108:111], a[16:31]\n"
"v_exp_f32 v19, v215\n"
"v_add_f32 v21, v21, v18\n"
"v_exp_f32 v16, v216\n"
"v_mfma_f32_32x32x16_bf16 a[32:47], a[200:203], v[100:103], a[32:47]\n"
"ds_read_b128 a[196:199], v11\n"
"v_add_f32 v21, v21, v19\n"
"v_cvt_pk_bf16_f32 v91, v18, v19\n"
"v_exp_f32 v17, v217\n"
"v_mfma_f32_32x32x16_bf16 a[48:63], a[200:203], v[108:111], a[48:63]\n"
"v_add_f32 v21, v21, v16\n"
"v_exp_f32 v18, v218\n"
"v_add_f32 v21, v21, v17\n"
"v_cvt_pk_bf16_f32 v92, v16, v17\n"
"v_mfma_f32_32x32x16_bf16 a[64:79], a[204:207], v[100:103], a[64:79]\n"
"ds_read_b128 a[200:203], v12\n"
"v_exp_f32 v19, v219\n"
"v_add_f32 v21, v21, v18\n"
"v_mfma_f32_32x32x16_bf16 a[80:95], a[204:207], v[108:111], a[80:95]\n"
"v_exp_f32 v16, v220\n"
"v_add_f32 v21, v21, v19\n"
"v_cvt_pk_bf16_f32 v93, v18, v19\n"
"v_mfma_f32_32x32x16_bf16 a[96:111], a[208:211], v[100:103], a[96:111]\n"
"ds_read_b128 a[204:207], v13\n"
"v_exp_f32 v17, v221\n"
"v_add_f32 v21, v21, v16\n"
"v_exp_f32 v18, v222\n"
"v_mfma_f32_32x32x16_bf16 a[112:127], a[208:211], v[108:111], a[112:127]\n"
"v_add_f32 v21, v21, v17\n"
"v_cvt_pk_bf16_f32 v94, v16, v17\n"
"v_exp_f32 v19, v223\n"
"v_add_f32 v21, v21, v18\n"
"; A(4)\n"
"s_waitcnt lgkmcnt(2)\n"
"v_mfma_f32_32x32x16_bf16 v[128:143], a[212:215], a[128:131], v[32:47]\n"
"ds_read_b128 a[208:211], v14\n"
"s_add_u32 m0, s40, 0x14000\n"
"s_mov_b32 s75, s41\n"
"s_add_u32 s41, s41, 0x4000\n"
"s_and_b32 s41, s41, 0x1ffff\n"
"v_mfma_f32_32x32x16_bf16 v[144:159], a[212:215], a[160:163], v[48:63]\n"
"buffer_load_dwordx4 v2, s[20:23], s41 offen lds\n"
"v_add_f32 v21, v21, v19\n"
"v_cvt_pk_bf16_f32 v95, v18, v19\n"
"v_exp_f32 v16, v224\n"
"v_mfma_f32_32x32x16_bf16 v[128:143], a[216:219], a[132:135], v[128:143]\n"
"ds_read_b128 a[212:215], v15\n"
"v_exp_f32 v17, v225\n"
"v_add_f32 v20, v20, v16\n"
"v_mfma_f32_32x32x16_bf16 v[144:159], a[216:219], a[164:167], v[144:159]\n"
"buffer_load_dwordx4 v3, s[20:23], s41 offen offset:1024 lds\n"
"v_exp_f32 v18, v226\n"
"v_add_f32 v20, v20, v17\n"
"v_cvt_pk_bf16_f32 v96, v16, v17\n"
"v_mfma_f32_32x32x16_bf16 v[128:143], a[192:195], a[136:139], v[128:143]\n"
"ds_read_b128 a[216:219], v8 offset:8192\n"
"v_exp_f32 v19, v227\n"
"v_add_f32 v20, v20, v18\n"
"v_exp_f32 v16, v228\n"
"v_mfma_f32_32x32x16_bf16 v[144:159], a[192:195], a[168:171], v[144:159]\n"
"buffer_load_dwordx4 v4, s[20:23], s41 offen offset:2048 lds\n"
"v_add_f32 v20, v20, v19\n"
"v_cvt_pk_bf16_f32 v97, v18, v19\n"
"v_exp_f32 v17, v229\n"
"v_mfma_f32_32x32x16_bf16 v[128:143], a[196:199], a[140:143], v[128:143]\n"
"ds_read_b128 a[192:195], v9 offset:8192\n"
"v_add_f32 v20, v20, v16\n"
"v_exp_f32 v18, v230\n"
"v_add_f32 v20, v20, v17\n"
"v_mfma_f32_32x32x16_bf16 v[144:159], a[196:199], a[172:175], v[144:159]\n"
"buffer_load_dwordx4 v5, s[20:23], s41 offen offset:3072 lds\n"
"v_cvt_pk_bf16_f32 v98, v16, v17\n"
"v_exp_f32 v19, v231\n"
"v_add_f32 v20, v20, v18\n"
"s_waitcnt lgkmcnt(2)\n"
"v_mfma_f32_32x32x16_bf16 v[128:143], a[200:203], a[144:147], v[128:143]\n"
"ds_read_b128 a[196:199], v10 offset:8192\n"
"v_exp_f32 v16, v232\n"
"v_add_f32 v20, v20, v19\n"
"s_add_u32 m0, s40, 0x18000\n"
"v_mfma_f32_32x32x16_bf16 v[144:159], a[200:203], a[176:179], v[144:159]\n"
"buffer_load_dwordx4 v6, s[24:27], s75 offen lds\n"
"v_cvt_pk_bf16_f32 v99, v18, v19\n"
"v_exp_f32 v17, v233\n"
"v_add_f32 v20, v20, v16\n"
"v_mfma_f32_32x32x16_bf16 v[128:143], a[204:207], a[148:151], v[128:143]\n"
"ds_read_b128 a[200:203], v11 offset:8192\n"
"v_exp_f32 v18, v234\n"
"v_add_f32 v20, v20, v17\n"
"v_cvt_pk_bf16_f32 v100, v16, v17\n"
"v_mfma_f32_32x32x16_bf16 v[144:159], a[204:207], a[180:183], v[144:159]\n"
"buffer_load_dwordx4 v7, s[24:27], s75 offen offset:1024 lds\n"
"v_exp_f32 v19, v235\n"
"v_add_f32 v20, v20, v18\n"
"v_mfma_f32_32x32x16_bf16 v[128:143], a[208:211], a[152:155], v[128:143]\n"
"ds_read_b128 a[204:207], v12 offset:8192\n"
"v_exp_f32 v16, v236\n"
"v_add_f32 v20, v20, v19\n"
"v_cvt_pk_bf16_f32 v101, v18, v19\n"
"v_mfma_f32_32x32x16_bf16 v[144:159], a[208:211], a[184:187], v[144:159]\n"
"buffer_load_dwordx4 v6, s[24:27], s75 offen offset:2048 lds\n"
"v_exp_f32 v17, v237\n"
"v_add_f32 v20, v20, v16\n"
"v_exp_f32 v18, v238\n"
"v_mfma_f32_32x32x16_bf16 v[128:143], a[212:215], a[156:159], v[128:143]\n"
"ds_read_b128 a[208:211], v13 offset:8192\n"
"v_add_f32 v20, v20, v17\n"
"v_cvt_pk_bf16_f32 v102, v16, v17\n"
"v_exp_f32 v19, v239\n"
"v_mfma_f32_32x32x16_bf16 v[144:159], a[212:215], a[188:191], v[144:159]\n"
"buffer_load_dwordx4 v7, s[24:27], s75 offen offset:3072 lds\n"
"v_add_f32 v20, v20, v18\n"
"v_exp_f32 v16, v240\n"
"s_waitcnt lgkmcnt(2)\n"
"v_mfma_f32_32x32x16_bf16 v[160:175], a[216:219], a[128:131], v[32:47]\n"
"ds_read_b128 a[212:215], v14 offset:8192\n"
"v_add_f32 v20, v20, v19\n"
"v_cvt_pk_bf16_f32 v103, v18, v19\n"
"v_exp_f32 v17, v241\n"
"v_mfma_f32_32x32x16_bf16 v[176:191], a[216:219], a[160:163], v[48:63]\n"
"v_add_f32 v21, v21, v16\n"
"v_exp_f32 v18, v242\n"
"v_add_f32 v21, v21, v17\n"
"s_add_u32 s64, s85, 0xc000\n"
"v_mfma_f32_32x32x16_bf16 v[160:175], a[192:195], a[132:135], v[160:175]\n"
"ds_read_b128 a[216:219], v15 offset:8192\n"
"buffer_store_dwordx4 a[252:255], v113, s[28:31], s64 offen sc1\n"
"v_cvt_pk_bf16_f32 v104, v16, v17\n"
"v_exp_f32 v19, v243\n"
"v_mfma_f32_32x32x16_bf16 v[176:191], a[192:195], a[164:167], v[176:191]\n"
"v_add_f32 v21, v21, v18\n"
"v_exp_f32 v16, v244\n"
"v_add_f32 v21, v21, v19\n"
"v_cvt_pk_bf16_f32 v105, v18, v19\n"
"v_mfma_f32_32x32x16_bf16 v[160:175], a[196:199], a[136:139], v[160:175]\n"
"ds_read_b64_tr_b16 a[192:193], v112 offset:16384\n"
"v_exp_f32 v17, v245\n"
"v_add_f32 v21, v21, v16\n"
"v_mfma_f32_32x32x16_bf16 v[176:191], a[196:199], a[168:171], v[176:191]\n"
"ds_read_b64_tr_b16 a[194:195], v112 offset:18432\n"
"v_exp_f32 v18, v246\n"
"v_add_f32 v21, v21, v17\n"
"v_cvt_pk_bf16_f32 v106, v16, v17\n"
"v_mfma_f32_32x32x16_bf16 v[160:175], a[200:203], a[140:143], v[160:175]\n"
"ds_read_b64_tr_b16 a[196:197], v112 offset:16896\n"
"v_exp_f32 v19, v247\n"
"v_add_f32 v21, v21, v18\n"
"v_exp_f32 v16, v248\n"
"v_mfma_f32_32x32x16_bf16 v[176:191], a[200:203], a[172:175], v[176:191]\n"
"ds_read_b64_tr_b16 a[198:199], v112 offset:18944\n"
"v_add_f32 v21, v21, v19\n"
"v_cvt_pk_bf16_f32 v107, v18, v19\n"
"v_exp_f32 v17, v249\n"
"s_waitcnt lgkmcnt(4)\n"
"v_mfma_f32_32x32x16_bf16 v[160:175], a[204:207], a[144:147], v[160:175]\n"
"ds_read_b64_tr_b16 a[200:201], v112 offset:17408\n"
"v_add_f32 v21, v21, v16\n"
"v_exp_f32 v18, v250\n"
"s_add_u32 s64, s85, 0x2c000\n"
"v_mfma_f32_32x32x16_bf16 v[176:191], a[204:207], a[176:179], v[176:191]\n"
"ds_read_b64_tr_b16 a[202:203], v112 offset:19456\n"
"buffer_store_dwordx4 a[252:255], v113, s[28:31], s64 offen sc1\n"
"v_add_f32 v21, v21, v17\n"
"v_cvt_pk_bf16_f32 v108, v16, v17\n"
"v_mfma_f32_32x32x16_bf16 v[160:175], a[208:211], a[148:151], v[160:175]\n"
"ds_read_b64_tr_b16 a[204:205], v112 offset:17920\n"
"v_exp_f32 v19, v251\n"
"v_add_f32 v21, v21, v18\n"
"v_exp_f32 v16, v252\n"
"v_mfma_f32_32x32x16_bf16 v[176:191], a[208:211], a[180:183], v[176:191]\n"
"ds_read_b64_tr_b16 a[206:207], v112 offset:19968\n"
"v_add_f32 v21, v21, v19\n"
"v_cvt_pk_bf16_f32 v109, v18, v19\n"
"v_exp_f32 v17, v253\n"
"v_mfma_f32_32x32x16_bf16 v[160:175], a[212:215], a[152:155], v[160:175]\n"
"ds_read_b64_tr_b16 a[208:209], v112 offset:20480\n"
"v_add_f32 v21, v21, v16\n"
"v_exp_f32 v18, v254\n"
"v_add_f32 v21, v21, v17\n"
"v_mfma_f32_32x32x16_bf16 v[176:191], a[212:215], a[184:187], v[176:191]\n"
"ds_read_b64_tr_b16 a[210:211], v112 offset:22528\n"
"v_cvt_pk_bf16_f32 v110, v16, v17\n"
"v_exp_f32 v19, v255\n"
"v_add_f32 v21, v21, v18\n"
"v_mfma_f32_32x32x16_bf16 v[160:175], a[216:219], a[156:159], v[160:175]\n"
"ds_read_b64_tr_b16 a[212:213], v112 offset:20992\n"
"v_add_f32 v21, v21, v19\n"
"v_cvt_pk_bf16_f32 v111, v18, v19\n"
"v_max_f32 v24, v20, v21\n"
"v_cmp_nge_f32 vcc, s43, v24\n"
"v_mfma_f32_32x32x16_bf16 v[176:191], a[216:219], a[188:191], v[176:191]\n"
"ds_read_b64_tr_b16 a[214:215], v112 offset:23040\n"
"s_cbranch_vccnz L_rare3_%=\n"
"L_back3_%=:\n"
"; B(4)\n"
"s_waitcnt lgkmcnt(4)\n"
"v_mfma_f32_32x32x16_bf16 a[0:15], a[192:195], v[80:83], a[0:15]\n"
"ds_read_b64_tr_b16 a[216:217], v112 offset:21504\n"
"v_add_f32 v22, v22, v20\n"
"v_add_f32 v23, v23, v21\n"
"v_exp_f32 v16, v128\n"
"v_mfma_f32_32x32x16_bf16 a[16:31], a[192:195], v[88:91], a[16:31]\n"
"ds_read_b64_tr_b16 a[218:219], v112 offset:23552\n"
"v_exp_f32 v17, v129\n"
"v_mfma_f32_32x32x16_bf16 a[32:47], a[196:199], v[80:83], a[32:47]\n"
"ds_read_b64_tr_b16 a[192:193], v112 offset:22016\n"
"v_exp_f32 v18, v130\n"
"v_add_f32 v20, v16, v17\n"
"v_cvt_pk_bf16_f32 v64, v16, v17\n"
"v_mfma_f32_32x32x16_bf16 a[48:63], a[196:199], v[88:91], a[48:63]\n"
"ds_read_b64_tr_b16 a[194:195], v112 offset:24064\n"
"buffer_load_dwordx4 a[224:227], v115, s[88:91], s13 offen\n"
"v_exp_f32 v19, v131\n"
"v_add_f32 v20, v20, v18\n"
"v_mfma_f32_32x32x16_bf16 a[64:79], a[200:203], v[80:83], a[64:79]\n"
"ds_read_b64_tr_b16 a[196:197], v112 offset:24576\n"
"v_exp_f32 v16, v132\n"
"v_add_f32 v20, v20, v19\n"
"v_cvt_pk_bf16_f32 v65, v18, v19\n"
"v_mfma_f32_32x32x16_bf16 a[80:95], a[200:203], v[88:91], a[80:95]\n"
"ds_read_b64_tr_b16 a[198:199], v112 offset:26624\n"
"v_exp_f32 v17, v133\n"
"v_add_f32 v20, v20, v16\n"
"v_mfma_f32_32x32x16_bf16 a[96:111], a[204:207], v[80:83], a[96:111]\n"
"ds_read_b64_tr_b16 a[200:201], v112 offset:25088\n"
"v_exp_f32 v18, v134\n"
"v_add_f32 v20, v20, v17\n"
"v_cvt_pk_bf16_f32 v66, v16, v17\n"
"v_mfma_f32_32x32x16_bf16 a[112:127], a[204:207], v[88:91], a[112:127]\n"
"ds_read_b64_tr_b16 a[202:203], v112 offset:27136\n"
"v_exp_f32 v19, v135\n"
"v_add_f32 v20, v20, v18\n"
"s_waitcnt lgkmcnt(4)\n"
"v_mfma_f32_32x32x16_bf16 a[0:15], a[208:211], v[84:87], a[0:15]\n"
"ds_read_b64_tr_b16 a[204:205], v112 offset:25600\n"
"v_exp_f32 v16, v136\n"
"v_add_f32 v20, v20, v19\n"
"v_cvt_pk_bf16_f32 v67, v18, v19\n"
"v_mfma_f32_32x32x16_bf16 a[16:31], a[208:211], v[92:95], a[16:31]\n"
"ds_read_b64_tr_b16 a[206:207], v112 offset:27648\n"
"v_exp_f32 v17, v137\n"
"v_add_f32 v20, v20, v16\n"
"v_mfma_f32_32x32x16_bf16 a[32:47], a[212:215], v[84:87], a[32:47]\n"
"ds_read_b64_tr_b16 a[208:209], v112 offset:26112\n"
"buffer_load_dwordx4 a[228:231], v115, s[92:95], s13 offen\n"
"s_add_u32 s13, s13, 0x1c00\n"
"s_add_u32 s68, s58, s65\n"
"s_addc_u32 s69, s59, 0\n"
"s_and_b32 s69, s69, 0xffff\n"
"s_mov_b32 s70, 0x40000\n"
"s_mov_b32 s71, 0x20000\n"
"v_mfma_f32_32x32x16_bf16 a[48:63], a[212:215], v[92:95], a[48:63]\n"
"ds_read_b64_tr_b16 a[210:211], v112 offset:28160\n"
"v_exp_f32 v18, v138\n"
"v_mfma_f32_32x32x16_bf16 a[64:79], a[216:219], v[84:87], a[64:79]\n"
"ds_read_b64_tr_b16 a[212:213], v112 offset:28672\n"
"v_add_f32 v20, v20, v17\n"
"v_cvt_pk_bf16_f32 v68, v16, v17\n"
"v_mfma_f32_32x32x16_bf16 a[80:95], a[216:219], v[92:95], a[80:95]\n"
"ds_read_b64_tr_b16 a[214:215], v112 offset:30720\n"
"v_exp_f32 v19, v139\n"
"v_add_f32 v20, v20, v18\n"
"v_exp_f32 v16, v140\n"
"v_mfma_f32_32x32x16_bf16 a[96:111], a[192:195], v[84:87], a[96:111]\n"
"ds_read_b64_tr_b16 a[216:217], v112 offset:29184\n"
"v_add_f32 v20, v20, v19\n"
"v_cvt_pk_bf16_f32 v69, v18, v19\n"
"v_exp_f32 v17, v141\n"
"v_mfma_f32_32x32x16_bf16 a[112:127], a[192:195], v[92:95], a[112:127]\n"
"ds_read_b64_tr_b16 a[218:219], v112 offset:31232\n"
"v_add_f32 v20, v20, v16\n"
"v_exp_f32 v18, v142\n"
"s_waitcnt lgkmcnt(4)\n"
"v_mfma_f32_32x32x16_bf16 a[0:15], a[196:199], v[96:99], a[0:15]\n"
"ds_read_b64_tr_b16 a[192:193], v112 offset:29696\n"
"v_add_f32 v20, v20, v17\n"
"v_cvt_pk_bf16_f32 v70, v16, v17\n"
"v_mfma_f32_32x32x16_bf16 a[16:31], a[196:199], v[104:107], a[16:31]\n"
"ds_read_b64_tr_b16 a[194:195], v112 offset:31744\n"
"v_exp_f32 v19, v143\n"
"v_add_f32 v20, v20, v18\n"
"v_exp_f32 v16, v144\n"
"v_mfma_f32_32x32x16_bf16 a[32:47], a[200:203], v[96:99], a[32:47]\n"
"ds_read_b64_tr_b16 a[196:197], v112 offset:30208\n"
"v_add_f32 v20, v20, v19\n"
"v_cvt_pk_bf16_f32 v71, v18, v19\n"
"v_exp_f32 v17, v145\n"
"v_mfma_f32_32x32x16_bf16 a[48:63], a[200:203], v[104:107], a[48:63]\n"
"ds_read_b64_tr_b16 a[198:199], v112 offset:32256\n"
"v_exp_f32 v18, v146\n"
"v_add_f32 v21, v16, v17\n"
"v_mfma_f32_32x32x16_bf16 a[64:79], a[204:207], v[96:99], a[64:79]\n"
"v_cvt_pk_bf16_f32 v72, v16, v17\n"
"v_exp_f32 v19, v147\n"
"v_add_f32 v21, v21, v18\n"
"v_mfma_f32_32x32x16_bf16 a[80:95], a[204:207], v[104:107], a[80:95]\n"
"v_exp_f32 v16, v148\n"
"s_waitcnt vmcnt(4)\n"
"s_waitcnt lgkmcnt(0)\n"
"s_barrier\n"
"v_mfma_f32_32x32x16_bf16 a[96:111], a[208:211], v[96:99], a[96:111]\n"
"ds_read_b128 a[200:203], v8 offset:16384\n"
"v_add_f32 v21, v21, v19\n"
"v_cvt_pk_bf16_f32 v73, v18, v19\n"
"v_exp_f32 v17, v149\n"
"v_mfma_f32_32x32x16_bf16 a[112:127], a[208:211], v[104:107], a[112:127]\n"
"ds_read_b128 a[204:207], v9 offset:16384\n"
"v_add_f32 v21, v21, v16\n"
"v_mfma_f32_32x32x16_bf16 a[0:15], a[212:215], v[100:103], a[0:15]\n"
"ds_read_b128 a[208:211], v10 offset:16384\n"
"v_exp_f32 v18, v150\n"
"v_add_f32 v21, v21, v17\n"
"v_cvt_pk_bf16_f32 v74, v16, v17\n"
"v_mfma_f32_32x32x16_bf16 a[16:31], a[212:215], v[108:111], a[16:31]\n"
"v_exp_f32 v19, v151\n"
"v_add_f32 v21, v21, v18\n"
"v_exp_f32 v16, v152\n"
"v_add_f32 v21, v21, v19\n"
"v_mfma_f32_32x32x16_bf16 a[32:47], a[216:219], v[100:103], a[32:47]\n"
"ds_read_b128 a[212:215], v11 offset:16384\n"
"v_cvt_pk_bf16_f32 v75, v18, v19\n"
"v_exp_f32 v17, v153\n"
"v_mfma_f32_32x32x16_bf16 a[48:63], a[216:219], v[108:111], a[48:63]\n"
"v_add_f32 v21, v21, v16\n"
"v_exp_f32 v18, v154\n"
"v_add_f32 v21, v21, v17\n"
"v_cvt_pk_bf16_f32 v76, v16, v17\n"
"v_mfma_f32_32x32x16_bf16 a[64:79], a[192:195], v[100:103], a[64:79]\n"
"ds_read_b128 a[216:219], v12 offset:16384\n"
"v_exp_f32 v19, v155\n"
"v_add_f32 v21, v21, v18\n"
"v_mfma_f32_32x32x16_bf16 a[80:95], a[192:195], v[108:111], a[80:95]\n"
"v_exp_f32 v16, v156\n"
"v_add_f32 v21, v21, v19\n"
"v_cvt_pk_bf16_f32 v77, v18, v19\n"
"v_exp_f32 v17, v157\n"
"v_mfma_f32_32x32x16_bf16 a[96:111], a[196:199], v[100:103], a[96:111]\n"
"ds_read_b128 a[192:195], v13 offset:16384\n"
"v_add_f32 v21, v21, v16\n"
"v_exp_f32 v18, v158\n"
"v_mfma_f32_32x32x16_bf16 a[112:127], a[196:199], v[108:111], a[112:127]\n"
"v_add_f32 v21, v21, v17\n"
"v_cvt_pk_bf16_f32 v78, v16, v17\n"
"v_exp_f32 v19, v159\n"
"v_add_f32 v21, v21, v18\n"
"; A(5)\n"
"s_waitcnt lgkmcnt(2)\n"
"v_mfma_f32_32x32x16_bf16 v[192:207], a[200:203], a[128:131], v[32:47]\n"
"ds_read_b128 a[196:199], v14 offset:16384\n"
"s_add_u32 m0, s40, 0x10000\n"
"s_mov_b32 s75, s41\n"
"s_add_u32 s41, s41, 0x4000\n"
"s_and_b32 s41, s41, 0x1ffff\n"
"v_mfma_f32_32x32x16_bf16 v[208:223], a[200:203], a[160:163], v[48:63]\n"
"buffer_load_dwordx4 v2, s[20:23], s41 offen lds\n"
"v_add_f32 v21, v21, v19\n"
"v_cvt_pk_bf16_f32 v79, v18, v19\n"
"v_exp_f32 v16, v160\n"
"v_mfma_f32_32x32x16_bf16 v[192:207], a[204:207], a[132:135], v[192:207]\n"
"ds_read_b128 a[200:203], v15 offset:16384\n"
"v_exp_f32 v17, v161\n"
"v_add_f32 v20, v20, v16\n"
"v_mfma_f32_32x32x16_bf16 v[208:223], a[204:207], a[164:167], v[208:223]\n"
"buffer_load_dwordx4 v3, s[20:23], s41 offen offset:1024 lds\n"
"v_exp_f32 v18, v162\n"
"v_add_f32 v20, v20, v17\n"
"v_cvt_pk_bf16_f32 v96, v16, v17\n"
"v_mfma_f32_32x32x16_bf16 v[192:207], a[208:211], a[136:139], v[192:207]\n"
"ds_read_b128 a[204:207], v8 offset:24576\n"
"v_exp_f32 v19, v163\n"
"v_add_f32 v20, v20, v18\n"
"v_exp_f32 v16, v164\n"
"v_mfma_f32_32x32x16_bf16 v[208:223], a[208:211], a[168:171], v[208:223]\n"
"buffer_load_dwordx4 v4, s[20:23], s41 offen offset:2048 lds\n"
"v_add_f32 v20, v20, v19\n"
"v_cvt_pk_bf16_f32 v97, v18, v19\n"
"v_exp_f32 v17, v165\n"
"v_mfma_f32_32x32x16_bf16 v[192:207], a[212:215], a[140:143], v[192:207]\n"
"ds_read_b128 a[208:211], v9 offset:24576\n"
"v_add_f32 v20, v20, v16\n"
"v_exp_f32 v18, v166\n"
"v_add_f32 v20, v20, v17\n"
"v_mfma_f32_32x32x16_bf16 v[208:223], a[212:215], a[172:175], v[208:223]\n"
"buffer_load_dwordx4 v5, s[20:23], s41 offen offset:3072 lds\n"
"v_cvt_pk_bf16_f32 v98, v16, v17\n"
"v_exp_f32 v19, v167\n"
"v_add_f32 v20, v20, v18\n"
"s_waitcnt lgkmcnt(2)\n"
"v_mfma_f32_32x32x16_bf16 v[192:207], a[216:219], a[144:147], v[192:207]\n"
"ds_read_b128 a[212:215], v10 offset:24576\n"
"v_exp_f32 v16, v168\n"
"v_add_f32 v20, v20, v19\n"
"s_add_u32 m0, s40, 0x1c000\n"
"v_mfma_f32_32x32x16_bf16 v[208:223], a[216:219], a[176:179], v[208:223]\n"
"buffer_load_dwordx4 v6, s[24:27], s75 offen lds\n"
"v_cvt_pk_bf16_f32 v99, v18, v19\n"
"v_exp_f32 v17, v169\n"
"v_add_f32 v20, v20, v16\n"
"v_mfma_f32_32x32x16_bf16 v[192:207], a[192:195], a[148:151], v[192:207]\n"
"ds_read_b128 a[216:219], v11 offset:24576\n"
"v_exp_f32 v18, v170\n"
"v_add_f32 v20, v20, v17\n"
"v_cvt_pk_bf16_f32 v100, v16, v17\n"
"v_mfma_f32_32x32x16_bf16 v[208:223], a[192:195], a[180:183], v[208:223]\n"
"buffer_load_dwordx4 v7, s[24:27], s75 offen offset:1024 lds\n"
"v_exp_f32 v19, v171\n"
"v_add_f32 v20, v20, v18\n"
"v_mfma_f32_32x32x16_bf16 v[192:207], a[196:199], a[152:155], v[192:207]\n"
"ds_read_b128 a[192:195], v12 offset:24576\n"
"v_exp_f32 v16, v172\n"
"v_add_f32 v20, v20, v19\n"
"v_cvt_pk_bf16_f32 v101, v18, v19\n"
"v_mfma_f32_32x32x16_bf16 v[208:223], a[196:199], a[184:187], v[208:223]\n"
"buffer_load_dwordx4 v6, s[24:27], s75 offen offset:2048 lds\n"
"v_exp_f32 v17, v173\n"
"v_add_f32 v20, v20, v16\n"
"v_exp_f32 v18, v174\n"
"v_mfma_f32_32x32x16_bf16 v[192:207], a[200:203], a[156:159], v[192:207]\n"
"ds_read_b128 a[196:199], v13 offset:24576\n"
"v_add_f32 v20, v20, v17\n"
"v_cvt_pk_bf16_f32 v102, v16, v17\n"
"v_exp_f32 v19, v175\n"
"v_mfma_f32_32x32x16_bf16 v[208:223], a[200:203], a[188:191], v[208:223]\n"
"buffer_load_dwordx4 v7, s[24:27], s75 offen offset:3072 lds\n"
"v_add_f32 v20, v20, v18\n"
"v_exp_f32 v16, v176\n"
"s_waitcnt lgkmcnt(2)\n"
"v_mfma_f32_32x32x16_bf16 v[224:239], a[204:207], a[128:131], v[32:47]\n"
"ds_read_b128 a[200:203], v14 offset:24576\n"
"v_add_f32 v20, v20, v19\n"
"v_cvt_pk_bf16_f32 v103, v18, v19\n"
"v_exp_f32 v17, v177\n"
"v_mfma_f32_32x32x16_bf16 v[240:255], a[204:207], a[160:163], v[48:63]\n"
"v_add_f32 v21, v21, v16\n"
"v_exp_f32 v18, v178\n"
"v_add_f32 v21, v21, v17\n"
"s_add_u32 s64, s85, 0x4c000\n"
"v_mfma_f32_32x32x16_bf16 v[224:239], a[208:211], a[132:135], v[224:239]\n"
"ds_read_b128 a[204:207], v15 offset:24576\n"
"buffer_store_dwordx4 a[252:255], v113, s[28:31], s64 offen sc1\n"
"v_cvt_pk_bf16_f32 v104, v16, v17\n"
"v_exp_f32 v19, v179\n"
"v_mfma_f32_32x32x16_bf16 v[240:255], a[208:211], a[164:167], v[240:255]\n"
"v_add_f32 v21, v21, v18\n"
"v_exp_f32 v16, v180\n"
"v_add_f32 v21, v21, v19\n"
"v_cvt_pk_bf16_f32 v105, v18, v19\n"
"v_mfma_f32_32x32x16_bf16 v[224:239], a[212:215], a[136:139], v[224:239]\n"
"ds_read_b64_tr_b16 a[208:209], v112\n"
"v_exp_f32 v17, v181\n"
"v_add_f32 v21, v21, v16\n"
"v_mfma_f32_32x32x16_bf16 v[240:255], a[212:215], a[168:171], v[240:255]\n"
"ds_read_b64_tr_b16 a[210:211], v112 offset:2048\n"
"v_exp_f32 v18, v182\n"
"v_add_f32 v21, v21, v17\n"
"v_cvt_pk_bf16_f32 v106, v16, v17\n"
"v_mfma_f32_32x32x16_bf16 v[224:239], a[216:219], a[140:143], v[224:239]\n"
"ds_read_b64_tr_b16 a[212:213], v112 offset:512\n"
"v_exp_f32 v19, v183\n"
"v_add_f32 v21, v21, v18\n"
"v_exp_f32 v16, v184\n"
"v_mfma_f32_32x32x16_bf16 v[240:255], a[216:219], a[172:175], v[240:255]\n"
"ds_read_b64_tr_b16 a[214:215], v112 offset:2560\n"
"v_add_f32 v21, v21, v19\n"
"v_cvt_pk_bf16_f32 v107, v18, v19\n"
"v_exp_f32 v17, v185\n"
"s_waitcnt lgkmcnt(4)\n"
"v_mfma_f32_32x32x16_bf16 v[224:239], a[192:195], a[144:147], v[224:239]\n"
"ds_read_b64_tr_b16 a[216:217], v112 offset:1024\n"
"v_add_f32 v21, v21, v16\n"
"v_exp_f32 v18, v186\n"
"v_add_f32 v21, v21, v17\n"
"v_mfma_f32_32x32x16_bf16 v[240:255], a[192:195], a[176:179], v[240:255]\n"
"ds_read_b64_tr_b16 a[218:219], v112 offset:3072\n"
"v_cvt_pk_bf16_f32 v108, v16, v17\n"
"v_exp_f32 v19, v187\n"
"v_add_f32 v21, v21, v18\n"
"v_mfma_f32_32x32x16_bf16 v[224:239], a[196:199], a[148:151], v[224:239]\n"
"ds_read_b64_tr_b16 a[192:193], v112 offset:1536\n"
"v_exp_f32 v16, v188\n"
"v_add_f32 v21, v21, v19\n"
"s_add_u32 s64, s85, 0x6c000\n"
"v_mfma_f32_32x32x16_bf16 v[240:255], a[196:199], a[180:183], v[240:255]\n"
"ds_read_b64_tr_b16 a[194:195], v112 offset:3584\n"
"buffer_store_dwordx4 a[252:255], v113, s[28:31], s64 offen sc1\n"
"v_cvt_pk_bf16_f32 v109, v18, v19\n"
"v_exp_f32 v17, v189\n"
"v_mfma_f32_32x32x16_bf16 v[224:239], a[200:203], a[152:155], v[224:239]\n"
"ds_read_b64_tr_b16 a[196:197], v112 offset:4096\n"
"v_add_f32 v21, v21, v16\n"
"v_exp_f32 v18, v190\n"
"v_add_f32 v21, v21, v17\n"
"v_mfma_f32_32x32x16_bf16 v[240:255], a[200:203], a[184:187], v[240:255]\n"
"ds_read_b64_tr_b16 a[198:199], v112 offset:6144\n"
"v_cvt_pk_bf16_f32 v110, v16, v17\n"
"v_exp_f32 v19, v191\n"
"v_add_f32 v21, v21, v18\n"
"v_mfma_f32_32x32x16_bf16 v[224:239], a[204:207], a[156:159], v[224:239]\n"
"ds_read_b64_tr_b16 a[200:201], v112 offset:4608\n"
"v_add_f32 v21, v21, v19\n"
"v_cvt_pk_bf16_f32 v111, v18, v19\n"
"v_max_f32 v24, v20, v21\n"
"v_cmp_nge_f32 vcc, s43, v24\n"
"v_mfma_f32_32x32x16_bf16 v[240:255], a[204:207], a[188:191], v[240:255]\n"
"ds_read_b64_tr_b16 a[202:203], v112 offset:6656\n"
"s_cbranch_vccnz L_rare4_%=\n"
"L_back4_%=:\n"
"; B(5)\n"
"s_waitcnt lgkmcnt(4)\n"
"v_mfma_f32_32x32x16_bf16 a[0:15], a[208:211], v[64:67], a[0:15]\n"
"ds_read_b64_tr_b16 a[204:205], v112 offset:5120\n"
"v_add_f32 v22, v22, v20\n"
"v_add_f32 v23, v23, v21\n"
"v_exp_f32 v16, v192\n"
"v_mfma_f32_32x32x16_bf16 a[16:31], a[208:211], v[72:75], a[16:31]\n"
"ds_read_b64_tr_b16 a[206:207], v112 offset:7168\n"
"v_exp_f32 v17, v193\n"
"v_mfma_f32_32x32x16_bf16 a[32:47], a[212:215], v[64:67], a[32:47]\n"
"ds_read_b64_tr_b16 a[208:209], v112 offset:5632\n"
"v_exp_f32 v18, v194\n"
"v_add_f32 v20, v16, v17\n"
"v_cvt_pk_bf16_f32 v80, v16, v17\n"
"v_mfma_f32_32x32x16_bf16 a[48:63], a[212:215], v[72:75], a[48:63]\n"
"ds_read_b64_tr_b16 a[210:211], v112 offset:7680\n"
"v_exp_f32 v19, v195\n"
"v_add_f32 v20, v20, v18\n"
"v_mfma_f32_32x32x16_bf16 a[64:79], a[216:219], v[64:67], a[64:79]\n"
"ds_read_b64_tr_b16 a[212:213], v112 offset:8192\n"
"v_exp_f32 v16, v196\n"
"v_add_f32 v20, v20, v19\n"
"v_cvt_pk_bf16_f32 v81, v18, v19\n"
"v_mfma_f32_32x32x16_bf16 a[80:95], a[216:219], v[72:75], a[80:95]\n"
"ds_read_b64_tr_b16 a[214:215], v112 offset:10240\n"
"v_exp_f32 v17, v197\n"
"v_add_f32 v20, v20, v16\n"
"v_mfma_f32_32x32x16_bf16 a[96:111], a[192:195], v[64:67], a[96:111]\n"
"ds_read_b64_tr_b16 a[216:217], v112 offset:8704\n"
"v_exp_f32 v18, v198\n"
"v_add_f32 v20, v20, v17\n"
"v_cvt_pk_bf16_f32 v82, v16, v17\n"
"v_mfma_f32_32x32x16_bf16 a[112:127], a[192:195], v[72:75], a[112:127]\n"
"ds_read_b64_tr_b16 a[218:219], v112 offset:10752\n"
"buffer_load_dwordx4 a[232:235], v1, s[68:71], s12 offen\n"
"v_exp_f32 v19, v199\n"
"s_waitcnt lgkmcnt(4)\n"
"v_mfma_f32_32x32x16_bf16 a[0:15], a[196:199], v[68:71], a[0:15]\n"
"ds_read_b64_tr_b16 a[192:193], v112 offset:9216\n"
"v_add_f32 v20, v20, v18\n"
"v_exp_f32 v16, v200\n"
"v_add_f32 v20, v20, v19\n"
"v_mfma_f32_32x32x16_bf16 a[16:31], a[196:199], v[76:79], a[16:31]\n"
"ds_read_b64_tr_b16 a[194:195], v112 offset:11264\n"
"v_cvt_pk_bf16_f32 v83, v18, v19\n"
"v_exp_f32 v17, v201\n"
"v_mfma_f32_32x32x16_bf16 a[32:47], a[200:203], v[68:71], a[32:47]\n"
"ds_read_b64_tr_b16 a[196:197], v112 offset:9728\n"
"v_add_f32 v20, v20, v16\n"
"v_exp_f32 v18, v202\n"
"v_add_f32 v20, v20, v17\n"
"v_mfma_f32_32x32x16_bf16 a[48:63], a[200:203], v[76:79], a[48:63]\n"
"ds_read_b64_tr_b16 a[198:199], v112 offset:11776\n"
"v_cvt_pk_bf16_f32 v84, v16, v17\n"
"v_exp_f32 v19, v203\n"
"v_mfma_f32_32x32x16_bf16 a[64:79], a[204:207], v[68:71], a[64:79]\n"
"ds_read_b64_tr_b16 a[200:201], v112 offset:12288\n"
"v_add_f32 v20, v20, v18\n"
"v_exp_f32 v16, v204\n"
"v_add_f32 v20, v20, v19\n"
"v_mfma_f32_32x32x16_bf16 a[80:95], a[204:207], v[76:79], a[80:95]\n"
"ds_read_b64_tr_b16 a[202:203], v112 offset:14336\n"
"v_cvt_pk_bf16_f32 v85, v18, v19\n"
"v_exp_f32 v17, v205\n"
"v_mfma_f32_32x32x16_bf16 a[96:111], a[208:211], v[68:71], a[96:111]\n"
"ds_read_b64_tr_b16 a[204:205], v112 offset:12800\n"
"v_add_f32 v20, v20, v16\n"
"v_exp_f32 v18, v206\n"
"v_add_f32 v20, v20, v17\n"
"v_mfma_f32_32x32x16_bf16 a[112:127], a[208:211], v[76:79], a[112:127]\n"
"ds_read_b64_tr_b16 a[206:207], v112 offset:14848\n"
"v_cvt_pk_bf16_f32 v86, v16, v17\n"
"s_waitcnt lgkmcnt(4)\n"
"v_mfma_f32_32x32x16_bf16 a[0:15], a[212:215], v[96:99], a[0:15]\n"
"ds_read_b64_tr_b16 a[208:209], v112 offset:13312\n"
"v_exp_f32 v19, v207\n"
"v_add_f32 v20, v20, v18\n"
"v_exp_f32 v16, v208\n"
"v_mfma_f32_32x32x16_bf16 a[16:31], a[212:215], v[104:107], a[16:31]\n"
"ds_read_b64_tr_b16 a[210:211], v112 offset:15360\n"
"v_add_f32 v20, v20, v19\n"
"v_cvt_pk_bf16_f32 v87, v18, v19\n"
"v_mfma_f32_32x32x16_bf16 a[32:47], a[216:219], v[96:99], a[32:47]\n"
"ds_read_b64_tr_b16 a[212:213], v112 offset:13824\n"
"v_exp_f32 v17, v209\n"
"v_exp_f32 v18, v210\n"
"v_add_f32 v21, v16, v17\n"
"v_mfma_f32_32x32x16_bf16 a[48:63], a[216:219], v[104:107], a[48:63]\n"
"ds_read_b64_tr_b16 a[214:215], v112 offset:15872\n"
"v_cvt_pk_bf16_f32 v88, v16, v17\n"
"v_exp_f32 v19, v211\n"
"v_mfma_f32_32x32x16_bf16 a[64:79], a[192:195], v[96:99], a[64:79]\n"
"v_add_f32 v21, v21, v18\n"
"v_exp_f32 v16, v212\n"
"v_add_f32 v21, v21, v19\n"
"v_cvt_pk_bf16_f32 v89, v18, v19\n"
"v_mfma_f32_32x32x16_bf16 a[80:95], a[192:195], v[104:107], a[80:95]\n"
"s_waitcnt vmcnt(3)\n"
"s_waitcnt lgkmcnt(0)\n"
"s_barrier\n"
"v_mfma_f32_32x32x16_bf16 a[96:111], a[196:199], v[96:99], a[96:111]\n"
"ds_read_b128 a[216:219], v8\n"
"v_exp_f32 v17, v213\n"
"v_add_f32 v21, v21, v16\n"
"v_mfma_f32_32x32x16_bf16 a[112:127], a[196:199], v[104:107], a[112:127]\n"
"ds_read_b128 a[192:195], v9\n"
"v_exp_f32 v18, v214\n"
"v_add_f32 v21, v21, v17\n"
"v_mfma_f32_32x32x16_bf16 a[0:15], a[200:203], v[100:103], a[0:15]\n"
"ds_read_b128 a[196:199], v10\n"
"v_cvt_pk_bf16_f32 v90, v16, v17\n"
"v_exp_f32 v19, v215\n"
"v_add_f32 v21, v21, v18\n"
"v_mfma_f32_32x32x16_bf16 a[16:31], a[200:203], v[108:111], a[16:31]\n"
"v_exp_f32 v16, v216\n"
"v_add_f32 v21, v21, v19\n"
"v_cvt_pk_bf16_f32 v91, v18, v19\n"
"v_mfma_f32_32x32x16_bf16 a[32:47], a[204:207], v[100:103], a[32:47]\n"
"ds_read_b128 a[200:203], v11\n"
"v_exp_f32 v17, v217\n"
"v_add_f32 v21, v21, v16\n"
"v_mfma_f32_32x32x16_bf16 a[48:63], a[204:207], v[108:111], a[48:63]\n"
"v_exp_f32 v18, v218\n"
"v_add_f32 v21, v21, v17\n"
"v_cvt_pk_bf16_f32 v92, v16, v17\n"
"v_mfma_f32_32x32x16_bf16 a[64:79], a[208:211], v[100:103], a[64:79]\n"
"ds_read_b128 a[204:207], v12\n"
"v_exp_f32 v19, v219\n"
"v_add_f32 v21, v21, v18\n"
"v_exp_f32 v16, v220\n"
"v_mfma_f32_32x32x16_bf16 a[80:95], a[208:211], v[108:111], a[80:95]\n"
"v_add_f32 v21, v21, v19\n"
"v_cvt_pk_bf16_f32 v93, v18, v19\n"
"v_exp_f32 v17, v221\n"
"v_mfma_f32_32x32x16_bf16 a[96:111], a[212:215], v[100:103], a[96:111]\n"
"ds_read_b128 a[208:211], v13\n"
"v_add_f32 v21, v21, v16\n"
"v_exp_f32 v18, v222\n"
"v_add_f32 v21, v21, v17\n"
"v_mfma_f32_32x32x16_bf16 a[112:127], a[212:215], v[108:111], a[112:127]\n"
"v_cvt_pk_bf16_f32 v94, v16, v17\n"
"v_exp_f32 v19, v223\n"
"v_add_f32 v21, v21, v18\n"
"; A(6)\n"
"s_waitcnt lgkmcnt(2)\n"
"v_mfma_f32_32x32x16_bf16 v[128:143], a[216:219], a[128:131], v[32:47]\n"
"ds_read_b128 a[212:215], v14\n"
"s_add_u32 m0, s40, 0x14000\n"
"s_mov_b32 s75, s41\n"
"s_add_u32 s41, s41, 0x4000\n"
"s_and_b32 s41, s41, 0x1ffff\n"
"v_mfma_f32_32x32x16_bf16 v[144:159], a[216:219], a[160:163], v[48:63]\n"
"buffer_load_dwordx4 v2, s[20:23], s41 offen lds\n"
"v_add_f32 v21, v21, v19\n"
"v_cvt_pk_bf16_f32 v95, v18, v19\n"
"v_exp_f32 v16, v224\n"
"v_mfma_f32_32x32x16_bf16 v[128:143], a[192:195], a[132:135], v[128:143]\n"
"ds_read_b128 a[216:219], v15\n"
"v_exp_f32 v17, v225\n"
"v_add_f32 v20, v20, v16\n"
"v_mfma_f32_32x32x16_bf16 v[144:159], a[192:195], a[164:167], v[144:159]\n"
"buffer_load_dwordx4 v3, s[20:23], s41 offen offset:1024 lds\n"
"v_exp_f32 v18, v226\n"
"v_add_f32 v20, v20, v17\n"
"v_cvt_pk_bf16_f32 v96, v16, v17\n"
"v_mfma_f32_32x32x16_bf16 v[128:143], a[196:199], a[136:139], v[128:143]\n"
"ds_read_b128 a[192:195], v8 offset:8192\n"
"v_exp_f32 v19, v227\n"
"v_add_f32 v20, v20, v18\n"
"v_exp_f32 v16, v228\n"
"v_mfma_f32_32x32x16_bf16 v[144:159], a[196:199], a[168:171], v[144:159]\n"
"buffer_load_dwordx4 v4, s[20:23], s41 offen offset:2048 lds\n"
"v_add_f32 v20, v20, v19\n"
"v_cvt_pk_bf16_f32 v97, v18, v19\n"
"v_exp_f32 v17, v229\n"
"v_mfma_f32_32x32x16_bf16 v[128:143], a[200:203], a[140:143], v[128:143]\n"
"ds_read_b128 a[196:199], v9 offset:8192\n"
"v_add_f32 v20, v20, v16\n"
"v_exp_f32 v18, v230\n"
"v_add_f32 v20, v20, v17\n"
"v_mfma_f32_32x32x16_bf16 v[144:159], a[200:203], a[172:175], v[144:159]\n"
"buffer_load_dwordx4 v5, s[20:23], s41 offen offset:3072 lds\n"
"v_cvt_pk_bf16_f32 v98, v16, v17\n"
"v_exp_f32 v19, v231\n"
"s_waitcnt lgkmcnt(2)\n"
"v_mfma_f32_32x32x16_bf16 v[128:143], a[204:207], a[144:147], v[128:143]\n"
"ds_read_b128 a[200:203], v10 offset:8192\n"
"v_add_f32 v20, v20, v18\n"
"v_exp_f32 v16, v232\n"
"s_add_u32 m0, s40, 0x18000\n"
"v_mfma_f32_32x32x16_bf16 v[144:159], a[204:207], a[176:179], v[144:159]\n"
"buffer_load_dwordx4 v6, s[24:27], s75 offen lds\n"
"v_add_f32 v20, v20, v19\n"
"v_cvt_pk_bf16_f32 v99, v18, v19\n"
"v_exp_f32 v17, v233\n"
"v_mfma_f32_32x32x16_bf16 v[128:143], a[208:211], a[148:151], v[128:143]\n"
"ds_read_b128 a[204:207], v11 offset:8192\n"
"v_add_f32 v20, v20, v16\n"
"v_exp_f32 v18, v234\n"
"v_add_f32 v20, v20, v17\n"
"v_mfma_f32_32x32x16_bf16 v[144:159], a[208:211], a[180:183], v[144:159]\n"
"buffer_load_dwordx4 v7, s[24:27], s75 offen offset:1024 lds\n"
"v_cvt_pk_bf16_f32 v100, v16, v17\n"
"v_exp_f32 v19, v235\n"
"v_add_f32 v20, v20, v18\n"
"v_mfma_f32_32x32x16_bf16 v[128:143], a[212:215], a[152:155], v[128:143]\n"
"ds_read_b128 a[208:211], v12 offset:8192\n"
"v_exp_f32 v16, v236\n"
"v_add_f32 v20, v20, v19\n"
"v_cvt_pk_bf16_f32 v101, v18, v19\n"
"v_mfma_f32_32x32x16_bf16 v[144:159], a[212:215], a[184:187], v[144:159]\n"
"buffer_load_dwordx4 v6, s[24:27], s75 offen offset:2048 lds\n"
"v_exp_f32 v17, v237\n"
"v_add_f32 v20, v20, v16\n"
"v_mfma_f32_32x32x16_bf16 v[128:143], a[216:219], a[156:159], v[128:143]\n"
"ds_read_b128 a[212:215], v13 offset:8192\n"
"v_exp_f32 v18, v238\n"
"v_add_f32 v20, v20, v17\n"
"v_cvt_pk_bf16_f32 v102, v16, v17\n"
"v_mfma_f32_32x32x16_bf16 v[144:159], a[216:219], a[188:191], v[144:159]\n"
"buffer_load_dwordx4 v7, s[24:27], s75 offen offset:3072 lds\n"
"v_exp_f32 v19, v239\n"
"v_add_f32 v20, v20, v18\n"
"s_waitcnt lgkmcnt(2)\n"
"v_mfma_f32_32x32x16_bf16 v[160:175], a[192:195], a[128:131], v[32:47]\n"
"ds_read_b128 a[216:219], v14 offset:8192\n"
"v_exp_f32 v16, v240\n"
"v_add_f32 v20, v20, v19\n"
"v_cvt_pk_bf16_f32 v103, v18, v19\n"
"v_mfma_f32_32x32x16_bf16 v[176:191], a[192:195], a[160:163], v[48:63]\n"
"v_exp_f32 v17, v241\n"
"v_add_f32 v21, v21, v16\n"
"v_exp_f32 v18, v242\n"
"v_add_f32 v21, v21, v17\n"
"v_mfma_f32_32x32x16_bf16 v[160:175], a[196:199], a[132:135], v[160:175]\n"
"ds_read_b128 a[192:195], v15 offset:8192\n"
"buffer_store_dwordx4 a[252:255], v114, s[28:31], s86 offen sc1\n"
"v_cvt_pk_bf16_f32 v104, v16, v17\n"
"v_exp_f32 v19, v243\n"
"v_mfma_f32_32x32x16_bf16 v[176:191], a[196:199], a[164:167], v[176:191]\n"
"v_add_f32 v21, v21, v18\n"
"v_exp_f32 v16, v244\n"
"v_add_f32 v21, v21, v19\n"
"v_cvt_pk_bf16_f32 v105, v18, v19\n"
"v_mfma_f32_32x32x16_bf16 v[160:175], a[200:203], a[136:139], v[160:175]\n"
"ds_read_b64_tr_b16 a[196:197], v112 offset:16384\n"
"v_exp_f32 v17, v245\n"
"v_add_f32 v21, v21, v16\n"
"v_mfma_f32_32x32x16_bf16 v[176:191], a[200:203], a[168:171], v[176:191]\n"
"ds_read_b64_tr_b16 a[198:199], v112 offset:18432\n"
"v_exp_f32 v18, v246\n"
"v_add_f32 v21, v21, v17\n"
"v_cvt_pk_bf16_f32 v106, v16, v17\n"
"v_mfma_f32_32x32x16_bf16 v[160:175], a[204:207], a[140:143], v[160:175]\n"
"ds_read_b64_tr_b16 a[200:201], v112 offset:16896\n"
"v_exp_f32 v19, v247\n"
"v_add_f32 v21, v21, v18\n"
"v_exp_f32 v16, v248\n"
"v_mfma_f32_32x32x16_bf16 v[176:191], a[204:207], a[172:175], v[176:191]\n"
"ds_read_b64_tr_b16 a[202:203], v112 offset:18944\n"
"v_add_f32 v21, v21, v19\n"
"v_cvt_pk_bf16_f32 v107, v18, v19\n"
"s_waitcnt lgkmcnt(4)\n"
"v_mfma_f32_32x32x16_bf16 v[160:175], a[208:211], a[144:147], v[160:175]\n"
"ds_read_b64_tr_b16 a[204:205], v112 offset:17408\n"
"v_exp_f32 v17, v249\n"
"v_add_f32 v21, v21, v16\n"
"v_exp_f32 v18, v250\n"
"v_mfma_f32_32x32x16_bf16 v[176:191], a[208:211], a[176:179], v[176:191]\n"
"ds_read_b64_tr_b16 a[206:207], v112 offset:19456\n"
"v_add_f32 v21, v21, v17\n"
"v_cvt_pk_bf16_f32 v108, v16, v17\n"
"v_exp_f32 v19, v251\n"
"v_mfma_f32_32x32x16_bf16 v[160:175], a[212:215], a[148:151], v[160:175]\n"
"ds_read_b64_tr_b16 a[208:209], v112 offset:17920\n"
"v_add_f32 v21, v21, v18\n"
"v_exp_f32 v16, v252\n"
"v_add_f32 v21, v21, v19\n"
"v_mfma_f32_32x32x16_bf16 v[176:191], a[212:215], a[180:183], v[176:191]\n"
"ds_read_b64_tr_b16 a[210:211], v112 offset:19968\n"
"buffer_store_dwordx4 a[252:255], v114, s[28:31], s86 offen offset:2048 sc1\n"
"v_cvt_pk_bf16_f32 v109, v18, v19\n"
"v_exp_f32 v17, v253\n"
"v_mfma_f32_32x32x16_bf16 v[160:175], a[216:219], a[152:155], v[160:175]\n"
"ds_read_b64_tr_b16 a[212:213], v112 offset:20480\n"
"v_add_f32 v21, v21, v16\n"
"v_exp_f32 v18, v254\n"
"v_add_f32 v21, v21, v17\n"
"v_mfma_f32_32x32x16_bf16 v[176:191], a[216:219], a[184:187], v[176:191]\n"
"ds_read_b64_tr_b16 a[214:215], v112 offset:22528\n"
"v_cvt_pk_bf16_f32 v110, v16, v17\n"
"v_exp_f32 v19, v255\n"
"v_add_f32 v21, v21, v18\n"
"v_mfma_f32_32x32x16_bf16 v[160:175], a[192:195], a[156:159], v[160:175]\n"
"ds_read_b64_tr_b16 a[216:217], v112 offset:20992\n"
"v_add_f32 v21, v21, v19\n"
"v_cvt_pk_bf16_f32 v111, v18, v19\n"
"v_max_f32 v24, v20, v21\n"
"v_cmp_nge_f32 vcc, s43, v24\n"
"v_mfma_f32_32x32x16_bf16 v[176:191], a[192:195], a[188:191], v[176:191]\n"
"ds_read_b64_tr_b16 a[218:219], v112 offset:23040\n"
"s_cbranch_vccnz L_rare5_%=\n"
"L_back5_%=:\n"
"; B(6)\n"
"s_waitcnt lgkmcnt(4)\n"
"v_mfma_f32_32x32x16_bf16 a[0:15], a[196:199], v[80:83], a[0:15]\n"
"ds_read_b64_tr_b16 a[192:193], v112 offset:21504\n"
"v_add_f32 v22, v22, v20\n"
"v_add_f32 v23, v23, v21\n"
"v_exp_f32 v16, v128\n"
"v_mfma_f32_32x32x16_bf16 a[16:31], a[196:199], v[88:91], a[16:31]\n"
"ds_read_b64_tr_b16 a[194:195], v112 offset:23552\n"
"v_exp_f32 v17, v129\n"
"v_mfma_f32_32x32x16_bf16 a[32:47], a[200:203], v[80:83], a[32:47]\n"
"ds_read_b64_tr_b16 a[196:197], v112 offset:22016\n"
"v_exp_f32 v18, v130\n"
"v_add_f32 v20, v16, v17\n"
"v_cvt_pk_bf16_f32 v64, v16, v17\n"
"v_mfma_f32_32x32x16_bf16 a[48:63], a[200:203], v[88:91], a[48:63]\n"
"ds_read_b64_tr_b16 a[198:199], v112 offset:24064\n"
"v_exp_f32 v19, v131\n"
"v_add_f32 v20, v20, v18\n"
"v_mfma_f32_32x32x16_bf16 a[64:79], a[204:207], v[80:83], a[64:79]\n"
"ds_read_b64_tr_b16 a[200:201], v112 offset:24576\n"
"v_exp_f32 v16, v132\n"
"v_add_f32 v20, v20, v19\n"
"v_cvt_pk_bf16_f32 v65, v18, v19\n"
"v_mfma_f32_32x32x16_bf16 a[80:95], a[204:207], v[88:91], a[80:95]\n"
"ds_read_b64_tr_b16 a[202:203], v112 offset:26624\n"
"v_exp_f32 v17, v133\n"
"v_add_f32 v20, v20, v16\n"
"v_mfma_f32_32x32x16_bf16 a[96:111], a[208:211], v[80:83], a[96:111]\n"
"ds_read_b64_tr_b16 a[204:205], v112 offset:25088\n"
"v_exp_f32 v18, v134\n"
"v_add_f32 v20, v20, v17\n"
"v_cvt_pk_bf16_f32 v66, v16, v17\n"
"v_mfma_f32_32x32x16_bf16 a[112:127], a[208:211], v[88:91], a[112:127]\n"
"ds_read_b64_tr_b16 a[206:207], v112 offset:27136\n"
"buffer_load_dwordx4 a[236:239], v1, s[68:71], s12 offen offset:1024\n"
"v_exp_f32 v19, v135\n"
"s_waitcnt lgkmcnt(4)\n"
"v_mfma_f32_32x32x16_bf16 a[0:15], a[212:215], v[84:87], a[0:15]\n"
"ds_read_b64_tr_b16 a[208:209], v112 offset:25600\n"
"v_add_f32 v20, v20, v18\n"
"v_exp_f32 v16, v136\n"
"v_add_f32 v20, v20, v19\n"
"v_mfma_f32_32x32x16_bf16 a[16:31], a[212:215], v[92:95], a[16:31]\n"
"ds_read_b64_tr_b16 a[210:211], v112 offset:27648\n"
"v_cvt_pk_bf16_f32 v67, v18, v19\n"
"v_exp_f32 v17, v137\n"
"v_mfma_f32_32x32x16_bf16 a[32:47], a[216:219], v[84:87], a[32:47]\n"
"ds_read_b64_tr_b16 a[212:213], v112 offset:26112\n"
"v_add_f32 v20, v20, v16\n"
"v_exp_f32 v18, v138\n"
"v_add_f32 v20, v20, v17\n"
"v_mfma_f32_32x32x16_bf16 a[48:63], a[216:219], v[92:95], a[48:63]\n"
"ds_read_b64_tr_b16 a[214:215], v112 offset:28160\n"
"v_cvt_pk_bf16_f32 v68, v16, v17\n"
"v_exp_f32 v19, v139\n"
"v_mfma_f32_32x32x16_bf16 a[64:79], a[192:195], v[84:87], a[64:79]\n"
"ds_read_b64_tr_b16 a[216:217], v112 offset:28672\n"
"v_add_f32 v20, v20, v18\n"
"v_exp_f32 v16, v140\n"
"v_add_f32 v20, v20, v19\n"
"v_mfma_f32_32x32x16_bf16 a[80:95], a[192:195], v[92:95], a[80:95]\n"
"ds_read_b64_tr_b16 a[218:219], v112 offset:30720\n"
"v_cvt_pk_bf16_f32 v69, v18, v19\n"
"v_exp_f32 v17, v141\n"
"v_mfma_f32_32x32x16_bf16 a[96:111], a[196:199], v[84:87], a[96:111]\n"
"ds_read_b64_tr_b16 a[192:193], v112 offset:29184\n"
"v_add_f32 v20, v20, v16\n"
"v_exp_f32 v18, v142\n"
"v_add_f32 v20, v20, v17\n"
"v_mfma_f32_32x32x16_bf16 a[112:127], a[196:199], v[92:95], a[112:127]\n"
"ds_read_b64_tr_b16 a[194:195], v112 offset:31232\n"
"v_cvt_pk_bf16_f32 v70, v16, v17\n"
"v_exp_f32 v19, v143\n"
"s_waitcnt lgkmcnt(4)\n"
"v_mfma_f32_32x32x16_bf16 a[0:15], a[200:203], v[96:99], a[0:15]\n"
"ds_read_b64_tr_b16 a[196:197], v112 offset:29696\n"
"v_add_f32 v20, v20, v18\n"
"v_exp_f32 v16, v144\n"
"v_mfma_f32_32x32x16_bf16 a[16:31], a[200:203], v[104:107], a[16:31]\n"
"ds_read_b64_tr_b16 a[198:199], v112 offset:31744\n"
"v_add_f32 v20, v20, v19\n"
"v_cvt_pk_bf16_f32 v71, v18, v19\n"
"v_exp_f32 v17, v145\n"
"v_mfma_f32_32x32x16_bf16 a[32:47], a[204:207], v[96:99], a[32:47]\n"
"ds_read_b64_tr_b16 a[200:201], v112 offset:30208\n"
"v_exp_f32 v18, v146\n"
"v_add_f32 v21, v16, v17\n"
"v_mfma_f32_32x32x16_bf16 a[48:63], a[204:207], v[104:107], a[48:63]\n"
"ds_read_b64_tr_b16 a[202:203], v112 offset:32256\n"
"v_cvt_pk_bf16_f32 v72, v16, v17\n"
"v_exp_f32 v19, v147\n"
"v_add_f32 v21, v21, v18\n"
"v_mfma_f32_32x32x16_bf16 a[64:79], a[208:211], v[96:99], a[64:79]\n"
"global_load_dwordx4 a[248:251], v1, s[44:45]\n"
"v_exp_f32 v16, v148\n"
"v_add_f32 v21, v21, v19\n"
"v_mfma_f32_32x32x16_bf16 a[80:95], a[208:211], v[104:107], a[80:95]\n"
"v_cvt_pk_bf16_f32 v73, v18, v19\n"
"s_waitcnt vmcnt(4)\n"
"s_waitcnt lgkmcnt(0)\n"
"s_barrier\n"
"v_mfma_f32_32x32x16_bf16 a[96:111], a[212:215], v[96:99], a[96:111]\n"
"ds_read_b128 a[204:207], v8 offset:16384\n"
"v_exp_f32 v17, v149\n"
"v_add_f32 v21, v21, v16\n"
"v_mfma_f32_32x32x16_bf16 a[112:127], a[212:215], v[104:107], a[112:127]\n"
"ds_read_b128 a[208:211], v9 offset:16384\n"
"v_exp_f32 v18, v150\n"
"v_add_f32 v21, v21, v17\n"
"v_mfma_f32_32x32x16_bf16 a[0:15], a[216:219], v[100:103], a[0:15]\n"
"ds_read_b128 a[212:215], v10 offset:16384\n"
"v_cvt_pk_bf16_f32 v74, v16, v17\n"
"v_exp_f32 v19, v151\n"
"v_mfma_f32_32x32x16_bf16 a[16:31], a[216:219], v[108:111], a[16:31]\n"
"v_add_f32 v21, v21, v18\n"
"v_exp_f32 v16, v152\n"
"v_add_f32 v21, v21, v19\n"
"v_cvt_pk_bf16_f32 v75, v18, v19\n"
"v_mfma_f32_32x32x16_bf16 a[32:47], a[192:195], v[100:103], a[32:47]\n"
"ds_read_b128 a[216:219], v11 offset:16384\n"
"v_exp_f32 v17, v153\n"
"v_add_f32 v21, v21, v16\n"
"v_mfma_f32_32x32x16_bf16 a[48:63], a[192:195], v[108:111], a[48:63]\n"
"v_exp_f32 v18, v154\n"
"v_add_f32 v21, v21, v17\n"
"v_cvt_pk_bf16_f32 v76, v16, v17\n"
"v_mfma_f32_32x32x16_bf16 a[64:79], a[196:199], v[100:103], a[64:79]\n"
"ds_read_b128 a[192:195], v12 offset:16384\n"
"v_exp_f32 v19, v155\n"
"v_add_f32 v21, v21, v18\n"
"v_mfma_f32_32x32x16_bf16 a[80:95], a[196:199], v[108:111], a[80:95]\n"
"v_exp_f32 v16, v156\n"
"v_add_f32 v21, v21, v19\n"
"v_cvt_pk_bf16_f32 v77, v18, v19\n"
"v_exp_f32 v17, v157\n"
"v_mfma_f32_32x32x16_bf16 a[96:111], a[200:203], v[100:103], a[96:111]\n"
"ds_read_b128 a[196:199], v13 offset:16384\n"
"v_add_f32 v21, v21, v16\n"
"v_exp_f32 v18, v158\n"
"v_mfma_f32_32x32x16_bf16 a[112:127], a[200:203], v[108:111], a[112:127]\n"
"v_add_f32 v21, v21, v17\n"
"v_cvt_pk_bf16_f32 v78, v16, v17\n"
"v_exp_f32 v19, v159\n"
"v_add_f32 v21, v21, v18\n"
"; A(7)\n"
"s_waitcnt lgkmcnt(2)\n"
"v_mfma_f32_32x32x16_bf16 v[192:207], a[204:207], a[128:131], v[32:47]\n"
"ds_read_b128 a[200:203], v14 offset:16384\n"
"v_add_f32 v21, v21, v19\n"
"v_cvt_pk_bf16_f32 v79, v18, v19\n"
"s_mov_b32 s75, s41\n"
"v_mfma_f32_32x32x16_bf16 v[208:223], a[204:207], a[160:163], v[48:63]\n"
"v_exp_f32 v16, v160\n"
"v_exp_f32 v17, v161\n"
"v_add_f32 v20, v20, v16\n"
"v_mfma_f32_32x32x16_bf16 v[192:207], a[208:211], a[132:135], v[192:207]\n"
"ds_read_b128 a[204:207], v15 offset:16384\n"
"v_exp_f32 v18, v162\n"
"v_add_f32 v20, v20, v17\n"
"v_cvt_pk_bf16_f32 v96, v16, v17\n"
"v_mfma_f32_32x32x16_bf16 v[208:223], a[208:211], a[164:167], v[208:223]\n"
"v_exp_f32 v19, v163\n"
"v_add_f32 v20, v20, v18\n"
"v_exp_f32 v16, v164\n"
"v_mfma_f32_32x32x16_bf16 v[192:207], a[212:215], a[136:139], v[192:207]\n"
"ds_read_b128 a[208:211], v8 offset:24576\n"
"v_add_f32 v20, v20, v19\n"
"v_cvt_pk_bf16_f32 v97, v18, v19\n"
"v_exp_f32 v17, v165\n"
"v_mfma_f32_32x32x16_bf16 v[208:223], a[212:215], a[168:171], v[208:223]\n"
"v_add_f32 v20, v20, v16\n"
"v_exp_f32 v18, v166\n"
"v_add_f32 v20, v20, v17\n"
"v_mfma_f32_32x32x16_bf16 v[192:207], a[216:219], a[140:143], v[192:207]\n"
"ds_read_b128 a[212:215], v9 offset:24576\n"
"v_cvt_pk_bf16_f32 v98, v16, v17\n"
"v_exp_f32 v19, v167\n"
"v_add_f32 v20, v20, v18\n"
"v_mfma_f32_32x32x16_bf16 v[208:223], a[216:219], a[172:175], v[208:223]\n"
"v_exp_f32 v16, v168\n"
"v_add_f32 v20, v20, v19\n"
"v_cvt_pk_bf16_f32 v99, v18, v19\n"
"s_waitcnt lgkmcnt(2)\n"
"v_mfma_f32_32x32x16_bf16 v[192:207], a[192:195], a[144:147], v[192:207]\n"
"ds_read_b128 a[216:219], v10 offset:24576\n"
"v_exp_f32 v17, v169\n"
"s_add_u32 m0, s40, 0x1c000\n"
"v_mfma_f32_32x32x16_bf16 v[208:223], a[192:195], a[176:179], v[208:223]\n"
"buffer_load_dwordx4 v6, s[24:27], s75 offen lds\n"
"v_add_f32 v20, v20, v16\n"
"v_exp_f32 v18, v170\n"
"v_add_f32 v20, v20, v17\n"
"v_mfma_f32_32x32x16_bf16 v[192:207], a[196:199], a[148:151], v[192:207]\n"
"ds_read_b128 a[192:195], v11 offset:24576\n"
"v_cvt_pk_bf16_f32 v100, v16, v17\n"
"v_exp_f32 v19, v171\n"
"v_add_f32 v20, v20, v18\n"
"v_mfma_f32_32x32x16_bf16 v[208:223], a[196:199], a[180:183], v[208:223]\n"
"buffer_load_dwordx4 v7, s[24:27], s75 offen offset:1024 lds\n"
"v_exp_f32 v16, v172\n"
"v_add_f32 v20, v20, v19\n"
"v_cvt_pk_bf16_f32 v101, v18, v19\n"
"v_mfma_f32_32x32x16_bf16 v[192:207], a[200:203], a[152:155], v[192:207]\n"
"ds_read_b128 a[196:199], v12 offset:24576\n"
"v_exp_f32 v17, v173\n"
"v_add_f32 v20, v20, v16\n"
"v_mfma_f32_32x32x16_bf16 v[208:223], a[200:203], a[184:187], v[208:223]\n"
"buffer_load_dwordx4 v6, s[24:27], s75 offen offset:2048 lds\n"
"v_exp_f32 v18, v174\n"
"v_add_f32 v20, v20, v17\n"
"v_cvt_pk_bf16_f32 v102, v16, v17\n"
"v_mfma_f32_32x32x16_bf16 v[192:207], a[204:207], a[156:159], v[192:207]\n"
"ds_read_b128 a[200:203], v13 offset:24576\n"
"v_exp_f32 v19, v175\n"
"v_add_f32 v20, v20, v18\n"
"v_mfma_f32_32x32x16_bf16 v[208:223], a[204:207], a[188:191], v[208:223]\n"
"buffer_load_dwordx4 v7, s[24:27], s75 offen offset:3072 lds\n"
"v_exp_f32 v16, v176\n"
"v_add_f32 v20, v20, v19\n"
"s_waitcnt lgkmcnt(2)\n"
"v_mfma_f32_32x32x16_bf16 v[224:239], a[208:211], a[128:131], v[32:47]\n"
"ds_read_b128 a[204:207], v14 offset:24576\n"
"v_cvt_pk_bf16_f32 v103, v18, v19\n"
"v_exp_f32 v17, v177\n"
"v_add_f32 v21, v21, v16\n"
"v_mfma_f32_32x32x16_bf16 v[240:255], a[208:211], a[160:163], v[48:63]\n"
"v_exp_f32 v18, v178\n"
"v_add_f32 v21, v21, v17\n"
"v_cvt_pk_bf16_f32 v104, v16, v17\n"
"v_mfma_f32_32x32x16_bf16 v[224:239], a[212:215], a[132:135], v[224:239]\n"
"ds_read_b128 a[208:211], v15 offset:24576\n"
"v_exp_f32 v19, v179\n"
"v_add_f32 v21, v21, v18\n"
"v_mfma_f32_32x32x16_bf16 v[240:255], a[212:215], a[164:167], v[240:255]\n"
"v_exp_f32 v16, v180\n"
"v_add_f32 v21, v21, v19\n"
"v_cvt_pk_bf16_f32 v105, v18, v19\n"
"v_exp_f32 v17, v181\n"
"v_mfma_f32_32x32x16_bf16 v[224:239], a[216:219], a[136:139], v[224:239]\n"
"ds_read_b64_tr_b16 a[212:213], v112\n"
"v_add_f32 v21, v21, v16\n"
"v_exp_f32 v18, v182\n"
"v_add_f32 v21, v21, v17\n"
"v_mfma_f32_32x32x16_bf16 v[240:255], a[216:219], a[168:171], v[240:255]\n"
"ds_read_b64_tr_b16 a[214:215], v112 offset:2048\n"
"v_cvt_pk_bf16_f32 v106, v16, v17\n"
"v_exp_f32 v19, v183\n"
"v_add_f32 v21, v21, v18\n"
"v_mfma_f32_32x32x16_bf16 v[224:239], a[192:195], a[140:143], v[224:239]\n"
"ds_read_b64_tr_b16 a[216:217], v112 offset:512\n"
"v_exp_f32 v16, v184\n"
"v_add_f32 v21, v21, v19\n"
"v_mfma_f32_32x32x16_bf16 v[240:255], a[192:195], a[172:175], v[240:255]\n"
"ds_read_b64_tr_b16 a[218:219], v112 offset:2560\n"
"v_cvt_pk_bf16_f32 v107, v18, v19\n"
"v_exp_f32 v17, v185\n"
"s_waitcnt lgkmcnt(4)\n"
"v_mfma_f32_32x32x16_bf16 v[224:239], a[196:199], a[144:147], v[224:239]\n"
"ds_read_b64_tr_b16 a[192:193], v112 offset:1024\n"
"v_add_f32 v21, v21, v16\n"
"v_exp_f32 v18, v186\n"
"v_add_f32 v21, v21, v17\n"
"v_mfma_f32_32x32x16_bf16 v[240:255], a[196:199], a[176:179], v[240:255]\n"
"ds_read_b64_tr_b16 a[194:195], v112 offset:3072\n"
"v_cvt_pk_bf16_f32 v108, v16, v17\n"
"v_exp_f32 v19, v187\n"
"v_mfma_f32_32x32x16_bf16 v[224:239], a[200:203], a[148:151], v[224:239]\n"
"ds_read_b64_tr_b16 a[196:197], v112 offset:1536\n"
"v_add_f32 v21, v21, v18\n"
"v_exp_f32 v16, v188\n"
"v_add_f32 v21, v21, v19\n"
"v_mfma_f32_32x32x16_bf16 v[240:255], a[200:203], a[180:183], v[240:255]\n"
"ds_read_b64_tr_b16 a[198:199], v112 offset:3584\n"
"v_cvt_pk_bf16_f32 v109, v18, v19\n"
"v_exp_f32 v17, v189\n"
"v_add_f32 v21, v21, v16\n"
"v_mfma_f32_32x32x16_bf16 v[224:239], a[204:207], a[152:155], v[224:239]\n"
"ds_read_b64_tr_b16 a[200:201], v112 offset:4096\n"
"v_exp_f32 v18, v190\n"
"v_add_f32 v21, v21, v17\n"
"v_cvt_pk_bf16_f32 v110, v16, v17\n"
"v_mfma_f32_32x32x16_bf16 v[240:255], a[204:207], a[184:187], v[240:255]\n"
"ds_read_b64_tr_b16 a[202:203], v112 offset:6144\n"
"v_exp_f32 v19, v191\n"
"v_add_f32 v21, v21, v18\n"
"v_mfma_f32_32x32x16_bf16 v[224:239], a[208:211], a[156:159], v[224:239]\n"
"ds_read_b64_tr_b16 a[204:205], v112 offset:4608\n"
"v_add_f32 v21, v21, v19\n"
"v_cvt_pk_bf16_f32 v111, v18, v19\n"
"v_max_f32 v24, v20, v21\n"
"v_cmp_nge_f32 vcc, s43, v24\n"
"v_mfma_f32_32x32x16_bf16 v[240:255], a[208:211], a[188:191], v[240:255]\n"
"ds_read_b64_tr_b16 a[206:207], v112 offset:6656\n"
"s_cbranch_vccnz L_rare6_%=\n"
"L_back6_%=:\n"
"; B(7)\n"
"s_waitcnt lgkmcnt(4)\n"
"v_mfma_f32_32x32x16_bf16 a[0:15], a[212:215], v[64:67], a[0:15]\n"
"ds_read_b64_tr_b16 a[208:209], v112 offset:5120\n"
"v_add_f32 v22, v22, v20\n"
"v_add_f32 v23, v23, v21\n"
"v_exp_f32 v16, v192\n"
"v_mfma_f32_32x32x16_bf16 a[16:31], a[212:215], v[72:75], a[16:31]\n"
"ds_read_b64_tr_b16 a[210:211], v112 offset:7168\n"
"v_exp_f32 v17, v193\n"
"v_mfma_f32_32x32x16_bf16 a[32:47], a[216:219], v[64:67], a[32:47]\n"
"ds_read_b64_tr_b16 a[212:213], v112 offset:5632\n"
"v_exp_f32 v18, v194\n"
"v_add_f32 v20, v16, v17\n"
"v_cvt_pk_bf16_f32 v80, v16, v17\n"
"v_mfma_f32_32x32x16_bf16 a[48:63], a[216:219], v[72:75], a[48:63]\n"
"ds_read_b64_tr_b16 a[214:215], v112 offset:7680\n"
"v_exp_f32 v19, v195\n"
"v_add_f32 v20, v20, v18\n"
"v_exp_f32 v16, v196\n"
"v_mfma_f32_32x32x16_bf16 a[64:79], a[192:195], v[64:67], a[64:79]\n"
"ds_read_b64_tr_b16 a[216:217], v112 offset:8192\n"
"v_add_f32 v20, v20, v19\n"
"v_cvt_pk_bf16_f32 v81, v18, v19\n"
"v_mfma_f32_32x32x16_bf16 a[80:95], a[192:195], v[72:75], a[80:95]\n"
"ds_read_b64_tr_b16 a[218:219], v112 offset:10240\n"
"v_exp_f32 v17, v197\n"
"v_add_f32 v20, v20, v16\n"
"v_exp_f32 v18, v198\n"
"v_mfma_f32_32x32x16_bf16 a[96:111], a[196:199], v[64:67], a[96:111]\n"
"ds_read_b64_tr_b16 a[192:193], v112 offset:8704\n"
"v_add_f32 v20, v20, v17\n"
"v_cvt_pk_bf16_f32 v82, v16, v17\n"
"v_exp_f32 v19, v199\n"
"v_mfma_f32_32x32x16_bf16 a[112:127], a[196:199], v[72:75], a[112:127]\n"
"ds_read_b64_tr_b16 a[194:195], v112 offset:10752\n"
"v_add_f32 v20, v20, v18\n"
"s_waitcnt lgkmcnt(4)\n"
"v_mfma_f32_32x32x16_bf16 a[0:15], a[200:203], v[68:71], a[0:15]\n"
"ds_read_b64_tr_b16 a[196:197], v112 offset:9216\n"
"v_exp_f32 v16, v200\n"
"v_add_f32 v20, v20, v19\n"
"v_cvt_pk_bf16_f32 v83, v18, v19\n"
"v_mfma_f32_32x32x16_bf16 a[16:31], a[200:203], v[76:79], a[16:31]\n"
"ds_read_b64_tr_b16 a[198:199], v112 offset:11264\n"
"v_exp_f32 v17, v201\n"
"v_add_f32 v20, v20, v16\n"
"v_exp_f32 v18, v202\n"
"v_mfma_f32_32x32x16_bf16 a[32:47], a[204:207], v[68:71], a[32:47]\n"
"ds_read_b64_tr_b16 a[200:201], v112 offset:9728\n"
"v_add_f32 v20, v20, v17\n"
"v_cvt_pk_bf16_f32 v84, v16, v17\n"
"v_mfma_f32_32x32x16_bf16 a[48:63], a[204:207], v[76:79], a[48:63]\n"
"ds_read_b64_tr_b16 a[202:203], v112 offset:11776\n"
"v_exp_f32 v19, v203\n"
"v_add_f32 v20, v20, v18\n"
"v_exp_f32 v16, v204\n"
"v_mfma_f32_32x32x16_bf16 a[64:79], a[208:211], v[68:71], a[64:79]\n"
"ds_read_b64_tr_b16 a[204:205], v112 offset:12288\n"
"v_add_f32 v20, v20, v19\n"
"v_cvt_pk_bf16_f32 v85, v18, v19\n"
"v_exp_f32 v17, v205\n"
"v_mfma_f32_32x32x16_bf16 a[80:95], a[208:211], v[76:79], a[80:95]\n"
"ds_read_b64_tr_b16 a[206:207], v112 offset:14336\n"
"v_add_f32 v20, v20, v16\n"
"v_exp_f32 v18, v206\n"
"v_mfma_f32_32x32x16_bf16 a[96:111], a[212:215], v[68:71], a[96:111]\n"
"ds_read_b64_tr_b16 a[208:209], v112 offset:12800\n"
"v_add_f32 v20, v20, v17\n"
"v_cvt_pk_bf16_f32 v86, v16, v17\n"
"v_exp_f32 v19, v207\n"
"v_mfma_f32_32x32x16_bf16 a[112:127], a[212:215], v[76:79], a[112:127]\n"
"ds_read_b64_tr_b16 a[210:211], v112 offset:14848\n"
"v_add_f32 v20, v20, v18\n"
"v_exp_f32 v16, v208\n"
"s_waitcnt lgkmcnt(4)\n"
"v_mfma_f32_32x32x16_bf16 a[0:15], a[216:219], v[96:99], a[0:15]\n"
"ds_read_b64_tr_b16 a[212:213], v112 offset:13312\n"
"v_add_f32 v20, v20, v19\n"
"v_cvt_pk_bf16_f32 v87, v18, v19\n"
"v_mfma_f32_32x32x16_bf16 a[16:31], a[216:219], v[104:107], a[16:31]\n"
"ds_read_b64_tr_b16 a[214:215], v112 offset:15360\n"
"v_exp_f32 v17, v209\n"
"v_exp_f32 v18, v210\n"
"v_add_f32 v21, v16, v17\n"
"v_mfma_f32_32x32x16_bf16 a[32:47], a[192:195], v[96:99], a[32:47]\n"
"ds_read_b64_tr_b16 a[216:217], v112 offset:13824\n"
"v_cvt_pk_bf16_f32 v88, v16, v17\n"
"v_exp_f32 v19, v211\n"
"v_mfma_f32_32x32x16_bf16 a[48:63], a[192:195], v[104:107], a[48:63]\n"
"ds_read_b64_tr_b16 a[218:219], v112 offset:15872\n"
"v_add_f32 v21, v21, v18\n"
"v_exp_f32 v16, v212\n"
"v_add_f32 v21, v21, v19\n"
"v_mfma_f32_32x32x16_bf16 a[64:79], a[196:199], v[96:99], a[64:79]\n"
"v_cvt_pk_bf16_f32 v89, v18, v19\n"
"v_exp_f32 v17, v213\n"
"v_add_f32 v21, v21, v16\n"
"v_mfma_f32_32x32x16_bf16 a[80:95], a[196:199], v[104:107], a[80:95]\n"
"s_waitcnt vmcnt(0)\n"
"s_waitcnt lgkmcnt(0)\n"
"s_barrier\n"
"v_mfma_f32_32x32x16_bf16 a[96:111], a[200:203], v[96:99], a[96:111]\n"
"ds_read_b64_tr_b16 a[192:193], v112 offset:16384\n"
"v_exp_f32 v18, v214\n"
"v_add_f32 v21, v21, v17\n"
"v_cvt_pk_bf16_f32 v90, v16, v17\n"
"v_mfma_f32_32x32x16_bf16 a[112:127], a[200:203], v[104:107], a[112:127]\n"
"ds_read_b64_tr_b16 a[194:195], v112 offset:18432\n"
"v_exp_f32 v19, v215\n"
"v_add_f32 v21, v21, v18\n"
"v_mfma_f32_32x32x16_bf16 a[0:15], a[204:207], v[100:103], a[0:15]\n"
"ds_read_b64_tr_b16 a[196:197], v112 offset:20480\n"
"v_exp_f32 v16, v216\n"
"v_add_f32 v21, v21, v19\n"
"v_cvt_pk_bf16_f32 v91, v18, v19\n"
"v_mfma_f32_32x32x16_bf16 a[16:31], a[204:207], v[108:111], a[16:31]\n"
"ds_read_b64_tr_b16 a[198:199], v112 offset:22528\n"
"v_exp_f32 v17, v217\n"
"v_add_f32 v21, v21, v16\n"
"v_mfma_f32_32x32x16_bf16 a[32:47], a[208:211], v[100:103], a[32:47]\n"
"ds_read_b64_tr_b16 a[200:201], v112 offset:24576\n"
"v_exp_f32 v18, v218\n"
"v_add_f32 v21, v21, v17\n"
"v_cvt_pk_bf16_f32 v92, v16, v17\n"
"v_mfma_f32_32x32x16_bf16 a[48:63], a[208:211], v[108:111], a[48:63]\n"
"ds_read_b64_tr_b16 a[202:203], v112 offset:26624\n"
"v_exp_f32 v19, v219\n"
"v_add_f32 v21, v21, v18\n"
"v_mfma_f32_32x32x16_bf16 a[64:79], a[212:215], v[100:103], a[64:79]\n"
"ds_read_b64_tr_b16 a[204:205], v112 offset:28672\n"
"v_exp_f32 v16, v220\n"
"v_add_f32 v21, v21, v19\n"
"v_cvt_pk_bf16_f32 v93, v18, v19\n"
"v_mfma_f32_32x32x16_bf16 a[80:95], a[212:215], v[108:111], a[80:95]\n"
"ds_read_b64_tr_b16 a[206:207], v112 offset:30720\n"
"v_exp_f32 v17, v221\n"
"v_add_f32 v21, v21, v16\n"
"v_exp_f32 v18, v222\n"
"v_mfma_f32_32x32x16_bf16 a[96:111], a[216:219], v[100:103], a[96:111]\n"
"ds_read_b64_tr_b16 a[208:209], v112 offset:16896\n"
"v_add_f32 v21, v21, v17\n"
"v_cvt_pk_bf16_f32 v94, v16, v17\n"
"v_mfma_f32_32x32x16_bf16 a[112:127], a[216:219], v[108:111], a[112:127]\n"
"ds_read_b64_tr_b16 a[210:211], v112 offset:18944\n"
"ds_read_b64_tr_b16 a[212:213], v112 offset:20992\n"
"ds_read_b64_tr_b16 a[214:215], v112 offset:23040\n"
"v_exp_f32 v19, v223\n"
"v_add_f32 v21, v21, v18\n"
"; drain\n"
"v_add_f32 v21, v21, v19\n"
"v_cvt_pk_bf16_f32 v95, v18, v19\n"
"v_exp_f32 v16, v224\n"
"v_exp_f32 v17, v225\n"
"v_add_f32 v20, v20, v16\n"
"v_exp_f32 v18, v226\n"
"v_add_f32 v20, v20, v17\n"
"v_cvt_pk_bf16_f32 v96, v16, v17\n"
"v_exp_f32 v19, v227\n"
"v_add_f32 v20, v20, v18\n"
"v_exp_f32 v16, v228\n"
"v_add_f32 v20, v20, v19\n"
"v_cvt_pk_bf16_f32 v97, v18, v19\n"
"v_exp_f32 v17, v229\n"
"v_add_f32 v20, v20, v16\n"
"v_exp_f32 v18, v230\n"
"v_add_f32 v20, v20, v17\n"
"v_cvt_pk_bf16_f32 v98, v16, v17\n"
"v_exp_f32 v19, v231\n"
"v_add_f32 v20, v20, v18\n"
"v_exp_f32 v16, v232\n"
"v_add_f32 v20, v20, v19\n"
"v_cvt_pk_bf16_f32 v99, v18, v19\n"
"v_exp_f32 v17, v233\n"
"v_add_f32 v20, v20, v16\n"
"v_exp_f32 v18, v234\n"
"v_add_f32 v20, v20, v17\n"
"v_cvt_pk_bf16_f32 v100, v16, v17\n"
"v_exp_f32 v19, v235\n"
"v_add_f32 v20, v20, v18\n"
"v_exp_f32 v16, v236\n"
"v_add_f32 v20, v20, v19\n"
"v_cvt_pk_bf16_f32 v101, v18, v19\n"
"v_exp_f32 v17, v237\n"
"v_add_f32 v20, v20, v16\n"
"v_exp_f32 v18, v238\n"
"v_add_f32 v20, v20, v17\n"
"v_cvt_pk_bf16_f32 v102, v16, v17\n"
"v_exp_f32 v19, v239\n"
"v_add_f32 v20, v20, v18\n"
"v_exp_f32 v16, v240\n"
"v_add_f32 v20, v20, v19\n"
"v_cvt_pk_bf16_f32 v103, v18, v19\n"
"v_exp_f32 v17, v241\n"
"v_add_f32 v21, v21, v16\n"
"v_exp_f32 v18, v242\n"
"v_add_f32 v21, v21, v17\n"
"v_cvt_pk_bf16_f32 v104, v16, v17\n"
"v_exp_f32 v19, v243\n"
"v_add_f32 v21, v21, v18\n"
"v_exp_f32 v16, v244\n"
"v_add_f32 v21, v21, v19\n"
"v_cvt_pk_bf16_f32 v105, v18, v19\n"
"v_exp_f32 v17, v245\n"
"v_add_f32 v21, v21, v16\n"
"v_exp_f32 v18, v246\n"
"v_add_f32 v21, v21, v17\n"
"v_cvt_pk_bf16_f32 v106, v16, v17\n"
"v_exp_f32 v19, v247\n"
"v_add_f32 v21, v21, v18\n"
"v_exp_f32 v16, v248\n"
"v_add_f32 v21, v21, v19\n"
"v_cvt_pk_bf16_f32 v107, v18, v19\n"
"v_exp_f32 v17, v249\n"
"v_add_f32 v21, v21, v16\n"
"v_exp_f32 v18, v250\n"
"v_add_f32 v21, v21, v17\n"
"v_cvt_pk_bf16_f32 v108, v16, v17\n"
"v_exp_f32 v19, v251\n"
"v_add_f32 v21, v21, v18\n"
"v_exp_f32 v16, v252\n"
"v_add_f32 v21, v21, v19\n"
"v_cvt_pk_bf16_f32 v109, v18, v19\n"
"v_exp_f32 v17, v253\n"
"v_add_f32 v21, v21, v16\n"
"v_exp_f32 v18, v254\n"
"v_add_f32 v21, v21, v17\n"
"v_cvt_pk_bf16_f32 v110, v16, v17\n"
"v_exp_f32 v19, v255\n"
"v_add_f32 v21, v21, v18\n"
"v_add_f32 v21, v21, v19\n"
"v_cvt_pk_bf16_f32 v111, v18, v19\n"
"v_max_f32 v24, v20, v21\n"
"v_cmp_nge_f32 vcc, s43, v24\n"
"s_nop 0\n"
"s_cbranch_vccnz L_rare7_%=\n"
"L_back7_%=:\n"
"; B(8)\n"
"s_waitcnt lgkmcnt(4)\n"
"v_mfma_f32_32x32x16_bf16 a[0:15], a[192:195], v[80:83], a[0:15]\n"
"ds_read_b64_tr_b16 a[216:217], v112 offset:25088\n"
"v_add_f32 v22, v22, v20\n"
"v_add_f32 v23, v23, v21\n"
"s_add_u32 m0, s79, 0x10000\n"
"s_add_u32 s35, s74, 0\n"
"s_and_b32 s35, s35, 0x7ffff\n"
"v_mfma_f32_32x32x16_bf16 a[16:31], a[192:195], v[88:91], a[16:31]\n"
"ds_read_b64_tr_b16 a[218:219], v112 offset:27136\n"
"buffer_load_dwordx4 v116, s[16:19], s35 offen lds\n"
"v_mov_b32 v16, v22\n"
"v_mov_b32 v17, v23\n"
"s_nop 0\n"
"v_permlane32_swap_b32 v22, v16\n"
"v_mfma_f32_32x32x16_bf16 a[0:15], a[196:199], v[84:87], a[0:15]\n"
"ds_read_b64_tr_b16 a[192:193], v112 offset:29184\n"
"buffer_load_dwordx4 v117, s[16:19], s35 offen offset:1024 lds\n"
"v_permlane32_swap_b32 v23, v17\n"
"v_add_f32 v22, v22, v16\n"
"v_add_f32 v23, v23, v17\n"
"v_mfma_f32_32x32x16_bf16 a[16:31], a[196:199], v[92:95], a[16:31]\n"
"ds_read_b64_tr_b16 a[194:195], v112 offset:31232\n"
"v_rcp_f32 v20, v22\n"
"s_add_u32 m0, s79, 0x12000\n"
"s_add_u32 s35, s74, 0x8000\n"
"s_and_b32 s35, s35, 0x7ffff\n"
"v_mfma_f32_32x32x16_bf16 a[0:15], a[200:203], v[96:99], a[0:15]\n"
"ds_read_b64_tr_b16 a[196:197], v112 offset:17408\n"
"buffer_load_dwordx4 v116, s[16:19], s35 offen lds\n"
"v_rcp_f32 v21, v23\n"
"v_mfma_f32_32x32x16_bf16 a[16:31], a[200:203], v[104:107], a[16:31]\n"
"ds_read_b64_tr_b16 a[198:199], v112 offset:19456\n"
"buffer_load_dwordx4 v117, s[16:19], s35 offen offset:1024 lds\n"
"v_mfma_f32_32x32x16_bf16 a[0:15], a[204:207], v[100:103], a[0:15]\n"
"ds_read_b64_tr_b16 a[200:201], v112 offset:21504\n"
"s_add_u32 m0, s79, 0x14000\n"
"s_add_u32 s35, s74, 0x10000\n"
"s_and_b32 s35, s35, 0x7ffff\n"
"v_mfma_f32_32x32x16_bf16 a[16:31], a[204:207], v[108:111], a[16:31]\n"
"ds_read_b64_tr_b16 a[202:203], v112 offset:23552\n"
"buffer_load_dwordx4 v116, s[16:19], s35 offen lds\n"
"s_waitcnt lgkmcnt(4)\n"
"v_mfma_f32_32x32x16_bf16 a[32:47], a[208:211], v[80:83], a[32:47]\n"
"ds_read_b64_tr_b16 a[204:205], v112 offset:25600\n"
"buffer_load_dwordx4 v117, s[16:19], s35 offen offset:1024 lds\n"
"v_mfma_f32_32x32x16_bf16 a[48:63], a[208:211], v[88:91], a[48:63]\n"
"ds_read_b64_tr_b16 a[206:207], v112 offset:27648\n"
"s_add_u32 m0, s79, 0x16000\n"
"s_add_u32 s35, s74, 0x18000\n"
"s_and_b32 s35, s35, 0x7ffff\n"
"v_mfma_f32_32x32x16_bf16 a[32:47], a[212:215], v[84:87], a[32:47]\n"
"ds_read_b64_tr_b16 a[208:209], v112 offset:29696\n"
"buffer_load_dwordx4 v116, s[16:19], s35 offen lds\n"
"v_mfma_f32_32x32x16_bf16 a[48:63], a[212:215], v[92:95], a[48:63]\n"
"ds_read_b64_tr_b16 a[210:211], v112 offset:31744\n"
"buffer_load_dwordx4 v117, s[16:19], s35 offen offset:1024 lds\n"
"v_accvgpr_read_b32 v128, a0\n"
"v_accvgpr_read_b32 v129, a1\n"
"v_accvgpr_read_b32 v130, a2\n"
"v_accvgpr_read_b32 v131, a3\n"
"v_mfma_f32_32x32x16_bf16 a[32:47], a[216:219], v[96:99], a[32:47]\n"
"ds_read_b64_tr_b16 a[212:213], v112 offset:17920\n"
"v_accvgpr_read_b32 v132, a4\n"
"v_accvgpr_read_b32 v133, a5\n"
"s_add_u32 m0, s79, 0x20000\n"
"s_add_u32 s35, s74, 0x20000\n"
"s_and_b32 s35, s35, 0x7ffff\n"
"v_mfma_f32_32x32x16_bf16 a[48:63], a[216:219], v[104:107], a[48:63]\n"
"ds_read_b64_tr_b16 a[214:215], v112 offset:19968\n"
"buffer_load_dwordx4 v116, s[16:19], s35 offen lds\n"
"v_accvgpr_read_b32 v134, a6\n"
"v_accvgpr_read_b32 v135, a7\n"
"v_mul_f32 v128, v128, v20\n"
"v_mul_f32 v129, v129, v20\n"
"v_mfma_f32_32x32x16_bf16 a[32:47], a[192:195], v[100:103], a[32:47]\n"
"ds_read_b64_tr_b16 a[216:217], v112 offset:22016\n"
"buffer_load_dwordx4 v117, s[16:19], s35 offen offset:1024 lds\n"
"v_mul_f32 v130, v130, v20\n"
"v_mul_f32 v131, v131, v20\n"
"v_mul_f32 v132, v132, v20\n"
"v_mul_f32 v133, v133, v20\n"
"v_mfma_f32_32x32x16_bf16 a[48:63], a[192:195], v[108:111], a[48:63]\n"
"ds_read_b64_tr_b16 a[218:219], v112 offset:24064\n"
"v_mul_f32 v134, v134, v20\n"
"s_waitcnt lgkmcnt(4)\n"
"s_add_u32 m0, s79, 0x22000\n"
"s_add_u32 s35, s74, 0x28000\n"
"s_and_b32 s35, s35, 0x7ffff\n"
"v_mfma_f32_32x32x16_bf16 a[64:79], a[196:199], v[80:83], a[64:79]\n"
"ds_read_b64_tr_b16 a[192:193], v112 offset:26112\n"
"buffer_load_dwordx4 v116, s[16:19], s35 offen lds\n"
"v_mul_f32 v135, v135, v20\n"
"v_cvt_pk_bf16_f32 v128, v128, v129\n"
"v_cvt_pk_bf16_f32 v129, v130, v131\n"
"v_cvt_pk_bf16_f32 v130, v132, v133\n"
"v_mfma_f32_32x32x16_bf16 a[80:95], a[196:199], v[88:91], a[80:95]\n"
"ds_read_b64_tr_b16 a[194:195], v112 offset:28160\n"
"buffer_load_dwordx4 v117, s[16:19], s35 offen offset:1024 lds\n"
"v_cvt_pk_bf16_f32 v131, v134, v135\n"
"v_mfma_f32_32x32x16_bf16 a[64:79], a[200:203], v[84:87], a[64:79]\n"
"ds_read_b64_tr_b16 a[196:197], v112 offset:30208\n"
"ds_write_b128 v118, v[128:131]\n"
"v_accvgpr_read_b32 v136, a8\n"
"v_mfma_f32_32x32x16_bf16 a[80:95], a[200:203], v[92:95], a[80:95]\n"
"ds_read_b64_tr_b16 a[198:199], v112 offset:32256\n"
"v_accvgpr_read_b32 v137, a9\n"
"v_accvgpr_read_b32 v138, a10\n"
"v_accvgpr_read_b32 v139, a11\n"
"v_accvgpr_read_b32 v140, a12\n"
"v_accvgpr_read_b32 v141, a13\n"
"v_mfma_f32_32x32x16_bf16 a[64:79], a[204:207], v[96:99], a[64:79]\n"
"v_accvgpr_read_b32 v142, a14\n"
"v_accvgpr_read_b32 v143, a15\n"
"v_mul_f32 v136, v136, v20\n"
"v_mul_f32 v137, v137, v20\n"
"v_mul_f32 v138, v138, v20\n"
"v_mul_f32 v139, v139, v20\n"
"v_mfma_f32_32x32x16_bf16 a[80:95], a[204:207], v[104:107], a[80:95]\n"
"v_mul_f32 v140, v140, v20\n"
"v_mul_f32 v141, v141, v20\n"
"v_mul_f32 v142, v142, v20\n"
"v_mul_f32 v143, v143, v20\n"
"v_cvt_pk_bf16_f32 v136, v136, v137\n"
"v_cvt_pk_bf16_f32 v137, v138, v139\n"
"v_mfma_f32_32x32x16_bf16 a[64:79], a[208:211], v[100:103], a[64:79]\n"
"v_cvt_pk_bf16_f32 v138, v140, v141\n"
"v_cvt_pk_bf16_f32 v139, v142, v143\n"
"ds_write_b128 v118, v[136:139] offset:512\n"
"v_mfma_f32_32x32x16_bf16 a[80:95], a[208:211], v[108:111], a[80:95]\n"
"v_accvgpr_read_b32 v144, a16\n"
"v_accvgpr_read_b32 v145, a17\n"
"v_accvgpr_read_b32 v146, a18\n"
"v_accvgpr_read_b32 v147, a19\n"
"v_accvgpr_read_b32 v148, a20\n"
"s_waitcnt lgkmcnt(1)\n"
"v_mfma_f32_32x32x16_bf16 a[96:111], a[212:215], v[80:83], a[96:111]\n"
"v_accvgpr_read_b32 v149, a21\n"
"v_accvgpr_read_b32 v150, a22\n"
"v_accvgpr_read_b32 v151, a23\n"
"v_mul_f32 v144, v144, v21\n"
"v_mul_f32 v145, v145, v21\n"
"v_mul_f32 v146, v146, v21\n"
"v_mfma_f32_32x32x16_bf16 a[112:127], a[212:215], v[88:91], a[112:127]\n"
"v_mul_f32 v147, v147, v21\n"
"v_mul_f32 v148, v148, v21\n"
"v_mul_f32 v149, v149, v21\n"
"v_mul_f32 v150, v150, v21\n"
"v_mul_f32 v151, v151, v21\n"
"v_cvt_pk_bf16_f32 v144, v144, v145\n"
"v_mfma_f32_32x32x16_bf16 a[96:111], a[216:219], v[84:87], a[96:111]\n"
"v_cvt_pk_bf16_f32 v145, v146, v147\n"
"v_cvt_pk_bf16_f32 v146, v148, v149\n"
"v_cvt_pk_bf16_f32 v147, v150, v151\n"
"v_mfma_f32_32x32x16_bf16 a[112:127], a[216:219], v[92:95], a[112:127]\n"
"ds_write_b128 v118, v[144:147] offset:2048\n"
"v_accvgpr_read_b32 v152, a24\n"
"v_accvgpr_read_b32 v153, a25\n"
"v_mfma_f32_32x32x16_bf16 a[96:111], a[192:195], v[96:99], a[96:111]\n"
"v_accvgpr_read_b32 v154, a26\n"
"v_accvgpr_read_b32 v155, a27\n"
"v_accvgpr_read_b32 v156, a28\n"
"v_accvgpr_read_b32 v157, a29\n"
"v_accvgpr_read_b32 v158, a30\n"
"v_accvgpr_read_b32 v159, a31\n"
"v_mfma_f32_32x32x16_bf16 a[112:127], a[192:195], v[104:107], a[112:127]\n"
"v_mul_f32 v152, v152, v21\n"
"v_mul_f32 v153, v153, v21\n"
"v_mul_f32 v154, v154, v21\n"
"v_mul_f32 v155, v155, v21\n"
"v_mul_f32 v156, v156, v21\n"
"v_mul_f32 v157, v157, v21\n"
"v_mfma_f32_32x32x16_bf16 a[96:111], a[196:199], v[100:103], a[96:111]\n"
"v_mul_f32 v158, v158, v21\n"
"v_mul_f32 v159, v159, v21\n"
"v_cvt_pk_bf16_f32 v152, v152, v153\n"
"v_cvt_pk_bf16_f32 v153, v154, v155\n"
"v_cvt_pk_bf16_f32 v154, v156, v157\n"
"v_cvt_pk_bf16_f32 v155, v158, v159\n"
"v_mfma_f32_32x32x16_bf16 a[112:127], a[196:199], v[108:111], a[112:127]\n"
"ds_write_b128 v118, v[152:155] offset:2560\n"
"v_accvgpr_read_b32 v160, a32\n"
"v_accvgpr_read_b32 v161, a33\n"
"; attn end\n"
"v_accvgpr_read_b32 v162, a34\n"
"v_accvgpr_read_b32 v163, a35\n"
"v_accvgpr_read_b32 v164, a36\n"
"v_accvgpr_read_b32 v165, a37\n"
"v_accvgpr_read_b32 v166, a38\n"
"v_accvgpr_read_b32 v167, a39\n"
"v_mul_f32 v160, v160, v20\n"
"v_mul_f32 v161, v161, v20\n"
"v_mul_f32 v162, v162, v20\n"
"v_mul_f32 v163, v163, v20\n"
"v_mul_f32 v164, v164, v20\n"
"v_mul_f32 v165, v165, v20\n"
"v_mul_f32 v166, v166, v20\n"
"v_mul_f32 v167, v167, v20\n"
"v_cvt_pk_bf16_f32 v160, v160, v161\n"
"v_cvt_pk_bf16_f32 v161, v162, v163\n"
"v_cvt_pk_bf16_f32 v162, v164, v165\n"
"v_cvt_pk_bf16_f32 v163, v166, v167\n"
"ds_write_b128 v119, v[160:163]\n"
"v_accvgpr_read_b32 v168, a40\n"
"v_accvgpr_read_b32 v169, a41\n"
"v_accvgpr_read_b32 v170, a42\n"
"v_accvgpr_read_b32 v171, a43\n"
"v_accvgpr_read_b32 v172, a44\n"
"v_accvgpr_read_b32 v173, a45\n"
"v_accvgpr_read_b32 v174, a46\n"
"v_accvgpr_read_b32 v175, a47\n"
"v_mul_f32 v168, v168, v20\n"
"v_mul_f32 v169, v169, v20\n"
"v_mul_f32 v170, v170, v20\n"
"v_mul_f32 v171, v171, v20\n"
"v_mul_f32 v172, v172, v20\n"
"v_mul_f32 v173, v173, v20\n"
"v_mul_f32 v174, v174, v20\n"
"v_mul_f32 v175, v175, v20\n"
"v_cvt_pk_bf16_f32 v168, v168, v169\n"
"v_cvt_pk_bf16_f32 v169, v170, v171\n"
"v_cvt_pk_bf16_f32 v170, v172, v173\n"
"v_cvt_pk_bf16_f32 v171, v174, v175\n"
"ds_write_b128 v119, v[168:171] offset:512\n"
"v_accvgpr_read_b32 v176, a48\n"
"v_accvgpr_read_b32 v177, a49\n"
"v_accvgpr_read_b32 v178, a50\n"
"v_accvgpr_read_b32 v179, a51\n"
"v_accvgpr_read_b32 v180, a52\n"
"v_accvgpr_read_b32 v181, a53\n"
"v_accvgpr_read_b32 v182, a54\n"
"v_accvgpr_read_b32 v183, a55\n"
"v_mul_f32 v176, v176, v21\n"
"v_mul_f32 v177, v177, v21\n"
"v_mul_f32 v178, v178, v21\n"
"v_mul_f32 v179, v179, v21\n"
"v_mul_f32 v180, v180, v21\n"
"v_mul_f32 v181, v181, v21\n"
"v_mul_f32 v182, v182, v21\n"
"v_mul_f32 v183, v183, v21\n"
"v_cvt_pk_bf16_f32 v176, v176, v177\n"
"v_cvt_pk_bf16_f32 v177, v178, v179\n"
"v_cvt_pk_bf16_f32 v178, v180, v181\n"
"v_cvt_pk_bf16_f32 v179, v182, v183\n"
"ds_write_b128 v119, v[176:179] offset:2048\n"
"s_lshl_b32 s11, s7, 10\n"
"v_add_u32 v11, s11, v1\n"
"s_barrier\n"
"v_accvgpr_read_b32 v184, a56\n"
"v_accvgpr_read_b32 v185, a57\n"
"v_accvgpr_read_b32 v186, a58\n"
"v_accvgpr_read_b32 v187, a59\n"
"v_accvgpr_read_b32 v188, a60\n"
"v_accvgpr_read_b32 v189, a61\n"
"v_accvgpr_read_b32 v190, a62\n"
"v_accvgpr_read_b32 v191, a63\n"
"v_pk_mul_f32 v[184:185], v[184:185], v[20:21] op_sel:[0,1] op_sel_hi:[1,1]\n"
"v_pk_mul_f32 v[186:187], v[186:187], v[20:21] op_sel:[0,1] op_sel_hi:[1,1]\n"
"v_pk_mul_f32 v[188:189], v[188:189], v[20:21] op_sel:[0,1] op_sel_hi:[1,1]\n"
"v_pk_mul_f32 v[190:191], v[190:191], v[20:21] op_sel:[0,1] op_sel_hi:[1,1]\n"
"v_cvt_pk_bf16_f32 v184, v184, v185\n"
"v_cvt_pk_bf16_f32 v185, v186, v187\n"
"v_cvt_pk_bf16_f32 v186, v188, v189\n"
"v_cvt_pk_bf16_f32 v187, v190, v191\n"
"ds_write_b128 v119, v[184:187] offset:2560\n"
"global_load_dwordx4 v[80:83], v1, s[44:45]\n"
"s_mov_b32 s12, 0x22000\n"
"buffer_load_dwordx4 v[32:35], v11, s[28:31], s12 offen sc1\n"
"v_accvgpr_read_b32 v128, a64\n"
"v_accvgpr_read_b32 v129, a65\n"
"v_accvgpr_read_b32 v130, a66\n"
"v_accvgpr_read_b32 v131, a67\n"
"v_accvgpr_read_b32 v132, a68\n"
"v_accvgpr_read_b32 v133, a69\n"
"v_accvgpr_read_b32 v134, a70\n"
"v_accvgpr_read_b32 v135, a71\n"
"v_pk_mul_f32 v[128:129], v[128:129], v[20:21] op_sel_hi:[1,0]\n"
"v_pk_mul_f32 v[130:131], v[130:131], v[20:21] op_sel_hi:[1,0]\n"
"v_pk_mul_f32 v[132:133], v[132:133], v[20:21] op_sel_hi:[1,0]\n"
"v_pk_mul_f32 v[134:135], v[134:135], v[20:21] op_sel_hi:[1,0]\n"
"v_cvt_pk_bf16_f32 v128, v128, v129\n"
"v_cvt_pk_bf16_f32 v129, v130, v131\n"
"v_cvt_pk_bf16_f32 v130, v132, v133\n"
"v_cvt_pk_bf16_f32 v131, v134, v135\n"
"ds_write_b128 v120, v[128:131]\n"
"s_mov_b32 s12, 0x23000\n"
"buffer_load_dwordx4 v[36:39], v11, s[28:31], s12 offen sc1\n"
"s_mov_b32 s12, 0x28000\n"
"v_accvgpr_read_b32 v136, a72\n"
"v_accvgpr_read_b32 v137, a73\n"
"v_accvgpr_read_b32 v138, a74\n"
"v_accvgpr_read_b32 v139, a75\n"
"v_accvgpr_read_b32 v140, a76\n"
"v_accvgpr_read_b32 v141, a77\n"
"v_accvgpr_read_b32 v142, a78\n"
"v_accvgpr_read_b32 v143, a79\n"
"v_pk_mul_f32 v[136:137], v[136:137], v[20:21] op_sel_hi:[1,0]\n"
"v_pk_mul_f32 v[138:139], v[138:139], v[20:21] op_sel_hi:[1,0]\n"
"v_pk_mul_f32 v[140:141], v[140:141], v[20:21] op_sel_hi:[1,0]\n"
"v_pk_mul_f32 v[142:143], v[142:143], v[20:21] op_sel_hi:[1,0]\n"
"v_cvt_pk_bf16_f32 v136, v136, v137\n"
"v_cvt_pk_bf16_f32 v137, v138, v139\n"
"v_cvt_pk_bf16_f32 v138, v140, v141\n"
"v_cvt_pk_bf16_f32 v139, v142, v143\n"
"ds_write_b128 v120, v[136:139] offset:512\n"
"buffer_load_dwordx4 v[40:43], v11, s[28:31], s12 offen sc1\n"
"s_mov_b32 s12, 0x29000\n"
"buffer_load_dwordx4 v[44:47], v11, s[28:31], s12 offen sc1\n"
"v_accvgpr_read_b32 v144, a80\n"
"v_accvgpr_read_b32 v145, a81\n"
"v_accvgpr_read_b32 v146, a82\n"
"v_accvgpr_read_b32 v147, a83\n"
"v_accvgpr_read_b32 v148, a84\n"
"v_accvgpr_read_b32 v149, a85\n"
"v_accvgpr_read_b32 v150, a86\n"
"v_accvgpr_read_b32 v151, a87\n"
"v_pk_mul_f32 v[144:145], v[144:145], v[20:21] op_sel:[0,1] op_sel_hi:[1,1]\n"
"v_pk_mul_f32 v[146:147], v[146:147], v[20:21] op_sel:[0,1] op_sel_hi:[1,1]\n"
"v_pk_mul_f32 v[148:149], v[148:149], v[20:21] op_sel:[0,1] op_sel_hi:[1,1]\n"
"v_pk_mul_f32 v[150:151], v[150:151], v[20:21] op_sel:[0,1] op_sel_hi:[1,1]\n"
"v_cvt_pk_bf16_f32 v144, v144, v145\n"
"v_cvt_pk_bf16_f32 v145, v146, v147\n"
"v_cvt_pk_bf16_f32 v146, v148, v149\n"
"v_cvt_pk_bf16_f32 v147, v150, v151\n"
"ds_write_b128 v120, v[144:147] offset:2048\n"
"s_mov_b32 s12, 0x24000\n"
"buffer_load_dwordx4 v[64:67], v11, s[28:31], s12 offen sc1\n"
"s_mov_b32 s12, 0x25000\n"
"v_accvgpr_read_b32 v152, a88\n"
"v_accvgpr_read_b32 v153, a89\n"
"v_accvgpr_read_b32 v154, a90\n"
"v_accvgpr_read_b32 v155, a91\n"
"v_accvgpr_read_b32 v156, a92\n"
"v_accvgpr_read_b32 v157, a93\n"
"v_accvgpr_read_b32 v158, a94\n"
"v_accvgpr_read_b32 v159, a95\n"
"v_pk_mul_f32 v[152:153], v[152:153], v[20:21] op_sel:[0,1] op_sel_hi:[1,1]\n"
"v_pk_mul_f32 v[154:155], v[154:155], v[20:21] op_sel:[0,1] op_sel_hi:[1,1]\n"
"v_pk_mul_f32 v[156:157], v[156:157], v[20:21] op_sel:[0,1] op_sel_hi:[1,1]\n"
"v_pk_mul_f32 v[158:159], v[158:159], v[20:21] op_sel:[0,1] op_sel_hi:[1,1]\n"
"v_cvt_pk_bf16_f32 v152, v152, v153\n"
"v_cvt_pk_bf16_f32 v153, v154, v155\n"
"v_cvt_pk_bf16_f32 v154, v156, v157\n"
"v_cvt_pk_bf16_f32 v155, v158, v159\n"
"ds_write_b128 v120, v[152:155] offset:2560\n"
"buffer_load_dwordx4 v[68:71], v11, s[28:31], s12 offen sc1\n"
"s_mov_b32 s12, 0x2a000\n"
"buffer_load_dwordx4 v[72:75], v11, s[28:31], s12 offen sc1\n"
"v_accvgpr_read_b32 v160, a96\n"
"v_accvgpr_read_b32 v161, a97\n"
"v_accvgpr_read_b32 v162, a98\n"
"v_accvgpr_read_b32 v163, a99\n"
"v_accvgpr_read_b32 v164, a100\n"
"v_accvgpr_read_b32 v165, a101\n"
"v_accvgpr_read_b32 v166, a102\n"
"v_accvgpr_read_b32 v167, a103\n"
"v_pk_mul_f32 v[160:161], v[160:161], v[20:21] op_sel_hi:[1,0]\n"
"v_pk_mul_f32 v[162:163], v[162:163], v[20:21] op_sel_hi:[1,0]\n"
"v_pk_mul_f32 v[164:165], v[164:165], v[20:21] op_sel_hi:[1,0]\n"
"v_pk_mul_f32 v[166:167], v[166:167], v[20:21] op_sel_hi:[1,0]\n"
"v_cvt_pk_bf16_f32 v160, v160, v161\n"
"v_cvt_pk_bf16_f32 v161, v162, v163\n"
"v_cvt_pk_bf16_f32 v162, v164, v165\n"
"v_cvt_pk_bf16_f32 v163, v166, v167\n"
"ds_write_b128 v121, v[160:163]\n"
"s_mov_b32 s12, 0x2b000\n"
"buffer_load_dwordx4 v[76:79], v11, s[28:31], s12 offen sc1\n"
"s_add_u32 m0, s79, 0x18000\n"
"v_accvgpr_read_b32 v168, a104\n"
"v_accvgpr_read_b32 v169, a105\n"
"v_accvgpr_read_b32 v170, a106\n"
"v_accvgpr_read_b32 v171, a107\n"
"v_accvgpr_read_b32 v172, a108\n"
"v_accvgpr_read_b32 v173, a109\n"
"v_accvgpr_read_b32 v174, a110\n"
"v_accvgpr_read_b32 v175, a111\n"
"v_pk_mul_f32 v[168:169], v[168:169], v[20:21] op_sel_hi:[1,0]\n"
"v_pk_mul_f32 v[170:171], v[170:171], v[20:21] op_sel_hi:[1,0]\n"
"v_pk_mul_f32 v[172:173], v[172:173], v[20:21] op_sel_hi:[1,0]\n"
"v_pk_mul_f32 v[174:175], v[174:175], v[20:21] op_sel_hi:[1,0]\n"
"v_cvt_pk_bf16_f32 v168, v168, v169\n"
"v_cvt_pk_bf16_f32 v169, v170, v171\n"
"v_cvt_pk_bf16_f32 v170, v172, v173\n"
"v_cvt_pk_bf16_f32 v171, v174, v175\n"
"ds_write_b128 v121, v[168:171] offset:512\n"
"s_add_u32 s35, s74, 0x30000\n"
"s_and_b32 s35, s35, 0x7ffff\n"
"buffer_load_dwordx4 v116, s[16:19], s35 offen lds\n"
"buffer_load_dwordx4 v117, s[16:19], s35 offen offset:1024 lds\n"
"v_accvgpr_read_b32 v176, a112\n"
"v_accvgpr_read_b32 v177, a113\n"
"v_accvgpr_read_b32 v178, a114\n"
"v_accvgpr_read_b32 v179, a115\n"
"v_accvgpr_read_b32 v180, a116\n"
"v_accvgpr_read_b32 v181, a117\n"
"v_accvgpr_read_b32 v182, a118\n"
"v_accvgpr_read_b32 v183, a119\n"
"v_pk_mul_f32 v[176:177], v[176:177], v[20:21] op_sel:[0,1] op_sel_hi:[1,1]\n"
"v_pk_mul_f32 v[178:179], v[178:179], v[20:21] op_sel:[0,1] op_sel_hi:[1,1]\n"
"v_pk_mul_f32 v[180:181], v[180:181], v[20:21] op_sel:[0,1] op_sel_hi:[1,1]\n"
"v_pk_mul_f32 v[182:183], v[182:183], v[20:21] op_sel:[0,1] op_sel_hi:[1,1]\n"
"v_cvt_pk_bf16_f32 v176, v176, v177\n"
"v_cvt_pk_bf16_f32 v177, v178, v179\n"
"v_cvt_pk_bf16_f32 v178, v180, v181\n"
"v_cvt_pk_bf16_f32 v179, v182, v183\n"
"ds_write_b128 v121, v[176:179] offset:2048\n"
"s_add_u32 m0, s79, 0x1a000\n"
"s_add_u32 s35, s74, 0x38000\n"
"s_and_b32 s35, s35, 0x7ffff\n"
"buffer_load_dwordx4 v116, s[16:19], s35 offen lds\n"
"v_accvgpr_read_b32 v184, a120\n"
"v_accvgpr_read_b32 v185, a121\n"
"v_accvgpr_read_b32 v186, a122\n"
"v_accvgpr_read_b32 v187, a123\n"
"v_accvgpr_read_b32 v188, a124\n"
"v_accvgpr_read_b32 v189, a125\n"
"v_accvgpr_read_b32 v190, a126\n"
"v_accvgpr_read_b32 v191, a127\n"
"v_pk_mul_f32 v[184:185], v[184:185], v[20:21] op_sel:[0,1] op_sel_hi:[1,1]\n"
"v_pk_mul_f32 v[186:187], v[186:187], v[20:21] op_sel:[0,1] op_sel_hi:[1,1]\n"
"v_pk_mul_f32 v[188:189], v[188:189], v[20:21] op_sel:[0,1] op_sel_hi:[1,1]\n"
"v_pk_mul_f32 v[190:191], v[190:191], v[20:21] op_sel:[0,1] op_sel_hi:[1,1]\n"
"v_cvt_pk_bf16_f32 v184, v184, v185\n"
"v_cvt_pk_bf16_f32 v185, v186, v187\n"
"v_cvt_pk_bf16_f32 v186, v188, v189\n"
"v_cvt_pk_bf16_f32 v187, v190, v191\n"
"ds_write_b128 v121, v[184:187] offset:2560\n"
"buffer_load_dwordx4 v117, s[16:19], s35 offen offset:1024 lds\n"
"s_add_u32 m0, s79, 0x1c000\n"
"s_add_u32 s35, s74, 0x40000\n"
"s_and_b32 s35, s35, 0x7ffff\n"
"buffer_load_dwordx4 v116, s[16:19], s35 offen lds\n"
"buffer_load_dwordx4 v117, s[16:19], s35 offen offset:1024 lds\n"
"v_lshrrev_b32 v6, 4, v1\n"
"v_and_b32 v7, 15, v6\n"
"v_lshrrev_b32 v8, 4, v6\n"
"v_bfe_u32 v4, v6, 2, 3\n"
"v_lshlrev_b32 v4, 6, v4\n"
"v_and_b32 v5, 3, v6\n"
"v_lshl_add_u32 v4, v5, 3, v4\n"
"v_lshrrev_b32 v5, 5, v6\n"
"v_lshl_add_u32 v4, v5, 9, v4\n"
"v_bfe_u32 v5, v6, 4, 1\n"
"v_lshl_add_u32 v4, v5, 5, v4\n"
"s_lshl_b32 s11, s7, 11\n"
"s_add_u32 s11, s11, 0x10000\n"
"v_add_u32 v2, s11, v4\n"
"v_xor_b32 v3, 32, v2\n"
"v_add_u32 v12, 0x10000, v2\n"
"v_add_u32 v13, 0x10000, v3\n"
"v_mov_b32 v14, v116\n"
"v_mov_b32 v15, v117\n"
"s_waitcnt lgkmcnt(0)\n"
"s_barrier\n"
"ds_read_b128 a[0:3], v1\n"
"ds_read_b128 a[4:7], v1 offset:1024\n"
"ds_read_b128 a[8:11], v1 offset:2048\n"
"ds_read_b128 a[12:15], v1 offset:3072\n"
"s_waitcnt vmcnt(25)\n"
"ds_read_b64_tr_b16 v[16:17], v2\n"
"ds_read_b64_tr_b16 v[18:19], v2 offset:1024\n"
"ds_read_b64_tr_b16 v[20:21], v3\n"
"ds_read_b64_tr_b16 v[22:23], v3 offset:1024\n"
"ds_read_b128 a[16:19], v1 offset:4096\n"
"ds_read_b128 a[20:23], v1 offset:5120\n"
"ds_read_b128 a[24:27], v1 offset:6144\n"
"ds_read_b128 a[28:31], v1 offset:7168\n"
"; o pass 0\n"
"s_waitcnt lgkmcnt(4)\n"
"v_mfma_f32_16x16x32_bf16 v[128:131], v[16:19], a[0:3], 0\n"
"s_waitcnt vmcnt(23)\n"
"ds_read_b64_tr_b16 v[24:25], v2 offset:8192\n"
"v_mfma_f32_16x16x32_bf16 v[132:135], v[16:19], a[4:7], 0\n"
"ds_read_b64_tr_b16 v[26:27], v2 offset:9216\n"
"v_lshrrev_b32 v4, 3, v7\n"
"v_mfma_f32_16x16x32_bf16 v[136:139], v[16:19], a[8:11], 0\n"
"ds_read_b64_tr_b16 v[28:29], v3 offset:8192\n"
"s_add_u32 m0, s79, 0x1e000\n"
"v_mfma_f32_16x16x32_bf16 v[140:143], v[16:19], a[12:15], 0\n"
"ds_read_b64_tr_b16 v[30:31], v3 offset:9216\n"
"s_add_u32 s35, s74, 0x48000\n"
"s_and_b32 s35, s35, 0x7ffff\n"
"v_mfma_f32_16x16x32_bf16 v[144:147], v[20:23], a[0:3], 0\n"
"buffer_load_dwordx4 v14, s[16:19], s35 offen lds\n"
"ds_read_b128 a[40:43], v1 offset:10240\n"
"v_mfma_f32_16x16x32_bf16 v[148:151], v[20:23], a[4:7], 0\n"
"buffer_load_dwordx4 v15, s[16:19], s35 offen offset:1024 lds\n"
"ds_read_b128 a[44:47], v1 offset:11264\n"
"v_mfma_f32_16x16x32_bf16 v[152:155], v[20:23], a[8:11], 0\n"
"ds_read_b128 a[32:35], v1 offset:8192\n"
"v_lshlrev_b32 v4, 16, v4\n"
"v_mfma_f32_16x16x32_bf16 v[156:159], v[20:23], a[12:15], 0\n"
"ds_read_b128 a[36:39], v1 offset:9216\n"
"s_waitcnt lgkmcnt(4)\n"
"v_mfma_f32_16x16x32_bf16 v[128:131], v[24:27], a[16:19], v[128:131]\n"
"s_waitcnt vmcnt(23)\n"
"ds_read_b64_tr_b16 v[16:17], v2 offset:16384\n"
"v_mfma_f32_16x16x32_bf16 v[132:135], v[24:27], a[20:23], v[132:135]\n"
"ds_read_b64_tr_b16 v[18:19], v2 offset:17408\n"
"v_lshl_add_u32 v4, v8, 7, v4\n"
"v_mfma_f32_16x16x32_bf16 v[136:139], v[24:27], a[24:27], v[136:139]\n"
"ds_read_b64_tr_b16 v[20:21], v3 offset:16384\n"
"s_add_u32 m0, s79, 0x10000\n"
"v_mfma_f32_16x16x32_bf16 v[140:143], v[24:27], a[28:31], v[140:143]\n"
"ds_read_b64_tr_b16 v[22:23], v3 offset:17408\n"
"s_add_u32 s35, s74, 0x50000\n"
"s_and_b32 s35, s35, 0x7ffff\n"
"v_mfma_f32_16x16x32_bf16 v[144:147], v[28:31], a[16:19], v[144:147]\n"
"buffer_load_dwordx4 v14, s[16:19], s35 offen lds\n"
"ds_read_b128 a[56:59], v1 offset:14336\n"
"v_mfma_f32_16x16x32_bf16 v[148:151], v[28:31], a[20:23], v[148:151]\n"
"buffer_load_dwordx4 v15, s[16:19], s35 offen offset:1024 lds\n"
"ds_read_b128 a[60:63], v1 offset:15360\n"
"v_mfma_f32_16x16x32_bf16 v[152:155], v[28:31], a[24:27], v[152:155]\n"
"ds_read_b128 a[48:51], v1 offset:12288\n"
"v_and_b32 v5, 7, v7\n"
"v_mfma_f32_16x16x32_bf16 v[156:159], v[28:31], a[28:31], v[156:159]\n"
"ds_read_b128 a[52:55], v1 offset:13312\n"
"s_waitcnt lgkmcnt(4)\n"
"v_mfma_f32_16x16x32_bf16 v[128:131], v[16:19], a[32:35], v[128:131]\n"
"s_waitcnt vmcnt(23)\n"
"ds_read_b64_tr_b16 v[24:25], v2 offset:24576\n"
"v_mfma_f32_16x16x32_bf16 v[132:135], v[16:19], a[36:39], v[132:135]\n"
"ds_read_b64_tr_b16 v[26:27], v2 offset:25600\n"
"v_lshl_add_u32 v9, v5, 4, v4\n"
"v_mfma_f32_16x16x32_bf16 v[136:139], v[16:19], a[40:43], v[136:139]\n"
"ds_read_b64_tr_b16 v[28:29], v3 offset:24576\n"
"s_add_u32 m0, s79, 0x12000\n"
"v_mfma_f32_16x16x32_bf16 v[140:143], v[16:19], a[44:47], v[140:143]\n"
"ds_read_b64_tr_b16 v[30:31], v3 offset:25600\n"
"s_add_u32 s35, s74, 0x58000\n"
"s_and_b32 s35, s35, 0x7ffff\n"
"v_mfma_f32_16x16x32_bf16 v[144:147], v[20:23], a[32:35], v[144:147]\n"
"buffer_load_dwordx4 v14, s[16:19], s35 offen lds\n"
"ds_read_b128 a[72:75], v1 offset:18432\n"
"v_mfma_f32_16x16x32_bf16 v[148:151], v[20:23], a[36:39], v[148:151]\n"
"buffer_load_dwordx4 v15, s[16:19], s35 offen offset:1024 lds\n"
"ds_read_b128 a[76:79], v1 offset:19456\n"
"v_mfma_f32_16x16x32_bf16 v[152:155], v[20:23], a[40:43], v[152:155]\n"
"ds_read_b128 a[64:67], v1 offset:16384\n"
"v_lshrrev_b32 v4, 5, v6\n"
"v_mfma_f32_16x16x32_bf16 v[156:159], v[20:23], a[44:47], v[156:159]\n"
"ds_read_b128 a[68:71], v1 offset:17408\n"
"s_waitcnt lgkmcnt(4)\n"
"v_mfma_f32_16x16x32_bf16 v[128:131], v[24:27], a[48:51], v[128:131]\n"
"s_waitcnt vmcnt(23)\n"
"ds_read_b64_tr_b16 v[16:17], v12\n"
"v_mfma_f32_16x16x32_bf16 v[132:135], v[24:27], a[52:55], v[132:135]\n"
"ds_read_b64_tr_b16 v[18:19], v12 offset:1024\n"
"v_and_b32 v5, 0x1ff, v1\n"
"v_mfma_f32_16x16x32_bf16 v[136:139], v[24:27], a[56:59], v[136:139]\n"
"ds_read_b64_tr_b16 v[20:21], v13\n"
"s_add_u32 m0, s79, 0x14000\n"
"v_mfma_f32_16x16x32_bf16 v[140:143], v[24:27], a[60:63], v[140:143]\n"
"ds_read_b64_tr_b16 v[22:23], v13 offset:1024\n"
"s_add_u32 s35, s74, 0x60000\n"
"s_and_b32 s35, s35, 0x7ffff\n"
"v_mfma_f32_16x16x32_bf16 v[144:147], v[28:31], a[48:51], v[144:147]\n"
"buffer_load_dwordx4 v14, s[16:19], s35 offen lds\n"
"ds_read_b128 a[88:91], v1 offset:22528\n"
"v_mfma_f32_16x16x32_bf16 v[148:151], v[28:31], a[52:55], v[148:151]\n"
"buffer_load_dwordx4 v15, s[16:19], s35 offen offset:1024 lds\n"
"ds_read_b128 a[92:95], v1 offset:23552\n"
"v_mfma_f32_16x16x32_bf16 v[152:155], v[28:31], a[56:59], v[152:155]\n"
"ds_read_b128 a[80:83], v1 offset:20480\n"
"v_lshl_add_u32 v10, v4, 11, v5\n"
"v_mfma_f32_16x16x32_bf16 v[156:159], v[28:31], a[60:63], v[156:159]\n"
"ds_read_b128 a[84:87], v1 offset:21504\n"
"s_waitcnt lgkmcnt(4)\n"
"v_mfma_f32_16x16x32_bf16 v[128:131], v[16:19], a[64:67], v[128:131]\n"
"s_waitcnt vmcnt(23)\n"
"ds_read_b64_tr_b16 v[24:25], v12 offset:8192\n"
"v_mfma_f32_16x16x32_bf16 v[132:135], v[16:19], a[68:71], v[132:135]\n"
"ds_read_b64_tr_b16 v[26:27], v12 offset:9216\n"
"s_lshr_b32 s11, s8, 1\n"
"v_mfma_f32_16x16x32_bf16 v[136:139], v[16:19], a[72:75], v[136:139]\n"
"ds_read_b64_tr_b16 v[28:29], v13 offset:8192\n"
"s_add_u32 m0, s79, 0x16000\n"
"v_mfma_f32_16x16x32_bf16 v[140:143], v[16:19], a[76:79], v[140:143]\n"
"ds_read_b64_tr_b16 v[30:31], v13 offset:9216\n"
"s_add_u32 s35, s74, 0x68000\n"
"s_and_b32 s35, s35, 0x7ffff\n"
"v_mfma_f32_16x16x32_bf16 v[144:147], v[20:23], a[64:67], v[144:147]\n"
"buffer_load_dwordx4 v14, s[16:19], s35 offen lds\n"
"ds_read_b128 a[104:107], v1 offset:26624\n"
"v_mfma_f32_16x16x32_bf16 v[148:151], v[20:23], a[68:71], v[148:151]\n"
"buffer_load_dwordx4 v15, s[16:19], s35 offen offset:1024 lds\n"
"ds_read_b128 a[108:111], v1 offset:27648\n"
"v_mfma_f32_16x16x32_bf16 v[152:155], v[20:23], a[72:75], v[152:155]\n"
"ds_read_b128 a[96:99], v1 offset:24576\n"
"s_and_b32 s12, s8, 1\n"
"v_mfma_f32_16x16x32_bf16 v[156:159], v[20:23], a[76:79], v[156:159]\n"
"ds_read_b128 a[100:103], v1 offset:25600\n"
"s_waitcnt lgkmcnt(4)\n"
"v_mfma_f32_16x16x32_bf16 v[128:131], v[24:27], a[80:83], v[128:131]\n"
"s_waitcnt vmcnt(14)\n"
"ds_read_b64_tr_b16 v[16:17], v2 offset:32768\n"
"v_mfma_f32_16x16x32_bf16 v[132:135], v[24:27], a[84:87], v[132:135]\n"
"ds_read_b64_tr_b16 v[18:19], v2 offset:33792\n"
"v_lshrrev_b32 v4, 3, v7\n"
"v_mfma_f32_16x16x32_bf16 v[136:139], v[24:27], a[88:91], v[136:139]\n"
"ds_read_b64_tr_b16 v[20:21], v3 offset:32768\n"
"s_add_u32 m0, s79, 0x20000\n"
"v_mfma_f32_16x16x32_bf16 v[140:143], v[24:27], a[92:95], v[140:143]\n"
"ds_read_b64_tr_b16 v[22:23], v3 offset:33792\n"
"s_add_u32 s35, s74, 0x70000\n"
"s_and_b32 s35, s35, 0x7ffff\n"
"v_mfma_f32_16x16x32_bf16 v[144:147], v[28:31], a[80:83], v[144:147]\n"
"buffer_load_dwordx4 v14, s[16:19], s35 offen lds\n"
"ds_read_b128 a[120:123], v1 offset:30720\n"
"v_mfma_f32_16x16x32_bf16 v[148:151], v[28:31], a[84:87], v[148:151]\n"
"buffer_load_dwordx4 v15, s[16:19], s35 offen offset:1024 lds\n"
"ds_read_b128 a[124:127], v1 offset:31744\n"
"v_mfma_f32_16x16x32_bf16 v[152:155], v[28:31], a[88:91], v[152:155]\n"
"ds_read_b128 a[112:115], v1 offset:28672\n"
"v_cmp_eq_u32 vcc, s12, v4\n"
"v_mfma_f32_16x16x32_bf16 v[156:159], v[28:31], a[92:95], v[156:159]\n"
"ds_read_b128 a[116:119], v1 offset:29696\n"
"s_waitcnt lgkmcnt(4)\n"
"v_mfma_f32_16x16x32_bf16 v[128:131], v[16:19], a[96:99], v[128:131]\n"
"s_waitcnt vmcnt(14)\n"
"ds_read_b64_tr_b16 v[24:25], v2 offset:40960\n"
"v_mfma_f32_16x16x32_bf16 v[132:135], v[16:19], a[100:103], v[132:135]\n"
"ds_read_b64_tr_b16 v[26:27], v2 offset:41984\n"
"v_mov_b32 v5, 0x40000000\n"
"v_mfma_f32_16x16x32_bf16 v[136:139], v[16:19], a[104:107], v[136:139]\n"
"ds_read_b64_tr_b16 v[28:29], v3 offset:40960\n"
"s_add_u32 m0, s79, 0x22000\n"
"v_mfma_f32_16x16x32_bf16 v[140:143], v[16:19], a[108:111], v[140:143]\n"
"ds_read_b64_tr_b16 v[30:31], v3 offset:41984\n"
"s_add_u32 s35, s74, 0x78000\n"
"s_and_b32 s35, s35, 0x7ffff\n"
"v_mfma_f32_16x16x32_bf16 v[144:147], v[20:23], a[96:99], v[144:147]\n"
"buffer_load_dwordx4 v14, s[16:19], s35 offen lds\n"
"ds_read_b128 a[136:139], v1 offset:34816\n"
"v_mfma_f32_16x16x32_bf16 v[148:151], v[20:23], a[100:103], v[148:151]\n"
"buffer_load_dwordx4 v15, s[16:19], s35 offen offset:1024 lds\n"
"ds_read_b128 a[140:143], v1 offset:35840\n"
"v_mfma_f32_16x16x32_bf16 v[152:155], v[20:23], a[104:107], v[152:155]\n"
"ds_read_b128 a[128:131], v1 offset:32768\n"
"v_cndmask_b32 v4, 0, v5, vcc\n"
"v_mfma_f32_16x16x32_bf16 v[156:159], v[20:23], a[108:111], v[156:159]\n"
"ds_read_b128 a[132:135], v1 offset:33792\n"
"s_waitcnt lgkmcnt(4)\n"
"v_mfma_f32_16x16x32_bf16 v[128:131], v[24:27], a[112:115], v[128:131]\n"
"s_waitcnt vmcnt(14)\n"
"ds_read_b64_tr_b16 v[16:17], v2 offset:49152\n"
"v_mfma_f32_16x16x32_bf16 v[132:135], v[24:27], a[116:119], v[132:135]\n"
"ds_read_b64_tr_b16 v[18:19], v2 offset:50176\n"
"s_cmp_eq_u32 s11, 0\n"
"s_cselect_b32 s13, -1, 0\n"
"v_mfma_f32_16x16x32_bf16 v[136:139], v[24:27], a[120:123], v[136:139]\n"
"ds_read_b64_tr_b16 v[20:21], v3 offset:49152\n"
"s_add_u32 m0, s79, 0x18000\n"
"v_mfma_f32_16x16x32_bf16 v[140:143], v[24:27], a[124:127], v[140:143]\n"
"ds_read_b64_tr_b16 v[22:23], v3 offset:50176\n"
"s_add_u32 s35, s74, 0x100\n"
"s_and_b32 s35, s35, 0x7ffff\n"
"v_mfma_f32_16x16x32_bf16 v[144:147], v[28:31], a[112:115], v[144:147]\n"
"buffer_load_dwordx4 v14, s[16:19], s35 offen lds\n"
"ds_read_b128 a[152:155], v1 offset:38912\n"
"v_mfma_f32_16x16x32_bf16 v[148:151], v[28:31], a[116:119], v[148:151]\n"
"buffer_load_dwordx4 v15, s[16:19], s35 offen offset:1024 lds\n"
"ds_read_b128 a[156:159], v1 offset:39936\n"
"v_mfma_f32_16x16x32_bf16 v[152:155], v[28:31], a[120:123], v[152:155]\n"
"ds_read_b128 a[144:147], v1 offset:36864\n"
"v_and_or_b32 v108, v4, s13, v9\n"
"v_mfma_f32_16x16x32_bf16 v[156:159], v[28:31], a[124:127], v[156:159]\n"
"ds_read_b128 a[148:151], v1 offset:37888\n"
"s_waitcnt lgkmcnt(4)\n"
"v_mfma_f32_16x16x32_bf16 v[128:131], v[16:19], a[128:131], v[128:131]\n"
"s_waitcnt vmcnt(14)\n"
"ds_read_b64_tr_b16 v[24:25], v2 offset:57344\n"
"v_mfma_f32_16x16x32_bf16 v[132:135], v[16:19], a[132:135], v[132:135]\n"
"ds_read_b64_tr_b16 v[26:27], v2 offset:58368\n"
"s_cmp_eq_u32 s11, 1\n"
"s_cselect_b32 s13, -1, 0\n"
"v_mfma_f32_16x16x32_bf16 v[136:139], v[16:19], a[136:139], v[136:139]\n"
"ds_read_b64_tr_b16 v[28:29], v3 offset:57344\n"
"s_add_u32 m0, s79, 0x1a000\n"
"v_mfma_f32_16x16x32_bf16 v[140:143], v[16:19], a[140:143], v[140:143]\n"
"ds_read_b64_tr_b16 v[30:31], v3 offset:58368\n"
"s_add_u32 s35, s74, 0x8100\n"
"s_and_b32 s35, s35, 0x7ffff\n"
"v_mfma_f32_16x16x32_bf16 v[144:147], v[20:23], a[128:131], v[144:147]\n"
"buffer_load_dwordx4 v14, s[16:19], s35 offen lds\n"
"ds_read_b128 a[168:171], v1 offset:43008\n"
"v_mfma_f32_16x16x32_bf16 v[148:151], v[20:23], a[132:135], v[148:151]\n"
"buffer_load_dwordx4 v15, s[16:19], s35 offen offset:1024 lds\n"
"ds_read_b128 a[172:175], v1 offset:44032\n"
"v_mfma_f32_16x16x32_bf16 v[152:155], v[20:23], a[136:139], v[152:155]\n"
"ds_read_b128 a[160:163], v1 offset:40960\n"
"v_and_or_b32 v109, v4, s13, v9\n"
"v_mfma_f32_16x16x32_bf16 v[156:159], v[20:23], a[140:143], v[156:159]\n"
"ds_read_b128 a[164:167], v1 offset:41984\n"
"s_waitcnt lgkmcnt(4)\n"
"v_mfma_f32_16x16x32_bf16 v[128:131], v[24:27], a[144:147], v[128:131]\n"
"s_waitcnt vmcnt(14)\n"
"ds_read_b64_tr_b16 v[16:17], v2\n"
"v_mfma_f32_16x16x32_bf16 v[132:135], v[24:27], a[148:151], v[132:135]\n"
"ds_read_b64_tr_b16 v[18:19], v2 offset:1024\n"
"s_cmp_eq_u32 s11, 2\n"
"s_cselect_b32 s13, -1, 0\n"
"v_mfma_f32_16x16x32_bf16 v[136:139], v[24:27], a[152:155], v[136:139]\n"
"ds_read_b64_tr_b16 v[20:21], v3\n"
"s_add_u32 m0, s79, 0x1c000\n"
"v_mfma_f32_16x16x32_bf16 v[140:143], v[24:27], a[156:159], v[140:143]\n"
"ds_read_b64_tr_b16 v[22:23], v3 offset:1024\n"
"s_add_u32 s35, s74, 0x10100\n"
"s_and_b32 s35, s35, 0x7ffff\n"
"v_mfma_f32_16x16x32_bf16 v[144:147], v[28:31], a[144:147], v[144:147]\n"
"buffer_load_dwordx4 v14, s[16:19], s35 offen lds\n"
"ds_read_b128 a[184:187], v1 offset:47104\n"
"v_mfma_f32_16x16x32_bf16 v[148:151], v[28:31], a[148:151], v[148:151]\n"
"buffer_load_dwordx4 v15, s[16:19], s35 offen offset:1024 lds\n"
"ds_read_b128 a[188:191], v1 offset:48128\n"
"v_mfma_f32_16x16x32_bf16 v[152:155], v[28:31], a[152:155], v[152:155]\n"
"ds_read_b128 a[176:179], v1 offset:45056\n"
"v_and_or_b32 v110, v4, s13, v9\n"
"v_mfma_f32_16x16x32_bf16 v[156:159], v[28:31], a[156:159], v[156:159]\n"
"ds_read_b128 a[180:183], v1 offset:46080\n"
"s_waitcnt lgkmcnt(4)\n"
"v_mfma_f32_16x16x32_bf16 v[128:131], v[16:19], a[160:163], v[128:131]\n"
"s_waitcnt vmcnt(14)\n"
"ds_read_b64_tr_b16 v[24:25], v2 offset:8192\n"
"v_mfma_f32_16x16x32_bf16 v[132:135], v[16:19], a[164:167], v[132:135]\n"
"ds_read_b64_tr_b16 v[26:27], v2 offset:9216\n"
"s_cmp_eq_u32 s11, 3\n"
"s_cselect_b32 s13, -1, 0\n"
"v_mfma_f32_16x16x32_bf16 v[136:139], v[16:19], a[168:171], v[136:139]\n"
"ds_read_b64_tr_b16 v[28:29], v3 offset:8192\n"
"s_add_u32 m0, s79, 0x1e000\n"
"v_mfma_f32_16x16x32_bf16 v[140:143], v[16:19], a[172:175], v[140:143]\n"
"ds_read_b64_tr_b16 v[30:31], v3 offset:9216\n"
"s_add_u32 s35, s74, 0x18100\n"
"s_and_b32 s35, s35, 0x7ffff\n"
"v_mfma_f32_16x16x32_bf16 v[144:147], v[20:23], a[160:163], v[144:147]\n"
"buffer_load_dwordx4 v14, s[16:19], s35 offen lds\n"
"ds_read_b128 a[200:203], v1 offset:51200\n"
"v_mfma_f32_16x16x32_bf16 v[148:151], v[20:23], a[164:167], v[148:151]\n"
"buffer_load_dwordx4 v15, s[16:19], s35 offen offset:1024 lds\n"
"ds_read_b128 a[204:207], v1 offset:52224\n"
"v_mfma_f32_16x16x32_bf16 v[152:155], v[20:23], a[168:171], v[152:155]\n"
"ds_read_b128 a[192:195], v1 offset:49152\n"
"v_and_or_b32 v111, v4, s13, v9\n"
"v_mfma_f32_16x16x32_bf16 v[156:159], v[20:23], a[172:175], v[156:159]\n"
"ds_read_b128 a[196:199], v1 offset:50176\n"
"s_waitcnt lgkmcnt(4)\n"
"v_mfma_f32_16x16x32_bf16 v[128:131], v[24:27], a[176:179], v[128:131]\n"
"s_waitcnt vmcnt(14)\n"
"ds_read_b64_tr_b16 v[16:17], v2 offset:16384\n"
"v_mfma_f32_16x16x32_bf16 v[132:135], v[24:27], a[180:183], v[132:135]\n"
"ds_read_b64_tr_b16 v[18:19], v2 offset:17408\n"
"v_lshrrev_b32 v4, 5, v6\n"
"v_mfma_f32_16x16x32_bf16 v[136:139], v[24:27], a[184:187], v[136:139]\n"
"ds_read_b64_tr_b16 v[20:21], v3 offset:16384\n"
"s_add_u32 m0, s79, 0x10000\n"
"v_mfma_f32_16x16x32_bf16 v[140:143], v[24:27], a[188:191], v[140:143]\n"
"ds_read_b64_tr_b16 v[22:23], v3 offset:17408\n"
"s_add_u32 s35, s74, 0x20100\n"
"s_and_b32 s35, s35, 0x7ffff\n"
"v_mfma_f32_16x16x32_bf16 v[144:147], v[28:31], a[176:179], v[144:147]\n"
"buffer_load_dwordx4 v14, s[16:19], s35 offen lds\n"
"ds_read_b128 a[216:219], v1 offset:55296\n"
"v_mfma_f32_16x16x32_bf16 v[148:151], v[28:31], a[180:183], v[148:151]\n"
"buffer_load_dwordx4 v15, s[16:19], s35 offen offset:1024 lds\n"
"ds_read_b128 a[220:223], v1 offset:56320\n"
"v_mfma_f32_16x16x32_bf16 v[152:155], v[28:31], a[184:187], v[152:155]\n"
"ds_read_b128 a[208:211], v1 offset:53248\n"
"v_cmp_eq_u32 vcc, s12, v4\n"
"v_mfma_f32_16x16x32_bf16 v[156:159], v[28:31], a[188:191], v[156:159]\n"
"ds_read_b128 a[212:215], v1 offset:54272\n"
"s_waitcnt lgkmcnt(4)\n"
"v_mfma_f32_16x16x32_bf16 v[128:131], v[16:19], a[192:195], v[128:131]\n"
"s_waitcnt vmcnt(14)\n"
"ds_read_b64_tr_b16 v[24:25], v2 offset:24576\n"
"v_mfma_f32_16x16x32_bf16 v[132:135], v[16:19], a[196:199], v[132:135]\n"
"ds_read_b64_tr_b16 v[26:27], v2 offset:25600\n"
"v_mov_b32 v98, 0x3f80\n"
"v_mfma_f32_16x16x32_bf16 v[136:139], v[16:19], a[200:203], v[136:139]\n"
"ds_read_b64_tr_b16 v[28:29], v3 offset:24576\n"
"s_add_u32 m0, s79, 0x12000\n"
"v_mfma_f32_16x16x32_bf16 v[140:143], v[16:19], a[204:207], v[140:143]\n"
"ds_read_b64_tr_b16 v[30:31], v3 offset:25600\n"
"s_add_u32 s35, s74, 0x28100\n"
"s_and_b32 s35, s35, 0x7ffff\n"
"v_mfma_f32_16x16x32_bf16 v[144:147], v[20:23], a[192:195], v[144:147]\n"
"buffer_load_dwordx4 v14, s[16:19], s35 offen lds\n"
"ds_read_b128 a[232:235], v1 offset:59392\n"
"v_mfma_f32_16x16x32_bf16 v[148:151], v[20:23], a[196:199], v[148:151]\n"
"buffer_load_dwordx4 v15, s[16:19], s35 offen offset:1024 lds\n"
"ds_read_b128 a[236:239], v1 offset:60416\n"
"v_mfma_f32_16x16x32_bf16 v[152:155], v[20:23], a[200:203], v[152:155]\n"
"ds_read_b128 a[224:227], v1 offset:57344\n"
"v_cndmask_b32 v98, 0, v98, vcc\n"
"v_mfma_f32_16x16x32_bf16 v[156:159], v[20:23], a[204:207], v[156:159]\n"
"ds_read_b128 a[228:231], v1 offset:58368\n"
"s_waitcnt lgkmcnt(4)\n"
"v_mfma_f32_16x16x32_bf16 v[128:131], v[24:27], a[208:211], v[128:131]\n"
"s_waitcnt vmcnt(14)\n"
"ds_read_b64_tr_b16 v[16:17], v12\n"
"v_mfma_f32_16x16x32_bf16 v[132:135], v[24:27], a[212:215], v[132:135]\n"
"ds_read_b64_tr_b16 v[18:19], v12 offset:1024\n"
"v_cndmask_b32 v4, 0, v5, vcc\n"
"v_mfma_f32_16x16x32_bf16 v[136:139], v[24:27], a[216:219], v[136:139]\n"
"ds_read_b64_tr_b16 v[20:21], v13\n"
"s_add_u32 m0, s79, 0x14000\n"
"v_mfma_f32_16x16x32_bf16 v[140:143], v[24:27], a[220:223], v[140:143]\n"
"ds_read_b64_tr_b16 v[22:23], v13 offset:1024\n"
"s_add_u32 s35, s74, 0x30100\n"
"s_and_b32 s35, s35, 0x7ffff\n"
"v_mfma_f32_16x16x32_bf16 v[144:147], v[28:31], a[208:211], v[144:147]\n"
"buffer_load_dwordx4 v14, s[16:19], s35 offen lds\n"
"ds_read_b128 a[248:251], v1 offset:63488\n"
"v_mfma_f32_16x16x32_bf16 v[148:151], v[28:31], a[212:215], v[148:151]\n"
"buffer_load_dwordx4 v15, s[16:19], s35 offen offset:1024 lds\n"
"ds_read_b128 a[252:255], v1 offset:64512\n"
"v_mfma_f32_16x16x32_bf16 v[152:155], v[28:31], a[216:219], v[152:155]\n"
"ds_read_b128 a[240:243], v1 offset:61440\n"
"s_cmp_eq_u32 s11, 0\n"
"s_cselect_b32 s13, -1, 0\n"
"v_mfma_f32_16x16x32_bf16 v[156:159], v[28:31], a[220:223], v[156:159]\n"
"ds_read_b128 a[244:247], v1 offset:62464\n"
"s_waitcnt lgkmcnt(4)\n"
"v_mfma_f32_16x16x32_bf16 v[128:131], v[16:19], a[224:227], v[128:131]\n"
"s_waitcnt vmcnt(14)\n"
"ds_read_b64_tr_b16 v[24:25], v12 offset:8192\n"
"v_mfma_f32_16x16x32_bf16 v[132:135], v[16:19], a[228:231], v[132:135]\n"
"ds_read_b64_tr_b16 v[26:27], v12 offset:9216\n"
"v_and_or_b32 v124, v4, s13, v10\n"
"v_mfma_f32_16x16x32_bf16 v[136:139], v[16:19], a[232:235], v[136:139]\n"
"ds_read_b64_tr_b16 v[28:29], v13 offset:8192\n"
"s_add_u32 m0, s79, 0x16000\n"
"v_mfma_f32_16x16x32_bf16 v[140:143], v[16:19], a[236:239], v[140:143]\n"
"ds_read_b64_tr_b16 v[30:31], v13 offset:9216\n"
"s_add_u32 s35, s74, 0x38100\n"
"s_and_b32 s35, s35, 0x7ffff\n"
"v_mfma_f32_16x16x32_bf16 v[144:147], v[20:23], a[224:227], v[144:147]\n"
"buffer_load_dwordx4 v14, s[16:19], s35 offen lds\n"
"s_cmp_eq_u32 s11, 1\n"
"s_cselect_b32 s13, -1, 0\n"
"v_mfma_f32_16x16x32_bf16 v[148:151], v[20:23], a[228:231], v[148:151]\n"
"buffer_load_dwordx4 v15, s[16:19], s35 offen offset:1024 lds\n"
"v_and_or_b32 v125, v4, s13, v10\n"
"v_mfma_f32_16x16x32_bf16 v[152:155], v[20:23], a[232:235], v[152:155]\n"
"s_cmp_eq_u32 s11, 2\n"
"s_cselect_b32 s13, -1, 0\n"
"v_mfma_f32_16x16x32_bf16 v[156:159], v[20:23], a[236:239], v[156:159]\n"
"v_and_or_b32 v126, v4, s13, v10\n"
"s_waitcnt lgkmcnt(0)\n"
"v_mfma_f32_16x16x32_bf16 v[128:131], v[24:27], a[240:243], v[128:131]\n"
"s_waitcnt vmcnt(14)\n"
"ds_read_b64_tr_b16 v[16:17], v2 offset:32768\n"
"v_mfma_f32_16x16x32_bf16 v[132:135], v[24:27], a[244:247], v[132:135]\n"
"ds_read_b64_tr_b16 v[18:19], v2 offset:33792\n"
"s_cmp_eq_u32 s11, 3\n"
"s_cselect_b32 s13, -1, 0\n"
"v_mfma_f32_16x16x32_bf16 v[136:139], v[24:27], a[248:251], v[136:139]\n"
"ds_read_b64_tr_b16 v[20:21], v3 offset:32768\n"
"s_add_u32 m0, s79, 0x20000\n"
"v_mfma_f32_16x16x32_bf16 v[140:143], v[24:27], a[252:255], v[140:143]\n"
"ds_read_b64_tr_b16 v[22:23], v3 offset:33792\n"
"s_add_u32 s35, s74, 0x40100\n"
"s_and_b32 s35, s35, 0x7ffff\n"
"v_mfma_f32_16x16x32_bf16 v[144:147], v[28:31], a[240:243], v[144:147]\n"
"buffer_load_dwordx4 v14, s[16:19], s35 offen lds\n"
"v_and_or_b32 v127, v4, s13, v10\n"
"v_mfma_f32_16x16x32_bf16 v[148:151], v[28:31], a[244:247], v[148:151]\n"
"buffer_load_dwordx4 v15, s[16:19], s35 offen offset:1024 lds\n"
"v_lshlrev_b32 v99, 16, v98\n"
"v_mfma_f32_16x16x32_bf16 v[152:155], v[28:31], a[248:251], v[152:155]\n"
"v_mfma_f32_16x16x32_bf16 v[156:159], v[28:31], a[252:255], v[156:159]\n"
"; o pass 1\n"
"s_waitcnt lgkmcnt(0)\n"
"v_mfma_f32_16x16x32_bf16 v[160:163], v[16:19], a[0:3], 0\n"
"s_waitcnt vmcnt(14)\n"
"ds_read_b64_tr_b16 v[24:25], v2 offset:40960\n"
"v_mfma_f32_16x16x32_bf16 v[164:167], v[16:19], a[4:7], 0\n"
"ds_read_b64_tr_b16 v[26:27], v2 offset:41984\n"
"v_mfma_f32_16x16x32_bf16 v[168:171], v[16:19], a[8:11], 0\n"
"ds_read_b64_tr_b16 v[28:29], v3 offset:40960\n"
"s_add_u32 m0, s79, 0x22000\n"
"v_mfma_f32_16x16x32_bf16 v[172:175], v[16:19], a[12:15], 0\n"
"ds_read_b64_tr_b16 v[30:31], v3 offset:41984\n"
"s_add_u32 s35, s74, 0x48100\n"
"s_and_b32 s35, s35, 0x7ffff\n"
"v_mfma_f32_16x16x32_bf16 v[176:179], v[20:23], a[0:3], 0\n"
"buffer_load_dwordx4 v14, s[16:19], s35 offen lds\n"
"v_mfma_f32_16x16x32_bf16 v[180:183], v[20:23], a[4:7], 0\n"
"buffer_load_dwordx4 v15, s[16:19], s35 offen offset:1024 lds\n"
"v_mfma_f32_16x16x32_bf16 v[184:187], v[20:23], a[8:11], 0\n"
"v_mfma_f32_16x16x32_bf16 v[188:191], v[20:23], a[12:15], 0\n"
"s_waitcnt lgkmcnt(0)\n"
"v_mfma_f32_16x16x32_bf16 v[160:163], v[24:27], a[16:19], v[160:163]\n"
"s_waitcnt vmcnt(14)\n"
"ds_read_b64_tr_b16 v[16:17], v2 offset:49152\n"
"v_mfma_f32_16x16x32_bf16 v[164:167], v[24:27], a[20:23], v[164:167]\n"
"ds_read_b64_tr_b16 v[18:19], v2 offset:50176\n"
"v_cvt_pk_bf16_f32 v128, v128, v129\n"
"v_mfma_f32_16x16x32_bf16 v[168:171], v[24:27], a[24:27], v[168:171]\n"
"ds_read_b64_tr_b16 v[20:21], v3 offset:49152\n"
"s_add_u32 m0, s79, 0x18000\n"
"v_mfma_f32_16x16x32_bf16 v[172:175], v[24:27], a[28:31], v[172:175]\n"
"ds_read_b64_tr_b16 v[22:23], v3 offset:50176\n"
"s_add_u32 s35, s74, 0x50100\n"
"s_and_b32 s35, s35, 0x7ffff\n"
"v_mfma_f32_16x16x32_bf16 v[176:179], v[28:31], a[16:19], v[176:179]\n"
"buffer_load_dwordx4 v14, s[16:19], s35 offen lds\n"
"v_cvt_pk_bf16_f32 v129, v130, v131\n"
"v_mfma_f32_16x16x32_bf16 v[180:183], v[28:31], a[20:23], v[180:183]\n"
"buffer_load_dwordx4 v15, s[16:19], s35 offen offset:1024 lds\n"
"v_cvt_pk_bf16_f32 v130, v144, v145\n"
"v_mfma_f32_16x16x32_bf16 v[184:187], v[28:31], a[24:27], v[184:187]\n"
"v_cvt_pk_bf16_f32 v131, v146, v147\n"
"v_cvt_pk_bf16_f32 v132, v132, v133\n"
"v_mfma_f32_16x16x32_bf16 v[188:191], v[28:31], a[28:31], v[188:191]\n"
"v_cvt_pk_bf16_f32 v133, v134, v135\n"
"s_waitcnt lgkmcnt(0)\n"
"v_mfma_f32_16x16x32_bf16 v[160:163], v[16:19], a[32:35], v[160:163]\n"
"s_waitcnt vmcnt(14)\n"
"ds_read_b64_tr_b16 v[24:25], v2 offset:57344\n"
"v_mfma_f32_16x16x32_bf16 v[164:167], v[16:19], a[36:39], v[164:167]\n"
"ds_read_b64_tr_b16 v[26:27], v2 offset:58368\n"
"v_cvt_pk_bf16_f32 v134, v148, v149\n"
"v_mfma_f32_16x16x32_bf16 v[168:171], v[16:19], a[40:43], v[168:171]\n"
"ds_read_b64_tr_b16 v[28:29], v3 offset:57344\n"
"s_add_u32 m0, s79, 0x1a000\n"
"v_mfma_f32_16x16x32_bf16 v[172:175], v[16:19], a[44:47], v[172:175]\n"
"ds_read_b64_tr_b16 v[30:31], v3 offset:58368\n"
"s_add_u32 s35, s74, 0x58100\n"
"s_and_b32 s35, s35, 0x7ffff\n"
"v_mfma_f32_16x16x32_bf16 v[176:179], v[20:23], a[32:35], v[176:179]\n"
"buffer_load_dwordx4 v14, s[16:19], s35 offen lds\n"
"v_cvt_pk_bf16_f32 v135, v150, v151\n"
"v_mfma_f32_16x16x32_bf16 v[180:183], v[20:23], a[36:39], v[180:183]\n"
"buffer_load_dwordx4 v15, s[16:19], s35 offen offset:1024 lds\n"
"v_cvt_pk_bf16_f32 v136, v136, v137\n"
"v_mfma_f32_16x16x32_bf16 v[184:187], v[20:23], a[40:43], v[184:187]\n"
"v_cvt_pk_bf16_f32 v137, v138, v139\n"
"v_cvt_pk_bf16_f32 v138, v152, v153\n"
"v_mfma_f32_16x16x32_bf16 v[188:191], v[20:23], a[44:47], v[188:191]\n"
"v_cvt_pk_bf16_f32 v139, v154, v155\n"
"s_waitcnt lgkmcnt(0)\n"
"v_mfma_f32_16x16x32_bf16 v[160:163], v[24:27], a[48:51], v[160:163]\n"
"s_waitcnt vmcnt(14)\n"
"ds_read_b64_tr_b16 v[16:17], v2\n"
"v_mfma_f32_16x16x32_bf16 v[164:167], v[24:27], a[52:55], v[164:167]\n"
"ds_read_b64_tr_b16 v[18:19], v2 offset:1024\n"
"v_cvt_pk_bf16_f32 v140, v140, v141\n"
"v_mfma_f32_16x16x32_bf16 v[168:171], v[24:27], a[56:59], v[168:171]\n"
"ds_read_b64_tr_b16 v[20:21], v3\n"
"s_add_u32 m0, s79, 0x1c000\n"
"v_mfma_f32_16x16x32_bf16 v[172:175], v[24:27], a[60:63], v[172:175]\n"
"ds_read_b64_tr_b16 v[22:23], v3 offset:1024\n"
"s_add_u32 s35, s74, 0x60100\n"
"s_and_b32 s35, s35, 0x7ffff\n"
"v_mfma_f32_16x16x32_bf16 v[176:179], v[28:31], a[48:51], v[176:179]\n"
"buffer_load_dwordx4 v14, s[16:19], s35 offen lds\n"
"v_cvt_pk_bf16_f32 v141, v142, v143\n"
"v_mfma_f32_16x16x32_bf16 v[180:183], v[28:31], a[52:55], v[180:183]\n"
"buffer_load_dwordx4 v15, s[16:19], s35 offen offset:1024 lds\n"
"v_cvt_pk_bf16_f32 v142, v156, v157\n"
"v_mfma_f32_16x16x32_bf16 v[184:187], v[28:31], a[56:59], v[184:187]\n"
"v_cvt_pk_bf16_f32 v143, v158, v159\n"
"v_cmp_gt_u32 vcc, 8, v6\n"
"v_mfma_f32_16x16x32_bf16 v[188:191], v[28:31], a[60:63], v[188:191]\n"
"v_mov_b32 v5, 0x40000000\n"
"s_waitcnt lgkmcnt(0)\n"
"v_mfma_f32_16x16x32_bf16 v[160:163], v[16:19], a[64:67], v[160:163]\n"
"s_waitcnt vmcnt(14)\n"
"ds_read_b64_tr_b16 v[24:25], v2 offset:8192\n"
"v_mfma_f32_16x16x32_bf16 v[164:167], v[16:19], a[68:71], v[164:167]\n"
"ds_read_b64_tr_b16 v[26:27], v2 offset:9216\n"
"v_cndmask_b32 v96, v5, v1, vcc\n"
"v_mfma_f32_16x16x32_bf16 v[168:171], v[16:19], a[72:75], v[168:171]\n"
"ds_read_b64_tr_b16 v[28:29], v3 offset:8192\n"
"s_add_u32 m0, s79, 0x1e000\n"
"v_mfma_f32_16x16x32_bf16 v[172:175], v[16:19], a[76:79], v[172:175]\n"
"ds_read_b64_tr_b16 v[30:31], v3 offset:9216\n"
"s_add_u32 s35, s74, 0x68100\n"
"s_and_b32 s35, s35, 0x7ffff\n"
"v_mfma_f32_16x16x32_bf16 v[176:179], v[20:23], a[64:67], v[176:179]\n"
"buffer_load_dwordx4 v14, s[16:19], s35 offen lds\n"
"v_and_b32 v4, 7, v6\n"
"v_mfma_f32_16x16x32_bf16 v[180:183], v[20:23], a[68:71], v[180:183]\n"
"buffer_load_dwordx4 v15, s[16:19], s35 offen offset:1024 lds\n"
"v_lshlrev_b32 v97, 11, v4\n"
"v_mfma_f32_16x16x32_bf16 v[184:187], v[20:23], a[72:75], v[184:187]\n"
"v_mov_b32 v104, s10\n"
"v_mov_b32 v105, s10\n"
"v_mfma_f32_16x16x32_bf16 v[188:191], v[20:23], a[76:79], v[188:191]\n"
"v_mov_b32 v106, s10\n"
"s_waitcnt lgkmcnt(0)\n"
"v_mfma_f32_16x16x32_bf16 v[160:163], v[24:27], a[80:83], v[160:163]\n"
"s_waitcnt vmcnt(14)\n"
"ds_read_b64_tr_b16 v[16:17], v2 offset:16384\n"
"v_mfma_f32_16x16x32_bf16 v[164:167], v[24:27], a[84:87], v[164:167]\n"
"ds_read_b64_tr_b16 v[18:19], v2 offset:17408\n"
"v_mov_b32 v107, s10\n"
"v_mfma_f32_16x16x32_bf16 v[168:171], v[24:27], a[88:91], v[168:171]\n"
"ds_read_b64_tr_b16 v[20:21], v3 offset:16384\n"
"s_add_u32 m0, s79, 0x10000\n"
"v_mfma_f32_16x16x32_bf16 v[172:175], v[24:27], a[92:95], v[172:175]\n"
"ds_read_b64_tr_b16 v[22:23], v3 offset:17408\n"
"s_add_u32 s35, s74, 0x70100\n"
"s_and_b32 s35, s35, 0x7ffff\n"
"v_mfma_f32_16x16x32_bf16 v[176:179], v[28:31], a[80:83], v[176:179]\n"
"buffer_load_dwordx4 v14, s[16:19], s35 offen lds\n"
"s_lshl_b32 s69, s9, 14\n"
"v_mfma_f32_16x16x32_bf16 v[180:183], v[28:31], a[84:87], v[180:183]\n"
"buffer_load_dwordx4 v15, s[16:19], s35 offen offset:1024 lds\n"
"s_lshl_b32 s15, s8, 11\n"
"v_mfma_f32_16x16x32_bf16 v[184:187], v[28:31], a[88:91], v[184:187]\n"
"s_add_u32 s69, s69, s15\n"
"s_lshl_b32 s15, s7, 9\n"
"v_mfma_f32_16x16x32_bf16 v[188:191], v[28:31], a[92:95], v[188:191]\n"
"s_add_u32 s69, s69, s15\n"
"s_waitcnt lgkmcnt(0)\n"
"v_mfma_f32_16x16x32_bf16 v[160:163], v[16:19], a[96:99], v[160:163]\n"
"s_waitcnt vmcnt(14)\n"
"ds_read_b64_tr_b16 v[24:25], v2 offset:24576\n"
"v_mfma_f32_16x16x32_bf16 v[164:167], v[16:19], a[100:103], v[164:167]\n"
"ds_read_b64_tr_b16 v[26:27], v2 offset:25600\n"
"s_add_u32 s69, s69, 0x2900000\n"
"v_mfma_f32_16x16x32_bf16 v[168:171], v[16:19], a[104:107], v[168:171]\n"
"ds_read_b64_tr_b16 v[28:29], v3 offset:24576\n"
"s_add_u32 m0, s79, 0x12000\n"
"v_mfma_f32_16x16x32_bf16 v[172:175], v[16:19], a[108:111], v[172:175]\n"
"ds_read_b64_tr_b16 v[30:31], v3 offset:25600\n"
"s_add_u32 s35, s74, 0x78100\n"
"s_and_b32 s35, s35, 0x7ffff\n"
"v_mfma_f32_16x16x32_bf16 v[176:179], v[20:23], a[96:99], v[176:179]\n"
"buffer_load_dwordx4 v14, s[16:19], s35 offen lds\n"
"s_lshl_b32 s78, s9, 14\n"
"v_mfma_f32_16x16x32_bf16 v[180:183], v[20:23], a[100:103], v[180:183]\n"
"buffer_load_dwordx4 v15, s[16:19], s35 offen offset:1024 lds\n"
"s_lshl_b32 s15, s7, 9\n"
"v_mfma_f32_16x16x32_bf16 v[184:187], v[20:23], a[104:107], v[184:187]\n"
"s_add_u32 s78, s78, s15\n"
"s_add_u32 s78, s78, 0x2900000\n"
"v_mfma_f32_16x16x32_bf16 v[188:191], v[20:23], a[108:111], v[188:191]\n"
"s_lshl_b32 s88, s9, 19\n"
"s_waitcnt lgkmcnt(0)\n"
"v_mfma_f32_16x16x32_bf16 v[160:163], v[24:27], a[112:115], v[160:163]\n"
"s_waitcnt vmcnt(14)\n"
"ds_read_b64_tr_b16 v[16:17], v12\n"
"v_mfma_f32_16x16x32_bf16 v[164:167], v[24:27], a[116:119], v[164:167]\n"
"ds_read_b64_tr_b16 v[18:19], v12 offset:1024\n"
"s_lshl_b32 s15, s8, 16\n"
"v_mfma_f32_16x16x32_bf16 v[168:171], v[24:27], a[120:123], v[168:171]\n"
"ds_read_b64_tr_b16 v[20:21], v13\n"
"s_add_u32 m0, s79, 0x14000\n"
"v_mfma_f32_16x16x32_bf16 v[172:175], v[24:27], a[124:127], v[172:175]\n"
"ds_read_b64_tr_b16 v[22:23], v13 offset:1024\n"
"s_add_u32 s35, s74, 0x200\n"
"s_and_b32 s35, s35, 0x7ffff\n"
"v_mfma_f32_16x16x32_bf16 v[176:179], v[28:31], a[112:115], v[176:179]\n"
"buffer_load_dwordx4 v14, s[16:19], s35 offen lds\n"
"s_add_u32 s88, s88, s15\n"
"v_mfma_f32_16x16x32_bf16 v[180:183], v[28:31], a[116:119], v[180:183]\n"
"buffer_load_dwordx4 v15, s[16:19], s35 offen offset:1024 lds\n"
"s_lshl_b32 s15, s7, 9\n"
"v_mfma_f32_16x16x32_bf16 v[184:187], v[28:31], a[120:123], v[184:187]\n"
"s_add_u32 s88, s88, s15\n"
"s_add_u32 s88, s88, 0x100000\n"
"v_mfma_f32_16x16x32_bf16 v[188:191], v[28:31], a[124:127], v[188:191]\n"
"s_mov_b32 s14, 0x10000\n"
"s_waitcnt lgkmcnt(0)\n"
"v_mfma_f32_16x16x32_bf16 v[160:163], v[16:19], a[128:131], v[160:163]\n"
"s_waitcnt vmcnt(14)\n"
"ds_read_b64_tr_b16 v[24:25], v12 offset:8192\n"
"v_mfma_f32_16x16x32_bf16 v[164:167], v[16:19], a[132:135], v[164:167]\n"
"ds_read_b64_tr_b16 v[26:27], v12 offset:9216\n"
"s_mov_b32 s13, 0x10000\n"
"v_mfma_f32_16x16x32_bf16 v[168:171], v[16:19], a[136:139], v[168:171]\n"
"ds_read_b64_tr_b16 v[28:29], v13 offset:8192\n"
"s_add_u32 m0, s79, 0x16000\n"
"v_mfma_f32_16x16x32_bf16 v[172:175], v[16:19], a[140:143], v[172:175]\n"
"ds_read_b64_tr_b16 v[30:31], v13 offset:9216\n"
"s_add_u32 s35, s74, 0x8200\n"
"s_and_b32 s35, s35, 0x7ffff\n"
"v_mfma_f32_16x16x32_bf16 v[176:179], v[20:23], a[128:131], v[176:179]\n"
"buffer_load_dwordx4 v14, s[16:19], s35 offen lds\n"
"v_xor_b32 v4, s10, v33\n"
"v_mfma_f32_16x16x32_bf16 v[180:183], v[20:23], a[132:135], v[180:183]\n"
"buffer_load_dwordx4 v15, s[16:19], s35 offen offset:1024 lds\n"
"v_bitop3_b32 v4, v35, s10, v4 bitop3:0xbe\n"
"v_mfma_f32_16x16x32_bf16 v[184:187], v[20:23], a[136:139], v[184:187]\n"
"v_bitop3_b32 v4, v37, s10, v4 bitop3:0xbe\n"
"v_bitop3_b32 v4, v39, s10, v4 bitop3:0xbe\n"
"v_mfma_f32_16x16x32_bf16 v[188:191], v[20:23], a[140:143], v[188:191]\n"
"v_bitop3_b32 v4, v41, s10, v4 bitop3:0xbe\n"
"s_waitcnt lgkmcnt(0)\n"
"v_mfma_f32_16x16x32_bf16 v[160:163], v[24:27], a[144:147], v[160:163]\n"
"s_waitcnt vmcnt(14)\n"
"ds_read_b64_tr_b16 v[16:17], v2 offset:32768\n"
"v_mfma_f32_16x16x32_bf16 v[164:167], v[24:27], a[148:151], v[164:167]\n"
"ds_read_b64_tr_b16 v[18:19], v2 offset:33792\n"
"v_bitop3_b32 v4, v43, s10, v4 bitop3:0xbe\n"
"v_mfma_f32_16x16x32_bf16 v[168:171], v[24:27], a[152:155], v[168:171]\n"
"ds_read_b64_tr_b16 v[20:21], v3 offset:32768\n"
"s_add_u32 m0, s79, 0x20000\n"
"v_mfma_f32_16x16x32_bf16 v[172:175], v[24:27], a[156:159], v[172:175]\n"
"ds_read_b64_tr_b16 v[22:23], v3 offset:33792\n"
"s_add_u32 s35, s74, 0x10200\n"
"s_and_b32 s35, s35, 0x7ffff\n"
"v_mfma_f32_16x16x32_bf16 v[176:179], v[28:31], a[144:147], v[176:179]\n"
"buffer_load_dwordx4 v14, s[16:19], s35 offen lds\n"
"v_bitop3_b32 v4, v45, s10, v4 bitop3:0xbe\n"
"v_mfma_f32_16x16x32_bf16 v[180:183], v[28:31], a[148:151], v[180:183]\n"
"buffer_load_dwordx4 v15, s[16:19], s35 offen offset:1024 lds\n"
"s_add_u32 s76, s84, 0\n"
"v_mfma_f32_16x16x32_bf16 v[184:187], v[28:31], a[152:155], v[184:187]\n"
"buffer_store_dwordx4 v[128:131], v108, s[28:31], s76 offen sc1\n"
"v_bitop3_b32 v4, v47, s10, v4 bitop3:0xbe\n"
"v_mfma_f32_16x16x32_bf16 v[188:191], v[28:31], a[156:159], v[188:191]\n"
"v_cmp_ne_u32 vcc, 0, v4\n"
"s_waitcnt lgkmcnt(0)\n"
"v_mfma_f32_16x16x32_bf16 v[160:163], v[16:19], a[160:163], v[160:163]\n"
"s_waitcnt vmcnt(15)\n"
"ds_read_b64_tr_b16 v[24:25], v2 offset:40960\n"
"v_mfma_f32_16x16x32_bf16 v[164:167], v[16:19], a[164:167], v[164:167]\n"
"ds_read_b64_tr_b16 v[26:27], v2 offset:41984\n"
"s_cbranch_vccnz L_mgrb%=\n"
"L_mgdb%=:\n"
"v_mfma_f32_16x16x32_bf16 v[168:171], v[16:19], a[168:171], v[168:171]\n"
"ds_read_b64_tr_b16 v[28:29], v3 offset:40960\n"
"s_add_u32 m0, s79, 0x22000\n"
"v_mfma_f32_16x16x32_bf16 v[172:175], v[16:19], a[172:175], v[172:175]\n"
"ds_read_b64_tr_b16 v[30:31], v3 offset:41984\n"
"s_add_u32 s35, s74, 0x18200\n"
"s_and_b32 s35, s35, 0x7ffff\n"
"v_mfma_f32_16x16x32_bf16 v[176:179], v[20:23], a[160:163], v[176:179]\n"
"buffer_load_dwordx4 v14, s[16:19], s35 offen lds\n"
"v_lshrrev_b32 v4, 1, v11\n"
"v_mfma_f32_16x16x32_bf16 v[180:183], v[20:23], a[164:167], v[180:183]\n"
"buffer_load_dwordx4 v15, s[16:19], s35 offen offset:1024 lds\n"
"v_add_u32 v4, 0x24000, v4\n"
"v_mfma_f32_16x16x32_bf16 v[184:187], v[20:23], a[168:171], v[184:187]\n"
"s_mov_b32 s13, 0xffff0000\n"
"v_add_f32 v32, v32, v40\n"
"v_mfma_f32_16x16x32_bf16 v[188:191], v[20:23], a[172:175], v[188:191]\n"
"v_add_f32 v34, v34, v42\n"
"s_waitcnt lgkmcnt(0)\n"
"v_mfma_f32_16x16x32_bf16 v[160:163], v[24:27], a[176:179], v[160:163]\n"
"s_waitcnt vmcnt(15)\n"
"ds_read_b64_tr_b16 v[16:17], v2 offset:49152\n"
"v_mfma_f32_16x16x32_bf16 v[164:167], v[24:27], a[180:183], v[164:167]\n"
"ds_read_b64_tr_b16 v[18:19], v2 offset:50176\n"
"v_add_f32 v36, v36, v44\n"
"v_mfma_f32_16x16x32_bf16 v[168:171], v[24:27], a[184:187], v[168:171]\n"
"ds_read_b64_tr_b16 v[20:21], v3 offset:49152\n"
"s_add_u32 m0, s79, 0x18000\n"
"v_mfma_f32_16x16x32_bf16 v[172:175], v[24:27], a[188:191], v[172:175]\n"
"ds_read_b64_tr_b16 v[22:23], v3 offset:50176\n"
"s_add_u32 s35, s74, 0x20200\n"
"s_and_b32 s35, s35, 0x7ffff\n"
"v_mfma_f32_16x16x32_bf16 v[176:179], v[28:31], a[176:179], v[176:179]\n"
"buffer_load_dwordx4 v14, s[16:19], s35 offen lds\n"
"v_add_f32 v38, v38, v46\n"
"v_mfma_f32_16x16x32_bf16 v[180:183], v[28:31], a[180:183], v[180:183]\n"
"buffer_load_dwordx4 v15, s[16:19], s35 offen offset:1024 lds\n"
"v_cvt_pk_bf16_f32 v40, v32, v34\n"
"v_mfma_f32_16x16x32_bf16 v[184:187], v[28:31], a[184:187], v[184:187]\n"
"v_lshlrev_b32 v44, 16, v40\n"
"v_and_b32 v45, s13, v40\n"
"v_mfma_f32_16x16x32_bf16 v[188:191], v[28:31], a[188:191], v[188:191]\n"
"ds_write_b64 v4, v[44:45] offset:4096\n"
"s_waitcnt lgkmcnt(1)\n"
"v_mfma_f32_16x16x32_bf16 v[160:163], v[16:19], a[192:195], v[160:163]\n"
"s_waitcnt vmcnt(15)\n"
"ds_read_b64_tr_b16 v[24:25], v2 offset:57344\n"
"v_mfma_f32_16x16x32_bf16 v[164:167], v[16:19], a[196:199], v[164:167]\n"
"ds_read_b64_tr_b16 v[26:27], v2 offset:58368\n"
"v_cvt_pk_bf16_f32 v41, v36, v38\n"
"v_mfma_f32_16x16x32_bf16 v[168:171], v[16:19], a[200:203], v[168:171]\n"
"ds_read_b64_tr_b16 v[28:29], v3 offset:57344\n"
"s_add_u32 m0, s79, 0x1a000\n"
"v_mfma_f32_16x16x32_bf16 v[172:175], v[16:19], a[204:207], v[172:175]\n"
"ds_read_b64_tr_b16 v[30:31], v3 offset:58368\n"
"s_add_u32 s35, s74, 0x28200\n"
"s_and_b32 s35, s35, 0x7ffff\n"
"v_mfma_f32_16x16x32_bf16 v[176:179], v[20:23], a[192:195], v[176:179]\n"
"buffer_load_dwordx4 v14, s[16:19], s35 offen lds\n"
"v_lshlrev_b32 v46, 16, v41\n"
"v_mfma_f32_16x16x32_bf16 v[180:183], v[20:23], a[196:199], v[180:183]\n"
"buffer_load_dwordx4 v15, s[16:19], s35 offen offset:1024 lds\n"
"s_add_u32 s76, s84, 0x20000\n"
"v_mfma_f32_16x16x32_bf16 v[184:187], v[20:23], a[200:203], v[184:187]\n"
"buffer_store_dwordx4 v[132:135], v109, s[28:31], s76 offen sc1\n"
"v_and_b32 v47, s13, v41\n"
"v_mfma_f32_16x16x32_bf16 v[188:191], v[20:23], a[204:207], v[188:191]\n"
"ds_write_b64 v4, v[46:47] offset:8192\n"
"s_waitcnt lgkmcnt(1)\n"
"v_mfma_f32_16x16x32_bf16 v[160:163], v[24:27], a[208:211], v[160:163]\n"
"s_waitcnt vmcnt(16)\n"
"ds_read_b64_tr_b16 v[16:17], v2\n"
"v_mfma_f32_16x16x32_bf16 v[164:167], v[24:27], a[212:215], v[164:167]\n"
"ds_read_b64_tr_b16 v[18:19], v2 offset:1024\n"
"v_mfma_f32_16x16x32_bf16 v[168:171], v[24:27], a[216:219], v[168:171]\n"
"ds_read_b64_tr_b16 v[20:21], v3\n"
"s_add_u32 m0, s79, 0x1c000\n"
"v_mfma_f32_16x16x32_bf16 v[172:175], v[24:27], a[220:223], v[172:175]\n"
"ds_read_b64_tr_b16 v[22:23], v3 offset:1024\n"
"s_add_u32 s35, s74, 0x30200\n"
"s_and_b32 s35, s35, 0x7ffff\n"
"v_mfma_f32_16x16x32_bf16 v[176:179], v[28:31], a[208:211], v[176:179]\n"
"buffer_load_dwordx4 v14, s[16:19], s35 offen lds\n"
"v_mfma_f32_16x16x32_bf16 v[180:183], v[28:31], a[212:215], v[180:183]\n"
"buffer_load_dwordx4 v15, s[16:19], s35 offen offset:1024 lds\n"
"v_mfma_f32_16x16x32_bf16 v[184:187], v[28:31], a[216:219], v[184:187]\n"
"v_mfma_f32_16x16x32_bf16 v[188:191], v[28:31], a[220:223], v[188:191]\n"
"s_waitcnt lgkmcnt(0)\n"
"v_mfma_f32_16x16x32_bf16 v[160:163], v[16:19], a[224:227], v[160:163]\n"
"s_waitcnt vmcnt(16)\n"
"ds_read_b64_tr_b16 v[24:25], v2 offset:8192\n"
"v_mfma_f32_16x16x32_bf16 v[164:167], v[16:19], a[228:231], v[164:167]\n"
"ds_read_b64_tr_b16 v[26:27], v2 offset:9216\n"
"v_mfma_f32_16x16x32_bf16 v[168:171], v[16:19], a[232:235], v[168:171]\n"
"ds_read_b64_tr_b16 v[28:29], v3 offset:8192\n"
"s_add_u32 m0, s79, 0x1e000\n"
"v_mfma_f32_16x16x32_bf16 v[172:175], v[16:19], a[236:239], v[172:175]\n"
"ds_read_b64_tr_b16 v[30:31], v3 offset:9216\n"
"s_add_u32 s35, s74, 0x38200\n"
"s_and_b32 s35, s35, 0x7ffff\n"
"v_mfma_f32_16x16x32_bf16 v[176:179], v[20:23], a[224:227], v[176:179]\n"
"buffer_load_dwordx4 v14, s[16:19], s35 offen lds\n"
"v_mfma_f32_16x16x32_bf16 v[180:183], v[20:23], a[228:231], v[180:183]\n"
"buffer_load_dwordx4 v15, s[16:19], s35 offen offset:1024 lds\n"
"v_mfma_f32_16x16x32_bf16 v[184:187], v[20:23], a[232:235], v[184:187]\n"
"v_mfma_f32_16x16x32_bf16 v[188:191], v[20:23], a[236:239], v[188:191]\n"
"s_waitcnt lgkmcnt(0)\n"
"v_mfma_f32_16x16x32_bf16 v[160:163], v[24:27], a[240:243], v[160:163]\n"
"s_waitcnt vmcnt(16)\n"
"ds_read_b64_tr_b16 v[16:17], v2 offset:16384\n"
"v_mfma_f32_16x16x32_bf16 v[164:167], v[24:27], a[244:247], v[164:167]\n"
"ds_read_b64_tr_b16 v[18:19], v2 offset:17408\n"
"v_mfma_f32_16x16x32_bf16 v[168:171], v[24:27], a[248:251], v[168:171]\n"
"ds_read_b64_tr_b16 v[20:21], v3 offset:16384\n"
"s_add_u32 m0, s79, 0x10000\n"
"v_mfma_f32_16x16x32_bf16 v[172:175], v[24:27], a[252:255], v[172:175]\n"
"ds_read_b64_tr_b16 v[22:23], v3 offset:17408\n"
"s_add_u32 s35, s74, 0x40200\n"
"s_and_b32 s35, s35, 0x7ffff\n"
"v_mfma_f32_16x16x32_bf16 v[176:179], v[28:31], a[240:243], v[176:179]\n"
"buffer_load_dwordx4 v14, s[16:19], s35 offen lds\n"
"v_mfma_f32_16x16x32_bf16 v[180:183], v[28:31], a[244:247], v[180:183]\n"
"buffer_load_dwordx4 v15, s[16:19], s35 offen offset:1024 lds\n"
"s_add_u32 s76, s84, 0x40000\n"
"v_mfma_f32_16x16x32_bf16 v[184:187], v[28:31], a[248:251], v[184:187]\n"
"buffer_store_dwordx4 v[136:139], v110, s[28:31], s76 offen sc1\n"
"v_mfma_f32_16x16x32_bf16 v[188:191], v[28:31], a[252:255], v[188:191]\n"
"; o pass 2\n"
"s_waitcnt lgkmcnt(0)\n"
"v_mfma_f32_16x16x32_bf16 v[192:195], v[16:19], a[0:3], 0\n"
"s_waitcnt vmcnt(17)\n"
"ds_read_b64_tr_b16 v[24:25], v2 offset:24576\n"
"v_mfma_f32_16x16x32_bf16 v[196:199], v[16:19], a[4:7], 0\n"
"ds_read_b64_tr_b16 v[26:27], v2 offset:25600\n"
"v_mfma_f32_16x16x32_bf16 v[200:203], v[16:19], a[8:11], 0\n"
"ds_read_b64_tr_b16 v[28:29], v3 offset:24576\n"
"s_add_u32 m0, s79, 0x12000\n"
"v_mfma_f32_16x16x32_bf16 v[204:207], v[16:19], a[12:15], 0\n"
"ds_read_b64_tr_b16 v[30:31], v3 offset:25600\n"
"s_add_u32 s35, s74, 0x48200\n"
"s_and_b32 s35, s35, 0x7ffff\n"
"v_mfma_f32_16x16x32_bf16 v[208:211], v[20:23], a[0:3], 0\n"
"buffer_load_dwordx4 v14, s[16:19], s35 offen lds\n"
"v_mfma_f32_16x16x32_bf16 v[212:215], v[20:23], a[4:7], 0\n"
"buffer_load_dwordx4 v15, s[16:19], s35 offen offset:1024 lds\n"
"v_mfma_f32_16x16x32_bf16 v[216:219], v[20:23], a[8:11], 0\n"
"v_mfma_f32_16x16x32_bf16 v[220:223], v[20:23], a[12:15], 0\n"
"s_waitcnt lgkmcnt(0)\n"
"v_mfma_f32_16x16x32_bf16 v[192:195], v[24:27], a[16:19], v[192:195]\n"
"s_waitcnt vmcnt(17)\n"
"ds_read_b64_tr_b16 v[16:17], v12\n"
"v_mfma_f32_16x16x32_bf16 v[196:199], v[24:27], a[20:23], v[196:199]\n"
"ds_read_b64_tr_b16 v[18:19], v12 offset:1024\n"
"v_cvt_pk_bf16_f32 v160, v160, v161\n"
"v_mfma_f32_16x16x32_bf16 v[200:203], v[24:27], a[24:27], v[200:203]\n"
"ds_read_b64_tr_b16 v[20:21], v13\n"
"s_add_u32 m0, s79, 0x14000\n"
"v_mfma_f32_16x16x32_bf16 v[204:207], v[24:27], a[28:31], v[204:207]\n"
"ds_read_b64_tr_b16 v[22:23], v13 offset:1024\n"
"s_add_u32 s35, s74, 0x50200\n"
"s_and_b32 s35, s35, 0x7ffff\n"
"v_mfma_f32_16x16x32_bf16 v[208:211], v[28:31], a[16:19], v[208:211]\n"
"buffer_load_dwordx4 v14, s[16:19], s35 offen lds\n"
"v_cvt_pk_bf16_f32 v161, v162, v163\n"
"v_mfma_f32_16x16x32_bf16 v[212:215], v[28:31], a[20:23], v[212:215]\n"
"buffer_load_dwordx4 v15, s[16:19], s35 offen offset:1024 lds\n"
"v_cvt_pk_bf16_f32 v162, v176, v177\n"
"v_mfma_f32_16x16x32_bf16 v[216:219], v[28:31], a[24:27], v[216:219]\n"
"v_cvt_pk_bf16_f32 v163, v178, v179\n"
"v_cvt_pk_bf16_f32 v164, v164, v165\n"
"v_mfma_f32_16x16x32_bf16 v[220:223], v[28:31], a[28:31], v[220:223]\n"
"v_cvt_pk_bf16_f32 v165, v166, v167\n"
"s_waitcnt lgkmcnt(0)\n"
"v_mfma_f32_16x16x32_bf16 v[192:195], v[16:19], a[32:35], v[192:195]\n"
"s_waitcnt vmcnt(16)\n"
"ds_read_b64_tr_b16 v[24:25], v12 offset:8192\n"
"v_mfma_f32_16x16x32_bf16 v[196:199], v[16:19], a[36:39], v[196:199]\n"
"ds_read_b64_tr_b16 v[26:27], v12 offset:9216\n"
"v_cvt_pk_bf16_f32 v166, v180, v181\n"
"v_mfma_f32_16x16x32_bf16 v[200:203], v[16:19], a[40:43], v[200:203]\n"
"ds_read_b64_tr_b16 v[28:29], v13 offset:8192\n"
"s_add_u32 m0, s79, 0x16000\n"
"v_mfma_f32_16x16x32_bf16 v[204:207], v[16:19], a[44:47], v[204:207]\n"
"ds_read_b64_tr_b16 v[30:31], v13 offset:9216\n"
"s_add_u32 s35, s74, 0x58200\n"
"s_and_b32 s35, s35, 0x7ffff\n"
"v_mfma_f32_16x16x32_bf16 v[208:211], v[20:23], a[32:35], v[208:211]\n"
"buffer_load_dwordx4 v14, s[16:19], s35 offen lds\n"
"v_cvt_pk_bf16_f32 v167, v182, v183\n"
"v_mfma_f32_16x16x32_bf16 v[212:215], v[20:23], a[36:39], v[212:215]\n"
"buffer_load_dwordx4 v15, s[16:19], s35 offen offset:1024 lds\n"
"s_add_u32 s76, s84, 0x60000\n"
"v_mfma_f32_16x16x32_bf16 v[216:219], v[20:23], a[40:43], v[216:219]\n"
"buffer_store_dwordx4 v[140:143], v111, s[28:31], s76 offen sc1\n"
"v_cvt_pk_bf16_f32 v168, v168, v169\n"
"v_mfma_f32_16x16x32_bf16 v[220:223], v[20:23], a[44:47], v[220:223]\n"
"v_cvt_pk_bf16_f32 v169, v170, v171\n"
"s_waitcnt lgkmcnt(0)\n"
"v_mfma_f32_16x16x32_bf16 v[192:195], v[24:27], a[48:51], v[192:195]\n"
"s_waitcnt vmcnt(17)\n"
"ds_read_b64_tr_b16 v[16:17], v2 offset:32768\n"
"v_mfma_f32_16x16x32_bf16 v[196:199], v[24:27], a[52:55], v[196:199]\n"
"ds_read_b64_tr_b16 v[18:19], v2 offset:33792\n"
"v_cvt_pk_bf16_f32 v170, v184, v185\n"
"v_mfma_f32_16x16x32_bf16 v[200:203], v[24:27], a[56:59], v[200:203]\n"
"ds_read_b64_tr_b16 v[20:21], v3 offset:32768\n"
"s_add_u32 m0, s79, 0x20000\n"
"v_mfma_f32_16x16x32_bf16 v[204:207], v[24:27], a[60:63], v[204:207]\n"
"ds_read_b64_tr_b16 v[22:23], v3 offset:33792\n"
"s_add_u32 s35, s74, 0x60200\n"
"s_and_b32 s35, s35, 0x7ffff\n"
"v_mfma_f32_16x16x32_bf16 v[208:211], v[28:31], a[48:51], v[208:211]\n"
"buffer_load_dwordx4 v14, s[16:19], s35 offen lds\n"
"v_cvt_pk_bf16_f32 v171, v186, v187\n"
"v_mfma_f32_16x16x32_bf16 v[212:215], v[28:31], a[52:55], v[212:215]\n"
"buffer_load_dwordx4 v15, s[16:19], s35 offen offset:1024 lds\n"
"v_cvt_pk_bf16_f32 v172, v172, v173\n"
"v_mfma_f32_16x16x32_bf16 v[216:219], v[28:31], a[56:59], v[216:219]\n"
"v_cvt_pk_bf16_f32 v173, v174, v175\n"
"v_cvt_pk_bf16_f32 v174, v188, v189\n"
"v_mfma_f32_16x16x32_bf16 v[220:223], v[28:31], a[60:63], v[220:223]\n"
"v_cvt_pk_bf16_f32 v175, v190, v191\n"
"s_waitcnt lgkmcnt(0)\n"
"v_mfma_f32_16x16x32_bf16 v[192:195], v[16:19], a[64:67], v[192:195]\n"
"s_waitcnt vmcnt(17)\n"
"ds_read_b64_tr_b16 v[24:25], v2 offset:40960\n"
"v_mfma_f32_16x16x32_bf16 v[196:199], v[16:19], a[68:71], v[196:199]\n"
"ds_read_b64_tr_b16 v[26:27], v2 offset:41984\n"
"s_mov_b32 s13, 0x10000\n"
"v_mfma_f32_16x16x32_bf16 v[200:203], v[16:19], a[72:75], v[200:203]\n"
"ds_read_b64_tr_b16 v[28:29], v3 offset:40960\n"
"s_add_u32 m0, s79, 0x22000\n"
"v_mfma_f32_16x16x32_bf16 v[204:207], v[16:19], a[76:79], v[204:207]\n"
"ds_read_b64_tr_b16 v[30:31], v3 offset:41984\n"
"s_add_u32 s35, s74, 0x68200\n"
"s_and_b32 s35, s35, 0x7ffff\n"
"v_mfma_f32_16x16x32_bf16 v[208:211], v[20:23], a[64:67], v[208:211]\n"
"buffer_load_dwordx4 v14, s[16:19], s35 offen lds\n"
"v_xor_b32 v4, s10, v65\n"
"v_mfma_f32_16x16x32_bf16 v[212:215], v[20:23], a[68:71], v[212:215]\n"
"buffer_load_dwordx4 v15, s[16:19], s35 offen offset:1024 lds\n"
"v_bitop3_b32 v4, v67, s10, v4 bitop3:0xbe\n"
"v_mfma_f32_16x16x32_bf16 v[216:219], v[20:23], a[72:75], v[216:219]\n"
"v_bitop3_b32 v4, v69, s10, v4 bitop3:0xbe\n"
"v_bitop3_b32 v4, v71, s10, v4 bitop3:0xbe\n"
"v_mfma_f32_16x16x32_bf16 v[220:223], v[20:23], a[76:79], v[220:223]\n"
"v_bitop3_b32 v4, v73, s10, v4 bitop3:0xbe\n"
"s_waitcnt lgkmcnt(0)\n"
"v_mfma_f32_16x16x32_bf16 v[192:195], v[24:27], a[80:83], v[192:195]\n"
"s_waitcnt vmcnt(16)\n"
"ds_read_b64_tr_b16 v[16:17], v2 offset:49152\n"
"v_mfma_f32_16x16x32_bf16 v[196:199], v[24:27], a[84:87], v[196:199]\n"
"ds_read_b64_tr_b16 v[18:19], v2 offset:50176\n"
"v_bitop3_b32 v4, v75, s10, v4 bitop3:0xbe\n"
"v_mfma_f32_16x16x32_bf16 v[200:203], v[24:27], a[88:91], v[200:203]\n"
"ds_read_b64_tr_b16 v[20:21], v3 offset:49152\n"
"s_add_u32 m0, s79, 0x18000\n"
"v_mfma_f32_16x16x32_bf16 v[204:207], v[24:27], a[92:95], v[204:207]\n"
"ds_read_b64_tr_b16 v[22:23], v3 offset:50176\n"
"s_add_u32 s35, s74, 0x70200\n"
"s_and_b32 s35, s35, 0x7ffff\n"
"v_mfma_f32_16x16x32_bf16 v[208:211], v[28:31], a[80:83], v[208:211]\n"
"buffer_load_dwordx4 v14, s[16:19], s35 offen lds\n"
"v_bitop3_b32 v4, v77, s10, v4 bitop3:0xbe\n"
"v_mfma_f32_16x16x32_bf16 v[212:215], v[28:31], a[84:87], v[212:215]\n"
"buffer_load_dwordx4 v15, s[16:19], s35 offen offset:1024 lds\n"
"s_add_u32 s76, s84, 0x4000\n"
"v_mfma_f32_16x16x32_bf16 v[216:219], v[28:31], a[88:91], v[216:219]\n"
"buffer_store_dwordx4 v[160:163], v108, s[28:31], s76 offen sc1\n"
"v_bitop3_b32 v4, v79, s10, v4 bitop3:0xbe\n"
"v_mfma_f32_16x16x32_bf16 v[220:223], v[28:31], a[92:95], v[220:223]\n"
"v_cmp_ne_u32 vcc, 0, v4\n"
"s_waitcnt lgkmcnt(0)\n"
"v_mfma_f32_16x16x32_bf16 v[192:195], v[16:19], a[96:99], v[192:195]\n"
"s_waitcnt vmcnt(17)\n"
"ds_read_b64_tr_b16 v[24:25], v2 offset:57344\n"
"v_mfma_f32_16x16x32_bf16 v[196:199], v[16:19], a[100:103], v[196:199]\n"
"ds_read_b64_tr_b16 v[26:27], v2 offset:58368\n"
"s_cbranch_vccnz L_mgrc%=\n"
"L_mgdc%=:\n"
"v_mfma_f32_16x16x32_bf16 v[200:203], v[16:19], a[104:107], v[200:203]\n"
"ds_read_b64_tr_b16 v[28:29], v3 offset:57344\n"
"s_add_u32 m0, s79, 0x1a000\n"
"v_mfma_f32_16x16x32_bf16 v[204:207], v[16:19], a[108:111], v[204:207]\n"
"ds_read_b64_tr_b16 v[30:31], v3 offset:58368\n"
"s_add_u32 s35, s74, 0x78200\n"
"s_and_b32 s35, s35, 0x7ffff\n"
"v_mfma_f32_16x16x32_bf16 v[208:211], v[20:23], a[96:99], v[208:211]\n"
"buffer_load_dwordx4 v14, s[16:19], s35 offen lds\n"
"v_lshrrev_b32 v4, 1, v11\n"
"v_mfma_f32_16x16x32_bf16 v[212:215], v[20:23], a[100:103], v[212:215]\n"
"buffer_load_dwordx4 v15, s[16:19], s35 offen offset:1024 lds\n"
"v_add_u32 v4, 0x24000, v4\n"
"v_mfma_f32_16x16x32_bf16 v[216:219], v[20:23], a[104:107], v[216:219]\n"
"s_mov_b32 s13, 0xffff0000\n"
"v_add_f32 v64, v64, v72\n"
"v_mfma_f32_16x16x32_bf16 v[220:223], v[20:23], a[108:111], v[220:223]\n"
"v_add_f32 v66, v66, v74\n"
"s_waitcnt lgkmcnt(0)\n"
"v_mfma_f32_16x16x32_bf16 v[192:195], v[24:27], a[112:115], v[192:195]\n"
"s_waitcnt vmcnt(17)\n"
"ds_read_b64_tr_b16 v[16:17], v2\n"
"v_mfma_f32_16x16x32_bf16 v[196:199], v[24:27], a[116:119], v[196:199]\n"
"ds_read_b64_tr_b16 v[18:19], v2 offset:1024\n"
"v_add_f32 v68, v68, v76\n"
"v_mfma_f32_16x16x32_bf16 v[200:203], v[24:27], a[120:123], v[200:203]\n"
"ds_read_b64_tr_b16 v[20:21], v3\n"
"s_add_u32 m0, s79, 0x1c000\n"
"v_mfma_f32_16x16x32_bf16 v[204:207], v[24:27], a[124:127], v[204:207]\n"
"ds_read_b64_tr_b16 v[22:23], v3 offset:1024\n"
"s_add_u32 s35, s74, 0x300\n"
"s_and_b32 s35, s35, 0x7ffff\n"
"v_mfma_f32_16x16x32_bf16 v[208:211], v[28:31], a[112:115], v[208:211]\n"
"buffer_load_dwordx4 v14, s[16:19], s35 offen lds\n"
"v_add_f32 v70, v70, v78\n"
"v_mfma_f32_16x16x32_bf16 v[212:215], v[28:31], a[116:119], v[212:215]\n"
"buffer_load_dwordx4 v15, s[16:19], s35 offen offset:1024 lds\n"
"v_cvt_pk_bf16_f32 v72, v64, v66\n"
"v_mfma_f32_16x16x32_bf16 v[216:219], v[28:31], a[120:123], v[216:219]\n"
"v_lshlrev_b32 v76, 16, v72\n"
"v_and_b32 v77, s13, v72\n"
"v_mfma_f32_16x16x32_bf16 v[220:223], v[28:31], a[124:127], v[220:223]\n"
"v_add_f32 v76, 1.0, v76\n"
"s_waitcnt lgkmcnt(0)\n"
"v_mfma_f32_16x16x32_bf16 v[192:195], v[16:19], a[128:131], v[192:195]\n"
"s_waitcnt vmcnt(16)\n"
"ds_read_b64_tr_b16 v[24:25], v2 offset:8192\n"
"v_mfma_f32_16x16x32_bf16 v[196:199], v[16:19], a[132:135], v[196:199]\n"
"ds_read_b64_tr_b16 v[26:27], v2 offset:9216\n"
"v_add_f32 v77, 1.0, v77\n"
"v_mfma_f32_16x16x32_bf16 v[200:203], v[16:19], a[136:139], v[200:203]\n"
"ds_read_b64_tr_b16 v[28:29], v3 offset:8192\n"
"s_add_u32 m0, s79, 0x1e000\n"
"v_mfma_f32_16x16x32_bf16 v[204:207], v[16:19], a[140:143], v[204:207]\n"
"ds_read_b64_tr_b16 v[30:31], v3 offset:9216\n"
"s_add_u32 s35, s74, 0x8300\n"
"s_and_b32 s35, s35, 0x7ffff\n"
"v_mfma_f32_16x16x32_bf16 v[208:211], v[20:23], a[128:131], v[208:211]\n"
"buffer_load_dwordx4 v14, s[16:19], s35 offen lds\n"
"v_cvt_pk_bf16_f32 v72, v76, v77\n"
"v_mfma_f32_16x16x32_bf16 v[212:215], v[20:23], a[132:135], v[212:215]\n"
"buffer_load_dwordx4 v15, s[16:19], s35 offen offset:1024 lds\n"
"s_add_u32 s76, s84, 0x24000\n"
"v_mfma_f32_16x16x32_bf16 v[216:219], v[20:23], a[136:139], v[216:219]\n"
"buffer_store_dwordx4 v[164:167], v109, s[28:31], s76 offen sc1\n"
"v_lshlrev_b32 v76, 16, v72\n"
"v_mfma_f32_16x16x32_bf16 v[220:223], v[20:23], a[140:143], v[220:223]\n"
"v_and_b32 v77, s13, v72\n"
"s_waitcnt lgkmcnt(0)\n"
"v_mfma_f32_16x16x32_bf16 v[192:195], v[24:27], a[144:147], v[192:195]\n"
"s_waitcnt vmcnt(17)\n"
"ds_read_b64_tr_b16 v[16:17], v2 offset:16384\n"
"v_mfma_f32_16x16x32_bf16 v[196:199], v[24:27], a[148:151], v[196:199]\n"
"ds_read_b64_tr_b16 v[18:19], v2 offset:17408\n"
"ds_write_b64 v4, v[76:77] offset:6144\n"
"v_mfma_f32_16x16x32_bf16 v[200:203], v[24:27], a[152:155], v[200:203]\n"
"ds_read_b64_tr_b16 v[20:21], v3 offset:16384\n"
"s_add_u32 m0, s79, 0x10000\n"
"v_mfma_f32_16x16x32_bf16 v[204:207], v[24:27], a[156:159], v[204:207]\n"
"ds_read_b64_tr_b16 v[22:23], v3 offset:17408\n"
"s_add_u32 s35, s74, 0x10300\n"
"s_and_b32 s35, s35, 0x7ffff\n"
"v_mfma_f32_16x16x32_bf16 v[208:211], v[28:31], a[144:147], v[208:211]\n"
"buffer_load_dwordx4 v14, s[16:19], s35 offen lds\n"
"v_cvt_pk_bf16_f32 v73, v68, v70\n"
"v_mfma_f32_16x16x32_bf16 v[212:215], v[28:31], a[148:151], v[212:215]\n"
"buffer_load_dwordx4 v15, s[16:19], s35 offen offset:1024 lds\n"
"v_lshlrev_b32 v78, 16, v73\n"
"v_mfma_f32_16x16x32_bf16 v[216:219], v[28:31], a[152:155], v[216:219]\n"
"v_and_b32 v79, s13, v73\n"
"ds_write_b64 v4, v[78:79] offset:10240\n"
"v_mfma_f32_16x16x32_bf16 v[220:223], v[28:31], a[156:159], v[220:223]\n"
"s_waitcnt lgkmcnt(1)\n"
"v_mfma_f32_16x16x32_bf16 v[192:195], v[16:19], a[160:163], v[192:195]\n"
"s_waitcnt vmcnt(17)\n"
"ds_read_b64_tr_b16 v[24:25], v2 offset:24576\n"
"v_mfma_f32_16x16x32_bf16 v[196:199], v[16:19], a[164:167], v[196:199]\n"
"ds_read_b64_tr_b16 v[26:27], v2 offset:25600\n"
"v_mfma_f32_16x16x32_bf16 v[200:203], v[16:19], a[168:171], v[200:203]\n"
"ds_read_b64_tr_b16 v[28:29], v3 offset:24576\n"
"s_add_u32 m0, s79, 0x12000\n"
"v_mfma_f32_16x16x32_bf16 v[204:207], v[16:19], a[172:175], v[204:207]\n"
"ds_read_b64_tr_b16 v[30:31], v3 offset:25600\n"
"s_add_u32 s35, s74, 0x18300\n"
"s_and_b32 s35, s35, 0x7ffff\n"
"v_mfma_f32_16x16x32_bf16 v[208:211], v[20:23], a[160:163], v[208:211]\n"
"buffer_load_dwordx4 v14, s[16:19], s35 offen lds\n"
"v_mfma_f32_16x16x32_bf16 v[212:215], v[20:23], a[164:167], v[212:215]\n"
"buffer_load_dwordx4 v15, s[16:19], s35 offen offset:1024 lds\n"
"v_mfma_f32_16x16x32_bf16 v[216:219], v[20:23], a[168:171], v[216:219]\n"
"v_mfma_f32_16x16x32_bf16 v[220:223], v[20:23], a[172:175], v[220:223]\n"
"s_waitcnt lgkmcnt(0)\n"
"v_mfma_f32_16x16x32_bf16 v[192:195], v[24:27], a[176:179], v[192:195]\n"
"s_waitcnt vmcnt(16)\n"
"ds_read_b64_tr_b16 v[16:17], v12\n"
"v_mfma_f32_16x16x32_bf16 v[196:199], v[24:27], a[180:183], v[196:199]\n"
"ds_read_b64_tr_b16 v[18:19], v12 offset:1024\n"
"v_mfma_f32_16x16x32_bf16 v[200:203], v[24:27], a[184:187], v[200:203]\n"
"ds_read_b64_tr_b16 v[20:21], v13\n"
"s_add_u32 m0, s79, 0x14000\n"
"v_mfma_f32_16x16x32_bf16 v[204:207], v[24:27], a[188:191], v[204:207]\n"
"ds_read_b64_tr_b16 v[22:23], v13 offset:1024\n"
"s_add_u32 s35, s74, 0x20300\n"
"s_and_b32 s35, s35, 0x7ffff\n"
"v_mfma_f32_16x16x32_bf16 v[208:211], v[28:31], a[176:179], v[208:211]\n"
"buffer_load_dwordx4 v14, s[16:19], s35 offen lds\n"
"v_mfma_f32_16x16x32_bf16 v[212:215], v[28:31], a[180:183], v[212:215]\n"
"buffer_load_dwordx4 v15, s[16:19], s35 offen offset:1024 lds\n"
"s_add_u32 s76, s84, 0x44000\n"
"v_mfma_f32_16x16x32_bf16 v[216:219], v[28:31], a[184:187], v[216:219]\n"
"buffer_store_dwordx4 v[168:171], v110, s[28:31], s76 offen sc1\n"
"v_mfma_f32_16x16x32_bf16 v[220:223], v[28:31], a[188:191], v[220:223]\n"
"s_waitcnt lgkmcnt(0)\n"
"v_mfma_f32_16x16x32_bf16 v[192:195], v[16:19], a[192:195], v[192:195]\n"
"s_waitcnt vmcnt(17)\n"
"ds_read_b64_tr_b16 v[24:25], v12 offset:8192\n"
"v_mfma_f32_16x16x32_bf16 v[196:199], v[16:19], a[196:199], v[196:199]\n"
"ds_read_b64_tr_b16 v[26:27], v12 offset:9216\n"
"v_mfma_f32_16x16x32_bf16 v[200:203], v[16:19], a[200:203], v[200:203]\n"
"ds_read_b64_tr_b16 v[28:29], v13 offset:8192\n"
"s_add_u32 m0, s79, 0x16000\n"
"v_mfma_f32_16x16x32_bf16 v[204:207], v[16:19], a[204:207], v[204:207]\n"
"ds_read_b64_tr_b16 v[30:31], v13 offset:9216\n"
"s_add_u32 s35, s74, 0x28300\n"
"s_and_b32 s35, s35, 0x7ffff\n"
"v_mfma_f32_16x16x32_bf16 v[208:211], v[20:23], a[192:195], v[208:211]\n"
"buffer_load_dwordx4 v14, s[16:19], s35 offen lds\n"
"v_mfma_f32_16x16x32_bf16 v[212:215], v[20:23], a[196:199], v[212:215]\n"
"buffer_load_dwordx4 v15, s[16:19], s35 offen offset:1024 lds\n"
"v_mfma_f32_16x16x32_bf16 v[216:219], v[20:23], a[200:203], v[216:219]\n"
"buffer_store_dwordx4 v[104:107], v96, s[28:31], s69 offen sc1\n"
"v_mfma_f32_16x16x32_bf16 v[220:223], v[20:23], a[204:207], v[220:223]\n"
"s_waitcnt lgkmcnt(0)\n"
"v_mfma_f32_16x16x32_bf16 v[192:195], v[24:27], a[208:211], v[192:195]\n"
"s_waitcnt vmcnt(18)\n"
"ds_read_b64_tr_b16 v[16:17], v2 offset:32768\n"
"v_mfma_f32_16x16x32_bf16 v[196:199], v[24:27], a[212:215], v[196:199]\n"
"ds_read_b64_tr_b16 v[18:19], v2 offset:33792\n"
"v_mfma_f32_16x16x32_bf16 v[200:203], v[24:27], a[216:219], v[200:203]\n"
"ds_read_b64_tr_b16 v[20:21], v3 offset:32768\n"
"s_add_u32 m0, s79, 0x20000\n"
"v_mfma_f32_16x16x32_bf16 v[204:207], v[24:27], a[220:223], v[204:207]\n"
"ds_read_b64_tr_b16 v[22:23], v3 offset:33792\n"
"s_add_u32 s35, s74, 0x30300\n"
"s_and_b32 s35, s35, 0x7ffff\n"
"v_mfma_f32_16x16x32_bf16 v[208:211], v[28:31], a[208:211], v[208:211]\n"
"buffer_load_dwordx4 v14, s[16:19], s35 offen lds\n"
"v_mfma_f32_16x16x32_bf16 v[212:215], v[28:31], a[212:215], v[212:215]\n"
"buffer_load_dwordx4 v15, s[16:19], s35 offen offset:1024 lds\n"
"v_mfma_f32_16x16x32_bf16 v[216:219], v[28:31], a[216:219], v[216:219]\n"
"v_mfma_f32_16x16x32_bf16 v[220:223], v[28:31], a[220:223], v[220:223]\n"
"s_waitcnt lgkmcnt(0)\n"
"v_mfma_f32_16x16x32_bf16 v[192:195], v[16:19], a[224:227], v[192:195]\n"
"s_waitcnt vmcnt(17)\n"
"ds_read_b64_tr_b16 v[24:25], v2 offset:40960\n"
"v_mfma_f32_16x16x32_bf16 v[196:199], v[16:19], a[228:231], v[196:199]\n"
"ds_read_b64_tr_b16 v[26:27], v2 offset:41984\n"
"v_mfma_f32_16x16x32_bf16 v[200:203], v[16:19], a[232:235], v[200:203]\n"
"ds_read_b64_tr_b16 v[28:29], v3 offset:40960\n"
"s_add_u32 m0, s79, 0x22000\n"
"v_mfma_f32_16x16x32_bf16 v[204:207], v[16:19], a[236:239], v[204:207]\n"
"ds_read_b64_tr_b16 v[30:31], v3 offset:41984\n"
"s_add_u32 s35, s74, 0x38300\n"
"s_and_b32 s35, s35, 0x7ffff\n"
"v_mfma_f32_16x16x32_bf16 v[208:211], v[20:23], a[224:227], v[208:211]\n"
"buffer_load_dwordx4 v14, s[16:19], s35 offen lds\n"
"v_mfma_f32_16x16x32_bf16 v[212:215], v[20:23], a[228:231], v[212:215]\n"
"buffer_load_dwordx4 v15, s[16:19], s35 offen offset:1024 lds\n"
"s_add_u32 s76, s84, 0x64000\n"
"v_mfma_f32_16x16x32_bf16 v[216:219], v[20:23], a[232:235], v[216:219]\n"
"buffer_store_dwordx4 v[172:175], v111, s[28:31], s76 offen sc1\n"
"v_mfma_f32_16x16x32_bf16 v[220:223], v[20:23], a[236:239], v[220:223]\n"
"s_waitcnt lgkmcnt(0)\n"
"v_mfma_f32_16x16x32_bf16 v[192:195], v[24:27], a[240:243], v[192:195]\n"
"s_waitcnt vmcnt(18)\n"
"ds_read_b64_tr_b16 v[16:17], v2 offset:49152\n"
"v_mfma_f32_16x16x32_bf16 v[196:199], v[24:27], a[244:247], v[196:199]\n"
"ds_read_b64_tr_b16 v[18:19], v2 offset:50176\n"
"v_mfma_f32_16x16x32_bf16 v[200:203], v[24:27], a[248:251], v[200:203]\n"
"ds_read_b64_tr_b16 v[20:21], v3 offset:49152\n"
"s_add_u32 m0, s79, 0x18000\n"
"v_mfma_f32_16x16x32_bf16 v[204:207], v[24:27], a[252:255], v[204:207]\n"
"ds_read_b64_tr_b16 v[22:23], v3 offset:50176\n"
"s_add_u32 s35, s74, 0x40300\n"
"s_and_b32 s35, s35, 0x7ffff\n"
"v_mfma_f32_16x16x32_bf16 v[208:211], v[28:31], a[240:243], v[208:211]\n"
"buffer_load_dwordx4 v14, s[16:19], s35 offen lds\n"
"v_mfma_f32_16x16x32_bf16 v[212:215], v[28:31], a[244:247], v[212:215]\n"
"buffer_load_dwordx4 v15, s[16:19], s35 offen offset:1024 lds\n"
"v_mfma_f32_16x16x32_bf16 v[216:219], v[28:31], a[248:251], v[216:219]\n"
"v_mfma_f32_16x16x32_bf16 v[220:223], v[28:31], a[252:255], v[220:223]\n"
"; o pass 3\n"
"s_waitcnt lgkmcnt(0)\n"
"v_mfma_f32_16x16x32_bf16 v[224:227], v[16:19], a[0:3], 0\n"
"s_waitcnt vmcnt(18)\n"
"ds_read_b64_tr_b16 v[24:25], v2 offset:57344\n"
"v_mfma_f32_16x16x32_bf16 v[228:231], v[16:19], a[4:7], 0\n"
"ds_read_b64_tr_b16 v[26:27], v2 offset:58368\n"
"v_mfma_f32_16x16x32_bf16 v[232:235], v[16:19], a[8:11], 0\n"
"ds_read_b64_tr_b16 v[28:29], v3 offset:57344\n"
"s_add_u32 m0, s79, 0x1a000\n"
"v_mfma_f32_16x16x32_bf16 v[236:239], v[16:19], a[12:15], 0\n"
"ds_read_b64_tr_b16 v[30:31], v3 offset:58368\n"
"s_add_u32 s35, s74, 0x48300\n"
"s_and_b32 s35, s35, 0x7ffff\n"
"v_mfma_f32_16x16x32_bf16 v[240:243], v[20:23], a[0:3], 0\n"
"buffer_load_dwordx4 v14, s[16:19], s35 offen lds\n"
"v_mfma_f32_16x16x32_bf16 v[244:247], v[20:23], a[4:7], 0\n"
"buffer_load_dwordx4 v15, s[16:19], s35 offen offset:1024 lds\n"
"v_mfma_f32_16x16x32_bf16 v[248:251], v[20:23], a[8:11], 0\n"
"v_mfma_f32_16x16x32_bf16 v[252:255], v[20:23], a[12:15], 0\n"
"s_waitcnt lgkmcnt(0)\n"
"v_mfma_f32_16x16x32_bf16 v[224:227], v[24:27], a[16:19], v[224:227]\n"
"s_waitcnt vmcnt(17)\n"
"ds_read_b64_tr_b16 v[16:17], v2\n"
"v_mfma_f32_16x16x32_bf16 v[228:231], v[24:27], a[20:23], v[228:231]\n"
"ds_read_b64_tr_b16 v[18:19], v2 offset:1024\n"
"v_cvt_pk_bf16_f32 v192, v192, v193\n"
"v_mfma_f32_16x16x32_bf16 v[232:235], v[24:27], a[24:27], v[232:235]\n"
"ds_read_b64_tr_b16 v[20:21], v3\n"
"s_add_u32 m0, s79, 0x1c000\n"
"v_mfma_f32_16x16x32_bf16 v[236:239], v[24:27], a[28:31], v[236:239]\n"
"ds_read_b64_tr_b16 v[22:23], v3 offset:1024\n"
"s_add_u32 s35, s74, 0x50300\n"
"s_and_b32 s35, s35, 0x7ffff\n"
"v_mfma_f32_16x16x32_bf16 v[240:243], v[28:31], a[16:19], v[240:243]\n"
"buffer_load_dwordx4 v14, s[16:19], s35 offen lds\n"
"v_cvt_pk_bf16_f32 v193, v194, v195\n"
"v_mfma_f32_16x16x32_bf16 v[244:247], v[28:31], a[20:23], v[244:247]\n"
"buffer_load_dwordx4 v15, s[16:19], s35 offen offset:1024 lds\n"
"v_cvt_pk_bf16_f32 v194, v208, v209\n"
"v_mfma_f32_16x16x32_bf16 v[248:251], v[28:31], a[24:27], v[248:251]\n"
"v_cvt_pk_bf16_f32 v195, v210, v211\n"
"v_cvt_pk_bf16_f32 v196, v196, v197\n"
"v_mfma_f32_16x16x32_bf16 v[252:255], v[28:31], a[28:31], v[252:255]\n"
"v_cvt_pk_bf16_f32 v197, v198, v199\n"
"s_waitcnt lgkmcnt(0)\n"
"v_mfma_f32_16x16x32_bf16 v[224:227], v[16:19], a[32:35], v[224:227]\n"
"s_waitcnt vmcnt(17)\n"
"ds_read_b64_tr_b16 v[24:25], v2 offset:8192\n"
"v_mfma_f32_16x16x32_bf16 v[228:231], v[16:19], a[36:39], v[228:231]\n"
"ds_read_b64_tr_b16 v[26:27], v2 offset:9216\n"
"v_cvt_pk_bf16_f32 v198, v212, v213\n"
"v_mfma_f32_16x16x32_bf16 v[232:235], v[16:19], a[40:43], v[232:235]\n"
"ds_read_b64_tr_b16 v[28:29], v3 offset:8192\n"
"s_add_u32 m0, s79, 0x1e000\n"
"v_mfma_f32_16x16x32_bf16 v[236:239], v[16:19], a[44:47], v[236:239]\n"
"ds_read_b64_tr_b16 v[30:31], v3 offset:9216\n"
"s_add_u32 s35, s74, 0x58300\n"
"s_and_b32 s35, s35, 0x7ffff\n"
"v_mfma_f32_16x16x32_bf16 v[240:243], v[20:23], a[32:35], v[240:243]\n"
"buffer_load_dwordx4 v14, s[16:19], s35 offen lds\n"
"v_cvt_pk_bf16_f32 v199, v214, v215\n"
"v_mfma_f32_16x16x32_bf16 v[244:247], v[20:23], a[36:39], v[244:247]\n"
"buffer_load_dwordx4 v15, s[16:19], s35 offen offset:1024 lds\n"
"v_cvt_pk_bf16_f32 v200, v200, v201\n"
"v_mfma_f32_16x16x32_bf16 v[248:251], v[20:23], a[40:43], v[248:251]\n"
"v_cvt_pk_bf16_f32 v201, v202, v203\n"
"v_cvt_pk_bf16_f32 v202, v216, v217\n"
"v_mfma_f32_16x16x32_bf16 v[252:255], v[20:23], a[44:47], v[252:255]\n"
"v_cvt_pk_bf16_f32 v203, v218, v219\n"
"s_waitcnt lgkmcnt(0)\n"
"v_mfma_f32_16x16x32_bf16 v[224:227], v[24:27], a[48:51], v[224:227]\n"
"s_waitcnt vmcnt(17)\n"
"ds_read_b64_tr_b16 v[16:17], v2 offset:16384\n"
"v_mfma_f32_16x16x32_bf16 v[228:231], v[24:27], a[52:55], v[228:231]\n"
"ds_read_b64_tr_b16 v[18:19], v2 offset:17408\n"
"v_cvt_pk_bf16_f32 v204, v204, v205\n"
"v_mfma_f32_16x16x32_bf16 v[232:235], v[24:27], a[56:59], v[232:235]\n"
"ds_read_b64_tr_b16 v[20:21], v3 offset:16384\n"
"s_add_u32 m0, s79, 0x10000\n"
"v_mfma_f32_16x16x32_bf16 v[236:239], v[24:27], a[60:63], v[236:239]\n"
"ds_read_b64_tr_b16 v[22:23], v3 offset:17408\n"
"s_add_u32 s35, s74, 0x60300\n"
"s_and_b32 s35, s35, 0x7ffff\n"
"v_mfma_f32_16x16x32_bf16 v[240:243], v[28:31], a[48:51], v[240:243]\n"
"buffer_load_dwordx4 v14, s[16:19], s35 offen lds\n"
"v_cvt_pk_bf16_f32 v205, v206, v207\n"
"v_mfma_f32_16x16x32_bf16 v[244:247], v[28:31], a[52:55], v[244:247]\n"
"buffer_load_dwordx4 v15, s[16:19], s35 offen offset:1024 lds\n"
"v_cvt_pk_bf16_f32 v206, v220, v221\n"
"v_mfma_f32_16x16x32_bf16 v[248:251], v[28:31], a[56:59], v[248:251]\n"
"v_cvt_pk_bf16_f32 v207, v222, v223\n"
"v_mfma_f32_16x16x32_bf16 v[252:255], v[28:31], a[60:63], v[252:255]\n"
"s_waitcnt lgkmcnt(0)\n"
"v_mfma_f32_16x16x32_bf16 v[224:227], v[16:19], a[64:67], v[224:227]\n"
"s_waitcnt vmcnt(16)\n"
"ds_read_b64_tr_b16 v[24:25], v2 offset:24576\n"
"v_mfma_f32_16x16x32_bf16 v[228:231], v[16:19], a[68:71], v[228:231]\n"
"ds_read_b64_tr_b16 v[26:27], v2 offset:25600\n"
"v_mfma_f32_16x16x32_bf16 v[232:235], v[16:19], a[72:75], v[232:235]\n"
"ds_read_b64_tr_b16 v[28:29], v3 offset:24576\n"
"s_add_u32 m0, s79, 0x12000\n"
"v_mfma_f32_16x16x32_bf16 v[236:239], v[16:19], a[76:79], v[236:239]\n"
"ds_read_b64_tr_b16 v[30:31], v3 offset:25600\n"
"s_add_u32 s35, s74, 0x68300\n"
"s_and_b32 s35, s35, 0x7ffff\n"
"v_mfma_f32_16x16x32_bf16 v[240:243], v[20:23], a[64:67], v[240:243]\n"
"buffer_load_dwordx4 v14, s[16:19], s35 offen lds\n"
"v_mfma_f32_16x16x32_bf16 v[244:247], v[20:23], a[68:71], v[244:247]\n"
"buffer_load_dwordx4 v15, s[16:19], s35 offen offset:1024 lds\n"
"v_mfma_f32_16x16x32_bf16 v[248:251], v[20:23], a[72:75], v[248:251]\n"
"v_mfma_f32_16x16x32_bf16 v[252:255], v[20:23], a[76:79], v[252:255]\n"
"s_waitcnt lgkmcnt(0)\n"
"v_mfma_f32_16x16x32_bf16 v[224:227], v[24:27], a[80:83], v[224:227]\n"
"s_waitcnt vmcnt(15)\n"
"ds_read_b64_tr_b16 v[16:17], v12\n"
"v_mfma_f32_16x16x32_bf16 v[228:231], v[24:27], a[84:87], v[228:231]\n"
"ds_read_b64_tr_b16 v[18:19], v12 offset:1024\n"
"v_mfma_f32_16x16x32_bf16 v[232:235], v[24:27], a[88:91], v[232:235]\n"
"ds_read_b64_tr_b16 v[20:21], v13\n"
"s_add_u32 m0, s79, 0x14000\n"
"v_mfma_f32_16x16x32_bf16 v[236:239], v[24:27], a[92:95], v[236:239]\n"
"ds_read_b64_tr_b16 v[22:23], v13 offset:1024\n"
"s_add_u32 s35, s74, 0x70300\n"
"s_and_b32 s35, s35, 0x7ffff\n"
"v_mfma_f32_16x16x32_bf16 v[240:243], v[28:31], a[80:83], v[240:243]\n"
"buffer_load_dwordx4 v14, s[16:19], s35 offen lds\n"
"v_mfma_f32_16x16x32_bf16 v[244:247], v[28:31], a[84:87], v[244:247]\n"
"buffer_load_dwordx4 v15, s[16:19], s35 offen offset:1024 lds\n"
"v_mfma_f32_16x16x32_bf16 v[248:251], v[28:31], a[88:91], v[248:251]\n"
"v_mfma_f32_16x16x32_bf16 v[252:255], v[28:31], a[92:95], v[252:255]\n"
"s_waitcnt lgkmcnt(0)\n"
"v_mfma_f32_16x16x32_bf16 v[224:227], v[16:19], a[96:99], v[224:227]\n"
"s_waitcnt vmcnt(15)\n"
"ds_read_b64_tr_b16 v[24:25], v12 offset:8192\n"
"v_mfma_f32_16x16x32_bf16 v[228:231], v[16:19], a[100:103], v[228:231]\n"
"ds_read_b64_tr_b16 v[26:27], v12 offset:9216\n"
"v_mfma_f32_16x16x32_bf16 v[232:235], v[16:19], a[104:107], v[232:235]\n"
"ds_read_b64_tr_b16 v[28:29], v13 offset:8192\n"
"s_add_u32 m0, s79, 0x16000\n"
"v_mfma_f32_16x16x32_bf16 v[236:239], v[16:19], a[108:111], v[236:239]\n"
"ds_read_b64_tr_b16 v[30:31], v13 offset:9216\n"
"s_add_u32 s35, s74, 0x78300\n"
"s_and_b32 s35, s35, 0x7ffff\n"
"v_mfma_f32_16x16x32_bf16 v[240:243], v[20:23], a[96:99], v[240:243]\n"
"buffer_load_dwordx4 v14, s[16:19], s35 offen lds\n"
"v_mfma_f32_16x16x32_bf16 v[244:247], v[20:23], a[100:103], v[244:247]\n"
"buffer_load_dwordx4 v15, s[16:19], s35 offen offset:1024 lds\n"
"v_mfma_f32_16x16x32_bf16 v[248:251], v[20:23], a[104:107], v[248:251]\n"
"v_mfma_f32_16x16x32_bf16 v[252:255], v[20:23], a[108:111], v[252:255]\n"
"s_waitcnt lgkmcnt(0)\n"
"v_mfma_f32_16x16x32_bf16 v[224:227], v[24:27], a[112:115], v[224:227]\n"
"s_waitcnt vmcnt(14)\n"
"ds_read_b64_tr_b16 v[16:17], v2 offset:32768\n"
"v_mfma_f32_16x16x32_bf16 v[228:231], v[24:27], a[116:119], v[228:231]\n"
"ds_read_b64_tr_b16 v[18:19], v2 offset:33792\n"
"v_mfma_f32_16x16x32_bf16 v[232:235], v[24:27], a[120:123], v[232:235]\n"
"ds_read_b64_tr_b16 v[20:21], v3 offset:32768\n"
"v_mfma_f32_16x16x32_bf16 v[236:239], v[24:27], a[124:127], v[236:239]\n"
"ds_read_b64_tr_b16 v[22:23], v3 offset:33792\n"
"s_add_u32 s76, s84, 0x8000\n"
"v_mfma_f32_16x16x32_bf16 v[240:243], v[28:31], a[112:115], v[240:243]\n"
"buffer_store_dwordx4 v[192:195], v108, s[28:31], s76 offen sc1\n"
"v_mfma_f32_16x16x32_bf16 v[244:247], v[28:31], a[116:119], v[244:247]\n"
"buffer_store_dwordx4 v[104:107], v96, s[28:31], s69 offen offset:128 sc1\n"
"v_mfma_f32_16x16x32_bf16 v[248:251], v[28:31], a[120:123], v[248:251]\n"
"v_mfma_f32_16x16x32_bf16 v[252:255], v[28:31], a[124:127], v[252:255]\n"
"s_waitcnt lgkmcnt(0)\n"
"v_mfma_f32_16x16x32_bf16 v[224:227], v[16:19], a[128:131], v[224:227]\n"
"s_waitcnt vmcnt(14)\n"
"ds_read_b64_tr_b16 v[24:25], v2 offset:40960\n"
"v_mfma_f32_16x16x32_bf16 v[228:231], v[16:19], a[132:135], v[228:231]\n"
"ds_read_b64_tr_b16 v[26:27], v2 offset:41984\n"
"v_mfma_f32_16x16x32_bf16 v[232:235], v[16:19], a[136:139], v[232:235]\n"
"ds_read_b64_tr_b16 v[28:29], v3 offset:40960\n"
"v_mfma_f32_16x16x32_bf16 v[236:239], v[16:19], a[140:143], v[236:239]\n"
"ds_read_b64_tr_b16 v[30:31], v3 offset:41984\n"
"s_add_u32 s76, s84, 0x28000\n"
"v_mfma_f32_16x16x32_bf16 v[240:243], v[20:23], a[128:131], v[240:243]\n"
"buffer_store_dwordx4 v[196:199], v109, s[28:31], s76 offen sc1\n"
"v_mfma_f32_16x16x32_bf16 v[244:247], v[20:23], a[132:135], v[244:247]\n"
"v_mfma_f32_16x16x32_bf16 v[248:251], v[20:23], a[136:139], v[248:251]\n"
"v_mfma_f32_16x16x32_bf16 v[252:255], v[20:23], a[140:143], v[252:255]\n"
"s_waitcnt lgkmcnt(0)\n"
"v_mfma_f32_16x16x32_bf16 v[224:227], v[24:27], a[144:147], v[224:227]\n"
"s_waitcnt vmcnt(13)\n"
"ds_read_b64_tr_b16 v[16:17], v2 offset:49152\n"
"v_mfma_f32_16x16x32_bf16 v[228:231], v[24:27], a[148:151], v[228:231]\n"
"ds_read_b64_tr_b16 v[18:19], v2 offset:50176\n"
"v_mfma_f32_16x16x32_bf16 v[232:235], v[24:27], a[152:155], v[232:235]\n"
"ds_read_b64_tr_b16 v[20:21], v3 offset:49152\n"
"v_mfma_f32_16x16x32_bf16 v[236:239], v[24:27], a[156:159], v[236:239]\n"
"ds_read_b64_tr_b16 v[22:23], v3 offset:50176\n"
"s_add_u32 s76, s84, 0x48000\n"
"v_mfma_f32_16x16x32_bf16 v[240:243], v[28:31], a[144:147], v[240:243]\n"
"buffer_store_dwordx4 v[200:203], v110, s[28:31], s76 offen sc1\n"
"v_mfma_f32_16x16x32_bf16 v[244:247], v[28:31], a[148:151], v[244:247]\n"
"v_mfma_f32_16x16x32_bf16 v[248:251], v[28:31], a[152:155], v[248:251]\n"
"v_mfma_f32_16x16x32_bf16 v[252:255], v[28:31], a[156:159], v[252:255]\n"
"s_waitcnt lgkmcnt(0)\n"
"v_mfma_f32_16x16x32_bf16 v[224:227], v[16:19], a[160:163], v[224:227]\n"
"s_waitcnt vmcnt(12)\n"
"ds_read_b64_tr_b16 v[24:25], v2 offset:57344\n"
"v_mfma_f32_16x16x32_bf16 v[228:231], v[16:19], a[164:167], v[228:231]\n"
"ds_read_b64_tr_b16 v[26:27], v2 offset:58368\n"
"v_mfma_f32_16x16x32_bf16 v[232:235], v[16:19], a[168:171], v[232:235]\n"
"ds_read_b64_tr_b16 v[28:29], v3 offset:57344\n"
"v_mfma_f32_16x16x32_bf16 v[236:239], v[16:19], a[172:175], v[236:239]\n"
"ds_read_b64_tr_b16 v[30:31], v3 offset:58368\n"
"s_add_u32 s76, s84, 0x68000\n"
"v_mfma_f32_16x16x32_bf16 v[240:243], v[20:23], a[160:163], v[240:243]\n"
"buffer_store_dwordx4 v[204:207], v111, s[28:31], s76 offen sc1\n"
"v_mfma_f32_16x16x32_bf16 v[244:247], v[20:23], a[164:167], v[244:247]\n"
"buffer_load_dword v100, v97, s[28:31], s78 offen sc1\n"
"v_mfma_f32_16x16x32_bf16 v[248:251], v[20:23], a[168:171], v[248:251]\n"
"v_mfma_f32_16x16x32_bf16 v[252:255], v[20:23], a[172:175], v[252:255]\n"
"s_waitcnt lgkmcnt(0)\n"
"v_mfma_f32_16x16x32_bf16 v[224:227], v[24:27], a[176:179], v[224:227]\n"
"s_waitcnt vmcnt(12)\n"
"ds_read_b64_tr_b16 v[16:17], v2\n"
"v_mfma_f32_16x16x32_bf16 v[228:231], v[24:27], a[180:183], v[228:231]\n"
"ds_read_b64_tr_b16 v[18:19], v2 offset:1024\n"
"v_mfma_f32_16x16x32_bf16 v[232:235], v[24:27], a[184:187], v[232:235]\n"
"ds_read_b64_tr_b16 v[20:21], v3\n"
"v_mfma_f32_16x16x32_bf16 v[236:239], v[24:27], a[188:191], v[236:239]\n"
"ds_read_b64_tr_b16 v[22:23], v3 offset:1024\n"
"v_mfma_f32_16x16x32_bf16 v[240:243], v[28:31], a[176:179], v[240:243]\n"
"v_mfma_f32_16x16x32_bf16 v[244:247], v[28:31], a[180:183], v[244:247]\n"
"v_mfma_f32_16x16x32_bf16 v[248:251], v[28:31], a[184:187], v[248:251]\n"
"v_mfma_f32_16x16x32_bf16 v[252:255], v[28:31], a[188:191], v[252:255]\n"
"s_waitcnt lgkmcnt(0)\n"
"v_mfma_f32_16x16x32_bf16 v[224:227], v[16:19], a[192:195], v[224:227]\n"
"s_waitcnt vmcnt(10)\n"
"ds_read_b64_tr_b16 v[24:25], v2 offset:8192\n"
"v_mfma_f32_16x16x32_bf16 v[228:231], v[16:19], a[196:199], v[228:231]\n"
"ds_read_b64_tr_b16 v[26:27], v2 offset:9216\n"
"v_mfma_f32_16x16x32_bf16 v[232:235], v[16:19], a[200:203], v[232:235]\n"
"ds_read_b64_tr_b16 v[28:29], v3 offset:8192\n"
"v_mfma_f32_16x16x32_bf16 v[236:239], v[16:19], a[204:207], v[236:239]\n"
"ds_read_b64_tr_b16 v[30:31], v3 offset:9216\n"
"v_mfma_f32_16x16x32_bf16 v[240:243], v[20:23], a[192:195], v[240:243]\n"
"v_mfma_f32_16x16x32_bf16 v[244:247], v[20:23], a[196:199], v[244:247]\n"
"v_mfma_f32_16x16x32_bf16 v[248:251], v[20:23], a[200:203], v[248:251]\n"
"v_mfma_f32_16x16x32_bf16 v[252:255], v[20:23], a[204:207], v[252:255]\n"
"s_waitcnt lgkmcnt(0)\n"
"v_mfma_f32_16x16x32_bf16 v[224:227], v[24:27], a[208:211], v[224:227]\n"
"s_waitcnt vmcnt(8)\n"
"ds_read_b64_tr_b16 v[16:17], v2 offset:16384\n"
"v_mfma_f32_16x16x32_bf16 v[228:231], v[24:27], a[212:215], v[228:231]\n"
"ds_read_b64_tr_b16 v[18:19], v2 offset:17408\n"
"v_mfma_f32_16x16x32_bf16 v[232:235], v[24:27], a[216:219], v[232:235]\n"
"ds_read_b64_tr_b16 v[20:21], v3 offset:16384\n"
"v_mfma_f32_16x16x32_bf16 v[236:239], v[24:27], a[220:223], v[236:239]\n"
"ds_read_b64_tr_b16 v[22:23], v3 offset:17408\n"
"v_mfma_f32_16x16x32_bf16 v[240:243], v[28:31], a[208:211], v[240:243]\n"
"v_mfma_f32_16x16x32_bf16 v[244:247], v[28:31], a[212:215], v[244:247]\n"
"v_mfma_f32_16x16x32_bf16 v[248:251], v[28:31], a[216:219], v[248:251]\n"
"v_mfma_f32_16x16x32_bf16 v[252:255], v[28:31], a[220:223], v[252:255]\n"
"s_waitcnt lgkmcnt(0)\n"
"v_mfma_f32_16x16x32_bf16 v[224:227], v[16:19], a[224:227], v[224:227]\n"
"s_waitcnt vmcnt(6)\n"
"ds_read_b64_tr_b16 v[24:25], v2 offset:24576\n"
"v_mfma_f32_16x16x32_bf16 v[228:231], v[16:19], a[228:231], v[228:231]\n"
"ds_read_b64_tr_b16 v[26:27], v2 offset:25600\n"
"v_mfma_f32_16x16x32_bf16 v[232:235], v[16:19], a[232:235], v[232:235]\n"
"ds_read_b64_tr_b16 v[28:29], v3 offset:24576\n"
"v_mfma_f32_16x16x32_bf16 v[236:239], v[16:19], a[236:239], v[236:239]\n"
"ds_read_b64_tr_b16 v[30:31], v3 offset:25600\n"
"v_mfma_f32_16x16x32_bf16 v[240:243], v[20:23], a[224:227], v[240:243]\n"
"v_mfma_f32_16x16x32_bf16 v[244:247], v[20:23], a[228:231], v[244:247]\n"
"v_mfma_f32_16x16x32_bf16 v[248:251], v[20:23], a[232:235], v[248:251]\n"
"v_mfma_f32_16x16x32_bf16 v[252:255], v[20:23], a[236:239], v[252:255]\n"
"s_waitcnt lgkmcnt(0)\n"
"v_mfma_f32_16x16x32_bf16 v[224:227], v[24:27], a[240:243], v[224:227]\n"
"v_mfma_f32_16x16x32_bf16 v[228:231], v[24:27], a[244:247], v[228:231]\n"
"buffer_load_dword v101, v97, s[28:31], s78 offen offset:128 sc1\n"
"v_mfma_f32_16x16x32_bf16 v[232:235], v[24:27], a[248:251], v[232:235]\n"
"v_mfma_f32_16x16x32_bf16 v[236:239], v[24:27], a[252:255], v[236:239]\n"
"v_mfma_f32_16x16x32_bf16 v[240:243], v[28:31], a[240:243], v[240:243]\n"
"v_mfma_f32_16x16x32_bf16 v[244:247], v[28:31], a[244:247], v[244:247]\n"
"v_mfma_f32_16x16x32_bf16 v[248:251], v[28:31], a[248:251], v[248:251]\n"
"v_mfma_f32_16x16x32_bf16 v[252:255], v[28:31], a[252:255], v[252:255]\n"
"; o end\n"
"v_cvt_pk_bf16_f32 v224, v224, v225\n"
"v_cvt_pk_bf16_f32 v225, v226, v227\n"
"s_nop 2\n"
"v_cvt_pk_bf16_f32 v226, v240, v241\n"
"v_cvt_pk_bf16_f32 v227, v242, v243\n"
"v_cvt_pk_bf16_f32 v228, v228, v229\n"
"v_cvt_pk_bf16_f32 v229, v230, v231\n"
"v_cvt_pk_bf16_f32 v230, v244, v245\n"
"v_cvt_pk_bf16_f32 v231, v246, v247\n"
"v_cvt_pk_bf16_f32 v232, v232, v233\n"
"v_cvt_pk_bf16_f32 v233, v234, v235\n"
"v_cvt_pk_bf16_f32 v234, v248, v249\n"
"v_cvt_pk_bf16_f32 v235, v250, v251\n"
"v_cvt_pk_bf16_f32 v236, v236, v237\n"
"v_cvt_pk_bf16_f32 v237, v238, v239\n"
"v_cvt_pk_bf16_f32 v238, v252, v253\n"
"v_cvt_pk_bf16_f32 v239, v254, v255\n"
"s_add_u32 s76, s84, 0xc000\n"
"buffer_store_dwordx4 v[224:227], v108, s[28:31], s76 offen sc1\n"
"s_add_u32 s76, s84, 0x2c000\n"
"buffer_store_dwordx4 v[228:231], v109, s[28:31], s76 offen sc1\n"
"s_add_u32 s76, s84, 0x4c000\n"
"buffer_store_dwordx4 v[232:235], v110, s[28:31], s76 offen sc1\n"
"s_add_u32 s76, s84, 0x6c000\n"
"buffer_store_dwordx4 v[236:239], v111, s[28:31], s76 offen sc1\n"
"v_accvgpr_write_b32 a18, v108\n"
"v_accvgpr_write_b32 a19, v109\n"
"v_accvgpr_write_b32 a20, v110\n"
"v_accvgpr_write_b32 a21, v111\n"
"v_accvgpr_write_b32 a22, v124\n"
"v_accvgpr_write_b32 a23, v125\n"
"v_accvgpr_write_b32 a24, v126\n"
"v_accvgpr_write_b32 a25, v127\n"
"v_accvgpr_write_b32 a26, v98\n"
"v_accvgpr_write_b32 a27, v96\n"
"v_accvgpr_write_b32 a28, v97\n"
"L_x1p0c%=:\n"
"s_waitcnt vmcnt(5)\n"
"v_cmp_ne_u32 vcc, s10, v100\n"
"s_nop 0\n"
"s_and_b32 s11, vcc_lo, 255\n"
"s_cbranch_scc1 L_x1p0r%=\n"
"L_x1p0d%=:\n"
"s_add_u32 s77, s88, 0\n"
"buffer_load_dwordx4 a[192:195], v124, s[28:31], s77 offen sc1\n"
"s_add_u32 s77, s88, 0x1000\n"
"buffer_load_dwordx4 a[196:199], v125, s[28:31], s77 offen sc1\n"
"s_add_u32 s77, s88, 0x2000\n"
"buffer_load_dwordx4 a[200:203], v126, s[28:31], s77 offen sc1\n"
"s_add_u32 s77, s88, 0x3000\n"
"buffer_load_dwordx4 a[204:207], v127, s[28:31], s77 offen sc1\n"
"s_lshr_b32 s11, s8, 1\n"
"s_lshl_b32 s11, s11, 2\n"
"s_and_b32 s12, s8, 1\n"
"s_lshl_b32 s12, s12, 5\n"
"v_bfe_u32 v4, v6, 3, 2\n"
"v_lshlrev_b32 v4, 6, v4\n"
"v_and_b32 v5, 7, v6\n"
"v_lshl_add_u32 v4, v5, 2, v4\n"
"v_add_u32 v4, s12, v4\n"
"s_set_gpr_idx_on s11, gpr_idx(SRC0)\n"
"v_mov_b32 v108, v128\n"
"v_mov_b32 v109, v129\n"
"v_mov_b32 v110, v130\n"
"v_mov_b32 v111, v131\n"
"v_mov_b32 v112, v160\n"
"v_mov_b32 v113, v161\n"
"v_mov_b32 v114, v162\n"
"v_mov_b32 v115, v163\n"
"v_mov_b32 v116, v192\n"
"v_mov_b32 v117, v193\n"
"v_mov_b32 v118, v194\n"
"v_mov_b32 v119, v195\n"
"v_mov_b32 v120, v224\n"
"v_mov_b32 v121, v225\n"
"v_mov_b32 v122, v226\n"
"v_mov_b32 v123, v227\n"
"s_set_gpr_idx_off\n"
"s_nop 1\n"
"ds_bpermute_b32 v108, v4, v108\n"
"ds_bpermute_b32 v109, v4, v109\n"
"ds_bpermute_b32 v110, v4, v110\n"
"ds_bpermute_b32 v111, v4, v111\n"
"ds_bpermute_b32 v112, v4, v112\n"
"ds_bpermute_b32 v113, v4, v113\n"
"ds_bpermute_b32 v114, v4, v114\n"
"ds_bpermute_b32 v115, v4, v115\n"
"ds_bpermute_b32 v116, v4, v116\n"
"ds_bpermute_b32 v117, v4, v117\n"
"ds_bpermute_b32 v118, v4, v118\n"
"ds_bpermute_b32 v119, v4, v119\n"
"ds_bpermute_b32 v120, v4, v120\n"
"ds_bpermute_b32 v121, v4, v121\n"
"ds_bpermute_b32 v122, v4, v122\n"
"ds_bpermute_b32 v123, v4, v123\n"
"global_load_dwordx4 a[180:183], v1, s[44:45]\n"
"v_and_b32 v4, 8, v7\n"
"v_cmp_eq_u32 vcc, 0, v4\n"
"v_mov_b32 v16, s56\n"
"v_mov_b32 v17, s54\n"
"v_cndmask_b32 v2, v16, v17, vcc\n"
"v_mov_b32 v16, s57\n"
"v_mov_b32 v17, s55\n"
"v_cndmask_b32 v3, v16, v17, vcc\n"
"v_and_b32 v4, 7, v7\n"
"v_lshlrev_b32 v4, 4, v4\n"
"s_mov_b32 s11, 0x3800\n"
"v_mad_u32_u24 v4, v8, s11, v4\n"
"s_lshl_b32 s11, s87, 9\n"
"s_lshl_b32 s15, s7, 7\n"
"s_add_u32 s11, s11, s15\n"
"s_mul_i32 s12, s83, 0x1c000\n"
"s_add_u32 s11, s11, s12\n"
"v_add_u32 v4, s11, v4\n"
"v_mov_b32 v5, 0\n"
"v_lshl_add_u64 v[2:3], v[4:5], 0, v[2:3]\n"
"s_mov_b32 s64, 0x1c00\n"
"s_mov_b32 s65, 0\n"
"s_mov_b32 s66, 0xe000\n"
"s_mov_b32 s67, 0\n"
"v_lshl_add_u64 v[10:11], v[2:3], 0, s[64:65]\n"
"v_lshl_add_u64 v[12:13], v[2:3], 0, s[66:67]\n"
"v_lshl_add_u64 v[14:15], v[10:11], 0, s[66:67]\n"
"s_mov_b32 s64, s83\n"
"s_mov_b32 s65, 0x1c000\n"
"global_load_dwordx4 v[32:35], v[2:3], off sc1\n"
"global_load_dwordx4 v[36:39], v[2:3], off offset:3584 sc1\n"
"global_load_dwordx4 v[40:43], v[10:11], off sc1\n"
"global_load_dwordx4 v[44:47], v[10:11], off offset:3584 sc1\n"
"global_load_dwordx4 v[48:51], v[12:13], off sc1\n"
"global_load_dwordx4 v[52:55], v[12:13], off offset:3584 sc1\n"
"global_load_dwordx4 v[56:59], v[14:15], off sc1\n"
"global_load_dwordx4 v[60:63], v[14:15], off offset:3584 sc1\n"
"s_add_u32 s64, s64, 1\n"
"s_and_b32 s64, s64, 15\n"
"s_cselect_b32 s66, s65, 0xffe5c000\n"
"s_cselect_b32 s67, 0, -1\n"
"v_lshl_add_u64 v[2:3], v[2:3], 0, s[66:67]\n"
"v_lshl_add_u64 v[10:11], v[10:11], 0, s[66:67]\n"
"v_lshl_add_u64 v[12:13], v[12:13], 0, s[66:67]\n"
"v_lshl_add_u64 v[14:15], v[14:15], 0, s[66:67]\n"
"global_load_dwordx4 v[64:67], v[2:3], off sc1\n"
"global_load_dwordx4 v[68:71], v[2:3], off offset:3584 sc1\n"
"global_load_dwordx4 v[72:75], v[10:11], off sc1\n"
"global_load_dwordx4 v[76:79], v[10:11], off offset:3584 sc1\n"
"global_load_dwordx4 v[80:83], v[12:13], off sc1\n"
"global_load_dwordx4 v[84:87], v[12:13], off offset:3584 sc1\n"
"global_load_dwordx4 v[88:91], v[14:15], off sc1\n"
"global_load_dwordx4 v[92:95], v[14:15], off offset:3584 sc1\n"
"s_add_u32 s64, s64, 1\n"
"s_and_b32 s64, s64, 15\n"
"s_cselect_b32 s66, s65, 0xffe5c000\n"
"s_cselect_b32 s67, 0, -1\n"
"v_lshl_add_u64 v[2:3], v[2:3], 0, s[66:67]\n"
"v_lshl_add_u64 v[10:11], v[10:11], 0, s[66:67]\n"
"v_lshl_add_u64 v[12:13], v[12:13], 0, s[66:67]\n"
"v_lshl_add_u64 v[14:15], v[14:15], 0, s[66:67]\n"
"s_add_u32 m0, s79, 0x10000\n"
"s_nop 0\n"
"global_load_lds_dwordx4 v[2:3], off sc1\n"
"s_add_u32 m0, s79, 0xf600\n"
"s_nop 0\n"
"global_load_lds_dwordx4 v[2:3], off offset:3584 sc1\n"
"s_add_u32 m0, s79, 0x12000\n"
"s_nop 0\n"
"global_load_lds_dwordx4 v[10:11], off sc1\n"
"s_add_u32 m0, s79, 0x11600\n"
"s_nop 0\n"
"global_load_lds_dwordx4 v[10:11], off offset:3584 sc1\n"
"s_add_u32 m0, s79, 0x14000\n"
"s_nop 0\n"
"global_load_lds_dwordx4 v[12:13], off sc1\n"
"s_add_u32 m0, s79, 0x13600\n"
"s_nop 0\n"
"global_load_lds_dwordx4 v[12:13], off offset:3584 sc1\n"
"s_add_u32 m0, s79, 0x16000\n"
"s_nop 0\n"
"global_load_lds_dwordx4 v[14:15], off sc1\n"
"s_add_u32 m0, s79, 0x15600\n"
"s_nop 0\n"
"global_load_lds_dwordx4 v[14:15], off offset:3584 sc1\n"
"v_add_u32 v0, s79, v1\n"
"v_add_u32 v0, 0x10000, v0\n"
"L_x1p1c%=:\n"
"s_waitcnt vmcnt(33)\n"
"v_cmp_ne_u32 vcc, s10, v101\n"
"s_nop 0\n"
"s_and_b32 s11, vcc_lo, 255\n"
"s_cbranch_scc1 L_x1p1r%=\n"
"L_x1p1d%=:\n"
"s_add_u32 s77, s88, 0x4000\n"
"buffer_load_dwordx4 a[208:211], v124, s[28:31], s77 offen sc1\n"
"s_add_u32 s77, s88, 0x5000\n"
"buffer_load_dwordx4 a[212:215], v125, s[28:31], s77 offen sc1\n"
"s_add_u32 s77, s88, 0x6000\n"
"buffer_load_dwordx4 a[216:219], v126, s[28:31], s77 offen sc1\n"
"s_add_u32 s77, s88, 0x7000\n"
"buffer_load_dwordx4 a[220:223], v127, s[28:31], s77 offen sc1\n"
"v_and_b32 v4, 7, v6\n"
"v_lshlrev_b32 v4, 10, v4\n"
"v_lshrrev_b32 v5, 5, v6\n"
"v_lshl_add_u32 v4, v5, 9, v4\n"
"v_bfe_u32 v5, v6, 3, 2\n"
"v_lshl_add_u32 v9, v5, 3, v4\n"
"s_lshl_b32 s68, s8, 13\n"
"s_lshl_b32 s15, s7, 6\n"
"s_add_u32 s68, s68, s15\n"
"buffer_load_dwordx2 v[16:17], v9, s[36:39], s68 offen\n"
"buffer_load_dwordx2 v[18:19], v9, s[36:39], s68 offen offset:32\n"
"buffer_load_dwordx2 v[20:21], v9, s[36:39], s68 offen offset:256\n"
"buffer_load_dwordx2 v[22:23], v9, s[36:39], s68 offen offset:288\n"
"v_accvgpr_write_b32 a16, v7\n"
"v_accvgpr_write_b32 a17, v9\n"
"v_lshrrev_b32 v4, 5, v6\n"
"v_lshlrev_b32 v4, 10, v4\n"
"v_bfe_u32 v5, v6, 3, 2\n"
"v_lshl_add_u32 v4, v5, 4, v4\n"
"s_lshl_b32 s11, s7, 7\n"
"s_add_u32 s11, s11, 0x24000\n"
"v_add_u32 v8, s11, v4\n"
"v_lshrrev_b32 v4, 5, v6\n"
"v_and_b32 v5, 0x1ff, v1\n"
"v_lshl_add_u32 v7, v4, 12, v5\n"
"s_mov_b32 s70, 0x3f80\n"
"s_mov_b32 s71, 0x3f800000\n"
"s_mov_b32 s72, 0xffff0000\n"
"s_mov_b32 s73, 0x3b000000\n"
"v_and_b32 v184, 7, v6\n"
"v_lshlrev_b32 v184, 5, v184\n"
"v_add_u32 v184, 0x27000, v184\n"
"s_lshl_b32 s11, s7, 3\n"
"v_add_u32 v185, s11, v184\n"
"s_waitcnt lgkmcnt(0)\n"
"s_barrier\n"
"ds_read_b128 v[192:195], v8 offset:4096\n"
"ds_read_b128 v[196:199], v8 offset:4160\n"
"ds_read_b128 v[200:203], v8 offset:4608\n"
"ds_read_b128 v[204:207], v8 offset:4672\n"
"ds_read_b128 v[208:211], v8 offset:6144\n"
"ds_read_b128 v[212:215], v8 offset:6208\n"
"ds_read_b128 v[216:219], v8 offset:6656\n"
"ds_read_b128 v[220:223], v8 offset:6720\n"
"ds_read_b128 v[224:227], v8 offset:8192\n"
"ds_read_b128 v[228:231], v8 offset:8256\n"
"ds_read_b128 v[232:235], v8 offset:8704\n"
"ds_read_b128 v[236:239], v8 offset:8768\n"
"v_dot2_f32_bf16 v128, v108, v98, 0\n"
"v_dot2_f32_bf16 v129, v108, v99, 0\n"
"v_dot2_f32_bf16 v130, v109, v98, 0\n"
"v_dot2_f32_bf16 v131, v109, v99, 0\n"
"v_dot2_f32_bf16 v132, v110, v98, 0\n"
"v_dot2_f32_bf16 v133, v110, v99, 0\n"
"v_dot2_f32_bf16 v134, v111, v98, 0\n"
"v_dot2_f32_bf16 v135, v111, v99, 0\n"
"v_dot2_f32_bf16 v136, v112, v98, 0\n"
"v_dot2_f32_bf16 v137, v112, v99, 0\n"
"v_dot2_f32_bf16 v138, v113, v98, 0\n"
"v_dot2_f32_bf16 v139, v113, v99, 0\n"
"v_dot2_f32_bf16 v140, v114, v98, 0\n"
"v_dot2_f32_bf16 v141, v114, v99, 0\n"
"v_dot2_f32_bf16 v142, v115, v98, 0\n"
"v_dot2_f32_bf16 v143, v115, v99, 0\n"
"v_dot2_f32_bf16 v144, v116, v98, 0\n"
"v_dot2_f32_bf16 v145, v116, v99, 0\n"
"v_dot2_f32_bf16 v146, v117, v98, 0\n"
"v_dot2_f32_bf16 v147, v117, v99, 0\n"
"v_dot2_f32_bf16 v148, v118, v98, 0\n"
"v_dot2_f32_bf16 v149, v118, v99, 0\n"
"v_dot2_f32_bf16 v150, v119, v98, 0\n"
"v_dot2_f32_bf16 v151, v119, v99, 0\n"
"v_dot2_f32_bf16 v152, v120, v98, 0\n"
"v_dot2_f32_bf16 v153, v120, v99, 0\n"
"v_dot2_f32_bf16 v154, v121, v98, 0\n"
"v_dot2_f32_bf16 v155, v121, v99, 0\n"
"v_dot2_f32_bf16 v156, v122, v98, 0\n"
"v_dot2_f32_bf16 v157, v122, v99, 0\n"
"v_dot2_f32_bf16 v158, v123, v98, 0\n"
"v_dot2_f32_bf16 v159, v123, v99, 0\n"
"s_add_u32 s77, s88, 0x8000\n"
"buffer_load_dwordx4 a[224:227], v124, s[28:31], s77 offen sc1\n"
"s_add_u32 s77, s88, 0x9000\n"
"buffer_load_dwordx4 a[228:231], v125, s[28:31], s77 offen sc1\n"
"s_add_u32 s77, s88, 0xa000\n"
"buffer_load_dwordx4 a[232:235], v126, s[28:31], s77 offen sc1\n"
"s_add_u32 s77, s88, 0xb000\n"
"buffer_load_dwordx4 a[236:239], v127, s[28:31], s77 offen sc1\n"
"s_cmp_eq_u32 s14, 0x10000\n"
"s_cbranch_scc1 L_x1ns%=\n"
"s_sleep 12\n"
"L_x1ns%=:\n"
"s_add_u32 s77, s88, 0xc000\n"
"buffer_load_dwordx4 v[108:111], v124, s[28:31], s77 offen sc1\n"
"s_add_u32 s77, s88, 0xd000\n"
"buffer_load_dwordx4 v[112:115], v125, s[28:31], s77 offen sc1\n"
"s_add_u32 s77, s88, 0xe000\n"
"buffer_load_dwordx4 v[116:119], v126, s[28:31], s77 offen sc1\n"
"s_add_u32 s77, s88, 0xf000\n"
"buffer_load_dwordx4 v[120:123], v127, s[28:31], s77 offen sc1\n"
"s_waitcnt vmcnt(11)\n"
"v_lshlrev_b32 v160, 16, v16\n"
"v_and_b32 v161, s72, v16\n"
"v_lshlrev_b32 v162, 16, v17\n"
"v_and_b32 v163, s72, v17\n"
"s_waitcnt vmcnt(10)\n"
"v_lshlrev_b32 v164, 16, v18\n"
"v_and_b32 v165, s72, v18\n"
"v_lshlrev_b32 v166, 16, v19\n"
"v_and_b32 v167, s72, v19\n"
"s_waitcnt vmcnt(9)\n"
"v_lshlrev_b32 v168, 16, v20\n"
"v_and_b32 v169, s72, v20\n"
"v_lshlrev_b32 v170, 16, v21\n"
"v_and_b32 v171, s72, v21\n"
"s_waitcnt vmcnt(8)\n"
"v_lshlrev_b32 v172, 16, v22\n"
"v_and_b32 v173, s72, v22\n"
"v_lshlrev_b32 v174, 16, v23\n"
"v_and_b32 v175, s72, v23\n"
"v_accvgpr_read_b32 v240, a192\n"
"v_accvgpr_read_b32 v241, a193\n"
"v_accvgpr_read_b32 v242, a194\n"
"v_accvgpr_read_b32 v243, a195\n"
"v_accvgpr_read_b32 v244, a196\n"
"v_accvgpr_read_b32 v245, a197\n"
"v_accvgpr_read_b32 v246, a198\n"
"v_accvgpr_read_b32 v247, a199\n"
"v_accvgpr_read_b32 v248, a200\n"
"v_accvgpr_read_b32 v249, a201\n"
"v_accvgpr_read_b32 v250, a202\n"
"v_accvgpr_read_b32 v251, a203\n"
"v_accvgpr_read_b32 v252, a204\n"
"v_accvgpr_read_b32 v253, a205\n"
"v_accvgpr_read_b32 v254, a206\n"
"v_accvgpr_read_b32 v255, a207\n"
"v_dot2_f32_bf16 v128, v240, s70, v128\n"
"v_dot2_f32_bf16 v129, v240, s71, v129\n"
"v_dot2_f32_bf16 v130, v241, s70, v130\n"
"v_dot2_f32_bf16 v131, v241, s71, v131\n"
"v_dot2_f32_bf16 v132, v242, s70, v132\n"
"v_dot2_f32_bf16 v133, v242, s71, v133\n"
"v_dot2_f32_bf16 v134, v243, s70, v134\n"
"v_dot2_f32_bf16 v135, v243, s71, v135\n"
"v_dot2_f32_bf16 v128, v244, s70, v128\n"
"v_dot2_f32_bf16 v129, v244, s71, v129\n"
"v_dot2_f32_bf16 v130, v245, s70, v130\n"
"v_dot2_f32_bf16 v131, v245, s71, v131\n"
"v_dot2_f32_bf16 v132, v246, s70, v132\n"
"v_dot2_f32_bf16 v133, v246, s71, v133\n"
"v_dot2_f32_bf16 v134, v247, s70, v134\n"
"v_dot2_f32_bf16 v135, v247, s71, v135\n"
"v_dot2_f32_bf16 v128, v248, s70, v128\n"
"v_dot2_f32_bf16 v129, v248, s71, v129\n"
"v_dot2_f32_bf16 v130, v249, s70, v130\n"
"v_dot2_f32_bf16 v131, v249, s71, v131\n"
"v_dot2_f32_bf16 v132, v250, s70, v132\n"
"v_dot2_f32_bf16 v133, v250, s71, v133\n"
"v_dot2_f32_bf16 v134, v251, s70, v134\n"
"v_dot2_f32_bf16 v135, v251, s71, v135\n"
"v_dot2_f32_bf16 v128, v252, s70, v128\n"
"v_dot2_f32_bf16 v129, v252, s71, v129\n"
"v_dot2_f32_bf16 v130, v253, s70, v130\n"
"v_dot2_f32_bf16 v131, v253, s71, v131\n"
"v_dot2_f32_bf16 v132, v254, s70, v132\n"
"v_dot2_f32_bf16 v133, v254, s71, v133\n"
"v_dot2_f32_bf16 v134, v255, s70, v134\n"
"v_dot2_f32_bf16 v135, v255, s71, v135\n"
"v_accvgpr_read_b32 v240, a208\n"
"v_accvgpr_read_b32 v241, a209\n"
"v_accvgpr_read_b32 v242, a210\n"
"v_accvgpr_read_b32 v243, a211\n"
"v_accvgpr_read_b32 v244, a212\n"
"v_accvgpr_read_b32 v245, a213\n"
"v_accvgpr_read_b32 v246, a214\n"
"v_accvgpr_read_b32 v247, a215\n"
"v_accvgpr_read_b32 v248, a216\n"
"v_accvgpr_read_b32 v249, a217\n"
"v_accvgpr_read_b32 v250, a218\n"
"v_accvgpr_read_b32 v251, a219\n"
"v_accvgpr_read_b32 v252, a220\n"
"v_accvgpr_read_b32 v253, a221\n"
"v_accvgpr_read_b32 v254, a222\n"
"v_accvgpr_read_b32 v255, a223\n"
"v_dot2_f32_bf16 v136, v240, s70, v136\n"
"v_dot2_f32_bf16 v137, v240, s71, v137\n"
"v_dot2_f32_bf16 v138, v241, s70, v138\n"
"v_dot2_f32_bf16 v139, v241, s71, v139\n"
"v_dot2_f32_bf16 v140, v242, s70, v140\n"
"v_dot2_f32_bf16 v141, v242, s71, v141\n"
"v_dot2_f32_bf16 v142, v243, s70, v142\n"
"v_dot2_f32_bf16 v143, v243, s71, v143\n"
"v_dot2_f32_bf16 v136, v244, s70, v136\n"
"v_dot2_f32_bf16 v137, v244, s71, v137\n"
"v_dot2_f32_bf16 v138, v245, s70, v138\n"
"v_dot2_f32_bf16 v139, v245, s71, v139\n"
"v_dot2_f32_bf16 v140, v246, s70, v140\n"
"v_dot2_f32_bf16 v141, v246, s71, v141\n"
"v_dot2_f32_bf16 v142, v247, s70, v142\n"
"v_dot2_f32_bf16 v143, v247, s71, v143\n"
"v_dot2_f32_bf16 v136, v248, s70, v136\n"
"v_dot2_f32_bf16 v137, v248, s71, v137\n"
"v_dot2_f32_bf16 v138, v249, s70, v138\n"
"v_dot2_f32_bf16 v139, v249, s71, v139\n"
"v_dot2_f32_bf16 v140, v250, s70, v140\n"
"v_dot2_f32_bf16 v141, v250, s71, v141\n"
"v_dot2_f32_bf16 v142, v251, s70, v142\n"
"v_dot2_f32_bf16 v143, v251, s71, v143\n"
"v_dot2_f32_bf16 v136, v252, s70, v136\n"
"v_dot2_f32_bf16 v137, v252, s71, v137\n"
"v_dot2_f32_bf16 v138, v253, s70, v138\n"
"v_dot2_f32_bf16 v139, v253, s71, v139\n"
"v_dot2_f32_bf16 v140, v254, s70, v140\n"
"v_dot2_f32_bf16 v141, v254, s71, v141\n"
"v_dot2_f32_bf16 v142, v255, s70, v142\n"
"v_dot2_f32_bf16 v143, v255, s71, v143\n"
"s_waitcnt vmcnt(7)\n"
"v_accvgpr_read_b32 v240, a224\n"
"v_accvgpr_read_b32 v241, a225\n"
"v_accvgpr_read_b32 v242, a226\n"
"v_accvgpr_read_b32 v243, a227\n"
"s_waitcnt vmcnt(6)\n"
"v_accvgpr_read_b32 v244, a228\n"
"v_accvgpr_read_b32 v245, a229\n"
"v_accvgpr_read_b32 v246, a230\n"
"v_accvgpr_read_b32 v247, a231\n"
"s_waitcnt vmcnt(5)\n"
"v_accvgpr_read_b32 v248, a232\n"
"v_accvgpr_read_b32 v249, a233\n"
"v_accvgpr_read_b32 v250, a234\n"
"v_accvgpr_read_b32 v251, a235\n"
"s_waitcnt vmcnt(4)\n"
"v_accvgpr_read_b32 v252, a236\n"
"v_accvgpr_read_b32 v253, a237\n"
"v_accvgpr_read_b32 v254, a238\n"
"v_accvgpr_read_b32 v255, a239\n"
"L_x1v2c%=:\n"
"v_max3_u32 v4, v240, v241, v242\n"
"v_max3_u32 v5, v243, v244, v245\n"
"v_max3_u32 v4, v4, v246, v247\n"
"v_max3_u32 v5, v5, v248, v249\n"
"v_max3_u32 v4, v4, v250, v251\n"
"v_max3_u32 v5, v5, v252, v253\n"
"v_max3_u32 v4, v4, v254, v255\n"
"v_max_u32 v4, v4, v5\n"
"v_cmp_eq_u32 vcc, -1, v4\n"
"s_nop 0\n"
"s_cbranch_vccnz L_x1v2r%=\n"
"L_x1v2d%=:\n"
"v_dot2_f32_bf16 v144, v240, s70, v144\n"
"v_dot2_f32_bf16 v145, v240, s71, v145\n"
"v_dot2_f32_bf16 v146, v241, s70, v146\n"
"v_dot2_f32_bf16 v147, v241, s71, v147\n"
"v_dot2_f32_bf16 v148, v242, s70, v148\n"
"v_dot2_f32_bf16 v149, v242, s71, v149\n"
"v_dot2_f32_bf16 v150, v243, s70, v150\n"
"v_dot2_f32_bf16 v151, v243, s71, v151\n"
"v_dot2_f32_bf16 v144, v244, s70, v144\n"
"v_dot2_f32_bf16 v145, v244, s71, v145\n"
"v_dot2_f32_bf16 v146, v245, s70, v146\n"
"v_dot2_f32_bf16 v147, v245, s71, v147\n"
"v_dot2_f32_bf16 v148, v246, s70, v148\n"
"v_dot2_f32_bf16 v149, v246, s71, v149\n"
"v_dot2_f32_bf16 v150, v247, s70, v150\n"
"v_dot2_f32_bf16 v151, v247, s71, v151\n"
"v_dot2_f32_bf16 v144, v248, s70, v144\n"
"v_dot2_f32_bf16 v145, v248, s71, v145\n"
"v_dot2_f32_bf16 v146, v249, s70, v146\n"
"v_dot2_f32_bf16 v147, v249, s71, v147\n"
"v_dot2_f32_bf16 v148, v250, s70, v148\n"
"v_dot2_f32_bf16 v149, v250, s71, v149\n"
"v_dot2_f32_bf16 v150, v251, s70, v150\n"
"v_dot2_f32_bf16 v151, v251, s71, v151\n"
"v_dot2_f32_bf16 v144, v252, s70, v144\n"
"v_dot2_f32_bf16 v145, v252, s71, v145\n"
"v_dot2_f32_bf16 v146, v253, s70, v146\n"
"v_dot2_f32_bf16 v147, v253, s71, v147\n"
"v_dot2_f32_bf16 v148, v254, s70, v148\n"
"v_dot2_f32_bf16 v149, v254, s71, v149\n"
"v_dot2_f32_bf16 v150, v255, s70, v150\n"
"v_dot2_f32_bf16 v151, v255, s71, v151\n"
"v_permlane32_swap_b32 v128, v144\n"
"v_permlane32_swap_b32 v129, v145\n"
"v_permlane32_swap_b32 v130, v146\n"
"v_permlane32_swap_b32 v131, v147\n"
"v_permlane32_swap_b32 v132, v148\n"
"v_permlane32_swap_b32 v133, v149\n"
"v_permlane32_swap_b32 v134, v150\n"
"v_permlane32_swap_b32 v135, v151\n"
"v_pk_add_f32 v[128:129], v[128:129], v[144:145]\n"
"v_pk_add_f32 v[130:131], v[130:131], v[146:147]\n"
"v_pk_add_f32 v[132:133], v[132:133], v[148:149]\n"
"v_pk_add_f32 v[134:135], v[134:135], v[150:151]\n"
"s_waitcnt lgkmcnt(11)\n"
"v_pk_fma_f32 v[160:161], v[128:129], v[192:193], v[160:161]\n"
"v_pk_fma_f32 v[162:163], v[130:131], v[194:195], v[162:163]\n"
"s_waitcnt lgkmcnt(10)\n"
"v_pk_fma_f32 v[164:165], v[132:133], v[196:197], v[164:165]\n"
"v_pk_fma_f32 v[166:167], v[134:135], v[198:199], v[166:167]\n"
"v_pk_add_f32 v[186:187], v[160:161], 0\n"
"v_pk_mul_f32 v[188:189], v[160:161], v[160:161]\n"
"v_pk_add_f32 v[186:187], v[186:187], v[162:163]\n"
"v_pk_fma_f32 v[188:189], v[162:163], v[162:163], v[188:189]\n"
"v_pk_add_f32 v[186:187], v[186:187], v[164:165]\n"
"v_pk_fma_f32 v[188:189], v[164:165], v[164:165], v[188:189]\n"
"v_pk_add_f32 v[186:187], v[186:187], v[166:167]\n"
"v_pk_fma_f32 v[188:189], v[166:167], v[166:167], v[188:189]\n"
"v_accvgpr_write_b32 a0, v160\n"
"v_accvgpr_write_b32 a1, v161\n"
"v_accvgpr_write_b32 a2, v162\n"
"v_accvgpr_write_b32 a3, v163\n"
"v_accvgpr_write_b32 a4, v164\n"
"v_accvgpr_write_b32 a5, v165\n"
"v_accvgpr_write_b32 a6, v166\n"
"v_accvgpr_write_b32 a7, v167\n"
"; p3\n"
"L_x1v3c%=:\n"
"s_waitcnt vmcnt(3)\n"
"v_max3_u32 v4, v108, v109, v110\n"
"s_waitcnt vmcnt(2)\n"
"v_max3_u32 v5, v111, v112, v113\n"
"v_max3_u32 v4, v4, v114, v115\n"
"s_waitcnt vmcnt(1)\n"
"v_max3_u32 v5, v5, v116, v117\n"
"v_max3_u32 v4, v4, v118, v119\n"
"s_waitcnt vmcnt(0)\n"
"v_max3_u32 v5, v5, v120, v121\n"
"v_max3_u32 v4, v4, v122, v123\n"
"v_max_u32 v4, v4, v5\n"
"v_cmp_eq_u32 vcc, -1, v4\n"
"s_nop 0\n"
"s_cbranch_vccnz L_x1v3r%=\n"
"L_x1v3d%=:\n"
"v_dot2_f32_bf16 v152, v108, s70, v152\n"
"v_dot2_f32_bf16 v153, v108, s71, v153\n"
"v_dot2_f32_bf16 v154, v109, s70, v154\n"
"v_dot2_f32_bf16 v155, v109, s71, v155\n"
"v_dot2_f32_bf16 v156, v110, s70, v156\n"
"v_dot2_f32_bf16 v157, v110, s71, v157\n"
"v_dot2_f32_bf16 v158, v111, s70, v158\n"
"v_dot2_f32_bf16 v159, v111, s71, v159\n"
"v_dot2_f32_bf16 v152, v112, s70, v152\n"
"v_dot2_f32_bf16 v153, v112, s71, v153\n"
"v_dot2_f32_bf16 v154, v113, s70, v154\n"
"v_dot2_f32_bf16 v155, v113, s71, v155\n"
"v_dot2_f32_bf16 v156, v114, s70, v156\n"
"v_dot2_f32_bf16 v157, v114, s71, v157\n"
"v_dot2_f32_bf16 v158, v115, s70, v158\n"
"v_dot2_f32_bf16 v159, v115, s71, v159\n"
"v_dot2_f32_bf16 v152, v116, s70, v152\n"
"v_dot2_f32_bf16 v153, v116, s71, v153\n"
"v_dot2_f32_bf16 v154, v117, s70, v154\n"
"v_dot2_f32_bf16 v155, v117, s71, v155\n"
"v_dot2_f32_bf16 v156, v118, s70, v156\n"
"v_dot2_f32_bf16 v157, v118, s71, v157\n"
"v_dot2_f32_bf16 v158, v119, s70, v158\n"
"v_dot2_f32_bf16 v159, v119, s71, v159\n"
"v_dot2_f32_bf16 v152, v120, s70, v152\n"
"v_dot2_f32_bf16 v153, v120, s71, v153\n"
"v_dot2_f32_bf16 v154, v121, s70, v154\n"
"v_dot2_f32_bf16 v155, v121, s71, v155\n"
"v_dot2_f32_bf16 v156, v122, s70, v156\n"
"v_dot2_f32_bf16 v157, v122, s71, v157\n"
"v_dot2_f32_bf16 v158, v123, s70, v158\n"
"v_dot2_f32_bf16 v159, v123, s71, v159\n"
"v_permlane32_swap_b32 v136, v152\n"
"v_permlane32_swap_b32 v137, v153\n"
"v_permlane32_swap_b32 v138, v154\n"
"v_permlane32_swap_b32 v139, v155\n"
"v_permlane32_swap_b32 v140, v156\n"
"v_permlane32_swap_b32 v141, v157\n"
"v_permlane32_swap_b32 v142, v158\n"
"v_permlane32_swap_b32 v143, v159\n"
"v_pk_add_f32 v[136:137], v[136:137], v[152:153]\n"
"v_pk_add_f32 v[138:139], v[138:139], v[154:155]\n"
"v_pk_add_f32 v[140:141], v[140:141], v[156:157]\n"
"v_pk_add_f32 v[142:143], v[142:143], v[158:159]\n"
"s_waitcnt lgkmcnt(9)\n"
"v_pk_fma_f32 v[168:169], v[136:137], v[200:201], v[168:169]\n"
"v_pk_fma_f32 v[170:171], v[138:139], v[202:203], v[170:171]\n"
"s_waitcnt lgkmcnt(8)\n"
"v_pk_fma_f32 v[172:173], v[140:141], v[204:205], v[172:173]\n"
"v_pk_fma_f32 v[174:175], v[142:143], v[206:207], v[174:175]\n"
"v_pk_add_f32 v[186:187], v[186:187], v[168:169]\n"
"v_pk_fma_f32 v[188:189], v[168:169], v[168:169], v[188:189]\n"
"v_pk_add_f32 v[186:187], v[186:187], v[170:171]\n"
"v_pk_fma_f32 v[188:189], v[170:171], v[170:171], v[188:189]\n"
"v_pk_add_f32 v[186:187], v[186:187], v[172:173]\n"
"v_pk_fma_f32 v[188:189], v[172:173], v[172:173], v[188:189]\n"
"v_pk_add_f32 v[186:187], v[186:187], v[174:175]\n"
"v_pk_fma_f32 v[188:189], v[174:175], v[174:175], v[188:189]\n"
"v_add_f32 v186, v186, v187\n"
"v_add_f32 v187, v188, v189\n"
"v_accvgpr_write_b32 a8, v168\n"
"v_add_f32_dpp v186, v186, v186 row_ror:8 row_mask:0xf bank_mask:0xf\n"
"v_add_f32_dpp v187, v187, v187 row_ror:8 row_mask:0xf bank_mask:0xf\n"
"v_mov_b32 v188, v186\n"
"v_mov_b32 v189, v187\n"
"v_accvgpr_write_b32 a9, v169\n"
"v_permlane16_swap_b32 v186, v188\n"
"v_permlane16_swap_b32 v187, v189\n"
"v_accvgpr_write_b32 a10, v170\n"
"v_add_f32 v186, v186, v188\n"
"v_add_f32 v187, v187, v189\n"
"v_accvgpr_write_b32 a11, v171\n"
"v_mov_b32 v188, v186\n"
"v_mov_b32 v189, v187\n"
"v_accvgpr_write_b32 a12, v172\n"
"v_permlane32_swap_b32 v186, v188\n"
"v_permlane32_swap_b32 v187, v189\n"
"v_accvgpr_write_b32 a13, v173\n"
"v_add_f32 v186, v186, v188\n"
"v_add_f32 v187, v187, v189\n"
"v_accvgpr_write_b32 a14, v174\n"
"ds_write_b64 v185, v[186:187]\n"
"v_accvgpr_write_b32 a15, v175\n"
"s_lshl_b32 s11, s81, 16\n"
"s_add_u32 s11, s11, 0x1400000\n"
"s_add_u32 s12, s83, s7\n"
"s_add_u32 s13, s12, 0\n"
"s_and_b32 s13, s13, 15\n"
"s_lshl_b32 s13, s13, 9\n"
"s_add_u32 s89, s11, s13\n"
"s_add_u32 s13, s12, 4\n"
"s_and_b32 s13, s13, 15\n"
"s_lshl_b32 s13, s13, 9\n"
"s_add_u32 s90, s11, s13\n"
"s_add_u32 s13, s12, 8\n"
"s_and_b32 s13, s13, 15\n"
"s_lshl_b32 s13, s13, 9\n"
"s_add_u32 s91, s11, s13\n"
"s_add_u32 s13, s12, 12\n"
"s_and_b32 s13, s13, 15\n"
"s_lshl_b32 s13, s13, 9\n"
"s_add_u32 s92, s11, s13\n"
"s_waitcnt lgkmcnt(0)\n"
"s_barrier\n"
"ds_read_b128 v[148:151], v184\n"
"ds_read_b128 v[152:155], v184 offset:16\n"
"s_waitcnt lgkmcnt(1)\n"
"v_pk_add_f32 v[148:149], v[148:149], v[150:151]\n"
"s_waitcnt lgkmcnt(0)\n"
"v_pk_add_f32 v[152:153], v[152:153], v[154:155]\n"
"v_pk_add_f32 v[148:149], v[148:149], v[152:153]\n"
"v_mul_f32 v148, 0x3b000000, v148\n"
"v_mul_f32 v150, v148, v148\n"
"v_fma_f32 v150, v149, s73, -v150\n"
"v_max_f32 v150, 0, v150\n"
"v_add_f32 v150, 0x3727c5ac, v150\n"
"v_rsq_f32 v156, v150\n"
"s_nop 0\n"
"v_mul_f32 v157, -v148, v156\n"
"v_pk_fma_f32 v[160:161], v[160:161], v[156:157], v[156:157] op_sel:[0,0,1] op_sel_hi:[1,0,1]\n"
"v_pk_fma_f32 v[162:163], v[162:163], v[156:157], v[156:157] op_sel:[0,0,1] op_sel_hi:[1,0,1]\n"
"v_pk_fma_f32 v[164:165], v[164:165], v[156:157], v[156:157] op_sel:[0,0,1] op_sel_hi:[1,0,1]\n"
"v_pk_fma_f32 v[166:167], v[166:167], v[156:157], v[156:157] op_sel:[0,0,1] op_sel_hi:[1,0,1]\n"
"v_pk_fma_f32 v[168:169], v[168:169], v[156:157], v[156:157] op_sel:[0,0,1] op_sel_hi:[1,0,1]\n"
"v_pk_fma_f32 v[170:171], v[170:171], v[156:157], v[156:157] op_sel:[0,0,1] op_sel_hi:[1,0,1]\n"
"v_pk_fma_f32 v[172:173], v[172:173], v[156:157], v[156:157] op_sel:[0,0,1] op_sel_hi:[1,0,1]\n"
"v_pk_fma_f32 v[174:175], v[174:175], v[156:157], v[156:157] op_sel:[0,0,1] op_sel_hi:[1,0,1]\n"
"v_pk_fma_f32 v[160:161], v[160:161], v[208:209], v[224:225]\n"
"v_pk_fma_f32 v[162:163], v[162:163], v[210:211], v[226:227]\n"
"v_pk_fma_f32 v[164:165], v[164:165], v[212:213], v[228:229]\n"
"v_pk_fma_f32 v[166:167], v[166:167], v[214:215], v[230:231]\n"
"v_pk_fma_f32 v[168:169], v[168:169], v[216:217], v[232:233]\n"
"v_pk_fma_f32 v[170:171], v[170:171], v[218:219], v[234:235]\n"
"v_pk_fma_f32 v[172:173], v[172:173], v[220:221], v[236:237]\n"
"v_pk_fma_f32 v[174:175], v[174:175], v[222:223], v[238:239]\n"
"v_cvt_pk_bf16_f32 v160, v160, v161\n"
"v_cvt_pk_bf16_f32 v161, v162, v163\n"
"v_cvt_pk_bf16_f32 v162, v164, v165\n"
"v_cvt_pk_bf16_f32 v163, v166, v167\n"
"v_cvt_pk_bf16_f32 v164, v168, v169\n"
"v_cvt_pk_bf16_f32 v165, v170, v171\n"
"v_cvt_pk_bf16_f32 v166, v172, v173\n"
"v_cvt_pk_bf16_f32 v167, v174, v175\n"
"buffer_store_dwordx4 v[160:163], v7, s[28:31], s86 offen sc1\n"
"buffer_store_dwordx4 v[164:167], v7, s[28:31], s86 offen offset:2048 sc1\n"
"s_cmp_eq_u32 s80, 7\n"
"s_cbranch_scc1 L_skip%=\n"
"v_lshrrev_b32 v4, 5, v6\n"
"v_and_b32 v5, 0x1ff, v1\n"
"v_lshl_add_u32 v7, v4, 13, v5\n"
"v_bfe_u32 v5, v6, 3, 2\n"
"v_lshlrev_b32 v5, 8, v5\n"
"v_and_b32 v4, 7, v6\n"
"v_lshl_add_u32 v5, v4, 4, v5\n"
"v_lshrrev_b32 v4, 5, v6\n"
"v_lshl_add_u32 v5, v4, 7, v5\n"
"s_lshl_b32 s11, s7, 12\n"
"v_add_u32 v9, s11, v5\n"
"s_or_b32 s11, s6, s7\n"
"s_cmp_eq_u32 s11, 0\n"
"s_cbranch_scc1 L_eu%=\n"
"L_eud%=:\n"
"s_add_u32 s64, s64, 1\n"
"s_and_b32 s64, s64, 15\n"
"s_cselect_b32 s66, s65, 0xffe5c000\n"
"s_cselect_b32 s67, 0, -1\n"
"v_lshl_add_u64 v[2:3], v[2:3], 0, s[66:67]\n"
"v_lshl_add_u64 v[10:11], v[10:11], 0, s[66:67]\n"
"v_lshl_add_u64 v[12:13], v[12:13], 0, s[66:67]\n"
"v_lshl_add_u64 v[14:15], v[14:15], 0, s[66:67]\n"
"s_add_u32 m0, s79, 0x18000\n"
"s_nop 0\n"
"global_load_lds_dwordx4 v[2:3], off sc1\n"
"s_add_u32 m0, s79, 0x17600\n"
"s_nop 0\n"
"global_load_lds_dwordx4 v[2:3], off offset:3584 sc1\n"
"s_add_u32 m0, s79, 0x1a000\n"
"s_nop 0\n"
"global_load_lds_dwordx4 v[10:11], off sc1\n"
"s_add_u32 m0, s79, 0x19600\n"
"s_nop 0\n"
"global_load_lds_dwordx4 v[10:11], off offset:3584 sc1\n"
"s_add_u32 m0, s79, 0x1c000\n"
"s_nop 0\n"
"global_load_lds_dwordx4 v[12:13], off sc1\n"
"s_add_u32 m0, s79, 0x1b600\n"
"s_nop 0\n"
"global_load_lds_dwordx4 v[12:13], off offset:3584 sc1\n"
"s_add_u32 m0, s79, 0x1e000\n"
"s_nop 0\n"
"global_load_lds_dwordx4 v[14:15], off sc1\n"
"s_add_u32 m0, s79, 0x1d600\n"
"s_nop 0\n"
"global_load_lds_dwordx4 v[14:15], off offset:3584 sc1\n"
"ds_read_b128 v[96:99], v0\n"
"ds_read_b128 v[100:103], v0 offset:1024\n"
"ds_read_b128 v[104:107], v0 offset:8192\n"
"ds_read_b128 v[108:111], v0 offset:9216\n"
"ds_read_b128 v[112:115], v0 offset:16384\n"
"ds_read_b128 v[116:119], v0 offset:17408\n"
"ds_read_b128 v[120:123], v0 offset:24576\n"
"ds_read_b128 v[124:127], v0 offset:25600\n"
"s_sleep 7\n"
"s_add_u32 s77, s89, 0\n"
"buffer_load_dwordx4 v[192:195], v7, s[28:31], s77 offen sc1\n"
"s_add_u32 s77, s89, 0x4000\n"
"buffer_load_dwordx4 v[196:199], v7, s[28:31], s77 offen sc1\n"
"s_add_u32 s77, s89, 0x8000\n"
"buffer_load_dwordx4 v[200:203], v7, s[28:31], s77 offen sc1\n"
"s_add_u32 s77, s89, 0xc000\n"
"buffer_load_dwordx4 v[204:207], v7, s[28:31], s77 offen sc1\n"
"L_h0c%=:\n"
"s_waitcnt vmcnt(3)\n"
"v_max3_u32 v4, v192, v193, v194\n"
"s_waitcnt vmcnt(2)\n"
"v_max3_u32 v5, v195, v196, v197\n"
"v_max3_u32 v4, v4, v198, v199\n"
"s_waitcnt vmcnt(1)\n"
"v_max3_u32 v5, v5, v200, v201\n"
"v_max3_u32 v4, v4, v202, v203\n"
"s_waitcnt vmcnt(0)\n"
"v_max3_u32 v5, v5, v204, v205\n"
"v_max3_u32 v4, v4, v206, v207\n"
"v_max_u32 v4, v4, v5\n"
"v_cmp_eq_u32 vcc, -1, v4\n"
"s_nop 0\n"
"s_cbranch_vccnz L_h0r%=\n"
"L_h0d%=:\n"
"s_add_u32 s77, s90, 0\n"
"buffer_load_dwordx4 a[64:67], v7, s[28:31], s77 offen sc1\n"
"s_add_u32 s77, s90, 0x4000\n"
"buffer_load_dwordx4 a[68:71], v7, s[28:31], s77 offen sc1\n"
"s_add_u32 s77, s90, 0x8000\n"
"buffer_load_dwordx4 a[72:75], v7, s[28:31], s77 offen sc1\n"
"s_add_u32 s77, s90, 0xc000\n"
"buffer_load_dwordx4 a[76:79], v7, s[28:31], s77 offen sc1\n"
"ds_write_b128 v9, v[192:195]\n"
"ds_write_b128 v9, v[196:199] offset:1024\n"
"ds_write_b128 v9, v[200:203] offset:2048\n"
"ds_write_b128 v9, v[204:207] offset:3072\n"
"s_waitcnt lgkmcnt(0)\n"
"s_barrier\n"
"ds_read_b128 a[224:227], v1\n"
"ds_read_b128 a[228:231], v1 offset:1024\n"
"ds_read_b128 a[232:235], v1 offset:2048\n"
"ds_read_b128 a[236:239], v1 offset:3072\n"
"v_perm_b32 v16, v36, v32, s33\n"
"v_perm_b32 v17, v44, v40, s33\n"
"v_perm_b32 v18, v52, v48, s33\n"
"v_perm_b32 v19, v60, v56, s33\n"
"v_perm_b32 v20, v36, v32, s34\n"
"v_perm_b32 v21, v44, v40, s34\n"
"v_perm_b32 v22, v52, v48, s34\n"
"v_perm_b32 v23, v60, v56, s34\n"
"; f ks=0\n"
"s_waitcnt lgkmcnt(0)\n"
"v_mfma_f32_16x16x32_bf16 v[128:131], v[16:19], a[224:227], 0\n"
"v_perm_b32 v24, v37, v33, s33\n"
"v_mfma_f32_16x16x32_bf16 v[132:135], v[16:19], a[228:231], 0\n"
"v_perm_b32 v25, v45, v41, s33\n"
"s_add_u32 s77, s91, 0\n"
"v_mfma_f32_16x16x32_bf16 v[136:139], v[16:19], a[232:235], 0\n"
"buffer_load_dwordx4 a[80:83], v7, s[28:31], s77 offen sc1\n"
"v_perm_b32 v26, v53, v49, s33\n"
"v_mfma_f32_16x16x32_bf16 v[140:143], v[16:19], a[236:239], 0\n"
"v_perm_b32 v27, v61, v57, s33\n"
"s_add_u32 s77, s91, 0x4000\n"
"v_mfma_f32_16x16x32_bf16 v[144:147], v[20:23], a[224:227], 0\n"
"buffer_load_dwordx4 a[84:87], v7, s[28:31], s77 offen sc1\n"
"v_perm_b32 v28, v37, v33, s34\n"
"v_mfma_f32_16x16x32_bf16 v[148:151], v[20:23], a[228:231], 0\n"
"v_perm_b32 v29, v45, v41, s34\n"
"s_add_u32 s77, s91, 0x8000\n"
"v_mfma_f32_16x16x32_bf16 v[152:155], v[20:23], a[232:235], 0\n"
"buffer_load_dwordx4 a[88:91], v7, s[28:31], s77 offen sc1\n"
"v_perm_b32 v30, v53, v49, s34\n"
"v_mfma_f32_16x16x32_bf16 v[156:159], v[20:23], a[236:239], 0\n"
"v_perm_b32 v31, v61, v57, s34\n"
"s_add_u32 s77, s91, 0xc000\n"
"v_mfma_f32_16x16x32_bf16 v[160:163], v[24:27], a[224:227], 0\n"
"buffer_load_dwordx4 a[92:95], v7, s[28:31], s77 offen sc1\n"
"v_perm_b32 v16, v38, v34, s33\n"
"v_mfma_f32_16x16x32_bf16 v[164:167], v[24:27], a[228:231], 0\n"
"ds_read_b128 a[240:243], v1 offset:4096\n"
"v_perm_b32 v17, v46, v42, s33\n"
"s_add_u32 s77, s92, 0\n"
"v_mfma_f32_16x16x32_bf16 v[168:171], v[24:27], a[232:235], 0\n"
"buffer_load_dwordx4 a[96:99], v7, s[28:31], s77 offen sc1\n"
"v_perm_b32 v18, v54, v50, s33\n"
"v_mfma_f32_16x16x32_bf16 v[172:175], v[24:27], a[236:239], 0\n"
"v_perm_b32 v19, v62, v58, s33\n"
"s_add_u32 s77, s92, 0x4000\n"
"v_mfma_f32_16x16x32_bf16 v[176:179], v[28:31], a[224:227], 0\n"
"buffer_load_dwordx4 a[100:103], v7, s[28:31], s77 offen sc1\n"
"v_perm_b32 v20, v38, v34, s34\n"
"v_mfma_f32_16x16x32_bf16 v[180:183], v[28:31], a[228:231], 0\n"
"ds_read_b128 a[244:247], v1 offset:5120\n"
"v_perm_b32 v21, v46, v42, s34\n"
"s_add_u32 s77, s92, 0x8000\n"
"v_mfma_f32_16x16x32_bf16 v[184:187], v[28:31], a[232:235], 0\n"
"buffer_load_dwordx4 a[104:107], v7, s[28:31], s77 offen sc1\n"
"v_perm_b32 v22, v54, v50, s34\n"
"v_mfma_f32_16x16x32_bf16 v[188:191], v[28:31], a[236:239], 0\n"
"v_perm_b32 v23, v62, v58, s34\n"
"s_add_u32 s77, s92, 0xc000\n"
"v_mfma_f32_16x16x32_bf16 v[192:195], v[16:19], a[224:227], 0\n"
"buffer_load_dwordx4 a[108:111], v7, s[28:31], s77 offen sc1\n"
"v_perm_b32 v24, v39, v35, s33\n"
"v_mfma_f32_16x16x32_bf16 v[196:199], v[16:19], a[228:231], 0\n"
"ds_read_b128 a[248:251], v1 offset:6144\n"
"v_perm_b32 v25, v47, v43, s33\n"
"v_mfma_f32_16x16x32_bf16 v[200:203], v[16:19], a[232:235], 0\n"
"v_perm_b32 v26, v55, v51, s33\n"
"v_mfma_f32_16x16x32_bf16 v[204:207], v[16:19], a[236:239], 0\n"
"v_perm_b32 v27, v63, v59, s33\n"
"v_mfma_f32_16x16x32_bf16 v[208:211], v[20:23], a[224:227], 0\n"
"v_perm_b32 v28, v39, v35, s34\n"
"v_mfma_f32_16x16x32_bf16 v[212:215], v[20:23], a[228:231], 0\n"
"ds_read_b128 a[252:255], v1 offset:7168\n"
"v_perm_b32 v29, v47, v43, s34\n"
"v_mfma_f32_16x16x32_bf16 v[216:219], v[20:23], a[232:235], 0\n"
"v_perm_b32 v30, v55, v51, s34\n"
"v_mfma_f32_16x16x32_bf16 v[220:223], v[20:23], a[236:239], 0\n"
"v_perm_b32 v31, v63, v59, s34\n"
"v_mfma_f32_16x16x32_bf16 v[224:227], v[24:27], a[224:227], 0\n"
"ds_read_b128 v[32:35], v0 offset:32768\n"
"v_perm_b32 v16, v68, v64, s33\n"
"v_mfma_f32_16x16x32_bf16 v[228:231], v[24:27], a[228:231], 0\n"
"v_perm_b32 v17, v76, v72, s33\n"
"v_mfma_f32_16x16x32_bf16 v[232:235], v[24:27], a[232:235], 0\n"
"v_perm_b32 v18, v84, v80, s33\n"
"v_mfma_f32_16x16x32_bf16 v[236:239], v[24:27], a[236:239], 0\n"
"ds_read_b128 v[36:39], v0 offset:33792\n"
"v_perm_b32 v19, v92, v88, s33\n"
"v_mfma_f32_16x16x32_bf16 v[240:243], v[28:31], a[224:227], 0\n"
"v_perm_b32 v20, v68, v64, s34\n"
"v_mfma_f32_16x16x32_bf16 v[244:247], v[28:31], a[228:231], 0\n"
"v_perm_b32 v21, v76, v72, s34\n"
"v_mfma_f32_16x16x32_bf16 v[248:251], v[28:31], a[232:235], 0\n"
"ds_read_b128 v[40:43], v0 offset:40960\n"
"v_perm_b32 v22, v84, v80, s34\n"
"v_mfma_f32_16x16x32_bf16 v[252:255], v[28:31], a[236:239], 0\n"
"v_perm_b32 v23, v92, v88, s34\n"
"; f ks=1\n"
"s_waitcnt lgkmcnt(3)\n"
"v_mfma_f32_16x16x32_bf16 v[128:131], v[16:19], a[240:243], v[128:131]\n"
"v_perm_b32 v24, v69, v65, s33\n"
"s_waitcnt vmcnt(11)\n"
"v_accvgpr_read_b32 v4, a64\n"
"v_mfma_f32_16x16x32_bf16 v[132:135], v[16:19], a[244:247], v[132:135]\n"
"ds_read_b128 v[44:47], v0 offset:41984\n"
"v_perm_b32 v25, v77, v73, s33\n"
"v_mfma_f32_16x16x32_bf16 v[136:139], v[16:19], a[248:251], v[136:139]\n"
"v_perm_b32 v26, v85, v81, s33\n"
"v_accvgpr_read_b32 v5, a65\n"
"v_mfma_f32_16x16x32_bf16 v[140:143], v[16:19], a[252:255], v[140:143]\n"
"v_perm_b32 v27, v93, v89, s33\n"
"v_max_u32 v6, v4, v5\n"
"v_mfma_f32_16x16x32_bf16 v[144:147], v[20:23], a[240:243], v[144:147]\n"
"ds_read_b128 v[48:51], v0 offset:49152\n"
"v_perm_b32 v28, v69, v65, s34\n"
"v_mfma_f32_16x16x32_bf16 v[148:151], v[20:23], a[244:247], v[148:151]\n"
"v_perm_b32 v29, v77, v73, s34\n"
"v_accvgpr_read_b32 v4, a66\n"
"v_mfma_f32_16x16x32_bf16 v[152:155], v[20:23], a[248:251], v[152:155]\n"
"v_perm_b32 v30, v85, v81, s34\n"
"v_accvgpr_read_b32 v5, a67\n"
"v_mfma_f32_16x16x32_bf16 v[156:159], v[20:23], a[252:255], v[156:159]\n"
"ds_read_b128 v[52:55], v0 offset:50176\n"
"v_perm_b32 v31, v93, v89, s34\n"
"v_mfma_f32_16x16x32_bf16 v[160:163], v[24:27], a[240:243], v[160:163]\n"
"v_perm_b32 v16, v70, v66, s33\n"
"v_max3_u32 v6, v6, v4, v5\n"
"v_mfma_f32_16x16x32_bf16 v[164:167], v[24:27], a[244:247], v[164:167]\n"
"ds_read_b128 a[224:227], v1 offset:8192\n"
"v_perm_b32 v17, v78, v74, s33\n"
"v_mfma_f32_16x16x32_bf16 v[168:171], v[24:27], a[248:251], v[168:171]\n"
"ds_read_b128 v[56:59], v0 offset:57344\n"
"v_perm_b32 v18, v86, v82, s33\n"
"v_mfma_f32_16x16x32_bf16 v[172:175], v[24:27], a[252:255], v[172:175]\n"
"v_perm_b32 v19, v94, v90, s33\n"
"s_waitcnt vmcnt(10)\n"
"v_accvgpr_read_b32 v4, a68\n"
"v_mfma_f32_16x16x32_bf16 v[176:179], v[28:31], a[240:243], v[176:179]\n"
"v_perm_b32 v20, v70, v66, s34\n"
"v_accvgpr_read_b32 v5, a69\n"
"v_mfma_f32_16x16x32_bf16 v[180:183], v[28:31], a[244:247], v[180:183]\n"
"ds_read_b128 v[60:63], v0 offset:58368\n"
"v_perm_b32 v21, v78, v74, s34\n"
"v_mfma_f32_16x16x32_bf16 v[184:187], v[28:31], a[248:251], v[184:187]\n"
"ds_read_b128 a[228:231], v1 offset:9216\n"
"v_perm_b32 v22, v86, v82, s34\n"
"v_mfma_f32_16x16x32_bf16 v[188:191], v[28:31], a[252:255], v[188:191]\n"
"v_perm_b32 v23, v94, v90, s34\n"
"s_add_u32 s64, s64, 1\n"
"s_and_b32 s64, s64, 15\n"
"s_cselect_b32 s66, s65, 0xffe5c000\n"
"s_cselect_b32 s67, 0, -1\n"
"v_mfma_f32_16x16x32_bf16 v[192:195], v[16:19], a[240:243], v[192:195]\n"
"v_perm_b32 v24, v71, v67, s33\n"
"v_max3_u32 v6, v6, v4, v5\n"
"v_mfma_f32_16x16x32_bf16 v[196:199], v[16:19], a[244:247], v[196:199]\n"
"ds_read_b128 a[232:235], v1 offset:10240\n"
"v_perm_b32 v25, v79, v75, s33\n"
"v_lshl_add_u64 v[2:3], v[2:3], 0, s[66:67]\n"
"v_mfma_f32_16x16x32_bf16 v[200:203], v[16:19], a[248:251], v[200:203]\n"
"v_perm_b32 v26, v87, v83, s33\n"
"v_accvgpr_read_b32 v4, a70\n"
"v_mfma_f32_16x16x32_bf16 v[204:207], v[16:19], a[252:255], v[204:207]\n"
"v_perm_b32 v27, v95, v91, s33\n"
"v_lshl_add_u64 v[10:11], v[10:11], 0, s[66:67]\n"
"v_mfma_f32_16x16x32_bf16 v[208:211], v[20:23], a[240:243], v[208:211]\n"
"v_perm_b32 v28, v71, v67, s34\n"
"v_accvgpr_read_b32 v5, a71\n"
"v_mfma_f32_16x16x32_bf16 v[212:215], v[20:23], a[244:247], v[212:215]\n"
"ds_read_b128 a[236:239], v1 offset:11264\n"
"v_perm_b32 v29, v79, v75, s34\n"
"v_lshl_add_u64 v[12:13], v[12:13], 0, s[66:67]\n"
"v_mfma_f32_16x16x32_bf16 v[216:219], v[20:23], a[248:251], v[216:219]\n"
"v_perm_b32 v30, v87, v83, s34\n"
"v_max3_u32 v6, v6, v4, v5\n"
"v_mfma_f32_16x16x32_bf16 v[220:223], v[20:23], a[252:255], v[220:223]\n"
"v_perm_b32 v31, v95, v91, s34\n"
"v_lshl_add_u64 v[14:15], v[14:15], 0, s[66:67]\n"
"v_mfma_f32_16x16x32_bf16 v[224:227], v[24:27], a[240:243], v[224:227]\n"
"global_load_dwordx4 v[64:67], v[2:3], off sc1\n"
"v_perm_b32 v16, v100, v96, s33\n"
"v_mfma_f32_16x16x32_bf16 v[228:231], v[24:27], a[244:247], v[228:231]\n"
"v_perm_b32 v17, v108, v104, s33\n"
"s_waitcnt vmcnt(10)\n"
"v_accvgpr_read_b32 v4, a72\n"
"v_mfma_f32_16x16x32_bf16 v[232:235], v[24:27], a[248:251], v[232:235]\n"
"v_perm_b32 v18, v116, v112, s33\n"
"v_accvgpr_read_b32 v5, a73\n"
"v_mfma_f32_16x16x32_bf16 v[236:239], v[24:27], a[252:255], v[236:239]\n"
"global_load_dwordx4 v[68:71], v[2:3], off offset:3584 sc1\n"
"v_perm_b32 v19, v124, v120, s33\n"
"v_mfma_f32_16x16x32_bf16 v[240:243], v[28:31], a[240:243], v[240:243]\n"
"v_perm_b32 v20, v100, v96, s34\n"
"v_max3_u32 v6, v6, v4, v5\n"
"v_mfma_f32_16x16x32_bf16 v[244:247], v[28:31], a[244:247], v[244:247]\n"
"v_perm_b32 v21, v108, v104, s34\n"
"v_accvgpr_read_b32 v4, a74\n"
"v_mfma_f32_16x16x32_bf16 v[248:251], v[28:31], a[248:251], v[248:251]\n"
"global_load_dwordx4 v[72:75], v[10:11], off sc1\n"
"v_perm_b32 v22, v116, v112, s34\n"
"v_mfma_f32_16x16x32_bf16 v[252:255], v[28:31], a[252:255], v[252:255]\n"
"v_perm_b32 v23, v124, v120, s34\n"
"; f ks=2\n"
"s_waitcnt lgkmcnt(0)\n"
"v_mfma_f32_16x16x32_bf16 v[128:131], v[16:19], a[224:227], v[128:131]\n"
"v_perm_b32 v24, v101, v97, s33\n"
"v_accvgpr_read_b32 v5, a75\n"
"v_mfma_f32_16x16x32_bf16 v[132:135], v[16:19], a[228:231], v[132:135]\n"
"global_load_dwordx4 v[76:79], v[10:11], off offset:3584 sc1\n"
"v_perm_b32 v25, v109, v105, s33\n"
"v_mfma_f32_16x16x32_bf16 v[136:139], v[16:19], a[232:235], v[136:139]\n"
"v_perm_b32 v26, v117, v113, s33\n"
"v_max3_u32 v6, v6, v4, v5\n"
"v_mfma_f32_16x16x32_bf16 v[140:143], v[16:19], a[236:239], v[140:143]\n"
"v_perm_b32 v27, v125, v121, s33\n"
"s_waitcnt vmcnt(12)\n"
"v_accvgpr_read_b32 v4, a76\n"
"v_mfma_f32_16x16x32_bf16 v[144:147], v[20:23], a[224:227], v[144:147]\n"
"global_load_dwordx4 v[80:83], v[12:13], off sc1\n"
"v_perm_b32 v28, v101, v97, s34\n"
"v_mfma_f32_16x16x32_bf16 v[148:151], v[20:23], a[228:231], v[148:151]\n"
"v_perm_b32 v29, v109, v105, s34\n"
"v_accvgpr_read_b32 v5, a77\n"
"v_mfma_f32_16x16x32_bf16 v[152:155], v[20:23], a[232:235], v[152:155]\n"
"v_perm_b32 v30, v117, v113, s34\n"
"v_max3_u32 v6, v6, v4, v5\n"
"v_mfma_f32_16x16x32_bf16 v[156:159], v[20:23], a[236:239], v[156:159]\n"
"global_load_dwordx4 v[84:87], v[12:13], off offset:3584 sc1\n"
"v_perm_b32 v31, v125, v121, s34\n"
"v_mfma_f32_16x16x32_bf16 v[160:163], v[24:27], a[224:227], v[160:163]\n"
"v_perm_b32 v16, v102, v98, s33\n"
"v_accvgpr_read_b32 v4, a78\n"
"v_mfma_f32_16x16x32_bf16 v[164:167], v[24:27], a[228:231], v[164:167]\n"
"ds_read_b128 a[240:243], v1 offset:12288\n"
"v_perm_b32 v17, v110, v106, s33\n"
"v_mfma_f32_16x16x32_bf16 v[168:171], v[24:27], a[232:235], v[168:171]\n"
"global_load_dwordx4 v[88:91], v[14:15], off sc1\n"
"v_perm_b32 v18, v118, v114, s33\n"
"v_mfma_f32_16x16x32_bf16 v[172:175], v[24:27], a[236:239], v[172:175]\n"
"v_perm_b32 v19, v126, v122, s33\n"
"v_accvgpr_read_b32 v5, a79\n"
"v_mfma_f32_16x16x32_bf16 v[176:179], v[28:31], a[224:227], v[176:179]\n"
"v_perm_b32 v20, v102, v98, s34\n"
"v_max3_u32 v6, v6, v4, v5\n"
"v_mfma_f32_16x16x32_bf16 v[180:183], v[28:31], a[228:231], v[180:183]\n"
"global_load_dwordx4 v[92:95], v[14:15], off offset:3584 sc1\n"
"v_perm_b32 v21, v110, v106, s34\n"
"v_mfma_f32_16x16x32_bf16 v[184:187], v[28:31], a[232:235], v[184:187]\n"
"ds_read_b128 a[244:247], v1 offset:13312\n"
"v_perm_b32 v22, v118, v114, s34\n"
"v_mfma_f32_16x16x32_bf16 v[188:191], v[28:31], a[236:239], v[188:191]\n"
"v_perm_b32 v23, v126, v122, s34\n"
"s_add_u32 s64, s64, 1\n"
"s_and_b32 s64, s64, 15\n"
"s_cselect_b32 s66, s65, 0xffe5c000\n"
"s_cselect_b32 s67, 0, -1\n"
"v_mfma_f32_16x16x32_bf16 v[192:195], v[16:19], a[224:227], v[192:195]\n"
"v_perm_b32 v24, v103, v99, s33\n"
"v_cmp_eq_u32 vcc, -1, v6\n"
"v_mfma_f32_16x16x32_bf16 v[196:199], v[16:19], a[228:231], v[196:199]\n"
"ds_read_b128 a[248:251], v1 offset:14336\n"
"v_perm_b32 v25, v111, v107, s33\n"
"v_lshl_add_u64 v[2:3], v[2:3], 0, s[66:67]\n"
"v_mfma_f32_16x16x32_bf16 v[200:203], v[16:19], a[232:235], v[200:203]\n"
"v_perm_b32 v26, v119, v115, s33\n"
"s_cbranch_vccnz L_hq1r%=\n"
"L_hq1d%=:\n"
"v_mfma_f32_16x16x32_bf16 v[204:207], v[16:19], a[236:239], v[204:207]\n"
"v_perm_b32 v27, v127, v123, s33\n"
"v_lshl_add_u64 v[10:11], v[10:11], 0, s[66:67]\n"
"v_mfma_f32_16x16x32_bf16 v[208:211], v[20:23], a[224:227], v[208:211]\n"
"v_perm_b32 v28, v103, v99, s34\n"
"ds_write_b128 v9, a[64:67] offset:16384\n"
"v_mfma_f32_16x16x32_bf16 v[212:215], v[20:23], a[228:231], v[212:215]\n"
"ds_read_b128 a[252:255], v1 offset:15360\n"
"v_perm_b32 v29, v111, v107, s34\n"
"v_lshl_add_u64 v[12:13], v[12:13], 0, s[66:67]\n"
"v_mfma_f32_16x16x32_bf16 v[216:219], v[20:23], a[232:235], v[216:219]\n"
"v_perm_b32 v30, v119, v115, s34\n"
"v_mfma_f32_16x16x32_bf16 v[220:223], v[20:23], a[236:239], v[220:223]\n"
"v_perm_b32 v31, v127, v123, s34\n"
"v_lshl_add_u64 v[14:15], v[14:15], 0, s[66:67]\n"
"v_mfma_f32_16x16x32_bf16 v[224:227], v[24:27], a[224:227], v[224:227]\n"
"global_load_dwordx4 v[96:99], v[2:3], off sc1\n"
"v_perm_b32 v16, v36, v32, s33\n"
"v_mfma_f32_16x16x32_bf16 v[228:231], v[24:27], a[228:231], v[228:231]\n"
"v_perm_b32 v17, v44, v40, s33\n"
"ds_write_b128 v9, a[68:71] offset:17408\n"
"v_mfma_f32_16x16x32_bf16 v[232:235], v[24:27], a[232:235], v[232:235]\n"
"v_perm_b32 v18, v52, v48, s33\n"
"v_mfma_f32_16x16x32_bf16 v[236:239], v[24:27], a[236:239], v[236:239]\n"
"global_load_dwordx4 v[100:103], v[2:3], off offset:3584 sc1\n"
"v_perm_b32 v19, v60, v56, s33\n"
"v_mfma_f32_16x16x32_bf16 v[240:243], v[28:31], a[224:227], v[240:243]\n"
"v_perm_b32 v20, v36, v32, s34\n"
"v_mfma_f32_16x16x32_bf16 v[244:247], v[28:31], a[228:231], v[244:247]\n"
"v_perm_b32 v21, v44, v40, s34\n"
"ds_write_b128 v9, a[72:75] offset:18432\n"
"v_mfma_f32_16x16x32_bf16 v[248:251], v[28:31], a[232:235], v[248:251]\n"
"global_load_dwordx4 v[104:107], v[10:11], off sc1\n"
"v_perm_b32 v22, v52, v48, s34\n"
"v_mfma_f32_16x16x32_bf16 v[252:255], v[28:31], a[236:239], v[252:255]\n"
"v_perm_b32 v23, v60, v56, s34\n"
"; f ks=3\n"
"s_waitcnt lgkmcnt(2)\n"
"v_mfma_f32_16x16x32_bf16 v[128:131], v[16:19], a[240:243], v[128:131]\n"
"v_perm_b32 v24, v37, v33, s33\n"
"v_mfma_f32_16x16x32_bf16 v[132:135], v[16:19], a[244:247], v[132:135]\n"
"global_load_dwordx4 v[108:111], v[10:11], off offset:3584 sc1\n"
"v_perm_b32 v25, v45, v41, s33\n"
"v_mfma_f32_16x16x32_bf16 v[136:139], v[16:19], a[248:251], v[136:139]\n"
"v_perm_b32 v26, v53, v49, s33\n"
"ds_write_b128 v9, a[76:79] offset:19456\n"
"v_mfma_f32_16x16x32_bf16 v[140:143], v[16:19], a[252:255], v[140:143]\n"
"v_perm_b32 v27, v61, v57, s33\n"
"v_mfma_f32_16x16x32_bf16 v[144:147], v[20:23], a[240:243], v[144:147]\n"
"global_load_dwordx4 v[112:115], v[12:13], off sc1\n"
"v_perm_b32 v28, v37, v33, s34\n"
"v_mfma_f32_16x16x32_bf16 v[148:151], v[20:23], a[244:247], v[148:151]\n"
"v_perm_b32 v29, v45, v41, s34\n"
"s_waitcnt lgkmcnt(0)\n"
"s_barrier\n"
"v_mfma_f32_16x16x32_bf16 v[152:155], v[20:23], a[248:251], v[152:155]\n"
"v_perm_b32 v30, v53, v49, s34\n"
"v_mfma_f32_16x16x32_bf16 v[156:159], v[20:23], a[252:255], v[156:159]\n"
"global_load_dwordx4 v[116:119], v[12:13], off offset:3584 sc1\n"
"v_perm_b32 v31, v61, v57, s34\n"
"v_mfma_f32_16x16x32_bf16 v[160:163], v[24:27], a[240:243], v[160:163]\n"
"v_perm_b32 v16, v38, v34, s33\n"
"v_mfma_f32_16x16x32_bf16 v[164:167], v[24:27], a[244:247], v[164:167]\n"
"ds_read_b128 a[224:227], v1 offset:16384\n"
"v_perm_b32 v17, v46, v42, s33\n"
"v_mfma_f32_16x16x32_bf16 v[168:171], v[24:27], a[248:251], v[168:171]\n"
"global_load_dwordx4 v[120:123], v[14:15], off sc1\n"
"v_perm_b32 v18, v54, v50, s33\n"
"v_mfma_f32_16x16x32_bf16 v[172:175], v[24:27], a[252:255], v[172:175]\n"
"v_perm_b32 v19, v62, v58, s33\n"
"v_mfma_f32_16x16x32_bf16 v[176:179], v[28:31], a[240:243], v[176:179]\n"
"v_perm_b32 v20, v38, v34, s34\n"
"v_mfma_f32_16x16x32_bf16 v[180:183], v[28:31], a[244:247], v[180:183]\n"
"global_load_dwordx4 v[124:127], v[14:15], off offset:3584 sc1\n"
"v_perm_b32 v21, v46, v42, s34\n"
"v_mfma_f32_16x16x32_bf16 v[184:187], v[28:31], a[248:251], v[184:187]\n"
"ds_read_b128 a[228:231], v1 offset:17408\n"
"v_perm_b32 v22, v54, v50, s34\n"
"v_mfma_f32_16x16x32_bf16 v[188:191], v[28:31], a[252:255], v[188:191]\n"
"v_perm_b32 v23, v62, v58, s34\n"
"s_add_u32 s64, s64, 1\n"
"s_and_b32 s64, s64, 15\n"
"s_cselect_b32 s66, s65, 0xffe5c000\n"
"s_cselect_b32 s67, 0, -1\n"
"v_mfma_f32_16x16x32_bf16 v[192:195], v[16:19], a[240:243], v[192:195]\n"
"v_perm_b32 v24, v39, v35, s33\n"
"v_mfma_f32_16x16x32_bf16 v[196:199], v[16:19], a[244:247], v[196:199]\n"
"ds_read_b128 a[232:235], v1 offset:18432\n"
"v_perm_b32 v25, v47, v43, s33\n"
"v_lshl_add_u64 v[2:3], v[2:3], 0, s[66:67]\n"
"v_mfma_f32_16x16x32_bf16 v[200:203], v[16:19], a[248:251], v[200:203]\n"
"v_perm_b32 v26, v55, v51, s33\n"
"v_mfma_f32_16x16x32_bf16 v[204:207], v[16:19], a[252:255], v[204:207]\n"
"v_perm_b32 v27, v63, v59, s33\n"
"v_lshl_add_u64 v[10:11], v[10:11], 0, s[66:67]\n"
"v_mfma_f32_16x16x32_bf16 v[208:211], v[20:23], a[240:243], v[208:211]\n"
"v_perm_b32 v28, v39, v35, s34\n"
"v_mfma_f32_16x16x32_bf16 v[212:215], v[20:23], a[244:247], v[212:215]\n"
"ds_read_b128 a[236:239], v1 offset:19456\n"
"v_perm_b32 v29, v47, v43, s34\n"
"v_lshl_add_u64 v[12:13], v[12:13], 0, s[66:67]\n"
"v_mfma_f32_16x16x32_bf16 v[216:219], v[20:23], a[248:251], v[216:219]\n"
"v_perm_b32 v30, v55, v51, s34\n"
"v_mfma_f32_16x16x32_bf16 v[220:223], v[20:23], a[252:255], v[220:223]\n"
"v_perm_b32 v31, v63, v59, s34\n"
"v_lshl_add_u64 v[14:15], v[14:15], 0, s[66:67]\n"
"v_mfma_f32_16x16x32_bf16 v[224:227], v[24:27], a[240:243], v[224:227]\n"
"global_load_dwordx4 v[32:35], v[2:3], off sc1\n"
"s_waitcnt vmcnt(9)\n"
"v_perm_b32 v16, v68, v64, s33\n"
"v_mfma_f32_16x16x32_bf16 v[228:231], v[24:27], a[244:247], v[228:231]\n"
"v_perm_b32 v17, v76, v72, s33\n"
"v_mfma_f32_16x16x32_bf16 v[232:235], v[24:27], a[248:251], v[232:235]\n"
"v_perm_b32 v18, v84, v80, s33\n"
"v_mfma_f32_16x16x32_bf16 v[236:239], v[24:27], a[252:255], v[236:239]\n"
"global_load_dwordx4 v[36:39], v[2:3], off offset:3584 sc1\n"
"v_perm_b32 v19, v92, v88, s33\n"
"v_mfma_f32_16x16x32_bf16 v[240:243], v[28:31], a[240:243], v[240:243]\n"
"v_perm_b32 v20, v68, v64, s34\n"
"v_mfma_f32_16x16x32_bf16 v[244:247], v[28:31], a[244:247], v[244:247]\n"
"v_perm_b32 v21, v76, v72, s34\n"
"v_mfma_f32_16x16x32_bf16 v[248:251], v[28:31], a[248:251], v[248:251]\n"
"global_load_dwordx4 v[40:43], v[10:11], off sc1\n"
"v_perm_b32 v22, v84, v80, s34\n"
"v_mfma_f32_16x16x32_bf16 v[252:255], v[28:31], a[252:255], v[252:255]\n"
"v_perm_b32 v23, v92, v88, s34\n"
"; f ks=4\n"
"s_waitcnt lgkmcnt(0)\n"
"v_mfma_f32_16x16x32_bf16 v[128:131], v[16:19], a[224:227], v[128:131]\n"
"v_perm_b32 v24, v69, v65, s33\n"
"v_accvgpr_read_b32 v4, a80\n"
"v_mfma_f32_16x16x32_bf16 v[132:135], v[16:19], a[228:231], v[132:135]\n"
"global_load_dwordx4 v[44:47], v[10:11], off offset:3584 sc1\n"
"v_perm_b32 v25, v77, v73, s33\n"
"v_mfma_f32_16x16x32_bf16 v[136:139], v[16:19], a[232:235], v[136:139]\n"
"v_perm_b32 v26, v85, v81, s33\n"
"v_accvgpr_read_b32 v5, a81\n"
"v_mfma_f32_16x16x32_bf16 v[140:143], v[16:19], a[236:239], v[140:143]\n"
"v_perm_b32 v27, v93, v89, s33\n"
"v_max_u32 v6, v4, v5\n"
"v_mfma_f32_16x16x32_bf16 v[144:147], v[20:23], a[224:227], v[144:147]\n"
"global_load_dwordx4 v[48:51], v[12:13], off sc1\n"
"v_perm_b32 v28, v69, v65, s34\n"
"v_mfma_f32_16x16x32_bf16 v[148:151], v[20:23], a[228:231], v[148:151]\n"
"v_perm_b32 v29, v77, v73, s34\n"
"v_accvgpr_read_b32 v4, a82\n"
"v_mfma_f32_16x16x32_bf16 v[152:155], v[20:23], a[232:235], v[152:155]\n"
"v_perm_b32 v30, v85, v81, s34\n"
"v_accvgpr_read_b32 v5, a83\n"
"v_mfma_f32_16x16x32_bf16 v[156:159], v[20:23], a[236:239], v[156:159]\n"
"global_load_dwordx4 v[52:55], v[12:13], off offset:3584 sc1\n"
"v_perm_b32 v31, v93, v89, s34\n"
"v_mfma_f32_16x16x32_bf16 v[160:163], v[24:27], a[224:227], v[160:163]\n"
"v_perm_b32 v16, v70, v66, s33\n"
"v_max3_u32 v6, v6, v4, v5\n"
"v_mfma_f32_16x16x32_bf16 v[164:167], v[24:27], a[228:231], v[164:167]\n"
"ds_read_b128 a[240:243], v1 offset:20480\n"
"v_perm_b32 v17, v78, v74, s33\n"
"v_mfma_f32_16x16x32_bf16 v[168:171], v[24:27], a[232:235], v[168:171]\n"
"global_load_dwordx4 v[56:59], v[14:15], off sc1\n"
"v_perm_b32 v18, v86, v82, s33\n"
"v_mfma_f32_16x16x32_bf16 v[172:175], v[24:27], a[236:239], v[172:175]\n"
"v_perm_b32 v19, v94, v90, s33\n"
"v_accvgpr_read_b32 v4, a84\n"
"v_mfma_f32_16x16x32_bf16 v[176:179], v[28:31], a[224:227], v[176:179]\n"
"v_perm_b32 v20, v70, v66, s34\n"
"v_accvgpr_read_b32 v5, a85\n"
"v_mfma_f32_16x16x32_bf16 v[180:183], v[28:31], a[228:231], v[180:183]\n"
"global_load_dwordx4 v[60:63], v[14:15], off offset:3584 sc1\n"
"v_perm_b32 v21, v78, v74, s34\n"
"v_mfma_f32_16x16x32_bf16 v[184:187], v[28:31], a[232:235], v[184:187]\n"
"ds_read_b128 a[244:247], v1 offset:21504\n"
"v_perm_b32 v22, v86, v82, s34\n"
"v_mfma_f32_16x16x32_bf16 v[188:191], v[28:31], a[236:239], v[188:191]\n"
"v_perm_b32 v23, v94, v90, s34\n"
"s_add_u32 s64, s64, 1\n"
"s_and_b32 s64, s64, 15\n"
"s_cselect_b32 s66, s65, 0xffe5c000\n"
"s_cselect_b32 s67, 0, -1\n"
"v_mfma_f32_16x16x32_bf16 v[192:195], v[16:19], a[224:227], v[192:195]\n"
"v_perm_b32 v24, v71, v67, s33\n"
"v_max3_u32 v6, v6, v4, v5\n"
"v_mfma_f32_16x16x32_bf16 v[196:199], v[16:19], a[228:231], v[196:199]\n"
"ds_read_b128 a[248:251], v1 offset:22528\n"
"v_perm_b32 v25, v79, v75, s33\n"
"v_lshl_add_u64 v[2:3], v[2:3], 0, s[66:67]\n"
"v_mfma_f32_16x16x32_bf16 v[200:203], v[16:19], a[232:235], v[200:203]\n"
"v_perm_b32 v26, v87, v83, s33\n"
"v_accvgpr_read_b32 v4, a86\n"
"v_mfma_f32_16x16x32_bf16 v[204:207], v[16:19], a[236:239], v[204:207]\n"
"v_perm_b32 v27, v95, v91, s33\n"
"v_lshl_add_u64 v[10:11], v[10:11], 0, s[66:67]\n"
"v_mfma_f32_16x16x32_bf16 v[208:211], v[20:23], a[224:227], v[208:211]\n"
"v_perm_b32 v28, v71, v67, s34\n"
"v_accvgpr_read_b32 v5, a87\n"
"v_mfma_f32_16x16x32_bf16 v[212:215], v[20:23], a[228:231], v[212:215]\n"
"ds_read_b128 a[252:255], v1 offset:23552\n"
"v_perm_b32 v29, v79, v75, s34\n"
"v_lshl_add_u64 v[12:13], v[12:13], 0, s[66:67]\n"
"v_mfma_f32_16x16x32_bf16 v[216:219], v[20:23], a[232:235], v[216:219]\n"
"v_perm_b32 v30, v87, v83, s34\n"
"v_max3_u32 v6, v6, v4, v5\n"
"v_mfma_f32_16x16x32_bf16 v[220:223], v[20:23], a[236:239], v[220:223]\n"
"v_perm_b32 v31, v95, v91, s34\n"
"v_lshl_add_u64 v[14:15], v[14:15], 0, s[66:67]\n"
"v_mfma_f32_16x16x32_bf16 v[224:227], v[24:27], a[224:227], v[224:227]\n"
"global_load_dwordx4 v[64:67], v[2:3], off sc1\n"
"s_waitcnt vmcnt(9)\n"
"v_perm_b32 v16, v100, v96, s33\n"
"v_mfma_f32_16x16x32_bf16 v[228:231], v[24:27], a[228:231], v[228:231]\n"
"v_perm_b32 v17, v108, v104, s33\n"
"v_accvgpr_read_b32 v4, a88\n"
"v_mfma_f32_16x16x32_bf16 v[232:235], v[24:27], a[232:235], v[232:235]\n"
"v_perm_b32 v18, v116, v112, s33\n"
"v_accvgpr_read_b32 v5, a89\n"
"v_mfma_f32_16x16x32_bf16 v[236:239], v[24:27], a[236:239], v[236:239]\n"
"global_load_dwordx4 v[68:71], v[2:3], off offset:3584 sc1\n"
"v_perm_b32 v19, v124, v120, s33\n"
"v_mfma_f32_16x16x32_bf16 v[240:243], v[28:31], a[224:227], v[240:243]\n"
"v_perm_b32 v20, v100, v96, s34\n"
"v_max3_u32 v6, v6, v4, v5\n"
"v_mfma_f32_16x16x32_bf16 v[244:247], v[28:31], a[228:231], v[244:247]\n"
"v_perm_b32 v21, v108, v104, s34\n"
"v_accvgpr_read_b32 v4, a90\n"
"v_mfma_f32_16x16x32_bf16 v[248:251], v[28:31], a[232:235], v[248:251]\n"
"global_load_dwordx4 v[72:75], v[10:11], off sc1\n"
"v_perm_b32 v22, v116, v112, s34\n"
"v_mfma_f32_16x16x32_bf16 v[252:255], v[28:31], a[236:239], v[252:255]\n"
"v_perm_b32 v23, v124, v120, s34\n"
"; f ks=5\n"
"s_waitcnt lgkmcnt(0)\n"
"v_mfma_f32_16x16x32_bf16 v[128:131], v[16:19], a[240:243], v[128:131]\n"
"v_perm_b32 v24, v101, v97, s33\n"
"v_accvgpr_read_b32 v5, a91\n"
"v_mfma_f32_16x16x32_bf16 v[132:135], v[16:19], a[244:247], v[132:135]\n"
"global_load_dwordx4 v[76:79], v[10:11], off offset:3584 sc1\n"
"v_perm_b32 v25, v109, v105, s33\n"
"v_mfma_f32_16x16x32_bf16 v[136:139], v[16:19], a[248:251], v[136:139]\n"
"v_perm_b32 v26, v117, v113, s33\n"
"v_max3_u32 v6, v6, v4, v5\n"
"v_mfma_f32_16x16x32_bf16 v[140:143], v[16:19], a[252:255], v[140:143]\n"
"v_perm_b32 v27, v125, v121, s33\n"
"v_accvgpr_read_b32 v4, a92\n"
"v_mfma_f32_16x16x32_bf16 v[144:147], v[20:23], a[240:243], v[144:147]\n"
"global_load_dwordx4 v[80:83], v[12:13], off sc1\n"
"v_perm_b32 v28, v101, v97, s34\n"
"v_mfma_f32_16x16x32_bf16 v[148:151], v[20:23], a[244:247], v[148:151]\n"
"v_perm_b32 v29, v109, v105, s34\n"
"v_accvgpr_read_b32 v5, a93\n"
"v_mfma_f32_16x16x32_bf16 v[152:155], v[20:23], a[248:251], v[152:155]\n"
"v_perm_b32 v30, v117, v113, s34\n"
"v_max3_u32 v6, v6, v4, v5\n"
"v_mfma_f32_16x16x32_bf16 v[156:159], v[20:23], a[252:255], v[156:159]\n"
"global_load_dwordx4 v[84:87], v[12:13], off offset:3584 sc1\n"
"v_perm_b32 v31, v125, v121, s34\n"
"v_mfma_f32_16x16x32_bf16 v[160:163], v[24:27], a[240:243], v[160:163]\n"
"v_perm_b32 v16, v102, v98, s33\n"
"v_accvgpr_read_b32 v4, a94\n"
"v_mfma_f32_16x16x32_bf16 v[164:167], v[24:27], a[244:247], v[164:167]\n"
"ds_read_b128 a[224:227], v1 offset:24576\n"
"v_perm_b32 v17, v110, v106, s33\n"
"v_mfma_f32_16x16x32_bf16 v[168:171], v[24:27], a[248:251], v[168:171]\n"
"global_load_dwordx4 v[88:91], v[14:15], off sc1\n"
"v_perm_b32 v18, v118, v114, s33\n"
"v_mfma_f32_16x16x32_bf16 v[172:175], v[24:27], a[252:255], v[172:175]\n"
"v_perm_b32 v19, v126, v122, s33\n"
"v_accvgpr_read_b32 v5, a95\n"
"v_mfma_f32_16x16x32_bf16 v[176:179], v[28:31], a[240:243], v[176:179]\n"
"v_perm_b32 v20, v102, v98, s34\n"
"v_max3_u32 v6, v6, v4, v5\n"
"v_mfma_f32_16x16x32_bf16 v[180:183], v[28:31], a[244:247], v[180:183]\n"
"global_load_dwordx4 v[92:95], v[14:15], off offset:3584 sc1\n"
"v_perm_b32 v21, v110, v106, s34\n"
"v_mfma_f32_16x16x32_bf16 v[184:187], v[28:31], a[248:251], v[184:187]\n"
"ds_read_b128 a[228:231], v1 offset:25600\n"
"v_perm_b32 v22, v118, v114, s34\n"
"v_mfma_f32_16x16x32_bf16 v[188:191], v[28:31], a[252:255], v[188:191]\n"
"v_perm_b32 v23, v126, v122, s34\n"
"s_add_u32 s64, s64, 1\n"
"s_and_b32 s64, s64, 15\n"
"s_cselect_b32 s66, s65, 0xffe5c000\n"
"s_cselect_b32 s67, 0, -1\n"
"v_mfma_f32_16x16x32_bf16 v[192:195], v[16:19], a[240:243], v[192:195]\n"
"v_perm_b32 v24, v103, v99, s33\n"
"v_cmp_eq_u32 vcc, -1, v6\n"
"v_mfma_f32_16x16x32_bf16 v[196:199], v[16:19], a[244:247], v[196:199]\n"
"ds_read_b128 a[232:235], v1 offset:26624\n"
"v_perm_b32 v25, v111, v107, s33\n"
"v_lshl_add_u64 v[2:3], v[2:3], 0, s[66:67]\n"
"v_mfma_f32_16x16x32_bf16 v[200:203], v[16:19], a[248:251], v[200:203]\n"
"v_perm_b32 v26, v119, v115, s33\n"
"s_cbranch_vccnz L_hq2r%=\n"
"L_hq2d%=:\n"
"v_mfma_f32_16x16x32_bf16 v[204:207], v[16:19], a[252:255], v[204:207]\n"
"v_perm_b32 v27, v127, v123, s33\n"
"v_lshl_add_u64 v[10:11], v[10:11], 0, s[66:67]\n"
"v_mfma_f32_16x16x32_bf16 v[208:211], v[20:23], a[240:243], v[208:211]\n"
"v_perm_b32 v28, v103, v99, s34\n"
"ds_write_b128 v9, a[80:83] offset:32768\n"
"v_mfma_f32_16x16x32_bf16 v[212:215], v[20:23], a[244:247], v[212:215]\n"
"ds_read_b128 a[236:239], v1 offset:27648\n"
"v_perm_b32 v29, v111, v107, s34\n"
"v_lshl_add_u64 v[12:13], v[12:13], 0, s[66:67]\n"
"v_mfma_f32_16x16x32_bf16 v[216:219], v[20:23], a[248:251], v[216:219]\n"
"v_perm_b32 v30, v119, v115, s34\n"
"v_mfma_f32_16x16x32_bf16 v[220:223], v[20:23], a[252:255], v[220:223]\n"
"v_perm_b32 v31, v127, v123, s34\n"
"v_lshl_add_u64 v[14:15], v[14:15], 0, s[66:67]\n"
"v_mfma_f32_16x16x32_bf16 v[224:227], v[24:27], a[240:243], v[224:227]\n"
"global_load_dwordx4 v[96:99], v[2:3], off sc1\n"
"s_waitcnt vmcnt(9)\n"
"v_perm_b32 v16, v36, v32, s33\n"
"v_mfma_f32_16x16x32_bf16 v[228:231], v[24:27], a[244:247], v[228:231]\n"
"v_perm_b32 v17, v44, v40, s33\n"
"ds_write_b128 v9, a[84:87] offset:33792\n"
"v_mfma_f32_16x16x32_bf16 v[232:235], v[24:27], a[248:251], v[232:235]\n"
"v_perm_b32 v18, v52, v48, s33\n"
"v_mfma_f32_16x16x32_bf16 v[236:239], v[24:27], a[252:255], v[236:239]\n"
"global_load_dwordx4 v[100:103], v[2:3], off offset:3584 sc1\n"
"v_perm_b32 v19, v60, v56, s33\n"
"v_mfma_f32_16x16x32_bf16 v[240:243], v[28:31], a[240:243], v[240:243]\n"
"v_perm_b32 v20, v36, v32, s34\n"
"v_mfma_f32_16x16x32_bf16 v[244:247], v[28:31], a[244:247], v[244:247]\n"
"v_perm_b32 v21, v44, v40, s34\n"
"ds_write_b128 v9, a[88:91] offset:34816\n"
"v_mfma_f32_16x16x32_bf16 v[248:251], v[28:31], a[248:251], v[248:251]\n"
"global_load_dwordx4 v[104:107], v[10:11], off sc1\n"
"v_perm_b32 v22, v52, v48, s34\n"
"v_mfma_f32_16x16x32_bf16 v[252:255], v[28:31], a[252:255], v[252:255]\n"
"v_perm_b32 v23, v60, v56, s34\n"
"; f ks=6\n"
"s_waitcnt lgkmcnt(2)\n"
"v_mfma_f32_16x16x32_bf16 v[128:131], v[16:19], a[224:227], v[128:131]\n"
"v_perm_b32 v24, v37, v33, s33\n"
"v_mfma_f32_16x16x32_bf16 v[132:135], v[16:19], a[228:231], v[132:135]\n"
"global_load_dwordx4 v[108:111], v[10:11], off offset:3584 sc1\n"
"v_perm_b32 v25, v45, v41, s33\n"
"v_mfma_f32_16x16x32_bf16 v[136:139], v[16:19], a[232:235], v[136:139]\n"
"v_perm_b32 v26, v53, v49, s33\n"
"ds_write_b128 v9, a[92:95] offset:35840\n"
"v_mfma_f32_16x16x32_bf16 v[140:143], v[16:19], a[236:239], v[140:143]\n"
"v_perm_b32 v27, v61, v57, s33\n"
"v_mfma_f32_16x16x32_bf16 v[144:147], v[20:23], a[224:227], v[144:147]\n"
"global_load_dwordx4 v[112:115], v[12:13], off sc1\n"
"v_perm_b32 v28, v37, v33, s34\n"
"v_mfma_f32_16x16x32_bf16 v[148:151], v[20:23], a[228:231], v[148:151]\n"
"v_perm_b32 v29, v45, v41, s34\n"
"s_waitcnt lgkmcnt(0)\n"
"s_barrier\n"
"v_mfma_f32_16x16x32_bf16 v[152:155], v[20:23], a[232:235], v[152:155]\n"
"v_perm_b32 v30, v53, v49, s34\n"
"v_mfma_f32_16x16x32_bf16 v[156:159], v[20:23], a[236:239], v[156:159]\n"
"global_load_dwordx4 v[116:119], v[12:13], off offset:3584 sc1\n"
"v_perm_b32 v31, v61, v57, s34\n"
"v_mfma_f32_16x16x32_bf16 v[160:163], v[24:27], a[224:227], v[160:163]\n"
"v_perm_b32 v16, v38, v34, s33\n"
"v_mfma_f32_16x16x32_bf16 v[164:167], v[24:27], a[228:231], v[164:167]\n"
"ds_read_b128 a[240:243], v1 offset:28672\n"
"v_perm_b32 v17, v46, v42, s33\n"
"v_mfma_f32_16x16x32_bf16 v[168:171], v[24:27], a[232:235], v[168:171]\n"
"global_load_dwordx4 v[120:123], v[14:15], off sc1\n"
"v_perm_b32 v18, v54, v50, s33\n"
"v_mfma_f32_16x16x32_bf16 v[172:175], v[24:27], a[236:239], v[172:175]\n"
"v_perm_b32 v19, v62, v58, s33\n"
"v_mfma_f32_16x16x32_bf16 v[176:179], v[28:31], a[224:227], v[176:179]\n"
"v_perm_b32 v20, v38, v34, s34\n"
"v_mfma_f32_16x16x32_bf16 v[180:183], v[28:31], a[228:231], v[180:183]\n"
"global_load_dwordx4 v[124:127], v[14:15], off offset:3584 sc1\n"
"v_perm_b32 v21, v46, v42, s34\n"
"v_mfma_f32_16x16x32_bf16 v[184:187], v[28:31], a[232:235], v[184:187]\n"
"ds_read_b128 a[244:247], v1 offset:29696\n"
"v_perm_b32 v22, v54, v50, s34\n"
"v_mfma_f32_16x16x32_bf16 v[188:191], v[28:31], a[236:239], v[188:191]\n"
"v_perm_b32 v23, v62, v58, s34\n"
"s_add_u32 s64, s64, 1\n"
"s_and_b32 s64, s64, 15\n"
"s_cselect_b32 s66, s65, 0xffe5c000\n"
"s_cselect_b32 s67, 0, -1\n"
"v_mfma_f32_16x16x32_bf16 v[192:195], v[16:19], a[224:227], v[192:195]\n"
"v_perm_b32 v24, v39, v35, s33\n"
"v_mfma_f32_16x16x32_bf16 v[196:199], v[16:19], a[228:231], v[196:199]\n"
"ds_read_b128 a[248:251], v1 offset:30720\n"
"v_perm_b32 v25, v47, v43, s33\n"
"v_lshl_add_u64 v[2:3], v[2:3], 0, s[66:67]\n"
"v_mfma_f32_16x16x32_bf16 v[200:203], v[16:19], a[232:235], v[200:203]\n"
"v_perm_b32 v26, v55, v51, s33\n"
"v_mfma_f32_16x16x32_bf16 v[204:207], v[16:19], a[236:239], v[204:207]\n"
"v_perm_b32 v27, v63, v59, s33\n"
"v_lshl_add_u64 v[10:11], v[10:11], 0, s[66:67]\n"
"v_mfma_f32_16x16x32_bf16 v[208:211], v[20:23], a[224:227], v[208:211]\n"
"v_perm_b32 v28, v39, v35, s34\n"
"v_mfma_f32_16x16x32_bf16 v[212:215], v[20:23], a[228:231], v[212:215]\n"
"ds_read_b128 a[252:255], v1 offset:31744\n"
"v_perm_b32 v29, v47, v43, s34\n"
"v_lshl_add_u64 v[12:13], v[12:13], 0, s[66:67]\n"
"v_mfma_f32_16x16x32_bf16 v[216:219], v[20:23], a[232:235], v[216:219]\n"
"v_perm_b32 v30, v55, v51, s34\n"
"v_mfma_f32_16x16x32_bf16 v[220:223], v[20:23], a[236:239], v[220:223]\n"
"v_perm_b32 v31, v63, v59, s34\n"
"v_lshl_add_u64 v[14:15], v[14:15], 0, s[66:67]\n"
"v_mfma_f32_16x16x32_bf16 v[224:227], v[24:27], a[224:227], v[224:227]\n"
"global_load_dwordx4 v[32:35], v[2:3], off sc1\n"
"s_waitcnt vmcnt(9)\n"
"v_perm_b32 v16, v68, v64, s33\n"
"v_mfma_f32_16x16x32_bf16 v[228:231], v[24:27], a[228:231], v[228:231]\n"
"v_perm_b32 v17, v76, v72, s33\n"
"v_mfma_f32_16x16x32_bf16 v[232:235], v[24:27], a[232:235], v[232:235]\n"
"v_perm_b32 v18, v84, v80, s33\n"
"v_mfma_f32_16x16x32_bf16 v[236:239], v[24:27], a[236:239], v[236:239]\n"
"global_load_dwordx4 v[36:39], v[2:3], off offset:3584 sc1\n"
"v_perm_b32 v19, v92, v88, s33\n"
"v_mfma_f32_16x16x32_bf16 v[240:243], v[28:31], a[224:227], v[240:243]\n"
"v_perm_b32 v20, v68, v64, s34\n"
"v_mfma_f32_16x16x32_bf16 v[244:247], v[28:31], a[228:231], v[244:247]\n"
"v_perm_b32 v21, v76, v72, s34\n"
"v_mfma_f32_16x16x32_bf16 v[248:251], v[28:31], a[232:235], v[248:251]\n"
"global_load_dwordx4 v[40:43], v[10:11], off sc1\n"
"v_perm_b32 v22, v84, v80, s34\n"
"v_mfma_f32_16x16x32_bf16 v[252:255], v[28:31], a[236:239], v[252:255]\n"
"v_perm_b32 v23, v92, v88, s34\n"
"; f ks=7\n"
"s_waitcnt lgkmcnt(0)\n"
"v_mfma_f32_16x16x32_bf16 v[128:131], v[16:19], a[240:243], v[128:131]\n"
"v_perm_b32 v24, v69, v65, s33\n"
"v_accvgpr_read_b32 v4, a96\n"
"v_mfma_f32_16x16x32_bf16 v[132:135], v[16:19], a[244:247], v[132:135]\n"
"global_load_dwordx4 v[44:47], v[10:11], off offset:3584 sc1\n"
"v_perm_b32 v25, v77, v73, s33\n"
"v_mfma_f32_16x16x32_bf16 v[136:139], v[16:19], a[248:251], v[136:139]\n"
"v_perm_b32 v26, v85, v81, s33\n"
"v_accvgpr_read_b32 v5, a97\n"
"v_mfma_f32_16x16x32_bf16 v[140:143], v[16:19], a[252:255], v[140:143]\n"
"v_perm_b32 v27, v93, v89, s33\n"
"v_max_u32 v6, v4, v5\n"
"v_mfma_f32_16x16x32_bf16 v[144:147], v[20:23], a[240:243], v[144:147]\n"
"global_load_dwordx4 v[48:51], v[12:13], off sc1\n"
"v_perm_b32 v28, v69, v65, s34\n"
"v_mfma_f32_16x16x32_bf16 v[148:151], v[20:23], a[244:247], v[148:151]\n"
"v_perm_b32 v29, v77, v73, s34\n"
"v_accvgpr_read_b32 v4, a98\n"
"v_mfma_f32_16x16x32_bf16 v[152:155], v[20:23], a[248:251], v[152:155]\n"
"v_perm_b32 v30, v85, v81, s34\n"
"v_accvgpr_read_b32 v5, a99\n"
"v_mfma_f32_16x16x32_bf16 v[156:159], v[20:23], a[252:255], v[156:159]\n"
"global_load_dwordx4 v[52:55], v[12:13], off offset:3584 sc1\n"
"v_perm_b32 v31, v93, v89, s34\n"
"v_mfma_f32_16x16x32_bf16 v[160:163], v[24:27], a[240:243], v[160:163]\n"
"v_perm_b32 v16, v70, v66, s33\n"
"v_max3_u32 v6, v6, v4, v5\n"
"v_mfma_f32_16x16x32_bf16 v[164:167], v[24:27], a[244:247], v[164:167]\n"
"ds_read_b128 a[224:227], v1 offset:32768\n"
"v_perm_b32 v17, v78, v74, s33\n"
"v_mfma_f32_16x16x32_bf16 v[168:171], v[24:27], a[248:251], v[168:171]\n"
"global_load_dwordx4 v[56:59], v[14:15], off sc1\n"
"v_perm_b32 v18, v86, v82, s33\n"
"v_mfma_f32_16x16x32_bf16 v[172:175], v[24:27], a[252:255], v[172:175]\n"
"v_perm_b32 v19, v94, v90, s33\n"
"v_accvgpr_read_b32 v4, a100\n"
"v_mfma_f32_16x16x32_bf16 v[176:179], v[28:31], a[240:243], v[176:179]\n"
"v_perm_b32 v20, v70, v66, s34\n"
"v_accvgpr_read_b32 v5, a101\n"
"v_mfma_f32_16x16x32_bf16 v[180:183], v[28:31], a[244:247], v[180:183]\n"
"global_load_dwordx4 v[60:63], v[14:15], off offset:3584 sc1\n"
"v_perm_b32 v21, v78, v74, s34\n"
"v_mfma_f32_16x16x32_bf16 v[184:187], v[28:31], a[248:251], v[184:187]\n"
"ds_read_b128 a[228:231], v1 offset:33792\n"
"v_perm_b32 v22, v86, v82, s34\n"
"v_mfma_f32_16x16x32_bf16 v[188:191], v[28:31], a[252:255], v[188:191]\n"
"v_perm_b32 v23, v94, v90, s34\n"
"s_add_u32 s64, s64, 1\n"
"s_and_b32 s64, s64, 15\n"
"s_cselect_b32 s66, s65, 0xffe5c000\n"
"s_cselect_b32 s67, 0, -1\n"
"v_mfma_f32_16x16x32_bf16 v[192:195], v[16:19], a[240:243], v[192:195]\n"
"v_perm_b32 v24, v71, v67, s33\n"
"v_max3_u32 v6, v6, v4, v5\n"
"v_mfma_f32_16x16x32_bf16 v[196:199], v[16:19], a[244:247], v[196:199]\n"
"ds_read_b128 a[232:235], v1 offset:34816\n"
"v_perm_b32 v25, v79, v75, s33\n"
"v_lshl_add_u64 v[2:3], v[2:3], 0, s[66:67]\n"
"v_mfma_f32_16x16x32_bf16 v[200:203], v[16:19], a[248:251], v[200:203]\n"
"v_perm_b32 v26, v87, v83, s33\n"
"v_accvgpr_read_b32 v4, a102\n"
"v_mfma_f32_16x16x32_bf16 v[204:207], v[16:19], a[252:255], v[204:207]\n"
"v_perm_b32 v27, v95, v91, s33\n"
"v_lshl_add_u64 v[10:11], v[10:11], 0, s[66:67]\n"
"v_mfma_f32_16x16x32_bf16 v[208:211], v[20:23], a[240:243], v[208:211]\n"
"v_perm_b32 v28, v71, v67, s34\n"
"v_accvgpr_read_b32 v5, a103\n"
"v_mfma_f32_16x16x32_bf16 v[212:215], v[20:23], a[244:247], v[212:215]\n"
"ds_read_b128 a[236:239], v1 offset:35840\n"
"v_perm_b32 v29, v79, v75, s34\n"
"v_lshl_add_u64 v[12:13], v[12:13], 0, s[66:67]\n"
"v_mfma_f32_16x16x32_bf16 v[216:219], v[20:23], a[248:251], v[216:219]\n"
"v_perm_b32 v30, v87, v83, s34\n"
"v_max3_u32 v6, v6, v4, v5\n"
"v_mfma_f32_16x16x32_bf16 v[220:223], v[20:23], a[252:255], v[220:223]\n"
"v_perm_b32 v31, v95, v91, s34\n"
"v_lshl_add_u64 v[14:15], v[14:15], 0, s[66:67]\n"
"v_mfma_f32_16x16x32_bf16 v[224:227], v[24:27], a[240:243], v[224:227]\n"
"global_load_dwordx4 v[64:67], v[2:3], off sc1\n"
"s_waitcnt vmcnt(9)\n"
"v_perm_b32 v16, v100, v96, s33\n"
"v_mfma_f32_16x16x32_bf16 v[228:231], v[24:27], a[244:247], v[228:231]\n"
"v_perm_b32 v17, v108, v104, s33\n"
"v_accvgpr_read_b32 v4, a104\n"
"v_mfma_f32_16x16x32_bf16 v[232:235], v[24:27], a[248:251], v[232:235]\n"
"v_perm_b32 v18, v116, v112, s33\n"
"v_accvgpr_read_b32 v5, a105\n"
"v_mfma_f32_16x16x32_bf16 v[236:239], v[24:27], a[252:255], v[236:239]\n"
"global_load_dwordx4 v[68:71], v[2:3], off offset:3584 sc1\n"
"v_perm_b32 v19, v124, v120, s33\n"
"v_mfma_f32_16x16x32_bf16 v[240:243], v[28:31], a[240:243], v[240:243]\n"
"v_perm_b32 v20, v100, v96, s34\n"
"v_max3_u32 v6, v6, v4, v5\n"
"v_mfma_f32_16x16x32_bf16 v[244:247], v[28:31], a[244:247], v[244:247]\n"
"v_perm_b32 v21, v108, v104, s34\n"
"v_accvgpr_read_b32 v4, a106\n"
"v_mfma_f32_16x16x32_bf16 v[248:251], v[28:31], a[248:251], v[248:251]\n"
"global_load_dwordx4 v[72:75], v[10:11], off sc1\n"
"v_perm_b32 v22, v116, v112, s34\n"
"v_mfma_f32_16x16x32_bf16 v[252:255], v[28:31], a[252:255], v[252:255]\n"
"v_perm_b32 v23, v124, v120, s34\n"
"; f ks=8\n"
"s_waitcnt lgkmcnt(0)\n"
"v_mfma_f32_16x16x32_bf16 v[128:131], v[16:19], a[224:227], v[128:131]\n"
"v_perm_b32 v24, v101, v97, s33\n"
"v_accvgpr_read_b32 v5, a107\n"
"v_mfma_f32_16x16x32_bf16 v[132:135], v[16:19], a[228:231], v[132:135]\n"
"global_load_dwordx4 v[76:79], v[10:11], off offset:3584 sc1\n"
"v_perm_b32 v25, v109, v105, s33\n"
"v_mfma_f32_16x16x32_bf16 v[136:139], v[16:19], a[232:235], v[136:139]\n"
"v_perm_b32 v26, v117, v113, s33\n"
"v_max3_u32 v6, v6, v4, v5\n"
"v_mfma_f32_16x16x32_bf16 v[140:143], v[16:19], a[236:239], v[140:143]\n"
"v_perm_b32 v27, v125, v121, s33\n"
"v_accvgpr_read_b32 v4, a108\n"
"v_mfma_f32_16x16x32_bf16 v[144:147], v[20:23], a[224:227], v[144:147]\n"
"global_load_dwordx4 v[80:83], v[12:13], off sc1\n"
"v_perm_b32 v28, v101, v97, s34\n"
"v_mfma_f32_16x16x32_bf16 v[148:151], v[20:23], a[228:231], v[148:151]\n"
"v_perm_b32 v29, v109, v105, s34\n"
"v_accvgpr_read_b32 v5, a109\n"
"v_mfma_f32_16x16x32_bf16 v[152:155], v[20:23], a[232:235], v[152:155]\n"
"v_perm_b32 v30, v117, v113, s34\n"
"v_max3_u32 v6, v6, v4, v5\n"
"v_mfma_f32_16x16x32_bf16 v[156:159], v[20:23], a[236:239], v[156:159]\n"
"global_load_dwordx4 v[84:87], v[12:13], off offset:3584 sc1\n"
"v_perm_b32 v31, v125, v121, s34\n"
"v_mfma_f32_16x16x32_bf16 v[160:163], v[24:27], a[224:227], v[160:163]\n"
"v_perm_b32 v16, v102, v98, s33\n"
"v_accvgpr_read_b32 v4, a110\n"
"v_mfma_f32_16x16x32_bf16 v[164:167], v[24:27], a[228:231], v[164:167]\n"
"ds_read_b128 a[240:243], v1 offset:36864\n"
"v_perm_b32 v17, v110, v106, s33\n"
"v_mfma_f32_16x16x32_bf16 v[168:171], v[24:27], a[232:235], v[168:171]\n"
"global_load_dwordx4 v[88:91], v[14:15], off sc1\n"
"v_perm_b32 v18, v118, v114, s33\n"
"v_mfma_f32_16x16x32_bf16 v[172:175], v[24:27], a[236:239], v[172:175]\n"
"v_perm_b32 v19, v126, v122, s33\n"
"v_accvgpr_read_b32 v5, a111\n"
"v_mfma_f32_16x16x32_bf16 v[176:179], v[28:31], a[224:227], v[176:179]\n"
"v_perm_b32 v20, v102, v98, s34\n"
"v_max3_u32 v6, v6, v4, v5\n"
"v_mfma_f32_16x16x32_bf16 v[180:183], v[28:31], a[228:231], v[180:183]\n"
"global_load_dwordx4 v[92:95], v[14:15], off offset:3584 sc1\n"
"v_perm_b32 v21, v110, v106, s34\n"
"v_mfma_f32_16x16x32_bf16 v[184:187], v[28:31], a[232:235], v[184:187]\n"
"ds_read_b128 a[244:247], v1 offset:37888\n"
"v_perm_b32 v22, v118, v114, s34\n"
"v_mfma_f32_16x16x32_bf16 v[188:191], v[28:31], a[236:239], v[188:191]\n"
"v_perm_b32 v23, v126, v122, s34\n"
"s_add_u32 s64, s64, 1\n"
"s_and_b32 s64, s64, 15\n"
"s_cselect_b32 s66, s65, 0xffe5c000\n"
"s_cselect_b32 s67, 0, -1\n"
"v_mfma_f32_16x16x32_bf16 v[192:195], v[16:19], a[224:227], v[192:195]\n"
"v_perm_b32 v24, v103, v99, s33\n"
"v_cmp_eq_u32 vcc, -1, v6\n"
"v_mfma_f32_16x16x32_bf16 v[196:199], v[16:19], a[228:231], v[196:199]\n"
"ds_read_b128 a[248:251], v1 offset:38912\n"
"v_perm_b32 v25, v111, v107, s33\n"
"v_lshl_add_u64 v[2:3], v[2:3], 0, s[66:67]\n"
"v_mfma_f32_16x16x32_bf16 v[200:203], v[16:19], a[232:235], v[200:203]\n"
"v_perm_b32 v26, v119, v115, s33\n"
"s_cbranch_vccnz L_hq3r%=\n"
"L_hq3d%=:\n"
"v_mfma_f32_16x16x32_bf16 v[204:207], v[16:19], a[236:239], v[204:207]\n"
"v_perm_b32 v27, v127, v123, s33\n"
"v_lshl_add_u64 v[10:11], v[10:11], 0, s[66:67]\n"
"v_mfma_f32_16x16x32_bf16 v[208:211], v[20:23], a[224:227], v[208:211]\n"
"v_perm_b32 v28, v103, v99, s34\n"
"ds_write_b128 v9, a[96:99] offset:49152\n"
"v_mfma_f32_16x16x32_bf16 v[212:215], v[20:23], a[228:231], v[212:215]\n"
"ds_read_b128 a[252:255], v1 offset:39936\n"
"v_perm_b32 v29, v111, v107, s34\n"
"v_lshl_add_u64 v[12:13], v[12:13], 0, s[66:67]\n"
"v_mfma_f32_16x16x32_bf16 v[216:219], v[20:23], a[232:235], v[216:219]\n"
"v_perm_b32 v30, v119, v115, s34\n"
"v_mfma_f32_16x16x32_bf16 v[220:223], v[20:23], a[236:239], v[220:223]\n"
"v_perm_b32 v31, v127, v123, s34\n"
"v_lshl_add_u64 v[14:15], v[14:15], 0, s[66:67]\n"
"v_mfma_f32_16x16x32_bf16 v[224:227], v[24:27], a[224:227], v[224:227]\n"
"global_load_dwordx4 v[96:99], v[2:3], off sc1\n"
"s_waitcnt vmcnt(9)\n"
"v_perm_b32 v16, v36, v32, s33\n"
"v_mfma_f32_16x16x32_bf16 v[228:231], v[24:27], a[228:231], v[228:231]\n"
"v_perm_b32 v17, v44, v40, s33\n"
"ds_write_b128 v9, a[100:103] offset:50176\n"
"v_mfma_f32_16x16x32_bf16 v[232:235], v[24:27], a[232:235], v[232:235]\n"
"v_perm_b32 v18, v52, v48, s33\n"
"v_mfma_f32_16x16x32_bf16 v[236:239], v[24:27], a[236:239], v[236:239]\n"
"global_load_dwordx4 v[100:103], v[2:3], off offset:3584 sc1\n"
"v_perm_b32 v19, v60, v56, s33\n"
"v_mfma_f32_16x16x32_bf16 v[240:243], v[28:31], a[224:227], v[240:243]\n"
"v_perm_b32 v20, v36, v32, s34\n"
"v_mfma_f32_16x16x32_bf16 v[244:247], v[28:31], a[228:231], v[244:247]\n"
"v_perm_b32 v21, v44, v40, s34\n"
"ds_write_b128 v9, a[104:107] offset:51200\n"
"v_mfma_f32_16x16x32_bf16 v[248:251], v[28:31], a[232:235], v[248:251]\n"
"global_load_dwordx4 v[104:107], v[10:11], off sc1\n"
"v_perm_b32 v22, v52, v48, s34\n"
"v_mfma_f32_16x16x32_bf16 v[252:255], v[28:31], a[236:239], v[252:255]\n"
"v_perm_b32 v23, v60, v56, s34\n"
"; f ks=9\n"
"s_waitcnt lgkmcnt(2)\n"
"v_mfma_f32_16x16x32_bf16 v[128:131], v[16:19], a[240:243], v[128:131]\n"
"v_perm_b32 v24, v37, v33, s33\n"
"v_mfma_f32_16x16x32_bf16 v[132:135], v[16:19], a[244:247], v[132:135]\n"
"global_load_dwordx4 v[108:111], v[10:11], off offset:3584 sc1\n"
"v_perm_b32 v25, v45, v41, s33\n"
"v_mfma_f32_16x16x32_bf16 v[136:139], v[16:19], a[248:251], v[136:139]\n"
"v_perm_b32 v26, v53, v49, s33\n"
"ds_write_b128 v9, a[108:111] offset:52224\n"
"v_mfma_f32_16x16x32_bf16 v[140:143], v[16:19], a[252:255], v[140:143]\n"
"v_perm_b32 v27, v61, v57, s33\n"
"v_mfma_f32_16x16x32_bf16 v[144:147], v[20:23], a[240:243], v[144:147]\n"
"global_load_dwordx4 v[112:115], v[12:13], off sc1\n"
"v_perm_b32 v28, v37, v33, s34\n"
"v_mfma_f32_16x16x32_bf16 v[148:151], v[20:23], a[244:247], v[148:151]\n"
"v_perm_b32 v29, v45, v41, s34\n"
"s_waitcnt lgkmcnt(0)\n"
"s_barrier\n"
"v_mfma_f32_16x16x32_bf16 v[152:155], v[20:23], a[248:251], v[152:155]\n"
"v_perm_b32 v30, v53, v49, s34\n"
"v_mfma_f32_16x16x32_bf16 v[156:159], v[20:23], a[252:255], v[156:159]\n"
"global_load_dwordx4 v[116:119], v[12:13], off offset:3584 sc1\n"
"v_perm_b32 v31, v61, v57, s34\n"
"v_mfma_f32_16x16x32_bf16 v[160:163], v[24:27], a[240:243], v[160:163]\n"
"v_perm_b32 v16, v38, v34, s33\n"
"v_mfma_f32_16x16x32_bf16 v[164:167], v[24:27], a[244:247], v[164:167]\n"
"ds_read_b128 a[224:227], v1 offset:40960\n"
"v_perm_b32 v17, v46, v42, s33\n"
"v_mfma_f32_16x16x32_bf16 v[168:171], v[24:27], a[248:251], v[168:171]\n"
"global_load_dwordx4 v[120:123], v[14:15], off sc1\n"
"v_perm_b32 v18, v54, v50, s33\n"
"v_mfma_f32_16x16x32_bf16 v[172:175], v[24:27], a[252:255], v[172:175]\n"
"v_perm_b32 v19, v62, v58, s33\n"
"v_mfma_f32_16x16x32_bf16 v[176:179], v[28:31], a[240:243], v[176:179]\n"
"v_perm_b32 v20, v38, v34, s34\n"
"v_mfma_f32_16x16x32_bf16 v[180:183], v[28:31], a[244:247], v[180:183]\n"
"global_load_dwordx4 v[124:127], v[14:15], off offset:3584 sc1\n"
"v_perm_b32 v21, v46, v42, s34\n"
"v_mfma_f32_16x16x32_bf16 v[184:187], v[28:31], a[248:251], v[184:187]\n"
"ds_read_b128 a[228:231], v1 offset:41984\n"
"v_perm_b32 v22, v54, v50, s34\n"
"v_mfma_f32_16x16x32_bf16 v[188:191], v[28:31], a[252:255], v[188:191]\n"
"v_perm_b32 v23, v62, v58, s34\n"
"s_add_u32 s64, s64, 1\n"
"s_and_b32 s64, s64, 15\n"
"s_cselect_b32 s66, s65, 0xffe5c000\n"
"s_cselect_b32 s67, 0, -1\n"
"v_mfma_f32_16x16x32_bf16 v[192:195], v[16:19], a[240:243], v[192:195]\n"
"v_perm_b32 v24, v39, v35, s33\n"
"v_mfma_f32_16x16x32_bf16 v[196:199], v[16:19], a[244:247], v[196:199]\n"
"ds_read_b128 a[232:235], v1 offset:43008\n"
"v_perm_b32 v25, v47, v43, s33\n"
"v_lshl_add_u64 v[2:3], v[2:3], 0, s[66:67]\n"
"v_mfma_f32_16x16x32_bf16 v[200:203], v[16:19], a[248:251], v[200:203]\n"
"v_perm_b32 v26, v55, v51, s33\n"
"v_mfma_f32_16x16x32_bf16 v[204:207], v[16:19], a[252:255], v[204:207]\n"
"v_perm_b32 v27, v63, v59, s33\n"
"v_lshl_add_u64 v[10:11], v[10:11], 0, s[66:67]\n"
"v_mfma_f32_16x16x32_bf16 v[208:211], v[20:23], a[240:243], v[208:211]\n"
"v_perm_b32 v28, v39, v35, s34\n"
"v_mfma_f32_16x16x32_bf16 v[212:215], v[20:23], a[244:247], v[212:215]\n"
"ds_read_b128 a[236:239], v1 offset:44032\n"
"v_perm_b32 v29, v47, v43, s34\n"
"v_lshl_add_u64 v[12:13], v[12:13], 0, s[66:67]\n"
"v_mfma_f32_16x16x32_bf16 v[216:219], v[20:23], a[248:251], v[216:219]\n"
"v_perm_b32 v30, v55, v51, s34\n"
"v_mfma_f32_16x16x32_bf16 v[220:223], v[20:23], a[252:255], v[220:223]\n"
"v_perm_b32 v31, v63, v59, s34\n"
"v_lshl_add_u64 v[14:15], v[14:15], 0, s[66:67]\n"
"v_mfma_f32_16x16x32_bf16 v[224:227], v[24:27], a[240:243], v[224:227]\n"
"global_load_dwordx4 v[32:35], v[2:3], off sc1\n"
"s_waitcnt vmcnt(9)\n"
"v_perm_b32 v16, v68, v64, s33\n"
"v_mfma_f32_16x16x32_bf16 v[228:231], v[24:27], a[244:247], v[228:231]\n"
"v_perm_b32 v17, v76, v72, s33\n"
"v_mfma_f32_16x16x32_bf16 v[232:235], v[24:27], a[248:251], v[232:235]\n"
"v_perm_b32 v18, v84, v80, s33\n"
"v_mfma_f32_16x16x32_bf16 v[236:239], v[24:27], a[252:255], v[236:239]\n"
"global_load_dwordx4 v[36:39], v[2:3], off offset:3584 sc1\n"
"v_perm_b32 v19, v92, v88, s33\n"
"v_mfma_f32_16x16x32_bf16 v[240:243], v[28:31], a[240:243], v[240:243]\n"
"v_perm_b32 v20, v68, v64, s34\n"
"v_mfma_f32_16x16x32_bf16 v[244:247], v[28:31], a[244:247], v[244:247]\n"
"v_perm_b32 v21, v76, v72, s34\n"
"v_mfma_f32_16x16x32_bf16 v[248:251], v[28:31], a[248:251], v[248:251]\n"
"global_load_dwordx4 v[40:43], v[10:11], off sc1\n"
"v_perm_b32 v22, v84, v80, s34\n"
"v_mfma_f32_16x16x32_bf16 v[252:255], v[28:31], a[252:255], v[252:255]\n"
"v_perm_b32 v23, v92, v88, s34\n"
"; f ks=10\n"
"s_waitcnt lgkmcnt(0)\n"
"v_mfma_f32_16x16x32_bf16 v[128:131], v[16:19], a[224:227], v[128:131]\n"
"v_perm_b32 v24, v69, v65, s33\n"
"v_mfma_f32_16x16x32_bf16 v[132:135], v[16:19], a[228:231], v[132:135]\n"
"global_load_dwordx4 v[44:47], v[10:11], off offset:3584 sc1\n"
"v_perm_b32 v25, v77, v73, s33\n"
"v_mfma_f32_16x16x32_bf16 v[136:139], v[16:19], a[232:235], v[136:139]\n"
"v_perm_b32 v26, v85, v81, s33\n"
"v_mfma_f32_16x16x32_bf16 v[140:143], v[16:19], a[236:239], v[140:143]\n"
"v_perm_b32 v27, v93, v89, s33\n"
"v_mfma_f32_16x16x32_bf16 v[144:147], v[20:23], a[224:227], v[144:147]\n"
"global_load_dwordx4 v[48:51], v[12:13], off sc1\n"
"v_perm_b32 v28, v69, v65, s34\n"
"v_mfma_f32_16x16x32_bf16 v[148:151], v[20:23], a[228:231], v[148:151]\n"
"v_perm_b32 v29, v77, v73, s34\n"
"v_mfma_f32_16x16x32_bf16 v[152:155], v[20:23], a[232:235], v[152:155]\n"
"v_perm_b32 v30, v85, v81, s34\n"
"v_mfma_f32_16x16x32_bf16 v[156:159], v[20:23], a[236:239], v[156:159]\n"
"global_load_dwordx4 v[52:55], v[12:13], off offset:3584 sc1\n"
"v_perm_b32 v31, v93, v89, s34\n"
"v_mfma_f32_16x16x32_bf16 v[160:163], v[24:27], a[224:227], v[160:163]\n"
"v_perm_b32 v16, v70, v66, s33\n"
"v_mfma_f32_16x16x32_bf16 v[164:167], v[24:27], a[228:231], v[164:167]\n"
"ds_read_b128 a[240:243], v1 offset:45056\n"
"v_perm_b32 v17, v78, v74, s33\n"
"v_mfma_f32_16x16x32_bf16 v[168:171], v[24:27], a[232:235], v[168:171]\n"
"global_load_dwordx4 v[56:59], v[14:15], off sc1\n"
"v_perm_b32 v18, v86, v82, s33\n"
"v_mfma_f32_16x16x32_bf16 v[172:175], v[24:27], a[236:239], v[172:175]\n"
"v_perm_b32 v19, v94, v90, s33\n"
"v_mfma_f32_16x16x32_bf16 v[176:179], v[28:31], a[224:227], v[176:179]\n"
"v_perm_b32 v20, v70, v66, s34\n"
"v_mfma_f32_16x16x32_bf16 v[180:183], v[28:31], a[228:231], v[180:183]\n"
"global_load_dwordx4 v[60:63], v[14:15], off offset:3584 sc1\n"
"v_perm_b32 v21, v78, v74, s34\n"
"v_mfma_f32_16x16x32_bf16 v[184:187], v[28:31], a[232:235], v[184:187]\n"
"ds_read_b128 a[244:247], v1 offset:46080\n"
"v_perm_b32 v22, v86, v82, s34\n"
"v_mfma_f32_16x16x32_bf16 v[188:191], v[28:31], a[236:239], v[188:191]\n"
"v_perm_b32 v23, v94, v90, s34\n"
"s_add_u32 s64, s64, 1\n"
"s_and_b32 s64, s64, 15\n"
"s_cselect_b32 s66, s65, 0xffe5c000\n"
"s_cselect_b32 s67, 0, -1\n"
"v_mfma_f32_16x16x32_bf16 v[192:195], v[16:19], a[224:227], v[192:195]\n"
"v_perm_b32 v24, v71, v67, s33\n"
"v_mfma_f32_16x16x32_bf16 v[196:199], v[16:19], a[228:231], v[196:199]\n"
"ds_read_b128 a[248:251], v1 offset:47104\n"
"v_perm_b32 v25, v79, v75, s33\n"
"v_lshl_add_u64 v[2:3], v[2:3], 0, s[66:67]\n"
"v_mfma_f32_16x16x32_bf16 v[200:203], v[16:19], a[232:235], v[200:203]\n"
"v_perm_b32 v26, v87, v83, s33\n"
"v_mfma_f32_16x16x32_bf16 v[204:207], v[16:19], a[236:239], v[204:207]\n"
"v_perm_b32 v27, v95, v91, s33\n"
"v_lshl_add_u64 v[10:11], v[10:11], 0, s[66:67]\n"
"v_mfma_f32_16x16x32_bf16 v[208:211], v[20:23], a[224:227], v[208:211]\n"
"v_perm_b32 v28, v71, v67, s34\n"
"v_mfma_f32_16x16x32_bf16 v[212:215], v[20:23], a[228:231], v[212:215]\n"
"ds_read_b128 a[252:255], v1 offset:48128\n"
"v_perm_b32 v29, v79, v75, s34\n"
"v_lshl_add_u64 v[12:13], v[12:13], 0, s[66:67]\n"
"v_mfma_f32_16x16x32_bf16 v[216:219], v[20:23], a[232:235], v[216:219]\n"
"v_perm_b32 v30, v87, v83, s34\n"
"v_mfma_f32_16x16x32_bf16 v[220:223], v[20:23], a[236:239], v[220:223]\n"
"v_perm_b32 v31, v95, v91, s34\n"
"v_lshl_add_u64 v[14:15], v[14:15], 0, s[66:67]\n"
"v_mfma_f32_16x16x32_bf16 v[224:227], v[24:27], a[224:227], v[224:227]\n"
"global_load_dwordx4 v[64:67], v[2:3], off sc1\n"
"s_waitcnt vmcnt(9)\n"
"v_perm_b32 v16, v100, v96, s33\n"
"v_mfma_f32_16x16x32_bf16 v[228:231], v[24:27], a[228:231], v[228:231]\n"
"v_perm_b32 v17, v108, v104, s33\n"
"v_mfma_f32_16x16x32_bf16 v[232:235], v[24:27], a[232:235], v[232:235]\n"
"v_perm_b32 v18, v116, v112, s33\n"
"v_mfma_f32_16x16x32_bf16 v[236:239], v[24:27], a[236:239], v[236:239]\n"
"global_load_dwordx4 v[68:71], v[2:3], off offset:3584 sc1\n"
"v_perm_b32 v19, v124, v120, s33\n"
"v_mfma_f32_16x16x32_bf16 v[240:243], v[28:31], a[224:227], v[240:243]\n"
"v_perm_b32 v20, v100, v96, s34\n"
"v_mfma_f32_16x16x32_bf16 v[244:247], v[28:31], a[228:231], v[244:247]\n"
"v_perm_b32 v21, v108, v104, s34\n"
"v_mfma_f32_16x16x32_bf16 v[248:251], v[28:31], a[232:235], v[248:251]\n"
"global_load_dwordx4 v[72:75], v[10:11], off sc1\n"
"v_perm_b32 v22, v116, v112, s34\n"
"v_mfma_f32_16x16x32_bf16 v[252:255], v[28:31], a[236:239], v[252:255]\n"
"v_perm_b32 v23, v124, v120, s34\n"
"; f ks=11\n"
"s_waitcnt lgkmcnt(0)\n"
"v_mfma_f32_16x16x32_bf16 v[128:131], v[16:19], a[240:243], v[128:131]\n"
"v_perm_b32 v24, v101, v97, s33\n"
"v_lshrrev_b32 v6, 4, v1\n"
"v_mfma_f32_16x16x32_bf16 v[132:135], v[16:19], a[244:247], v[132:135]\n"
"global_load_dwordx4 v[76:79], v[10:11], off offset:3584 sc1\n"
"v_perm_b32 v25, v109, v105, s33\n"
"v_mfma_f32_16x16x32_bf16 v[136:139], v[16:19], a[248:251], v[136:139]\n"
"v_perm_b32 v26, v117, v113, s33\n"
"s_and_b32 s74, s83, 3\n"
"v_mfma_f32_16x16x32_bf16 v[140:143], v[16:19], a[252:255], v[140:143]\n"
"v_perm_b32 v27, v125, v121, s33\n"
"s_lshl_b32 s74, s74, 16\n"
"v_mfma_f32_16x16x32_bf16 v[144:147], v[20:23], a[240:243], v[144:147]\n"
"global_load_dwordx4 v[80:83], v[12:13], off sc1\n"
"v_perm_b32 v28, v101, v97, s34\n"
"v_mfma_f32_16x16x32_bf16 v[148:151], v[20:23], a[244:247], v[148:151]\n"
"v_perm_b32 v29, v109, v105, s34\n"
"s_lshl_b32 s11, s80, 18\n"
"v_mfma_f32_16x16x32_bf16 v[152:155], v[20:23], a[248:251], v[152:155]\n"
"v_perm_b32 v30, v117, v113, s34\n"
"s_lshl_b32 s15, s7, 6\n"
"v_mfma_f32_16x16x32_bf16 v[156:159], v[20:23], a[252:255], v[156:159]\n"
"global_load_dwordx4 v[84:87], v[12:13], off offset:3584 sc1\n"
"v_perm_b32 v31, v125, v121, s34\n"
"v_mfma_f32_16x16x32_bf16 v[160:163], v[24:27], a[240:243], v[160:163]\n"
"v_perm_b32 v16, v102, v98, s33\n"
"s_add_u32 s11, s11, s15\n"
"v_mfma_f32_16x16x32_bf16 v[164:167], v[24:27], a[244:247], v[164:167]\n"
"ds_read_b128 a[224:227], v1 offset:49152\n"
"v_perm_b32 v17, v110, v106, s33\n"
"v_mfma_f32_16x16x32_bf16 v[168:171], v[24:27], a[248:251], v[168:171]\n"
"global_load_dwordx4 v[88:91], v[14:15], off sc1\n"
"v_perm_b32 v18, v118, v114, s33\n"
"v_mfma_f32_16x16x32_bf16 v[172:175], v[24:27], a[252:255], v[172:175]\n"
"v_perm_b32 v19, v126, v122, s33\n"
"s_add_u32 s16, s58, s11\n"
"s_addc_u32 s17, s59, 0\n"
"v_mfma_f32_16x16x32_bf16 v[176:179], v[28:31], a[240:243], v[176:179]\n"
"v_perm_b32 v20, v102, v98, s34\n"
"s_and_b32 s17, s17, 0xffff\n"
"v_mfma_f32_16x16x32_bf16 v[180:183], v[28:31], a[244:247], v[180:183]\n"
"global_load_dwordx4 v[92:95], v[14:15], off offset:3584 sc1\n"
"v_perm_b32 v21, v110, v106, s34\n"
"v_mfma_f32_16x16x32_bf16 v[184:187], v[28:31], a[248:251], v[184:187]\n"
"ds_read_b128 a[228:231], v1 offset:50176\n"
"v_perm_b32 v22, v118, v114, s34\n"
"v_mfma_f32_16x16x32_bf16 v[188:191], v[28:31], a[252:255], v[188:191]\n"
"v_perm_b32 v23, v126, v122, s34\n"
"s_add_u32 s64, s64, 1\n"
"s_and_b32 s64, s64, 15\n"
"s_cselect_b32 s66, s65, 0xffe5c000\n"
"s_cselect_b32 s67, 0, -1\n"
"v_mfma_f32_16x16x32_bf16 v[192:195], v[16:19], a[240:243], v[192:195]\n"
"v_perm_b32 v24, v103, v99, s33\n"
"s_mov_b32 s18, 0x3ff40\n"
"v_mfma_f32_16x16x32_bf16 v[196:199], v[16:19], a[244:247], v[196:199]\n"
"ds_read_b128 a[232:235], v1 offset:51200\n"
"v_perm_b32 v25, v111, v107, s33\n"
"v_lshl_add_u64 v[2:3], v[2:3], 0, s[66:67]\n"
"v_mfma_f32_16x16x32_bf16 v[200:203], v[16:19], a[248:251], v[200:203]\n"
"v_perm_b32 v26, v119, v115, s33\n"
"s_mov_b32 s19, 0x20000\n"
"v_mfma_f32_16x16x32_bf16 v[204:207], v[16:19], a[252:255], v[204:207]\n"
"v_perm_b32 v27, v127, v123, s33\n"
"v_lshl_add_u64 v[10:11], v[10:11], 0, s[66:67]\n"
"v_mfma_f32_16x16x32_bf16 v[208:211], v[20:23], a[240:243], v[208:211]\n"
"v_perm_b32 v28, v103, v99, s34\n"
"v_and_b32 v4, 3, v6\n"
"v_mfma_f32_16x16x32_bf16 v[212:215], v[20:23], a[244:247], v[212:215]\n"
"ds_read_b128 a[236:239], v1 offset:52224\n"
"v_perm_b32 v29, v111, v107, s34\n"
"v_lshl_add_u64 v[12:13], v[12:13], 0, s[66:67]\n"
"v_mfma_f32_16x16x32_bf16 v[216:219], v[20:23], a[248:251], v[216:219]\n"
"v_perm_b32 v30, v119, v115, s34\n"
"v_bfe_u32 v5, v6, 4, 1\n"
"v_mfma_f32_16x16x32_bf16 v[220:223], v[20:23], a[252:255], v[220:223]\n"
"v_perm_b32 v31, v127, v123, s34\n"
"v_lshl_add_u64 v[14:15], v[14:15], 0, s[66:67]\n"
"v_mfma_f32_16x16x32_bf16 v[224:227], v[24:27], a[240:243], v[224:227]\n"
"global_load_dwordx4 v[96:99], v[2:3], off sc1\n"
"s_waitcnt vmcnt(9)\n"
"v_perm_b32 v16, v36, v32, s33\n"
"v_mfma_f32_16x16x32_bf16 v[228:231], v[24:27], a[244:247], v[228:231]\n"
"v_perm_b32 v17, v44, v40, s33\n"
"v_lshlrev_b32 v5, 1, v5\n"
"v_mfma_f32_16x16x32_bf16 v[232:235], v[24:27], a[248:251], v[232:235]\n"
"v_perm_b32 v18, v52, v48, s33\n"
"v_xor_b32 v4, v4, v5\n"
"v_mfma_f32_16x16x32_bf16 v[236:239], v[24:27], a[252:255], v[236:239]\n"
"global_load_dwordx4 v[100:103], v[2:3], off offset:3584 sc1\n"
"v_perm_b32 v19, v60, v56, s33\n"
"v_mfma_f32_16x16x32_bf16 v[240:243], v[28:31], a[240:243], v[240:243]\n"
"v_perm_b32 v20, v36, v32, s34\n"
"v_lshlrev_b32 v7, 4, v4\n"
"v_mfma_f32_16x16x32_bf16 v[244:247], v[28:31], a[244:247], v[244:247]\n"
"v_perm_b32 v21, v44, v40, s34\n"
"v_bfe_u32 v4, v6, 4, 1\n"
"v_mfma_f32_16x16x32_bf16 v[248:251], v[28:31], a[248:251], v[248:251]\n"
"global_load_dwordx4 v[104:107], v[10:11], off sc1\n"
"v_perm_b32 v22, v52, v48, s34\n"
"v_mfma_f32_16x16x32_bf16 v[252:255], v[28:31], a[252:255], v[252:255]\n"
"v_perm_b32 v23, v60, v56, s34\n"
"; f ks=12\n"
"s_waitcnt lgkmcnt(0)\n"
"v_mfma_f32_16x16x32_bf16 v[128:131], v[16:19], a[224:227], v[128:131]\n"
"v_perm_b32 v24, v37, v33, s33\n"
"v_lshlrev_b32 v4, 5, v4\n"
"v_mfma_f32_16x16x32_bf16 v[132:135], v[16:19], a[228:231], v[132:135]\n"
"global_load_dwordx4 v[108:111], v[10:11], off offset:3584 sc1\n"
"v_perm_b32 v25, v45, v41, s33\n"
"v_mfma_f32_16x16x32_bf16 v[136:139], v[16:19], a[232:235], v[136:139]\n"
"v_perm_b32 v26, v53, v49, s33\n"
"v_lshrrev_b32 v5, 5, v6\n"
"v_mfma_f32_16x16x32_bf16 v[140:143], v[16:19], a[236:239], v[140:143]\n"
"v_perm_b32 v27, v61, v57, s33\n"
"v_lshl_add_u32 v4, v5, 4, v4\n"
"v_mfma_f32_16x16x32_bf16 v[144:147], v[20:23], a[224:227], v[144:147]\n"
"global_load_dwordx4 v[112:115], v[12:13], off sc1\n"
"v_perm_b32 v28, v37, v33, s34\n"
"v_mfma_f32_16x16x32_bf16 v[148:151], v[20:23], a[228:231], v[148:151]\n"
"v_perm_b32 v29, v45, v41, s34\n"
"v_bfe_u32 v5, v6, 2, 2\n"
"v_mfma_f32_16x16x32_bf16 v[152:155], v[20:23], a[232:235], v[152:155]\n"
"v_perm_b32 v30, v53, v49, s34\n"
"v_add_u32 v4, v4, v5\n"
"v_mfma_f32_16x16x32_bf16 v[156:159], v[20:23], a[236:239], v[156:159]\n"
"global_load_dwordx4 v[116:119], v[12:13], off offset:3584 sc1\n"
"v_perm_b32 v31, v61, v57, s34\n"
"v_mfma_f32_16x16x32_bf16 v[160:163], v[24:27], a[224:227], v[160:163]\n"
"v_perm_b32 v16, v38, v34, s33\n"
"v_lshl_add_u32 v7, v4, 10, v7\n"
"v_mfma_f32_16x16x32_bf16 v[164:167], v[24:27], a[228:231], v[164:167]\n"
"ds_read_b128 a[240:243], v1 offset:53248\n"
"v_perm_b32 v17, v46, v42, s33\n"
"v_mfma_f32_16x16x32_bf16 v[168:171], v[24:27], a[232:235], v[168:171]\n"
"global_load_dwordx4 v[120:123], v[14:15], off sc1\n"
"v_perm_b32 v18, v54, v50, s33\n"
"v_mfma_f32_16x16x32_bf16 v[172:175], v[24:27], a[236:239], v[172:175]\n"
"v_perm_b32 v19, v62, v58, s33\n"
"v_add_u32 v9, 0xc00, v7\n"
"v_mfma_f32_16x16x32_bf16 v[176:179], v[28:31], a[224:227], v[176:179]\n"
"v_perm_b32 v20, v38, v34, s34\n"
"v_mfma_f32_16x16x32_bf16 v[180:183], v[28:31], a[228:231], v[180:183]\n"
"global_load_dwordx4 v[124:127], v[14:15], off offset:3584 sc1\n"
"v_perm_b32 v21, v46, v42, s34\n"
"v_mfma_f32_16x16x32_bf16 v[184:187], v[28:31], a[232:235], v[184:187]\n"
"ds_read_b128 a[244:247], v1 offset:54272\n"
"v_perm_b32 v22, v54, v50, s34\n"
"v_mfma_f32_16x16x32_bf16 v[188:191], v[28:31], a[236:239], v[188:191]\n"
"v_perm_b32 v23, v62, v58, s34\n"
"s_add_u32 s64, s64, 1\n"
"s_and_b32 s64, s64, 15\n"
"s_cselect_b32 s66, s65, 0xffe5c000\n"
"s_cselect_b32 s67, 0, -1\n"
"v_mfma_f32_16x16x32_bf16 v[192:195], v[16:19], a[224:227], v[192:195]\n"
"v_perm_b32 v24, v39, v35, s33\n"
"v_mfma_f32_16x16x32_bf16 v[196:199], v[16:19], a[228:231], v[196:199]\n"
"ds_read_b128 a[248:251], v1 offset:55296\n"
"v_perm_b32 v25, v47, v43, s33\n"
"v_lshl_add_u64 v[2:3], v[2:3], 0, s[66:67]\n"
"v_mfma_f32_16x16x32_bf16 v[200:203], v[16:19], a[232:235], v[200:203]\n"
"v_perm_b32 v26, v55, v51, s33\n"
"v_mfma_f32_16x16x32_bf16 v[204:207], v[16:19], a[236:239], v[204:207]\n"
"v_perm_b32 v27, v63, v59, s33\n"
"v_lshl_add_u64 v[10:11], v[10:11], 0, s[66:67]\n"
"v_mfma_f32_16x16x32_bf16 v[208:211], v[20:23], a[224:227], v[208:211]\n"
"v_perm_b32 v28, v39, v35, s34\n"
"v_mfma_f32_16x16x32_bf16 v[212:215], v[20:23], a[228:231], v[212:215]\n"
"ds_read_b128 a[252:255], v1 offset:56320\n"
"v_perm_b32 v29, v47, v43, s34\n"
"v_lshl_add_u64 v[12:13], v[12:13], 0, s[66:67]\n"
"v_mfma_f32_16x16x32_bf16 v[216:219], v[20:23], a[232:235], v[216:219]\n"
"v_perm_b32 v30, v55, v51, s34\n"
"v_mfma_f32_16x16x32_bf16 v[220:223], v[20:23], a[236:239], v[220:223]\n"
"v_perm_b32 v31, v63, v59, s34\n"
"v_lshl_add_u64 v[14:15], v[14:15], 0, s[66:67]\n"
"v_mfma_f32_16x16x32_bf16 v[224:227], v[24:27], a[224:227], v[224:227]\n"
"global_load_dwordx4 v[32:35], v[2:3], off sc1\n"
"s_waitcnt vmcnt(9)\n"
"v_perm_b32 v16, v68, v64, s33\n"
"v_mfma_f32_16x16x32_bf16 v[228:231], v[24:27], a[228:231], v[228:231]\n"
"v_perm_b32 v17, v76, v72, s33\n"
"v_mfma_f32_16x16x32_bf16 v[232:235], v[24:27], a[232:235], v[232:235]\n"
"v_perm_b32 v18, v84, v80, s33\n"
"v_mfma_f32_16x16x32_bf16 v[236:239], v[24:27], a[236:239], v[236:239]\n"
"global_load_dwordx4 v[36:39], v[2:3], off offset:3584 sc1\n"
"v_perm_b32 v19, v92, v88, s33\n"
"v_mfma_f32_16x16x32_bf16 v[240:243], v[28:31], a[224:227], v[240:243]\n"
"v_perm_b32 v20, v68, v64, s34\n"
"v_mfma_f32_16x16x32_bf16 v[244:247], v[28:31], a[228:231], v[244:247]\n"
"v_perm_b32 v21, v76, v72, s34\n"
"v_mfma_f32_16x16x32_bf16 v[248:251], v[28:31], a[232:235], v[248:251]\n"
"global_load_dwordx4 v[40:43], v[10:11], off sc1\n"
"v_perm_b32 v22, v84, v80, s34\n"
"v_mfma_f32_16x16x32_bf16 v[252:255], v[28:31], a[236:239], v[252:255]\n"
"v_perm_b32 v23, v92, v88, s34\n"
"; f ks=13\n"
"s_waitcnt lgkmcnt(0)\n"
"v_mfma_f32_16x16x32_bf16 v[128:131], v[16:19], a[240:243], v[128:131]\n"
"v_perm_b32 v24, v69, v65, s33\n"
"v_mfma_f32_16x16x32_bf16 v[132:135], v[16:19], a[244:247], v[132:135]\n"
"global_load_dwordx4 v[44:47], v[10:11], off offset:3584 sc1\n"
"v_perm_b32 v25, v77, v73, s33\n"
"v_mfma_f32_16x16x32_bf16 v[136:139], v[16:19], a[248:251], v[136:139]\n"
"v_perm_b32 v26, v85, v81, s33\n"
"v_mfma_f32_16x16x32_bf16 v[140:143], v[16:19], a[252:255], v[140:143]\n"
"v_perm_b32 v27, v93, v89, s33\n"
"v_mfma_f32_16x16x32_bf16 v[144:147], v[20:23], a[240:243], v[144:147]\n"
"global_load_dwordx4 v[48:51], v[12:13], off sc1\n"
"v_perm_b32 v28, v69, v65, s34\n"
"v_mfma_f32_16x16x32_bf16 v[148:151], v[20:23], a[244:247], v[148:151]\n"
"v_perm_b32 v29, v77, v73, s34\n"
"v_mfma_f32_16x16x32_bf16 v[152:155], v[20:23], a[248:251], v[152:155]\n"
"v_perm_b32 v30, v85, v81, s34\n"
"v_mfma_f32_16x16x32_bf16 v[156:159], v[20:23], a[252:255], v[156:159]\n"
"global_load_dwordx4 v[52:55], v[12:13], off offset:3584 sc1\n"
"v_perm_b32 v31, v93, v89, s34\n"
"v_mfma_f32_16x16x32_bf16 v[160:163], v[24:27], a[240:243], v[160:163]\n"
"v_perm_b32 v16, v70, v66, s33\n"
"v_mfma_f32_16x16x32_bf16 v[164:167], v[24:27], a[244:247], v[164:167]\n"
"ds_read_b128 a[224:227], v1 offset:57344\n"
"v_perm_b32 v17, v78, v74, s33\n"
"v_mfma_f32_16x16x32_bf16 v[168:171], v[24:27], a[248:251], v[168:171]\n"
"global_load_dwordx4 v[56:59], v[14:15], off sc1\n"
"v_perm_b32 v18, v86, v82, s33\n"
"v_mfma_f32_16x16x32_bf16 v[172:175], v[24:27], a[252:255], v[172:175]\n"
"v_perm_b32 v19, v94, v90, s33\n"
"v_mfma_f32_16x16x32_bf16 v[176:179], v[28:31], a[240:243], v[176:179]\n"
"v_perm_b32 v20, v70, v66, s34\n"
"v_mfma_f32_16x16x32_bf16 v[180:183], v[28:31], a[244:247], v[180:183]\n"
"global_load_dwordx4 v[60:63], v[14:15], off offset:3584 sc1\n"
"v_perm_b32 v21, v78, v74, s34\n"
"v_mfma_f32_16x16x32_bf16 v[184:187], v[28:31], a[248:251], v[184:187]\n"
"ds_read_b128 a[228:231], v1 offset:58368\n"
"v_perm_b32 v22, v86, v82, s34\n"
"v_mfma_f32_16x16x32_bf16 v[188:191], v[28:31], a[252:255], v[188:191]\n"
"global_load_dwordx4 a[180:183], v1, s[44:45]\n"
"v_perm_b32 v23, v94, v90, s34\n"
"v_mfma_f32_16x16x32_bf16 v[192:195], v[16:19], a[240:243], v[192:195]\n"
"v_perm_b32 v24, v71, v67, s33\n"
"s_add_u32 m0, s79, 0x18000\n"
"v_mfma_f32_16x16x32_bf16 v[196:199], v[16:19], a[244:247], v[196:199]\n"
"ds_read_b128 a[232:235], v1 offset:59392\n"
"v_perm_b32 v25, v79, v75, s33\n"
"s_add_u32 s35, s74, 0\n"
"s_and_b32 s35, s35, 0x3ffff\n"
"v_mfma_f32_16x16x32_bf16 v[200:203], v[16:19], a[248:251], v[200:203]\n"
"buffer_load_dwordx4 v7, s[16:19], s35 offen lds\n"
"v_perm_b32 v26, v87, v83, s33\n"
"v_mfma_f32_16x16x32_bf16 v[204:207], v[16:19], a[252:255], v[204:207]\n"
"v_perm_b32 v27, v95, v91, s33\n"
"v_mfma_f32_16x16x32_bf16 v[208:211], v[20:23], a[240:243], v[208:211]\n"
"buffer_load_dwordx4 v9, s[16:19], s35 offen offset:1024 lds\n"
"v_perm_b32 v28, v71, v67, s34\n"
"v_mfma_f32_16x16x32_bf16 v[212:215], v[20:23], a[244:247], v[212:215]\n"
"ds_read_b128 a[236:239], v1 offset:60416\n"
"v_perm_b32 v29, v79, v75, s34\n"
"v_mfma_f32_16x16x32_bf16 v[216:219], v[20:23], a[248:251], v[216:219]\n"
"v_perm_b32 v30, v87, v83, s34\n"
"v_mfma_f32_16x16x32_bf16 v[220:223], v[20:23], a[252:255], v[220:223]\n"
"v_perm_b32 v31, v95, v91, s34\n"
"v_mfma_f32_16x16x32_bf16 v[224:227], v[24:27], a[240:243], v[224:227]\n"
"s_waitcnt vmcnt(11)\n"
"v_perm_b32 v16, v100, v96, s33\n"
"s_add_u32 m0, s79, 0x1a000\n"
"v_mfma_f32_16x16x32_bf16 v[228:231], v[24:27], a[244:247], v[228:231]\n"
"v_perm_b32 v17, v108, v104, s33\n"
"s_add_u32 s35, s74, 0x2000\n"
"s_and_b32 s35, s35, 0x3ffff\n"
"v_mfma_f32_16x16x32_bf16 v[232:235], v[24:27], a[248:251], v[232:235]\n"
"buffer_load_dwordx4 v7, s[16:19], s35 offen lds\n"
"v_perm_b32 v18, v116, v112, s33\n"
"v_mfma_f32_16x16x32_bf16 v[236:239], v[24:27], a[252:255], v[236:239]\n"
"v_perm_b32 v19, v124, v120, s33\n"
"v_mfma_f32_16x16x32_bf16 v[240:243], v[28:31], a[240:243], v[240:243]\n"
"buffer_load_dwordx4 v9, s[16:19], s35 offen offset:1024 lds\n"
"v_perm_b32 v20, v100, v96, s34\n"
"v_mfma_f32_16x16x32_bf16 v[244:247], v[28:31], a[244:247], v[244:247]\n"
"v_perm_b32 v21, v108, v104, s34\n"
"v_mfma_f32_16x16x32_bf16 v[248:251], v[28:31], a[248:251], v[248:251]\n"
"v_perm_b32 v22, v116, v112, s34\n"
"v_mfma_f32_16x16x32_bf16 v[252:255], v[28:31], a[252:255], v[252:255]\n"
"v_perm_b32 v23, v124, v120, s34\n"
"; f ks=14\n"
"s_waitcnt lgkmcnt(0)\n"
"v_mfma_f32_16x16x32_bf16 v[128:131], v[16:19], a[224:227], v[128:131]\n"
"v_perm_b32 v24, v101, v97, s33\n"
"s_add_u32 m0, s79, 0x1c000\n"
"v_mfma_f32_16x16x32_bf16 v[132:135], v[16:19], a[228:231], v[132:135]\n"
"v_perm_b32 v25, v109, v105, s33\n"
"s_add_u32 s35, s74, 0x10000\n"
"s_and_b32 s35, s35, 0x3ffff\n"
"v_mfma_f32_16x16x32_bf16 v[136:139], v[16:19], a[232:235], v[136:139]\n"
"buffer_load_dwordx4 v7, s[16:19], s35 offen lds\n"
"v_perm_b32 v26, v117, v113, s33\n"
"v_mfma_f32_16x16x32_bf16 v[140:143], v[16:19], a[236:239], v[140:143]\n"
"v_perm_b32 v27, v125, v121, s33\n"
"v_mfma_f32_16x16x32_bf16 v[144:147], v[20:23], a[224:227], v[144:147]\n"
"buffer_load_dwordx4 v9, s[16:19], s35 offen offset:1024 lds\n"
"v_perm_b32 v28, v101, v97, s34\n"
"v_mfma_f32_16x16x32_bf16 v[148:151], v[20:23], a[228:231], v[148:151]\n"
"v_perm_b32 v29, v109, v105, s34\n"
"v_mfma_f32_16x16x32_bf16 v[152:155], v[20:23], a[232:235], v[152:155]\n"
"v_perm_b32 v30, v117, v113, s34\n"
"v_mfma_f32_16x16x32_bf16 v[156:159], v[20:23], a[236:239], v[156:159]\n"
"v_perm_b32 v31, v125, v121, s34\n"
"v_mfma_f32_16x16x32_bf16 v[160:163], v[24:27], a[224:227], v[160:163]\n"
"v_perm_b32 v16, v102, v98, s33\n"
"s_add_u32 m0, s79, 0x1e000\n"
"v_mfma_f32_16x16x32_bf16 v[164:167], v[24:27], a[228:231], v[164:167]\n"
"ds_read_b128 a[240:243], v1 offset:61440\n"
"v_perm_b32 v17, v110, v106, s33\n"
"s_add_u32 s35, s74, 0x12000\n"
"s_and_b32 s35, s35, 0x3ffff\n"
"v_mfma_f32_16x16x32_bf16 v[168:171], v[24:27], a[232:235], v[168:171]\n"
"buffer_load_dwordx4 v7, s[16:19], s35 offen lds\n"
"v_perm_b32 v18, v118, v114, s33\n"
"v_mfma_f32_16x16x32_bf16 v[172:175], v[24:27], a[236:239], v[172:175]\n"
"v_perm_b32 v19, v126, v122, s33\n"
"v_mfma_f32_16x16x32_bf16 v[176:179], v[28:31], a[224:227], v[176:179]\n"
"buffer_load_dwordx4 v9, s[16:19], s35 offen offset:1024 lds\n"
"v_perm_b32 v20, v102, v98, s34\n"
"v_mfma_f32_16x16x32_bf16 v[180:183], v[28:31], a[228:231], v[180:183]\n"
"ds_read_b128 a[244:247], v1 offset:62464\n"
"v_perm_b32 v21, v110, v106, s34\n"
"v_mfma_f32_16x16x32_bf16 v[184:187], v[28:31], a[232:235], v[184:187]\n"
"v_perm_b32 v22, v118, v114, s34\n"
"v_mfma_f32_16x16x32_bf16 v[188:191], v[28:31], a[236:239], v[188:191]\n"
"v_perm_b32 v23, v126, v122, s34\n"
"v_mfma_f32_16x16x32_bf16 v[192:195], v[16:19], a[224:227], v[192:195]\n"
"v_perm_b32 v24, v103, v99, s33\n"
"s_add_u32 m0, s79, 0x20000\n"
"v_mfma_f32_16x16x32_bf16 v[196:199], v[16:19], a[228:231], v[196:199]\n"
"ds_read_b128 a[248:251], v1 offset:63488\n"
"v_perm_b32 v25, v111, v107, s33\n"
"s_add_u32 s35, s74, 0x20000\n"
"s_and_b32 s35, s35, 0x3ffff\n"
"v_mfma_f32_16x16x32_bf16 v[200:203], v[16:19], a[232:235], v[200:203]\n"
"buffer_load_dwordx4 v7, s[16:19], s35 offen lds\n"
"v_perm_b32 v26, v119, v115, s33\n"
"v_mfma_f32_16x16x32_bf16 v[204:207], v[16:19], a[236:239], v[204:207]\n"
"v_perm_b32 v27, v127, v123, s33\n"
"v_mfma_f32_16x16x32_bf16 v[208:211], v[20:23], a[224:227], v[208:211]\n"
"buffer_load_dwordx4 v9, s[16:19], s35 offen offset:1024 lds\n"
"v_perm_b32 v28, v103, v99, s34\n"
"v_mfma_f32_16x16x32_bf16 v[212:215], v[20:23], a[228:231], v[212:215]\n"
"ds_read_b128 a[252:255], v1 offset:64512\n"
"v_perm_b32 v29, v111, v107, s34\n"
"v_mfma_f32_16x16x32_bf16 v[216:219], v[20:23], a[232:235], v[216:219]\n"
"v_perm_b32 v30, v119, v115, s34\n"
"v_mfma_f32_16x16x32_bf16 v[220:223], v[20:23], a[236:239], v[220:223]\n"
"v_perm_b32 v31, v127, v123, s34\n"
"v_mfma_f32_16x16x32_bf16 v[224:227], v[24:27], a[224:227], v[224:227]\n"
"s_waitcnt vmcnt(11)\n"
"v_perm_b32 v16, v36, v32, s33\n"
"s_add_u32 m0, s79, 0x22000\n"
"v_mfma_f32_16x16x32_bf16 v[228:231], v[24:27], a[228:231], v[228:231]\n"
"v_perm_b32 v17, v44, v40, s33\n"
"s_add_u32 s35, s74, 0x22000\n"
"s_and_b32 s35, s35, 0x3ffff\n"
"v_mfma_f32_16x16x32_bf16 v[232:235], v[24:27], a[232:235], v[232:235]\n"
"buffer_load_dwordx4 v7, s[16:19], s35 offen lds\n"
"v_perm_b32 v18, v52, v48, s33\n"
"v_mfma_f32_16x16x32_bf16 v[236:239], v[24:27], a[236:239], v[236:239]\n"
"v_perm_b32 v19, v60, v56, s33\n"
"v_mfma_f32_16x16x32_bf16 v[240:243], v[28:31], a[224:227], v[240:243]\n"
"buffer_load_dwordx4 v9, s[16:19], s35 offen offset:1024 lds\n"
"v_perm_b32 v20, v36, v32, s34\n"
"v_mfma_f32_16x16x32_bf16 v[244:247], v[28:31], a[228:231], v[244:247]\n"
"v_perm_b32 v21, v44, v40, s34\n"
"v_mfma_f32_16x16x32_bf16 v[248:251], v[28:31], a[232:235], v[248:251]\n"
"v_perm_b32 v22, v52, v48, s34\n"
"v_mfma_f32_16x16x32_bf16 v[252:255], v[28:31], a[236:239], v[252:255]\n"
"v_perm_b32 v23, v60, v56, s34\n"
"; f ks=15\n"
"s_waitcnt lgkmcnt(0)\n"
"v_mfma_f32_16x16x32_bf16 v[128:131], v[16:19], a[240:243], v[128:131]\n"
"v_perm_b32 v24, v37, v33, s33\n"
"v_mfma_f32_16x16x32_bf16 v[132:135], v[16:19], a[244:247], v[132:135]\n"
"v_perm_b32 v25, v45, v41, s33\n"
"v_mfma_f32_16x16x32_bf16 v[136:139], v[16:19], a[248:251], v[136:139]\n"
"v_perm_b32 v26, v53, v49, s33\n"
"v_mfma_f32_16x16x32_bf16 v[140:143], v[16:19], a[252:255], v[140:143]\n"
"v_perm_b32 v27, v61, v57, s33\n"
"v_mfma_f32_16x16x32_bf16 v[144:147], v[20:23], a[240:243], v[144:147]\n"
"v_perm_b32 v28, v37, v33, s34\n"
"v_mfma_f32_16x16x32_bf16 v[148:151], v[20:23], a[244:247], v[148:151]\n"
"v_perm_b32 v29, v45, v41, s34\n"
"v_mfma_f32_16x16x32_bf16 v[152:155], v[20:23], a[248:251], v[152:155]\n"
"v_perm_b32 v30, v53, v49, s34\n"
"v_mfma_f32_16x16x32_bf16 v[156:159], v[20:23], a[252:255], v[156:159]\n"
"v_perm_b32 v31, v61, v57, s34\n"
"v_mfma_f32_16x16x32_bf16 v[160:163], v[24:27], a[240:243], v[160:163]\n"
"v_perm_b32 v16, v38, v34, s33\n"
"v_mfma_f32_16x16x32_bf16 v[164:167], v[24:27], a[244:247], v[164:167]\n"
"v_perm_b32 v17, v46, v42, s33\n"
"v_mfma_f32_16x16x32_bf16 v[168:171], v[24:27], a[248:251], v[168:171]\n"
"v_perm_b32 v18, v54, v50, s33\n"
"v_mfma_f32_16x16x32_bf16 v[172:175], v[24:27], a[252:255], v[172:175]\n"
"v_perm_b32 v19, v62, v58, s33\n"
"v_mfma_f32_16x16x32_bf16 v[176:179], v[28:31], a[240:243], v[176:179]\n"
"v_perm_b32 v20, v38, v34, s34\n"
"v_mfma_f32_16x16x32_bf16 v[180:183], v[28:31], a[244:247], v[180:183]\n"
"v_perm_b32 v21, v46, v42, s34\n"
"v_mfma_f32_16x16x32_bf16 v[184:187], v[28:31], a[248:251], v[184:187]\n"
"v_perm_b32 v22, v54, v50, s34\n"
"v_mfma_f32_16x16x32_bf16 v[188:191], v[28:31], a[252:255], v[188:191]\n"
"v_perm_b32 v23, v62, v58, s34\n"
"v_mfma_f32_16x16x32_bf16 v[192:195], v[16:19], a[240:243], v[192:195]\n"
"v_perm_b32 v24, v39, v35, s33\n"
"v_mfma_f32_16x16x32_bf16 v[196:199], v[16:19], a[244:247], v[196:199]\n"
"v_perm_b32 v25, v47, v43, s33\n"
"v_mfma_f32_16x16x32_bf16 v[200:203], v[16:19], a[248:251], v[200:203]\n"
"v_perm_b32 v26, v55, v51, s33\n"
"v_mfma_f32_16x16x32_bf16 v[204:207], v[16:19], a[252:255], v[204:207]\n"
"v_perm_b32 v27, v63, v59, s33\n"
"v_mfma_f32_16x16x32_bf16 v[208:211], v[20:23], a[240:243], v[208:211]\n"
"v_perm_b32 v28, v39, v35, s34\n"
"v_mfma_f32_16x16x32_bf16 v[212:215], v[20:23], a[244:247], v[212:215]\n"
"v_perm_b32 v29, v47, v43, s34\n"
"v_mfma_f32_16x16x32_bf16 v[216:219], v[20:23], a[248:251], v[216:219]\n"
"v_perm_b32 v30, v55, v51, s34\n"
"v_mfma_f32_16x16x32_bf16 v[220:223], v[20:23], a[252:255], v[220:223]\n"
"v_perm_b32 v31, v63, v59, s34\n"
"v_mfma_f32_16x16x32_bf16 v[224:227], v[24:27], a[240:243], v[224:227]\n"
"v_mfma_f32_16x16x32_bf16 v[228:231], v[24:27], a[244:247], v[228:231]\n"
"v_mfma_f32_16x16x32_bf16 v[232:235], v[24:27], a[248:251], v[232:235]\n"
"v_mfma_f32_16x16x32_bf16 v[236:239], v[24:27], a[252:255], v[236:239]\n"
"v_mfma_f32_16x16x32_bf16 v[240:243], v[28:31], a[240:243], v[240:243]\n"
"v_mfma_f32_16x16x32_bf16 v[244:247], v[28:31], a[244:247], v[244:247]\n"
"v_mfma_f32_16x16x32_bf16 v[248:251], v[28:31], a[248:251], v[248:251]\n"
"v_mfma_f32_16x16x32_bf16 v[252:255], v[28:31], a[252:255], v[252:255]\n"
"; f end\n"
"s_mov_b32 s64, 0xbdd2d3e8\n"
"s_mov_b32 s65, 0xbdd2d3e8\n"
"s_mov_b32 s66, 0x3f800000\n"
"s_mov_b32 s67, 0x3f800000\n"
"v_mov_b32 v10, 0xc0135761\n"
"v_mov_b32 v11, 0xc0135761\n"
"s_sub_u32 s11, s7, s83\n"
"s_and_b32 s11, s11, 3\n"
"s_lshl_b32 s11, s11, 13\n"
"s_add_u32 s11, s11, 0x10000\n"
"v_add_u32 v12, s11, v1\n"
"v_add_u32 v13, 0x10000, v1\n"
"s_barrier\n"
"s_add_u32 m0, s79, 0\n"
"s_add_u32 s35, s74, 0x30000\n"
"s_and_b32 s35, s35, 0x3ffff\n"
"buffer_load_dwordx4 v7, s[16:19], s35 offen lds\n"
"buffer_load_dwordx4 v9, s[16:19], s35 offen offset:1024 lds\n"
"v_permlane32_swap_b32 v128, v130\n"
"v_permlane32_swap_b32 v144, v146\n"
"v_permlane32_swap_b32 v160, v162\n"
"v_permlane32_swap_b32 v176, v178\n"
"v_permlane32_swap_b32 v129, v131\n"
"v_permlane32_swap_b32 v145, v147\n"
"v_permlane32_swap_b32 v161, v163\n"
"v_permlane32_swap_b32 v177, v179\n"
"v_pk_mul_f32 v[32:33], v[128:129], v[128:129]\n"
"v_pk_mul_f32 v[34:35], v[144:145], v[144:145]\n"
"v_pk_mul_f32 v[36:37], v[160:161], v[160:161]\n"
"v_pk_mul_f32 v[38:39], v[176:177], v[176:177]\n"
"v_pk_fma_f32 v[32:33], v[32:33], s[64:65], v[10:11]\n"
"v_pk_fma_f32 v[34:35], v[34:35], s[64:65], v[10:11]\n"
"v_pk_fma_f32 v[36:37], v[36:37], s[64:65], v[10:11]\n"
"v_pk_fma_f32 v[38:39], v[38:39], s[64:65], v[10:11]\n"
"v_pk_mul_f32 v[32:33], v[32:33], v[128:129]\n"
"v_pk_mul_f32 v[34:35], v[34:35], v[144:145]\n"
"v_pk_mul_f32 v[36:37], v[36:37], v[160:161]\n"
"v_pk_mul_f32 v[38:39], v[38:39], v[176:177]\n"
"v_exp_f32 v32, v32\n"
"v_exp_f32 v34, v34\n"
"v_exp_f32 v36, v36\n"
"v_exp_f32 v38, v38\n"
"v_exp_f32 v33, v33\n"
"v_exp_f32 v35, v35\n"
"v_exp_f32 v37, v37\n"
"v_exp_f32 v39, v39\n"
"v_pk_mul_f32 v[128:129], v[128:129], v[130:131]\n"
"v_pk_mul_f32 v[144:145], v[144:145], v[146:147]\n"
"v_pk_mul_f32 v[160:161], v[160:161], v[162:163]\n"
"v_pk_mul_f32 v[176:177], v[176:177], v[178:179]\n"
"v_pk_add_f32 v[32:33], v[32:33], s[66:67]\n"
"v_pk_add_f32 v[34:35], v[34:35], s[66:67]\n"
"v_pk_add_f32 v[36:37], v[36:37], s[66:67]\n"
"v_pk_add_f32 v[38:39], v[38:39], s[66:67]\n"
"v_rcp_f32 v32, v32\n"
"v_rcp_f32 v34, v34\n"
"v_rcp_f32 v36, v36\n"
"v_rcp_f32 v38, v38\n"
"v_rcp_f32 v33, v33\n"
"v_rcp_f32 v35, v35\n"
"v_rcp_f32 v37, v37\n"
"v_rcp_f32 v39, v39\n"
"v_pk_mul_f32 v[128:129], v[128:129], v[32:33]\n"
"v_pk_mul_f32 v[144:145], v[144:145], v[34:35]\n"
"v_pk_mul_f32 v[160:161], v[160:161], v[36:37]\n"
"v_pk_mul_f32 v[176:177], v[176:177], v[38:39]\n"
"s_add_u32 m0, s79, 0x2000\n"
"s_add_u32 s35, s74, 0x32000\n"
"s_and_b32 s35, s35, 0x3ffff\n"
"buffer_load_dwordx4 v7, s[16:19], s35 offen lds\n"
"buffer_load_dwordx4 v9, s[16:19], s35 offen offset:1024 lds\n"
"v_permlane32_swap_b32 v192, v194\n"
"v_permlane32_swap_b32 v208, v210\n"
"v_permlane32_swap_b32 v224, v226\n"
"v_permlane32_swap_b32 v240, v242\n"
"v_permlane32_swap_b32 v193, v195\n"
"v_permlane32_swap_b32 v209, v211\n"
"v_permlane32_swap_b32 v225, v227\n"
"v_permlane32_swap_b32 v241, v243\n"
"v_pk_mul_f32 v[40:41], v[192:193], v[192:193]\n"
"v_pk_mul_f32 v[42:43], v[208:209], v[208:209]\n"
"v_pk_mul_f32 v[44:45], v[224:225], v[224:225]\n"
"v_pk_mul_f32 v[46:47], v[240:241], v[240:241]\n"
"v_pk_fma_f32 v[40:41], v[40:41], s[64:65], v[10:11]\n"
"v_pk_fma_f32 v[42:43], v[42:43], s[64:65], v[10:11]\n"
"v_pk_fma_f32 v[44:45], v[44:45], s[64:65], v[10:11]\n"
"v_pk_fma_f32 v[46:47], v[46:47], s[64:65], v[10:11]\n"
"v_pk_mul_f32 v[40:41], v[40:41], v[192:193]\n"
"v_pk_mul_f32 v[42:43], v[42:43], v[208:209]\n"
"v_pk_mul_f32 v[44:45], v[44:45], v[224:225]\n"
"v_pk_mul_f32 v[46:47], v[46:47], v[240:241]\n"
"v_exp_f32 v40, v40\n"
"v_exp_f32 v42, v42\n"
"v_exp_f32 v44, v44\n"
"v_exp_f32 v46, v46\n"
"v_exp_f32 v41, v41\n"
"v_exp_f32 v43, v43\n"
"v_exp_f32 v45, v45\n"
"v_exp_f32 v47, v47\n"
"v_pk_mul_f32 v[192:193], v[192:193], v[194:195]\n"
"v_pk_mul_f32 v[208:209], v[208:209], v[210:211]\n"
"v_pk_mul_f32 v[224:225], v[224:225], v[226:227]\n"
"v_pk_mul_f32 v[240:241], v[240:241], v[242:243]\n"
"v_pk_add_f32 v[40:41], v[40:41], s[66:67]\n"
"v_pk_add_f32 v[42:43], v[42:43], s[66:67]\n"
"v_pk_add_f32 v[44:45], v[44:45], s[66:67]\n"
"v_pk_add_f32 v[46:47], v[46:47], s[66:67]\n"
"v_rcp_f32 v40, v40\n"
"v_rcp_f32 v42, v42\n"
"v_rcp_f32 v44, v44\n"
"v_rcp_f32 v46, v46\n"
"v_rcp_f32 v41, v41\n"
"v_rcp_f32 v43, v43\n"
"v_rcp_f32 v45, v45\n"
"v_rcp_f32 v47, v47\n"
"v_pk_mul_f32 v[192:193], v[192:193], v[40:41]\n"
"v_pk_mul_f32 v[208:209], v[208:209], v[42:43]\n"
"v_pk_mul_f32 v[224:225], v[224:225], v[44:45]\n"
"v_pk_mul_f32 v[240:241], v[240:241], v[46:47]\n"
"v_cvt_pk_bf16_f32 v64, v128, v144\n"
"v_cvt_pk_bf16_f32 v65, v160, v176\n"
"v_cvt_pk_bf16_f32 v66, v192, v208\n"
"v_cvt_pk_bf16_f32 v67, v224, v240\n"
"ds_write_b128 v12, v[64:67]\n"
"v_cvt_pk_bf16_f32 v68, v129, v145\n"
"v_cvt_pk_bf16_f32 v69, v161, v177\n"
"v_cvt_pk_bf16_f32 v70, v193, v209\n"
"v_cvt_pk_bf16_f32 v71, v225, v241\n"
"ds_write_b128 v12, v[68:71] offset:4096\n"
"s_add_u32 m0, s79, 0x4000\n"
"s_add_u32 s35, s74, 0x100\n"
"s_and_b32 s35, s35, 0x3ffff\n"
"buffer_load_dwordx4 v7, s[16:19], s35 offen lds\n"
"buffer_load_dwordx4 v9, s[16:19], s35 offen offset:1024 lds\n"
"v_permlane32_swap_b32 v132, v134\n"
"v_permlane32_swap_b32 v148, v150\n"
"v_permlane32_swap_b32 v164, v166\n"
"v_permlane32_swap_b32 v180, v182\n"
"v_permlane32_swap_b32 v133, v135\n"
"v_permlane32_swap_b32 v149, v151\n"
"v_permlane32_swap_b32 v165, v167\n"
"v_permlane32_swap_b32 v181, v183\n"
"v_pk_mul_f32 v[48:49], v[132:133], v[132:133]\n"
"v_pk_mul_f32 v[50:51], v[148:149], v[148:149]\n"
"v_pk_mul_f32 v[52:53], v[164:165], v[164:165]\n"
"v_pk_mul_f32 v[54:55], v[180:181], v[180:181]\n"
"v_pk_fma_f32 v[48:49], v[48:49], s[64:65], v[10:11]\n"
"v_pk_fma_f32 v[50:51], v[50:51], s[64:65], v[10:11]\n"
"v_pk_fma_f32 v[52:53], v[52:53], s[64:65], v[10:11]\n"
"v_pk_fma_f32 v[54:55], v[54:55], s[64:65], v[10:11]\n"
"v_pk_mul_f32 v[48:49], v[48:49], v[132:133]\n"
"v_pk_mul_f32 v[50:51], v[50:51], v[148:149]\n"
"v_pk_mul_f32 v[52:53], v[52:53], v[164:165]\n"
"v_pk_mul_f32 v[54:55], v[54:55], v[180:181]\n"
"v_exp_f32 v48, v48\n"
"v_exp_f32 v50, v50\n"
"v_exp_f32 v52, v52\n"
"v_exp_f32 v54, v54\n"
"v_exp_f32 v49, v49\n"
"v_exp_f32 v51, v51\n"
"v_exp_f32 v53, v53\n"
"v_exp_f32 v55, v55\n"
"v_pk_mul_f32 v[132:133], v[132:133], v[134:135]\n"
"v_pk_mul_f32 v[148:149], v[148:149], v[150:151]\n"
"v_pk_mul_f32 v[164:165], v[164:165], v[166:167]\n"
"v_pk_mul_f32 v[180:181], v[180:181], v[182:183]\n"
"v_pk_add_f32 v[48:49], v[48:49], s[66:67]\n"
"v_pk_add_f32 v[50:51], v[50:51], s[66:67]\n"
"v_pk_add_f32 v[52:53], v[52:53], s[66:67]\n"
"v_pk_add_f32 v[54:55], v[54:55], s[66:67]\n"
"v_rcp_f32 v48, v48\n"
"v_rcp_f32 v50, v50\n"
"v_rcp_f32 v52, v52\n"
"v_rcp_f32 v54, v54\n"
"v_rcp_f32 v49, v49\n"
"v_rcp_f32 v51, v51\n"
"v_rcp_f32 v53, v53\n"
"v_rcp_f32 v55, v55\n"
"v_pk_mul_f32 v[132:133], v[132:133], v[48:49]\n"
"v_pk_mul_f32 v[148:149], v[148:149], v[50:51]\n"
"v_pk_mul_f32 v[164:165], v[164:165], v[52:53]\n"
"v_pk_mul_f32 v[180:181], v[180:181], v[54:55]\n"
"s_add_u32 m0, s79, 0x6000\n"
"s_add_u32 s35, s74, 0x2100\n"
"s_and_b32 s35, s35, 0x3ffff\n"
"buffer_load_dwordx4 v7, s[16:19], s35 offen lds\n"
"buffer_load_dwordx4 v9, s[16:19], s35 offen offset:1024 lds\n"
"v_permlane32_swap_b32 v196, v198\n"
"v_permlane32_swap_b32 v212, v214\n"
"v_permlane32_swap_b32 v228, v230\n"
"v_permlane32_swap_b32 v244, v246\n"
"v_permlane32_swap_b32 v197, v199\n"
"v_permlane32_swap_b32 v213, v215\n"
"v_permlane32_swap_b32 v229, v231\n"
"v_permlane32_swap_b32 v245, v247\n"
"v_pk_mul_f32 v[56:57], v[196:197], v[196:197]\n"
"v_pk_mul_f32 v[58:59], v[212:213], v[212:213]\n"
"v_pk_mul_f32 v[60:61], v[228:229], v[228:229]\n"
"v_pk_mul_f32 v[62:63], v[244:245], v[244:245]\n"
"v_pk_fma_f32 v[56:57], v[56:57], s[64:65], v[10:11]\n"
"v_pk_fma_f32 v[58:59], v[58:59], s[64:65], v[10:11]\n"
"v_pk_fma_f32 v[60:61], v[60:61], s[64:65], v[10:11]\n"
"v_pk_fma_f32 v[62:63], v[62:63], s[64:65], v[10:11]\n"
"v_pk_mul_f32 v[56:57], v[56:57], v[196:197]\n"
"v_pk_mul_f32 v[58:59], v[58:59], v[212:213]\n"
"v_pk_mul_f32 v[60:61], v[60:61], v[228:229]\n"
"v_pk_mul_f32 v[62:63], v[62:63], v[244:245]\n"
"v_exp_f32 v56, v56\n"
"v_exp_f32 v58, v58\n"
"v_exp_f32 v60, v60\n"
"v_exp_f32 v62, v62\n"
"v_exp_f32 v57, v57\n"
"v_exp_f32 v59, v59\n"
"v_exp_f32 v61, v61\n"
"v_exp_f32 v63, v63\n"
"v_pk_mul_f32 v[196:197], v[196:197], v[198:199]\n"
"v_pk_mul_f32 v[212:213], v[212:213], v[214:215]\n"
"v_pk_mul_f32 v[228:229], v[228:229], v[230:231]\n"
"v_pk_mul_f32 v[244:245], v[244:245], v[246:247]\n"
"v_pk_add_f32 v[56:57], v[56:57], s[66:67]\n"
"v_pk_add_f32 v[58:59], v[58:59], s[66:67]\n"
"v_pk_add_f32 v[60:61], v[60:61], s[66:67]\n"
"v_pk_add_f32 v[62:63], v[62:63], s[66:67]\n"
"v_rcp_f32 v56, v56\n"
"v_rcp_f32 v58, v58\n"
"v_rcp_f32 v60, v60\n"
"v_rcp_f32 v62, v62\n"
"v_rcp_f32 v57, v57\n"
"v_rcp_f32 v59, v59\n"
"v_rcp_f32 v61, v61\n"
"v_rcp_f32 v63, v63\n"
"v_pk_mul_f32 v[196:197], v[196:197], v[56:57]\n"
"v_pk_mul_f32 v[212:213], v[212:213], v[58:59]\n"
"v_pk_mul_f32 v[228:229], v[228:229], v[60:61]\n"
"v_pk_mul_f32 v[244:245], v[244:245], v[62:63]\n"
"v_cvt_pk_bf16_f32 v72, v132, v148\n"
"v_cvt_pk_bf16_f32 v73, v164, v180\n"
"v_cvt_pk_bf16_f32 v74, v196, v212\n"
"v_cvt_pk_bf16_f32 v75, v228, v244\n"
"ds_write_b128 v12, v[72:75] offset:1024\n"
"v_cvt_pk_bf16_f32 v76, v133, v149\n"
"v_cvt_pk_bf16_f32 v77, v165, v181\n"
"v_cvt_pk_bf16_f32 v78, v197, v213\n"
"v_cvt_pk_bf16_f32 v79, v229, v245\n"
"ds_write_b128 v12, v[76:79] offset:5120\n"
"s_add_u32 m0, s79, 0x8000\n"
"s_add_u32 s35, s74, 0x10100\n"
"s_and_b32 s35, s35, 0x3ffff\n"
"buffer_load_dwordx4 v7, s[16:19], s35 offen lds\n"
"buffer_load_dwordx4 v9, s[16:19], s35 offen offset:1024 lds\n"
"v_permlane32_swap_b32 v136, v138\n"
"v_permlane32_swap_b32 v152, v154\n"
"v_permlane32_swap_b32 v168, v170\n"
"v_permlane32_swap_b32 v184, v186\n"
"v_permlane32_swap_b32 v137, v139\n"
"v_permlane32_swap_b32 v153, v155\n"
"v_permlane32_swap_b32 v169, v171\n"
"v_permlane32_swap_b32 v185, v187\n"
"v_pk_mul_f32 v[32:33], v[136:137], v[136:137]\n"
"v_pk_mul_f32 v[34:35], v[152:153], v[152:153]\n"
"v_pk_mul_f32 v[36:37], v[168:169], v[168:169]\n"
"v_pk_mul_f32 v[38:39], v[184:185], v[184:185]\n"
"v_pk_fma_f32 v[32:33], v[32:33], s[64:65], v[10:11]\n"
"v_pk_fma_f32 v[34:35], v[34:35], s[64:65], v[10:11]\n"
"v_pk_fma_f32 v[36:37], v[36:37], s[64:65], v[10:11]\n"
"v_pk_fma_f32 v[38:39], v[38:39], s[64:65], v[10:11]\n"
"v_pk_mul_f32 v[32:33], v[32:33], v[136:137]\n"
"v_pk_mul_f32 v[34:35], v[34:35], v[152:153]\n"
"v_pk_mul_f32 v[36:37], v[36:37], v[168:169]\n"
"v_pk_mul_f32 v[38:39], v[38:39], v[184:185]\n"
"v_exp_f32 v32, v32\n"
"v_exp_f32 v34, v34\n"
"v_exp_f32 v36, v36\n"
"v_exp_f32 v38, v38\n"
"v_exp_f32 v33, v33\n"
"v_exp_f32 v35, v35\n"
"v_exp_f32 v37, v37\n"
"v_exp_f32 v39, v39\n"
"v_pk_mul_f32 v[136:137], v[136:137], v[138:139]\n"
"v_pk_mul_f32 v[152:153], v[152:153], v[154:155]\n"
"v_pk_mul_f32 v[168:169], v[168:169], v[170:171]\n"
"v_pk_mul_f32 v[184:185], v[184:185], v[186:187]\n"
"v_pk_add_f32 v[32:33], v[32:33], s[66:67]\n"
"v_pk_add_f32 v[34:35], v[34:35], s[66:67]\n"
"v_pk_add_f32 v[36:37], v[36:37], s[66:67]\n"
"v_pk_add_f32 v[38:39], v[38:39], s[66:67]\n"
"v_rcp_f32 v32, v32\n"
"v_rcp_f32 v34, v34\n"
"v_rcp_f32 v36, v36\n"
"v_rcp_f32 v38, v38\n"
"v_rcp_f32 v33, v33\n"
"v_rcp_f32 v35, v35\n"
"v_rcp_f32 v37, v37\n"
"v_rcp_f32 v39, v39\n"
"v_pk_mul_f32 v[136:137], v[136:137], v[32:33]\n"
"v_pk_mul_f32 v[152:153], v[152:153], v[34:35]\n"
"v_pk_mul_f32 v[168:169], v[168:169], v[36:37]\n"
"v_pk_mul_f32 v[184:185], v[184:185], v[38:39]\n"
"s_add_u32 m0, s79, 0xa000\n"
"s_add_u32 s35, s74, 0x12100\n"
"s_and_b32 s35, s35, 0x3ffff\n"
"buffer_load_dwordx4 v7, s[16:19], s35 offen lds\n"
"buffer_load_dwordx4 v9, s[16:19], s35 offen offset:1024 lds\n"
"v_permlane32_swap_b32 v200, v202\n"
"v_permlane32_swap_b32 v216, v218\n"
"v_permlane32_swap_b32 v232, v234\n"
"v_permlane32_swap_b32 v248, v250\n"
"v_permlane32_swap_b32 v201, v203\n"
"v_permlane32_swap_b32 v217, v219\n"
"v_permlane32_swap_b32 v233, v235\n"
"v_permlane32_swap_b32 v249, v251\n"
"v_pk_mul_f32 v[40:41], v[200:201], v[200:201]\n"
"v_pk_mul_f32 v[42:43], v[216:217], v[216:217]\n"
"v_pk_mul_f32 v[44:45], v[232:233], v[232:233]\n"
"v_pk_mul_f32 v[46:47], v[248:249], v[248:249]\n"
"v_pk_fma_f32 v[40:41], v[40:41], s[64:65], v[10:11]\n"
"v_pk_fma_f32 v[42:43], v[42:43], s[64:65], v[10:11]\n"
"v_pk_fma_f32 v[44:45], v[44:45], s[64:65], v[10:11]\n"
"v_pk_fma_f32 v[46:47], v[46:47], s[64:65], v[10:11]\n"
"v_pk_mul_f32 v[40:41], v[40:41], v[200:201]\n"
"v_pk_mul_f32 v[42:43], v[42:43], v[216:217]\n"
"v_pk_mul_f32 v[44:45], v[44:45], v[232:233]\n"
"v_pk_mul_f32 v[46:47], v[46:47], v[248:249]\n"
"v_exp_f32 v40, v40\n"
"v_exp_f32 v42, v42\n"
"v_exp_f32 v44, v44\n"
"v_exp_f32 v46, v46\n"
"v_exp_f32 v41, v41\n"
"v_exp_f32 v43, v43\n"
"v_exp_f32 v45, v45\n"
"v_exp_f32 v47, v47\n"
"v_pk_mul_f32 v[200:201], v[200:201], v[202:203]\n"
"v_pk_mul_f32 v[216:217], v[216:217], v[218:219]\n"
"v_pk_mul_f32 v[232:233], v[232:233], v[234:235]\n"
"v_pk_mul_f32 v[248:249], v[248:249], v[250:251]\n"
"v_pk_add_f32 v[40:41], v[40:41], s[66:67]\n"
"v_pk_add_f32 v[42:43], v[42:43], s[66:67]\n"
"v_pk_add_f32 v[44:45], v[44:45], s[66:67]\n"
"v_pk_add_f32 v[46:47], v[46:47], s[66:67]\n"
"v_rcp_f32 v40, v40\n"
"v_rcp_f32 v42, v42\n"
"v_rcp_f32 v44, v44\n"
"v_rcp_f32 v46, v46\n"
"v_rcp_f32 v41, v41\n"
"v_rcp_f32 v43, v43\n"
"v_rcp_f32 v45, v45\n"
"v_rcp_f32 v47, v47\n"
"v_pk_mul_f32 v[200:201], v[200:201], v[40:41]\n"
"v_pk_mul_f32 v[216:217], v[216:217], v[42:43]\n"
"v_pk_mul_f32 v[232:233], v[232:233], v[44:45]\n"
"v_pk_mul_f32 v[248:249], v[248:249], v[46:47]\n"
"v_cvt_pk_bf16_f32 v64, v136, v152\n"
"v_cvt_pk_bf16_f32 v65, v168, v184\n"
"v_cvt_pk_bf16_f32 v66, v200, v216\n"
"v_cvt_pk_bf16_f32 v67, v232, v248\n"
"ds_write_b128 v12, v[64:67] offset:2048\n"
"v_cvt_pk_bf16_f32 v68, v137, v153\n"
"v_cvt_pk_bf16_f32 v69, v169, v185\n"
"v_cvt_pk_bf16_f32 v70, v201, v217\n"
"v_cvt_pk_bf16_f32 v71, v233, v249\n"
"ds_write_b128 v12, v[68:71] offset:6144\n"
"v_permlane32_swap_b32 v140, v142\n"
"v_permlane32_swap_b32 v156, v158\n"
"v_permlane32_swap_b32 v172, v174\n"
"v_permlane32_swap_b32 v188, v190\n"
"v_permlane32_swap_b32 v141, v143\n"
"v_permlane32_swap_b32 v157, v159\n"
"v_permlane32_swap_b32 v173, v175\n"
"v_permlane32_swap_b32 v189, v191\n"
"v_pk_mul_f32 v[48:49], v[140:141], v[140:141]\n"
"v_pk_mul_f32 v[50:51], v[156:157], v[156:157]\n"
"v_pk_mul_f32 v[52:53], v[172:173], v[172:173]\n"
"v_pk_mul_f32 v[54:55], v[188:189], v[188:189]\n"
"v_pk_fma_f32 v[48:49], v[48:49], s[64:65], v[10:11]\n"
"v_pk_fma_f32 v[50:51], v[50:51], s[64:65], v[10:11]\n"
"v_pk_fma_f32 v[52:53], v[52:53], s[64:65], v[10:11]\n"
"v_pk_fma_f32 v[54:55], v[54:55], s[64:65], v[10:11]\n"
"v_pk_mul_f32 v[48:49], v[48:49], v[140:141]\n"
"v_pk_mul_f32 v[50:51], v[50:51], v[156:157]\n"
"v_pk_mul_f32 v[52:53], v[52:53], v[172:173]\n"
"v_pk_mul_f32 v[54:55], v[54:55], v[188:189]\n"
"v_exp_f32 v48, v48\n"
"v_exp_f32 v50, v50\n"
"v_exp_f32 v52, v52\n"
"v_exp_f32 v54, v54\n"
"v_exp_f32 v49, v49\n"
"v_exp_f32 v51, v51\n"
"v_exp_f32 v53, v53\n"
"v_exp_f32 v55, v55\n"
"v_pk_mul_f32 v[140:141], v[140:141], v[142:143]\n"
"v_pk_mul_f32 v[156:157], v[156:157], v[158:159]\n"
"v_pk_mul_f32 v[172:173], v[172:173], v[174:175]\n"
"v_pk_mul_f32 v[188:189], v[188:189], v[190:191]\n"
"v_pk_add_f32 v[48:49], v[48:49], s[66:67]\n"
"v_pk_add_f32 v[50:51], v[50:51], s[66:67]\n"
"v_pk_add_f32 v[52:53], v[52:53], s[66:67]\n"
"v_pk_add_f32 v[54:55], v[54:55], s[66:67]\n"
"v_rcp_f32 v48, v48\n"
"v_rcp_f32 v50, v50\n"
"v_rcp_f32 v52, v52\n"
"v_rcp_f32 v54, v54\n"
"v_rcp_f32 v49, v49\n"
"v_rcp_f32 v51, v51\n"
"v_rcp_f32 v53, v53\n"
"v_rcp_f32 v55, v55\n"
"v_pk_mul_f32 v[140:141], v[140:141], v[48:49]\n"
"v_pk_mul_f32 v[156:157], v[156:157], v[50:51]\n"
"v_pk_mul_f32 v[172:173], v[172:173], v[52:53]\n"
"v_pk_mul_f32 v[188:189], v[188:189], v[54:55]\n"
"v_permlane32_swap_b32 v204, v206\n"
"v_permlane32_swap_b32 v220, v222\n"
"v_permlane32_swap_b32 v236, v238\n"
"v_permlane32_swap_b32 v252, v254\n"
"v_permlane32_swap_b32 v205, v207\n"
"v_permlane32_swap_b32 v221, v223\n"
"v_permlane32_swap_b32 v237, v239\n"
"v_permlane32_swap_b32 v253, v255\n"
"v_pk_mul_f32 v[56:57], v[204:205], v[204:205]\n"
"v_pk_mul_f32 v[58:59], v[220:221], v[220:221]\n"
"v_pk_mul_f32 v[60:61], v[236:237], v[236:237]\n"
"v_pk_mul_f32 v[62:63], v[252:253], v[252:253]\n"
"v_pk_fma_f32 v[56:57], v[56:57], s[64:65], v[10:11]\n"
"v_pk_fma_f32 v[58:59], v[58:59], s[64:65], v[10:11]\n"
"v_pk_fma_f32 v[60:61], v[60:61], s[64:65], v[10:11]\n"
"v_pk_fma_f32 v[62:63], v[62:63], s[64:65], v[10:11]\n"
"v_pk_mul_f32 v[56:57], v[56:57], v[204:205]\n"
"v_pk_mul_f32 v[58:59], v[58:59], v[220:221]\n"
"v_pk_mul_f32 v[60:61], v[60:61], v[236:237]\n"
"v_pk_mul_f32 v[62:63], v[62:63], v[252:253]\n"
"v_exp_f32 v56, v56\n"
"v_exp_f32 v58, v58\n"
"v_exp_f32 v60, v60\n"
"v_exp_f32 v62, v62\n"
"v_exp_f32 v57, v57\n"
"v_exp_f32 v59, v59\n"
"v_exp_f32 v61, v61\n"
"v_exp_f32 v63, v63\n"
"v_pk_mul_f32 v[204:205], v[204:205], v[206:207]\n"
"v_pk_mul_f32 v[220:221], v[220:221], v[222:223]\n"
"v_pk_mul_f32 v[236:237], v[236:237], v[238:239]\n"
"v_pk_mul_f32 v[252:253], v[252:253], v[254:255]\n"
"v_pk_add_f32 v[56:57], v[56:57], s[66:67]\n"
"v_pk_add_f32 v[58:59], v[58:59], s[66:67]\n"
"v_pk_add_f32 v[60:61], v[60:61], s[66:67]\n"
"v_pk_add_f32 v[62:63], v[62:63], s[66:67]\n"
"v_rcp_f32 v56, v56\n"
"v_rcp_f32 v58, v58\n"
"v_rcp_f32 v60, v60\n"
"v_rcp_f32 v62, v62\n"
"v_rcp_f32 v57, v57\n"
"v_rcp_f32 v59, v59\n"
"v_rcp_f32 v61, v61\n"
"v_rcp_f32 v63, v63\n"
"v_pk_mul_f32 v[204:205], v[204:205], v[56:57]\n"
"v_pk_mul_f32 v[220:221], v[220:221], v[58:59]\n"
"v_pk_mul_f32 v[236:237], v[236:237], v[60:61]\n"
"v_pk_mul_f32 v[252:253], v[252:253], v[62:63]\n"
"v_cvt_pk_bf16_f32 v72, v140, v156\n"
"v_cvt_pk_bf16_f32 v73, v172, v188\n"
"v_cvt_pk_bf16_f32 v74, v204, v220\n"
"v_cvt_pk_bf16_f32 v75, v236, v252\n"
"ds_write_b128 v12, v[72:75] offset:3072\n"
"v_cvt_pk_bf16_f32 v76, v141, v157\n"
"v_cvt_pk_bf16_f32 v77, v173, v189\n"
"v_cvt_pk_bf16_f32 v78, v205, v221\n"
"v_cvt_pk_bf16_f32 v79, v237, v253\n"
"ds_write_b128 v12, v[76:79] offset:7168\n"
"v_bfe_u32 v4, v6, 2, 3\n"
"v_lshlrev_b32 v4, 6, v4\n"
"v_and_b32 v5, 3, v6\n"
"v_lshl_add_u32 v4, v5, 3, v4\n"
"v_lshrrev_b32 v5, 5, v6\n"
"v_lshl_add_u32 v4, v5, 9, v4\n"
"v_bfe_u32 v5, v6, 4, 1\n"
"v_lshl_add_u32 v4, v5, 5, v4\n"
"s_lshl_b32 s11, s7, 11\n"
"s_add_u32 s11, s11, 0x18000\n"
"v_add_u32 v2, s11, v4\n"
"v_xor_b32 v3, 32, v2\n"
"v_subrev_u32 v32, 0x18000, v2\n"
"v_subrev_u32 v33, 0x18000, v3\n"
"s_waitcnt lgkmcnt(0)\n"
"s_barrier\n"
"ds_read_b128 a[32:35], v13\n"
"ds_read_b128 a[36:39], v13 offset:1024\n"
"ds_read_b128 a[40:43], v13 offset:2048\n"
"ds_read_b128 a[44:47], v13 offset:3072\n"
"s_waitcnt vmcnt(22)\n"
"ds_read_b64_tr_b16 v[16:17], v2\n"
"ds_read_b64_tr_b16 v[18:19], v2 offset:1024\n"
"ds_read_b64_tr_b16 v[20:21], v3\n"
"ds_read_b64_tr_b16 v[22:23], v3 offset:1024\n"
"ds_read_b128 a[48:51], v13 offset:4096\n"
"ds_read_b128 a[52:55], v13 offset:5120\n"
"ds_read_b128 a[56:59], v13 offset:6144\n"
"ds_read_b128 a[60:63], v13 offset:7168\n"
"; d pass 0\n"
"s_waitcnt lgkmcnt(4)\n"
"v_mfma_f32_16x16x32_bf16 v[128:131], v[16:19], a[32:35], 0\n"
"s_waitcnt vmcnt(20)\n"
"ds_read_b64_tr_b16 v[24:25], v2 offset:8192\n"
"v_mfma_f32_16x16x32_bf16 v[132:135], v[16:19], a[36:39], 0\n"
"ds_read_b64_tr_b16 v[26:27], v2 offset:9216\n"
"v_accvgpr_read_b32 v108, a18\n"
"v_mfma_f32_16x16x32_bf16 v[136:139], v[16:19], a[40:43], 0\n"
"ds_read_b64_tr_b16 v[28:29], v3 offset:8192\n"
"s_add_u32 m0, s79, 0xc000\n"
"v_mfma_f32_16x16x32_bf16 v[140:143], v[16:19], a[44:47], 0\n"
"ds_read_b64_tr_b16 v[30:31], v3 offset:9216\n"
"s_add_u32 s35, s74, 0x20100\n"
"s_and_b32 s35, s35, 0x3ffff\n"
"v_mfma_f32_16x16x32_bf16 v[144:147], v[20:23], a[32:35], 0\n"
"buffer_load_dwordx4 v7, s[16:19], s35 offen lds\n"
"ds_read_b128 a[72:75], v13 offset:10240\n"
"v_mfma_f32_16x16x32_bf16 v[148:151], v[20:23], a[36:39], 0\n"
"buffer_load_dwordx4 v9, s[16:19], s35 offen offset:1024 lds\n"
"ds_read_b128 a[76:79], v13 offset:11264\n"
"v_mfma_f32_16x16x32_bf16 v[152:155], v[20:23], a[40:43], 0\n"
"ds_read_b128 a[64:67], v13 offset:8192\n"
"v_accvgpr_read_b32 v109, a19\n"
"v_mfma_f32_16x16x32_bf16 v[156:159], v[20:23], a[44:47], 0\n"
"ds_read_b128 a[68:71], v13 offset:9216\n"
"s_waitcnt lgkmcnt(4)\n"
"v_mfma_f32_16x16x32_bf16 v[128:131], v[24:27], a[48:51], v[128:131]\n"
"s_waitcnt vmcnt(20)\n"
"ds_read_b64_tr_b16 v[16:17], v2 offset:16384\n"
"v_mfma_f32_16x16x32_bf16 v[132:135], v[24:27], a[52:55], v[132:135]\n"
"ds_read_b64_tr_b16 v[18:19], v2 offset:17408\n"
"v_accvgpr_read_b32 v110, a20\n"
"v_mfma_f32_16x16x32_bf16 v[136:139], v[24:27], a[56:59], v[136:139]\n"
"ds_read_b64_tr_b16 v[20:21], v3 offset:16384\n"
"s_add_u32 m0, s79, 0xe000\n"
"v_mfma_f32_16x16x32_bf16 v[140:143], v[24:27], a[60:63], v[140:143]\n"
"ds_read_b64_tr_b16 v[22:23], v3 offset:17408\n"
"s_add_u32 s35, s74, 0x22100\n"
"s_and_b32 s35, s35, 0x3ffff\n"
"v_mfma_f32_16x16x32_bf16 v[144:147], v[28:31], a[48:51], v[144:147]\n"
"buffer_load_dwordx4 v7, s[16:19], s35 offen lds\n"
"ds_read_b128 a[88:91], v13 offset:14336\n"
"v_mfma_f32_16x16x32_bf16 v[148:151], v[28:31], a[52:55], v[148:151]\n"
"buffer_load_dwordx4 v9, s[16:19], s35 offen offset:1024 lds\n"
"ds_read_b128 a[92:95], v13 offset:15360\n"
"v_mfma_f32_16x16x32_bf16 v[152:155], v[28:31], a[56:59], v[152:155]\n"
"ds_read_b128 a[80:83], v13 offset:12288\n"
"v_accvgpr_read_b32 v111, a21\n"
"v_mfma_f32_16x16x32_bf16 v[156:159], v[28:31], a[60:63], v[156:159]\n"
"ds_read_b128 a[84:87], v13 offset:13312\n"
"s_waitcnt lgkmcnt(4)\n"
"v_mfma_f32_16x16x32_bf16 v[128:131], v[16:19], a[64:67], v[128:131]\n"
"s_waitcnt vmcnt(20)\n"
"ds_read_b64_tr_b16 v[24:25], v2 offset:24576\n"
"v_mfma_f32_16x16x32_bf16 v[132:135], v[16:19], a[68:71], v[132:135]\n"
"ds_read_b64_tr_b16 v[26:27], v2 offset:25600\n"
"v_accvgpr_read_b32 v124, a22\n"
"v_mfma_f32_16x16x32_bf16 v[136:139], v[16:19], a[72:75], v[136:139]\n"
"ds_read_b64_tr_b16 v[28:29], v3 offset:24576\n"
"s_add_u32 m0, s79, 0x18000\n"
"v_mfma_f32_16x16x32_bf16 v[140:143], v[16:19], a[76:79], v[140:143]\n"
"ds_read_b64_tr_b16 v[30:31], v3 offset:25600\n"
"s_add_u32 s35, s74, 0x30100\n"
"s_and_b32 s35, s35, 0x3ffff\n"
"v_mfma_f32_16x16x32_bf16 v[144:147], v[20:23], a[64:67], v[144:147]\n"
"buffer_load_dwordx4 v7, s[16:19], s35 offen lds\n"
"ds_read_b128 a[104:107], v13 offset:18432\n"
"v_mfma_f32_16x16x32_bf16 v[148:151], v[20:23], a[68:71], v[148:151]\n"
"buffer_load_dwordx4 v9, s[16:19], s35 offen offset:1024 lds\n"
"ds_read_b128 a[108:111], v13 offset:19456\n"
"v_mfma_f32_16x16x32_bf16 v[152:155], v[20:23], a[72:75], v[152:155]\n"
"ds_read_b128 a[96:99], v13 offset:16384\n"
"v_accvgpr_read_b32 v125, a23\n"
"v_mfma_f32_16x16x32_bf16 v[156:159], v[20:23], a[76:79], v[156:159]\n"
"ds_read_b128 a[100:103], v13 offset:17408\n"
"s_waitcnt lgkmcnt(4)\n"
"v_mfma_f32_16x16x32_bf16 v[128:131], v[24:27], a[80:83], v[128:131]\n"
"s_waitcnt vmcnt(20)\n"
"ds_read_b64_tr_b16 v[16:17], v2 offset:32768\n"
"v_mfma_f32_16x16x32_bf16 v[132:135], v[24:27], a[84:87], v[132:135]\n"
"ds_read_b64_tr_b16 v[18:19], v2 offset:33792\n"
"v_accvgpr_read_b32 v126, a24\n"
"v_mfma_f32_16x16x32_bf16 v[136:139], v[24:27], a[88:91], v[136:139]\n"
"ds_read_b64_tr_b16 v[20:21], v3 offset:32768\n"
"s_add_u32 m0, s79, 0x1a000\n"
"v_mfma_f32_16x16x32_bf16 v[140:143], v[24:27], a[92:95], v[140:143]\n"
"ds_read_b64_tr_b16 v[22:23], v3 offset:33792\n"
"s_add_u32 s35, s74, 0x32100\n"
"s_and_b32 s35, s35, 0x3ffff\n"
"v_mfma_f32_16x16x32_bf16 v[144:147], v[28:31], a[80:83], v[144:147]\n"
"buffer_load_dwordx4 v7, s[16:19], s35 offen lds\n"
"ds_read_b128 a[120:123], v13 offset:22528\n"
"v_mfma_f32_16x16x32_bf16 v[148:151], v[28:31], a[84:87], v[148:151]\n"
"buffer_load_dwordx4 v9, s[16:19], s35 offen offset:1024 lds\n"
"ds_read_b128 a[124:127], v13 offset:23552\n"
"v_mfma_f32_16x16x32_bf16 v[152:155], v[28:31], a[88:91], v[152:155]\n"
"ds_read_b128 a[112:115], v13 offset:20480\n"
"v_accvgpr_read_b32 v127, a25\n"
"v_mfma_f32_16x16x32_bf16 v[156:159], v[28:31], a[92:95], v[156:159]\n"
"ds_read_b128 a[116:119], v13 offset:21504\n"
"s_waitcnt lgkmcnt(4)\n"
"v_mfma_f32_16x16x32_bf16 v[128:131], v[16:19], a[96:99], v[128:131]\n"
"s_waitcnt vmcnt(20)\n"
"ds_read_b64_tr_b16 v[24:25], v2 offset:40960\n"
"v_mfma_f32_16x16x32_bf16 v[132:135], v[16:19], a[100:103], v[132:135]\n"
"ds_read_b64_tr_b16 v[26:27], v2 offset:41984\n"
"v_accvgpr_read_b32 v98, a26\n"
"v_mfma_f32_16x16x32_bf16 v[136:139], v[16:19], a[104:107], v[136:139]\n"
"ds_read_b64_tr_b16 v[28:29], v3 offset:40960\n"
"s_add_u32 m0, s79, 0x1c000\n"
"v_mfma_f32_16x16x32_bf16 v[140:143], v[16:19], a[108:111], v[140:143]\n"
"ds_read_b64_tr_b16 v[30:31], v3 offset:41984\n"
"s_add_u32 s35, s74, 0x200\n"
"s_and_b32 s35, s35, 0x3ffff\n"
"v_mfma_f32_16x16x32_bf16 v[144:147], v[20:23], a[96:99], v[144:147]\n"
"buffer_load_dwordx4 v7, s[16:19], s35 offen lds\n"
"ds_read_b128 a[136:139], v13 offset:26624\n"
"v_mfma_f32_16x16x32_bf16 v[148:151], v[20:23], a[100:103], v[148:151]\n"
"buffer_load_dwordx4 v9, s[16:19], s35 offen offset:1024 lds\n"
"ds_read_b128 a[140:143], v13 offset:27648\n"
"v_mfma_f32_16x16x32_bf16 v[152:155], v[20:23], a[104:107], v[152:155]\n"
"ds_read_b128 a[128:131], v13 offset:24576\n"
"v_accvgpr_read_b32 v96, a27\n"
"v_mfma_f32_16x16x32_bf16 v[156:159], v[20:23], a[108:111], v[156:159]\n"
"ds_read_b128 a[132:135], v13 offset:25600\n"
"s_waitcnt lgkmcnt(4)\n"
"v_mfma_f32_16x16x32_bf16 v[128:131], v[24:27], a[112:115], v[128:131]\n"
"s_waitcnt vmcnt(20)\n"
"ds_read_b64_tr_b16 v[16:17], v32\n"
"v_mfma_f32_16x16x32_bf16 v[132:135], v[24:27], a[116:119], v[132:135]\n"
"ds_read_b64_tr_b16 v[18:19], v32 offset:1024\n"
"v_accvgpr_read_b32 v97, a28\n"
"v_mfma_f32_16x16x32_bf16 v[136:139], v[24:27], a[120:123], v[136:139]\n"
"ds_read_b64_tr_b16 v[20:21], v33\n"
"s_add_u32 m0, s79, 0x1e000\n"
"v_mfma_f32_16x16x32_bf16 v[140:143], v[24:27], a[124:127], v[140:143]\n"
"ds_read_b64_tr_b16 v[22:23], v33 offset:1024\n"
"s_add_u32 s35, s74, 0x2200\n"
"s_and_b32 s35, s35, 0x3ffff\n"
"v_mfma_f32_16x16x32_bf16 v[144:147], v[28:31], a[112:115], v[144:147]\n"
"buffer_load_dwordx4 v7, s[16:19], s35 offen lds\n"
"ds_read_b128 a[152:155], v13 offset:30720\n"
"v_mfma_f32_16x16x32_bf16 v[148:151], v[28:31], a[116:119], v[148:151]\n"
"buffer_load_dwordx4 v9, s[16:19], s35 offen offset:1024 lds\n"
"ds_read_b128 a[156:159], v13 offset:31744\n"
"v_mfma_f32_16x16x32_bf16 v[152:155], v[28:31], a[120:123], v[152:155]\n"
"ds_read_b128 a[144:147], v13 offset:28672\n"
"v_lshrrev_b32 v4, 5, v6\n"
"v_mfma_f32_16x16x32_bf16 v[156:159], v[28:31], a[124:127], v[156:159]\n"
"ds_read_b128 a[148:151], v13 offset:29696\n"
"s_waitcnt lgkmcnt(4)\n"
"v_mfma_f32_16x16x32_bf16 v[128:131], v[16:19], a[128:131], v[128:131]\n"
"s_waitcnt vmcnt(20)\n"
"ds_read_b64_tr_b16 v[24:25], v32 offset:8192\n"
"v_mfma_f32_16x16x32_bf16 v[132:135], v[16:19], a[132:135], v[132:135]\n"
"ds_read_b64_tr_b16 v[26:27], v32 offset:9216\n"
"v_lshl_or_b32 v127, v4, 30, v127\n"
"v_mfma_f32_16x16x32_bf16 v[136:139], v[16:19], a[136:139], v[136:139]\n"
"ds_read_b64_tr_b16 v[28:29], v33 offset:8192\n"
"s_add_u32 m0, s79, 0x20000\n"
"v_mfma_f32_16x16x32_bf16 v[140:143], v[16:19], a[140:143], v[140:143]\n"
"ds_read_b64_tr_b16 v[30:31], v33 offset:9216\n"
"s_add_u32 s35, s74, 0x10200\n"
"s_and_b32 s35, s35, 0x3ffff\n"
"v_mfma_f32_16x16x32_bf16 v[144:147], v[20:23], a[128:131], v[144:147]\n"
"buffer_load_dwordx4 v7, s[16:19], s35 offen lds\n"
"v_mfma_f32_16x16x32_bf16 v[148:151], v[20:23], a[132:135], v[148:151]\n"
"buffer_load_dwordx4 v9, s[16:19], s35 offen offset:1024 lds\n"
"v_mfma_f32_16x16x32_bf16 v[152:155], v[20:23], a[136:139], v[152:155]\n"
"v_mfma_f32_16x16x32_bf16 v[156:159], v[20:23], a[140:143], v[156:159]\n"
"s_waitcnt lgkmcnt(0)\n"
"v_mfma_f32_16x16x32_bf16 v[128:131], v[24:27], a[144:147], v[128:131]\n"
"s_waitcnt vmcnt(20)\n"
"ds_read_b64_tr_b16 v[16:17], v32 offset:16384\n"
"v_mfma_f32_16x16x32_bf16 v[132:135], v[24:27], a[148:151], v[132:135]\n"
"ds_read_b64_tr_b16 v[18:19], v32 offset:17408\n"
"v_mfma_f32_16x16x32_bf16 v[136:139], v[24:27], a[152:155], v[136:139]\n"
"ds_read_b64_tr_b16 v[20:21], v33 offset:16384\n"
"s_add_u32 m0, s79, 0x22000\n"
"v_mfma_f32_16x16x32_bf16 v[140:143], v[24:27], a[156:159], v[140:143]\n"
"ds_read_b64_tr_b16 v[22:23], v33 offset:17408\n"
"s_add_u32 s35, s74, 0x12200\n"
"s_and_b32 s35, s35, 0x3ffff\n"
"v_mfma_f32_16x16x32_bf16 v[144:147], v[28:31], a[144:147], v[144:147]\n"
"buffer_load_dwordx4 v7, s[16:19], s35 offen lds\n"
"v_mfma_f32_16x16x32_bf16 v[148:151], v[28:31], a[148:151], v[148:151]\n"
"buffer_load_dwordx4 v9, s[16:19], s35 offen offset:1024 lds\n"
"v_mfma_f32_16x16x32_bf16 v[152:155], v[28:31], a[152:155], v[152:155]\n"
"v_mfma_f32_16x16x32_bf16 v[156:159], v[28:31], a[156:159], v[156:159]\n"
"; d pass 1\n"
"s_waitcnt lgkmcnt(0)\n"
"v_mfma_f32_16x16x32_bf16 v[160:163], v[16:19], a[32:35], 0\n"
"s_waitcnt vmcnt(20)\n"
"ds_read_b64_tr_b16 v[24:25], v32 offset:24576\n"
"v_mfma_f32_16x16x32_bf16 v[164:167], v[16:19], a[36:39], 0\n"
"ds_read_b64_tr_b16 v[26:27], v32 offset:25600\n"
"v_mfma_f32_16x16x32_bf16 v[168:171], v[16:19], a[40:43], 0\n"
"ds_read_b64_tr_b16 v[28:29], v33 offset:24576\n"
"s_add_u32 m0, s79, 0\n"
"v_mfma_f32_16x16x32_bf16 v[172:175], v[16:19], a[44:47], 0\n"
"ds_read_b64_tr_b16 v[30:31], v33 offset:25600\n"
"s_add_u32 s35, s74, 0x20200\n"
"s_and_b32 s35, s35, 0x3ffff\n"
"v_mfma_f32_16x16x32_bf16 v[176:179], v[20:23], a[32:35], 0\n"
"buffer_load_dwordx4 v7, s[16:19], s35 offen lds\n"
"v_mfma_f32_16x16x32_bf16 v[180:183], v[20:23], a[36:39], 0\n"
"buffer_load_dwordx4 v9, s[16:19], s35 offen offset:1024 lds\n"
"v_mfma_f32_16x16x32_bf16 v[184:187], v[20:23], a[40:43], 0\n"
"v_mfma_f32_16x16x32_bf16 v[188:191], v[20:23], a[44:47], 0\n"
"s_waitcnt lgkmcnt(0)\n"
"v_mfma_f32_16x16x32_bf16 v[160:163], v[24:27], a[48:51], v[160:163]\n"
"s_waitcnt vmcnt(20)\n"
"ds_read_b64_tr_b16 v[16:17], v32 offset:32768\n"
"v_mfma_f32_16x16x32_bf16 v[164:167], v[24:27], a[52:55], v[164:167]\n"
"ds_read_b64_tr_b16 v[18:19], v32 offset:33792\n"
"v_cvt_pk_bf16_f32 v128, v128, v129\n"
"v_mfma_f32_16x16x32_bf16 v[168:171], v[24:27], a[56:59], v[168:171]\n"
"ds_read_b64_tr_b16 v[20:21], v33 offset:32768\n"
"s_add_u32 m0, s79, 0x2000\n"
"v_mfma_f32_16x16x32_bf16 v[172:175], v[24:27], a[60:63], v[172:175]\n"
"ds_read_b64_tr_b16 v[22:23], v33 offset:33792\n"
"s_add_u32 s35, s74, 0x22200\n"
"s_and_b32 s35, s35, 0x3ffff\n"
"v_mfma_f32_16x16x32_bf16 v[176:179], v[28:31], a[48:51], v[176:179]\n"
"buffer_load_dwordx4 v7, s[16:19], s35 offen lds\n"
"v_cvt_pk_bf16_f32 v129, v130, v131\n"
"v_mfma_f32_16x16x32_bf16 v[180:183], v[28:31], a[52:55], v[180:183]\n"
"buffer_load_dwordx4 v9, s[16:19], s35 offen offset:1024 lds\n"
"v_cvt_pk_bf16_f32 v130, v144, v145\n"
"v_mfma_f32_16x16x32_bf16 v[184:187], v[28:31], a[56:59], v[184:187]\n"
"v_cvt_pk_bf16_f32 v131, v146, v147\n"
"v_cvt_pk_bf16_f32 v132, v132, v133\n"
"v_mfma_f32_16x16x32_bf16 v[188:191], v[28:31], a[60:63], v[188:191]\n"
"v_cvt_pk_bf16_f32 v133, v134, v135\n"
"s_waitcnt lgkmcnt(0)\n"
"v_mfma_f32_16x16x32_bf16 v[160:163], v[16:19], a[64:67], v[160:163]\n"
"s_waitcnt vmcnt(20)\n"
"ds_read_b64_tr_b16 v[24:25], v32 offset:40960\n"
"v_mfma_f32_16x16x32_bf16 v[164:167], v[16:19], a[68:71], v[164:167]\n"
"ds_read_b64_tr_b16 v[26:27], v32 offset:41984\n"
"v_cvt_pk_bf16_f32 v134, v148, v149\n"
"v_mfma_f32_16x16x32_bf16 v[168:171], v[16:19], a[72:75], v[168:171]\n"
"ds_read_b64_tr_b16 v[28:29], v33 offset:40960\n"
"s_add_u32 m0, s79, 0x4000\n"
"v_mfma_f32_16x16x32_bf16 v[172:175], v[16:19], a[76:79], v[172:175]\n"
"ds_read_b64_tr_b16 v[30:31], v33 offset:41984\n"
"s_add_u32 s35, s74, 0x30200\n"
"s_and_b32 s35, s35, 0x3ffff\n"
"v_mfma_f32_16x16x32_bf16 v[176:179], v[20:23], a[64:67], v[176:179]\n"
"buffer_load_dwordx4 v7, s[16:19], s35 offen lds\n"
"v_cvt_pk_bf16_f32 v135, v150, v151\n"
"v_mfma_f32_16x16x32_bf16 v[180:183], v[20:23], a[68:71], v[180:183]\n"
"buffer_load_dwordx4 v9, s[16:19], s35 offen offset:1024 lds\n"
"s_add_u32 s76, s85, 0\n"
"v_mfma_f32_16x16x32_bf16 v[184:187], v[20:23], a[72:75], v[184:187]\n"
"buffer_store_dwordx4 v[128:131], v108, s[28:31], s76 offen sc1\n"
"v_cvt_pk_bf16_f32 v136, v136, v137\n"
"v_mfma_f32_16x16x32_bf16 v[188:191], v[20:23], a[76:79], v[188:191]\n"
"v_cvt_pk_bf16_f32 v137, v138, v139\n"
"s_waitcnt lgkmcnt(0)\n"
"v_mfma_f32_16x16x32_bf16 v[160:163], v[24:27], a[80:83], v[160:163]\n"
"s_waitcnt vmcnt(21)\n"
"ds_read_b64_tr_b16 v[16:17], v32 offset:49152\n"
"v_mfma_f32_16x16x32_bf16 v[164:167], v[24:27], a[84:87], v[164:167]\n"
"ds_read_b64_tr_b16 v[18:19], v32 offset:50176\n"
"v_cvt_pk_bf16_f32 v138, v152, v153\n"
"v_mfma_f32_16x16x32_bf16 v[168:171], v[24:27], a[88:91], v[168:171]\n"
"ds_read_b64_tr_b16 v[20:21], v33 offset:49152\n"
"s_add_u32 m0, s79, 0x6000\n"
"v_mfma_f32_16x16x32_bf16 v[172:175], v[24:27], a[92:95], v[172:175]\n"
"ds_read_b64_tr_b16 v[22:23], v33 offset:50176\n"
"s_add_u32 s35, s74, 0x32200\n"
"s_and_b32 s35, s35, 0x3ffff\n"
"v_mfma_f32_16x16x32_bf16 v[176:179], v[28:31], a[80:83], v[176:179]\n"
"buffer_load_dwordx4 v7, s[16:19], s35 offen lds\n"
"v_cvt_pk_bf16_f32 v139, v154, v155\n"
"v_mfma_f32_16x16x32_bf16 v[180:183], v[28:31], a[84:87], v[180:183]\n"
"buffer_load_dwordx4 v9, s[16:19], s35 offen offset:1024 lds\n"
"s_add_u32 s76, s85, 0x20000\n"
"v_mfma_f32_16x16x32_bf16 v[184:187], v[28:31], a[88:91], v[184:187]\n"
"buffer_store_dwordx4 v[132:135], v109, s[28:31], s76 offen sc1\n"
"v_cvt_pk_bf16_f32 v140, v140, v141\n"
"v_mfma_f32_16x16x32_bf16 v[188:191], v[28:31], a[92:95], v[188:191]\n"
"v_cvt_pk_bf16_f32 v141, v142, v143\n"
"s_waitcnt lgkmcnt(0)\n"
"v_mfma_f32_16x16x32_bf16 v[160:163], v[16:19], a[96:99], v[160:163]\n"
"s_waitcnt vmcnt(22)\n"
"ds_read_b64_tr_b16 v[24:25], v32 offset:57344\n"
"v_mfma_f32_16x16x32_bf16 v[164:167], v[16:19], a[100:103], v[164:167]\n"
"ds_read_b64_tr_b16 v[26:27], v32 offset:58368\n"
"v_cvt_pk_bf16_f32 v142, v156, v157\n"
"v_mfma_f32_16x16x32_bf16 v[168:171], v[16:19], a[104:107], v[168:171]\n"
"ds_read_b64_tr_b16 v[28:29], v33 offset:57344\n"
"s_add_u32 m0, s79, 0x8000\n"
"v_mfma_f32_16x16x32_bf16 v[172:175], v[16:19], a[108:111], v[172:175]\n"
"ds_read_b64_tr_b16 v[30:31], v33 offset:58368\n"
"s_add_u32 s35, s74, 0x300\n"
"s_and_b32 s35, s35, 0x3ffff\n"
"v_mfma_f32_16x16x32_bf16 v[176:179], v[20:23], a[96:99], v[176:179]\n"
"buffer_load_dwordx4 v7, s[16:19], s35 offen lds\n"
"v_cvt_pk_bf16_f32 v143, v158, v159\n"
"v_mfma_f32_16x16x32_bf16 v[180:183], v[20:23], a[100:103], v[180:183]\n"
"buffer_load_dwordx4 v9, s[16:19], s35 offen offset:1024 lds\n"
"s_add_u32 s76, s85, 0x40000\n"
"v_mfma_f32_16x16x32_bf16 v[184:187], v[20:23], a[104:107], v[184:187]\n"
"buffer_store_dwordx4 v[136:139], v110, s[28:31], s76 offen sc1\n"
"s_cmp_eq_u32 s8, 7\n"
"s_cselect_b32 s13, 0, -1\n"
"v_mfma_f32_16x16x32_bf16 v[188:191], v[20:23], a[108:111], v[188:191]\n"
"v_and_b32 v98, s13, v98\n"
"s_waitcnt lgkmcnt(0)\n"
"v_mfma_f32_16x16x32_bf16 v[160:163], v[24:27], a[112:115], v[160:163]\n"
"s_waitcnt vmcnt(23)\n"
"ds_read_b64_tr_b16 v[16:17], v2\n"
"v_mfma_f32_16x16x32_bf16 v[164:167], v[24:27], a[116:119], v[164:167]\n"
"ds_read_b64_tr_b16 v[18:19], v2 offset:1024\n"
"v_lshlrev_b32 v99, 16, v98\n"
"v_mfma_f32_16x16x32_bf16 v[168:171], v[24:27], a[120:123], v[168:171]\n"
"ds_read_b64_tr_b16 v[20:21], v3\n"
"s_add_u32 m0, s79, 0xa000\n"
"v_mfma_f32_16x16x32_bf16 v[172:175], v[24:27], a[124:127], v[172:175]\n"
"ds_read_b64_tr_b16 v[22:23], v3 offset:1024\n"
"s_add_u32 s35, s74, 0x2300\n"
"s_and_b32 s35, s35, 0x3ffff\n"
"v_mfma_f32_16x16x32_bf16 v[176:179], v[28:31], a[112:115], v[176:179]\n"
"buffer_load_dwordx4 v7, s[16:19], s35 offen lds\n"
"v_mov_b32 v104, s10\n"
"v_mfma_f32_16x16x32_bf16 v[180:183], v[28:31], a[116:119], v[180:183]\n"
"buffer_load_dwordx4 v9, s[16:19], s35 offen offset:1024 lds\n"
"s_add_u32 s76, s85, 0x60000\n"
"v_mfma_f32_16x16x32_bf16 v[184:187], v[28:31], a[120:123], v[184:187]\n"
"buffer_store_dwordx4 v[140:143], v111, s[28:31], s76 offen sc1\n"
"v_mov_b32 v105, s10\n"
"v_mfma_f32_16x16x32_bf16 v[188:191], v[28:31], a[124:127], v[188:191]\n"
"v_mov_b32 v106, s10\n"
"s_waitcnt lgkmcnt(0)\n"
"v_mfma_f32_16x16x32_bf16 v[160:163], v[16:19], a[128:131], v[160:163]\n"
"s_waitcnt vmcnt(24)\n"
"ds_read_b64_tr_b16 v[24:25], v2 offset:8192\n"
"v_mfma_f32_16x16x32_bf16 v[164:167], v[16:19], a[132:135], v[164:167]\n"
"ds_read_b64_tr_b16 v[26:27], v2 offset:9216\n"
"v_mov_b32 v107, s10\n"
"v_mfma_f32_16x16x32_bf16 v[168:171], v[16:19], a[136:139], v[168:171]\n"
"ds_read_b64_tr_b16 v[28:29], v3 offset:8192\n"
"s_add_u32 m0, s79, 0xc000\n"
"v_mfma_f32_16x16x32_bf16 v[172:175], v[16:19], a[140:143], v[172:175]\n"
"ds_read_b64_tr_b16 v[30:31], v3 offset:9216\n"
"s_add_u32 s35, s74, 0x10300\n"
"s_and_b32 s35, s35, 0x3ffff\n"
"v_mfma_f32_16x16x32_bf16 v[176:179], v[20:23], a[128:131], v[176:179]\n"
"buffer_load_dwordx4 v7, s[16:19], s35 offen lds\n"
"s_add_u32 s69, s69, 0x100000\n"
"v_mfma_f32_16x16x32_bf16 v[180:183], v[20:23], a[132:135], v[180:183]\n"
"buffer_load_dwordx4 v9, s[16:19], s35 offen offset:1024 lds\n"
"s_add_u32 s78, s78, 0x100000\n"
"v_mfma_f32_16x16x32_bf16 v[184:187], v[20:23], a[136:139], v[184:187]\n"
"s_lshl_b32 s88, s9, 19\n"
"s_lshl_b32 s15, s8, 16\n"
"v_mfma_f32_16x16x32_bf16 v[188:191], v[20:23], a[140:143], v[188:191]\n"
"s_add_u32 s88, s88, s15\n"
"s_waitcnt lgkmcnt(0)\n"
"v_mfma_f32_16x16x32_bf16 v[160:163], v[24:27], a[144:147], v[160:163]\n"
"s_waitcnt vmcnt(24)\n"
"ds_read_b64_tr_b16 v[16:17], v2 offset:16384\n"
"v_mfma_f32_16x16x32_bf16 v[164:167], v[24:27], a[148:151], v[164:167]\n"
"ds_read_b64_tr_b16 v[18:19], v2 offset:17408\n"
"s_lshl_b32 s15, s7, 9\n"
"v_mfma_f32_16x16x32_bf16 v[168:171], v[24:27], a[152:155], v[168:171]\n"
"ds_read_b64_tr_b16 v[20:21], v3 offset:16384\n"
"s_add_u32 m0, s79, 0xe000\n"
"v_mfma_f32_16x16x32_bf16 v[172:175], v[24:27], a[156:159], v[172:175]\n"
"ds_read_b64_tr_b16 v[22:23], v3 offset:17408\n"
"s_add_u32 s35, s74, 0x12300\n"
"s_and_b32 s35, s35, 0x3ffff\n"
"v_mfma_f32_16x16x32_bf16 v[176:179], v[28:31], a[144:147], v[176:179]\n"
"buffer_load_dwordx4 v7, s[16:19], s35 offen lds\n"
"s_add_u32 s88, s88, s15\n"
"v_mfma_f32_16x16x32_bf16 v[180:183], v[28:31], a[148:151], v[180:183]\n"
"buffer_load_dwordx4 v9, s[16:19], s35 offen offset:1024 lds\n"
"s_add_u32 s88, s88, 0x1800000\n"
"v_mfma_f32_16x16x32_bf16 v[184:187], v[28:31], a[152:155], v[184:187]\n"
"s_mov_b32 s14, 0x10000\n"
"v_mfma_f32_16x16x32_bf16 v[188:191], v[28:31], a[156:159], v[188:191]\n"
"; d pass 2\n"
"s_waitcnt lgkmcnt(0)\n"
"v_mfma_f32_16x16x32_bf16 v[192:195], v[16:19], a[32:35], 0\n"
"s_waitcnt vmcnt(24)\n"
"ds_read_b64_tr_b16 v[24:25], v2 offset:24576\n"
"v_mfma_f32_16x16x32_bf16 v[196:199], v[16:19], a[36:39], 0\n"
"ds_read_b64_tr_b16 v[26:27], v2 offset:25600\n"
"v_mfma_f32_16x16x32_bf16 v[200:203], v[16:19], a[40:43], 0\n"
"ds_read_b64_tr_b16 v[28:29], v3 offset:24576\n"
"s_add_u32 m0, s79, 0x18000\n"
"v_mfma_f32_16x16x32_bf16 v[204:207], v[16:19], a[44:47], 0\n"
"ds_read_b64_tr_b16 v[30:31], v3 offset:25600\n"
"s_add_u32 s35, s74, 0x20300\n"
"s_and_b32 s35, s35, 0x3ffff\n"
"v_mfma_f32_16x16x32_bf16 v[208:211], v[20:23], a[32:35], 0\n"
"buffer_load_dwordx4 v7, s[16:19], s35 offen lds\n"
"v_mfma_f32_16x16x32_bf16 v[212:215], v[20:23], a[36:39], 0\n"
"buffer_load_dwordx4 v9, s[16:19], s35 offen offset:1024 lds\n"
"v_mfma_f32_16x16x32_bf16 v[216:219], v[20:23], a[40:43], 0\n"
"v_mfma_f32_16x16x32_bf16 v[220:223], v[20:23], a[44:47], 0\n"
"s_waitcnt lgkmcnt(0)\n"
"v_mfma_f32_16x16x32_bf16 v[192:195], v[24:27], a[48:51], v[192:195]\n"
"s_waitcnt vmcnt(24)\n"
"ds_read_b64_tr_b16 v[16:17], v2 offset:32768\n"
"v_mfma_f32_16x16x32_bf16 v[196:199], v[24:27], a[52:55], v[196:199]\n"
"ds_read_b64_tr_b16 v[18:19], v2 offset:33792\n"
"v_cvt_pk_bf16_f32 v160, v160, v161\n"
"v_mfma_f32_16x16x32_bf16 v[200:203], v[24:27], a[56:59], v[200:203]\n"
"ds_read_b64_tr_b16 v[20:21], v3 offset:32768\n"
"s_add_u32 m0, s79, 0x1a000\n"
"v_mfma_f32_16x16x32_bf16 v[204:207], v[24:27], a[60:63], v[204:207]\n"
"ds_read_b64_tr_b16 v[22:23], v3 offset:33792\n"
"s_add_u32 s35, s74, 0x22300\n"
"s_and_b32 s35, s35, 0x3ffff\n"
"v_mfma_f32_16x16x32_bf16 v[208:211], v[28:31], a[48:51], v[208:211]\n"
"buffer_load_dwordx4 v7, s[16:19], s35 offen lds\n"
"v_cvt_pk_bf16_f32 v161, v162, v163\n"
"v_mfma_f32_16x16x32_bf16 v[212:215], v[28:31], a[52:55], v[212:215]\n"
"buffer_load_dwordx4 v9, s[16:19], s35 offen offset:1024 lds\n"
"v_cvt_pk_bf16_f32 v162, v176, v177\n"
"v_mfma_f32_16x16x32_bf16 v[216:219], v[28:31], a[56:59], v[216:219]\n"
"v_cvt_pk_bf16_f32 v163, v178, v179\n"
"v_cvt_pk_bf16_f32 v164, v164, v165\n"
"v_mfma_f32_16x16x32_bf16 v[220:223], v[28:31], a[60:63], v[220:223]\n"
"v_cvt_pk_bf16_f32 v165, v166, v167\n"
"s_waitcnt lgkmcnt(0)\n"
"v_mfma_f32_16x16x32_bf16 v[192:195], v[16:19], a[64:67], v[192:195]\n"
"s_waitcnt vmcnt(24)\n"
"ds_read_b64_tr_b16 v[24:25], v2 offset:40960\n"
"v_mfma_f32_16x16x32_bf16 v[196:199], v[16:19], a[68:71], v[196:199]\n"
"ds_read_b64_tr_b16 v[26:27], v2 offset:41984\n"
"v_cvt_pk_bf16_f32 v166, v180, v181\n"
"v_mfma_f32_16x16x32_bf16 v[200:203], v[16:19], a[72:75], v[200:203]\n"
"ds_read_b64_tr_b16 v[28:29], v3 offset:40960\n"
"s_add_u32 m0, s79, 0x1c000\n"
"v_mfma_f32_16x16x32_bf16 v[204:207], v[16:19], a[76:79], v[204:207]\n"
"ds_read_b64_tr_b16 v[30:31], v3 offset:41984\n"
"s_add_u32 s35, s74, 0x30300\n"
"s_and_b32 s35, s35, 0x3ffff\n"
"v_mfma_f32_16x16x32_bf16 v[208:211], v[20:23], a[64:67], v[208:211]\n"
"buffer_load_dwordx4 v7, s[16:19], s35 offen lds\n"
"v_cvt_pk_bf16_f32 v167, v182, v183\n"
"v_mfma_f32_16x16x32_bf16 v[212:215], v[20:23], a[68:71], v[212:215]\n"
"buffer_load_dwordx4 v9, s[16:19], s35 offen offset:1024 lds\n"
"s_add_u32 s76, s85, 0x4000\n"
"v_mfma_f32_16x16x32_bf16 v[216:219], v[20:23], a[72:75], v[216:219]\n"
"buffer_store_dwordx4 v[160:163], v108, s[28:31], s76 offen sc1\n"
"v_cvt_pk_bf16_f32 v168, v168, v169\n"
"v_mfma_f32_16x16x32_bf16 v[220:223], v[20:23], a[76:79], v[220:223]\n"
"v_cvt_pk_bf16_f32 v169, v170, v171\n"
"s_waitcnt lgkmcnt(0)\n"
"v_mfma_f32_16x16x32_bf16 v[192:195], v[24:27], a[80:83], v[192:195]\n"
"s_waitcnt vmcnt(25)\n"
"ds_read_b64_tr_b16 v[16:17], v32\n"
"v_mfma_f32_16x16x32_bf16 v[196:199], v[24:27], a[84:87], v[196:199]\n"
"ds_read_b64_tr_b16 v[18:19], v32 offset:1024\n"
"v_cvt_pk_bf16_f32 v170, v184, v185\n"
"v_mfma_f32_16x16x32_bf16 v[200:203], v[24:27], a[88:91], v[200:203]\n"
"ds_read_b64_tr_b16 v[20:21], v33\n"
"s_add_u32 m0, s79, 0x1e000\n"
"v_mfma_f32_16x16x32_bf16 v[204:207], v[24:27], a[92:95], v[204:207]\n"
"ds_read_b64_tr_b16 v[22:23], v33 offset:1024\n"
"s_add_u32 s35, s74, 0x32300\n"
"s_and_b32 s35, s35, 0x3ffff\n"
"v_mfma_f32_16x16x32_bf16 v[208:211], v[28:31], a[80:83], v[208:211]\n"
"buffer_load_dwordx4 v7, s[16:19], s35 offen lds\n"
"v_cvt_pk_bf16_f32 v171, v186, v187\n"
"v_mfma_f32_16x16x32_bf16 v[212:215], v[28:31], a[84:87], v[212:215]\n"
"buffer_load_dwordx4 v9, s[16:19], s35 offen offset:1024 lds\n"
"s_add_u32 s76, s85, 0x24000\n"
"v_mfma_f32_16x16x32_bf16 v[216:219], v[28:31], a[88:91], v[216:219]\n"
"buffer_store_dwordx4 v[164:167], v109, s[28:31], s76 offen sc1\n"
"s_waitcnt vmcnt(14)\n"
"v_mfma_f32_16x16x32_bf16 v[220:223], v[28:31], a[92:95], v[220:223]\n"
"buffer_store_dwordx4 v[104:107], v96, s[28:31], s69 offen sc1\n"
"s_waitcnt lgkmcnt(0)\n"
"v_mfma_f32_16x16x32_bf16 v[192:195], v[16:19], a[96:99], v[192:195]\n"
"ds_read_b64_tr_b16 v[24:25], v32 offset:8192\n"
"v_mfma_f32_16x16x32_bf16 v[196:199], v[16:19], a[100:103], v[196:199]\n"
"ds_read_b64_tr_b16 v[26:27], v32 offset:9216\n"
"v_cvt_pk_bf16_f32 v172, v172, v173\n"
"v_mfma_f32_16x16x32_bf16 v[200:203], v[16:19], a[104:107], v[200:203]\n"
"ds_read_b64_tr_b16 v[28:29], v33 offset:8192\n"
"v_cvt_pk_bf16_f32 v173, v174, v175\n"
"v_mfma_f32_16x16x32_bf16 v[204:207], v[16:19], a[108:111], v[204:207]\n"
"ds_read_b64_tr_b16 v[30:31], v33 offset:9216\n"
"s_add_u32 s76, s85, 0x44000\n"
"v_mfma_f32_16x16x32_bf16 v[208:211], v[20:23], a[96:99], v[208:211]\n"
"buffer_store_dwordx4 v[168:171], v110, s[28:31], s76 offen sc1\n"
"v_cvt_pk_bf16_f32 v174, v188, v189\n"
"v_mfma_f32_16x16x32_bf16 v[212:215], v[20:23], a[100:103], v[212:215]\n"
"v_cvt_pk_bf16_f32 v175, v190, v191\n"
"v_mfma_f32_16x16x32_bf16 v[216:219], v[20:23], a[104:107], v[216:219]\n"
"v_mfma_f32_16x16x32_bf16 v[220:223], v[20:23], a[108:111], v[220:223]\n"
"s_waitcnt lgkmcnt(0)\n"
"v_mfma_f32_16x16x32_bf16 v[192:195], v[24:27], a[112:115], v[192:195]\n"
"ds_read_b64_tr_b16 v[16:17], v32 offset:16384\n"
"v_mfma_f32_16x16x32_bf16 v[196:199], v[24:27], a[116:119], v[196:199]\n"
"ds_read_b64_tr_b16 v[18:19], v32 offset:17408\n"
"v_mfma_f32_16x16x32_bf16 v[200:203], v[24:27], a[120:123], v[200:203]\n"
"ds_read_b64_tr_b16 v[20:21], v33 offset:16384\n"
"v_mfma_f32_16x16x32_bf16 v[204:207], v[24:27], a[124:127], v[204:207]\n"
"ds_read_b64_tr_b16 v[22:23], v33 offset:17408\n"
"s_add_u32 s76, s85, 0x64000\n"
"v_mfma_f32_16x16x32_bf16 v[208:211], v[28:31], a[112:115], v[208:211]\n"
"buffer_store_dwordx4 v[172:175], v111, s[28:31], s76 offen sc1\n"
"v_mfma_f32_16x16x32_bf16 v[212:215], v[28:31], a[116:119], v[212:215]\n"
"v_mfma_f32_16x16x32_bf16 v[216:219], v[28:31], a[120:123], v[216:219]\n"
"v_mfma_f32_16x16x32_bf16 v[220:223], v[28:31], a[124:127], v[220:223]\n"
"s_waitcnt lgkmcnt(0)\n"
"v_mfma_f32_16x16x32_bf16 v[192:195], v[16:19], a[128:131], v[192:195]\n"
"ds_read_b64_tr_b16 v[24:25], v32 offset:24576\n"
"v_mfma_f32_16x16x32_bf16 v[196:199], v[16:19], a[132:135], v[196:199]\n"
"ds_read_b64_tr_b16 v[26:27], v32 offset:25600\n"
"v_mfma_f32_16x16x32_bf16 v[200:203], v[16:19], a[136:139], v[200:203]\n"
"ds_read_b64_tr_b16 v[28:29], v33 offset:24576\n"
"v_mfma_f32_16x16x32_bf16 v[204:207], v[16:19], a[140:143], v[204:207]\n"
"ds_read_b64_tr_b16 v[30:31], v33 offset:25600\n"
"v_mfma_f32_16x16x32_bf16 v[208:211], v[20:23], a[128:131], v[208:211]\n"
"v_mfma_f32_16x16x32_bf16 v[212:215], v[20:23], a[132:135], v[212:215]\n"
"v_mfma_f32_16x16x32_bf16 v[216:219], v[20:23], a[136:139], v[216:219]\n"
"v_mfma_f32_16x16x32_bf16 v[220:223], v[20:23], a[140:143], v[220:223]\n"
"s_waitcnt lgkmcnt(0)\n"
"v_mfma_f32_16x16x32_bf16 v[192:195], v[24:27], a[144:147], v[192:195]\n"
"ds_read_b64_tr_b16 v[16:17], v32 offset:32768\n"
"v_mfma_f32_16x16x32_bf16 v[196:199], v[24:27], a[148:151], v[196:199]\n"
"ds_read_b64_tr_b16 v[18:19], v32 offset:33792\n"
"v_mfma_f32_16x16x32_bf16 v[200:203], v[24:27], a[152:155], v[200:203]\n"
"ds_read_b64_tr_b16 v[20:21], v33 offset:32768\n"
"v_mfma_f32_16x16x32_bf16 v[204:207], v[24:27], a[156:159], v[204:207]\n"
"ds_read_b64_tr_b16 v[22:23], v33 offset:33792\n"
"v_mfma_f32_16x16x32_bf16 v[208:211], v[28:31], a[144:147], v[208:211]\n"
"v_mfma_f32_16x16x32_bf16 v[212:215], v[28:31], a[148:151], v[212:215]\n"
"v_mfma_f32_16x16x32_bf16 v[216:219], v[28:31], a[152:155], v[216:219]\n"
"v_mfma_f32_16x16x32_bf16 v[220:223], v[28:31], a[156:159], v[220:223]\n"
"; d pass 3\n"
"s_waitcnt lgkmcnt(0)\n"
"v_mfma_f32_16x16x32_bf16 v[224:227], v[16:19], a[32:35], 0\n"
"ds_read_b64_tr_b16 v[24:25], v32 offset:40960\n"
"v_mfma_f32_16x16x32_bf16 v[228:231], v[16:19], a[36:39], 0\n"
"ds_read_b64_tr_b16 v[26:27], v32 offset:41984\n"
"v_mfma_f32_16x16x32_bf16 v[232:235], v[16:19], a[40:43], 0\n"
"ds_read_b64_tr_b16 v[28:29], v33 offset:40960\n"
"v_mfma_f32_16x16x32_bf16 v[236:239], v[16:19], a[44:47], 0\n"
"ds_read_b64_tr_b16 v[30:31], v33 offset:41984\n"
"v_mfma_f32_16x16x32_bf16 v[240:243], v[20:23], a[32:35], 0\n"
"v_mfma_f32_16x16x32_bf16 v[244:247], v[20:23], a[36:39], 0\n"
"v_mfma_f32_16x16x32_bf16 v[248:251], v[20:23], a[40:43], 0\n"
"v_mfma_f32_16x16x32_bf16 v[252:255], v[20:23], a[44:47], 0\n"
"s_waitcnt lgkmcnt(0)\n"
"v_mfma_f32_16x16x32_bf16 v[224:227], v[24:27], a[48:51], v[224:227]\n"
"s_waitcnt vmcnt(15)\n"
"ds_read_b64_tr_b16 v[16:17], v32 offset:49152\n"
"v_mfma_f32_16x16x32_bf16 v[228:231], v[24:27], a[52:55], v[228:231]\n"
"ds_read_b64_tr_b16 v[18:19], v32 offset:50176\n"
"v_cvt_pk_bf16_f32 v192, v192, v193\n"
"v_mfma_f32_16x16x32_bf16 v[232:235], v[24:27], a[56:59], v[232:235]\n"
"ds_read_b64_tr_b16 v[20:21], v33 offset:49152\n"
"v_cvt_pk_bf16_f32 v193, v194, v195\n"
"v_mfma_f32_16x16x32_bf16 v[236:239], v[24:27], a[60:63], v[236:239]\n"
"ds_read_b64_tr_b16 v[22:23], v33 offset:50176\n"
"v_cvt_pk_bf16_f32 v194, v208, v209\n"
"v_mfma_f32_16x16x32_bf16 v[240:243], v[28:31], a[48:51], v[240:243]\n"
"v_cvt_pk_bf16_f32 v195, v210, v211\n"
"v_cvt_pk_bf16_f32 v196, v196, v197\n"
"v_mfma_f32_16x16x32_bf16 v[244:247], v[28:31], a[52:55], v[244:247]\n"
"v_cvt_pk_bf16_f32 v197, v198, v199\n"
"v_cvt_pk_bf16_f32 v198, v212, v213\n"
"v_mfma_f32_16x16x32_bf16 v[248:251], v[28:31], a[56:59], v[248:251]\n"
"v_cvt_pk_bf16_f32 v199, v214, v215\n"
"v_cvt_pk_bf16_f32 v200, v200, v201\n"
"v_mfma_f32_16x16x32_bf16 v[252:255], v[28:31], a[60:63], v[252:255]\n"
"v_cvt_pk_bf16_f32 v201, v202, v203\n"
"s_waitcnt lgkmcnt(0)\n"
"v_mfma_f32_16x16x32_bf16 v[224:227], v[16:19], a[64:67], v[224:227]\n"
"s_waitcnt vmcnt(13)\n"
"ds_read_b64_tr_b16 v[24:25], v32 offset:57344\n"
"v_mfma_f32_16x16x32_bf16 v[228:231], v[16:19], a[68:71], v[228:231]\n"
"ds_read_b64_tr_b16 v[26:27], v32 offset:58368\n"
"v_cvt_pk_bf16_f32 v202, v216, v217\n"
"v_mfma_f32_16x16x32_bf16 v[232:235], v[16:19], a[72:75], v[232:235]\n"
"ds_read_b64_tr_b16 v[28:29], v33 offset:57344\n"
"v_cvt_pk_bf16_f32 v203, v218, v219\n"
"v_mfma_f32_16x16x32_bf16 v[236:239], v[16:19], a[76:79], v[236:239]\n"
"ds_read_b64_tr_b16 v[30:31], v33 offset:58368\n"
"s_add_u32 s76, s85, 0x8000\n"
"v_mfma_f32_16x16x32_bf16 v[240:243], v[20:23], a[64:67], v[240:243]\n"
"buffer_store_dwordx4 v[192:195], v108, s[28:31], s76 offen sc1\n"
"v_cvt_pk_bf16_f32 v204, v204, v205\n"
"v_mfma_f32_16x16x32_bf16 v[244:247], v[20:23], a[68:71], v[244:247]\n"
"buffer_load_dword v100, v97, s[28:31], s78 offen sc1\n"
"v_cvt_pk_bf16_f32 v205, v206, v207\n"
"v_mfma_f32_16x16x32_bf16 v[248:251], v[20:23], a[72:75], v[248:251]\n"
"v_cvt_pk_bf16_f32 v206, v220, v221\n"
"v_cvt_pk_bf16_f32 v207, v222, v223\n"
"v_mfma_f32_16x16x32_bf16 v[252:255], v[20:23], a[76:79], v[252:255]\n"
"s_waitcnt lgkmcnt(0)\n"
"v_mfma_f32_16x16x32_bf16 v[224:227], v[24:27], a[80:83], v[224:227]\n"
"s_waitcnt vmcnt(13)\n"
"ds_read_b64_tr_b16 v[16:17], v2\n"
"v_mfma_f32_16x16x32_bf16 v[228:231], v[24:27], a[84:87], v[228:231]\n"
"ds_read_b64_tr_b16 v[18:19], v2 offset:1024\n"
"v_mfma_f32_16x16x32_bf16 v[232:235], v[24:27], a[88:91], v[232:235]\n"
"ds_read_b64_tr_b16 v[20:21], v3\n"
"v_mfma_f32_16x16x32_bf16 v[236:239], v[24:27], a[92:95], v[236:239]\n"
"ds_read_b64_tr_b16 v[22:23], v3 offset:1024\n"
"s_add_u32 s76, s85, 0x28000\n"
"v_mfma_f32_16x16x32_bf16 v[240:243], v[28:31], a[80:83], v[240:243]\n"
"buffer_store_dwordx4 v[196:199], v109, s[28:31], s76 offen sc1\n"
"v_mfma_f32_16x16x32_bf16 v[244:247], v[28:31], a[84:87], v[244:247]\n"
"v_mfma_f32_16x16x32_bf16 v[248:251], v[28:31], a[88:91], v[248:251]\n"
"v_mfma_f32_16x16x32_bf16 v[252:255], v[28:31], a[92:95], v[252:255]\n"
"s_waitcnt lgkmcnt(0)\n"
"v_mfma_f32_16x16x32_bf16 v[224:227], v[16:19], a[96:99], v[224:227]\n"
"s_waitcnt vmcnt(12)\n"
"ds_read_b64_tr_b16 v[24:25], v2 offset:8192\n"
"v_mfma_f32_16x16x32_bf16 v[228:231], v[16:19], a[100:103], v[228:231]\n"
"ds_read_b64_tr_b16 v[26:27], v2 offset:9216\n"
"v_mfma_f32_16x16x32_bf16 v[232:235], v[16:19], a[104:107], v[232:235]\n"
"ds_read_b64_tr_b16 v[28:29], v3 offset:8192\n"
"v_mfma_f32_16x16x32_bf16 v[236:239], v[16:19], a[108:111], v[236:239]\n"
"ds_read_b64_tr_b16 v[30:31], v3 offset:9216\n"
"s_add_u32 s76, s85, 0x48000\n"
"v_mfma_f32_16x16x32_bf16 v[240:243], v[20:23], a[96:99], v[240:243]\n"
"buffer_store_dwordx4 v[200:203], v110, s[28:31], s76 offen sc1\n"
"s_waitcnt vmcnt(4)\n"
"v_mfma_f32_16x16x32_bf16 v[244:247], v[20:23], a[100:103], v[244:247]\n"
"buffer_store_dwordx4 v[104:107], v96, s[28:31], s69 offen offset:128 sc1\n"
"v_mfma_f32_16x16x32_bf16 v[248:251], v[20:23], a[104:107], v[248:251]\n"
"v_mfma_f32_16x16x32_bf16 v[252:255], v[20:23], a[108:111], v[252:255]\n"
"s_waitcnt lgkmcnt(0)\n"
"v_mfma_f32_16x16x32_bf16 v[224:227], v[24:27], a[112:115], v[224:227]\n"
"ds_read_b64_tr_b16 v[16:17], v2 offset:16384\n"
"v_mfma_f32_16x16x32_bf16 v[228:231], v[24:27], a[116:119], v[228:231]\n"
"ds_read_b64_tr_b16 v[18:19], v2 offset:17408\n"
"v_mfma_f32_16x16x32_bf16 v[232:235], v[24:27], a[120:123], v[232:235]\n"
"ds_read_b64_tr_b16 v[20:21], v3 offset:16384\n"
"v_mfma_f32_16x16x32_bf16 v[236:239], v[24:27], a[124:127], v[236:239]\n"
"ds_read_b64_tr_b16 v[22:23], v3 offset:17408\n"
"s_add_u32 s76, s85, 0x68000\n"
"v_mfma_f32_16x16x32_bf16 v[240:243], v[28:31], a[112:115], v[240:243]\n"
"buffer_store_dwordx4 v[204:207], v111, s[28:31], s76 offen sc1\n"
"v_mfma_f32_16x16x32_bf16 v[244:247], v[28:31], a[116:119], v[244:247]\n"
"v_mfma_f32_16x16x32_bf16 v[248:251], v[28:31], a[120:123], v[248:251]\n"
"v_mfma_f32_16x16x32_bf16 v[252:255], v[28:31], a[124:127], v[252:255]\n"
"s_waitcnt lgkmcnt(0)\n"
"v_mfma_f32_16x16x32_bf16 v[224:227], v[16:19], a[128:131], v[224:227]\n"
"ds_read_b64_tr_b16 v[24:25], v2 offset:24576\n"
"v_mfma_f32_16x16x32_bf16 v[228:231], v[16:19], a[132:135], v[228:231]\n"
"ds_read_b64_tr_b16 v[26:27], v2 offset:25600\n"
"v_mfma_f32_16x16x32_bf16 v[232:235], v[16:19], a[136:139], v[232:235]\n"
"ds_read_b64_tr_b16 v[28:29], v3 offset:24576\n"
"v_mfma_f32_16x16x32_bf16 v[236:239], v[16:19], a[140:143], v[236:239]\n"
"ds_read_b64_tr_b16 v[30:31], v3 offset:25600\n"
"v_mfma_f32_16x16x32_bf16 v[240:243], v[20:23], a[128:131], v[240:243]\n"
"v_mfma_f32_16x16x32_bf16 v[244:247], v[20:23], a[132:135], v[244:247]\n"
"v_mfma_f32_16x16x32_bf16 v[248:251], v[20:23], a[136:139], v[248:251]\n"
"v_mfma_f32_16x16x32_bf16 v[252:255], v[20:23], a[140:143], v[252:255]\n"
"s_waitcnt lgkmcnt(0)\n"
"v_mfma_f32_16x16x32_bf16 v[224:227], v[24:27], a[144:147], v[224:227]\n"
"v_mfma_f32_16x16x32_bf16 v[228:231], v[24:27], a[148:151], v[228:231]\n"
"v_mfma_f32_16x16x32_bf16 v[232:235], v[24:27], a[152:155], v[232:235]\n"
"v_mfma_f32_16x16x32_bf16 v[236:239], v[24:27], a[156:159], v[236:239]\n"
"v_mfma_f32_16x16x32_bf16 v[240:243], v[28:31], a[144:147], v[240:243]\n"
"v_mfma_f32_16x16x32_bf16 v[244:247], v[28:31], a[148:151], v[244:247]\n"
"v_mfma_f32_16x16x32_bf16 v[248:251], v[28:31], a[152:155], v[248:251]\n"
"v_mfma_f32_16x16x32_bf16 v[252:255], v[28:31], a[156:159], v[252:255]\n"
"; d end\n"
"s_nop 0\n"
"v_cvt_pk_bf16_f32 v224, v224, v225\n"
"v_cvt_pk_bf16_f32 v225, v226, v227\n"
"s_nop 1\n"
"v_cvt_pk_bf16_f32 v226, v240, v241\n"
"v_cvt_pk_bf16_f32 v227, v242, v243\n"
"v_cvt_pk_bf16_f32 v228, v228, v229\n"
"v_cvt_pk_bf16_f32 v229, v230, v231\n"
"v_cvt_pk_bf16_f32 v230, v244, v245\n"
"v_cvt_pk_bf16_f32 v231, v246, v247\n"
"v_cvt_pk_bf16_f32 v232, v232, v233\n"
"v_cvt_pk_bf16_f32 v233, v234, v235\n"
"v_cvt_pk_bf16_f32 v234, v248, v249\n"
"v_cvt_pk_bf16_f32 v235, v250, v251\n"
"v_cvt_pk_bf16_f32 v236, v236, v237\n"
"v_cvt_pk_bf16_f32 v237, v238, v239\n"
"v_cvt_pk_bf16_f32 v238, v252, v253\n"
"v_cvt_pk_bf16_f32 v239, v254, v255\n"
"s_add_u32 s76, s85, 0xc000\n"
"buffer_store_dwordx4 v[224:227], v108, s[28:31], s76 offen sc1\n"
"s_add_u32 s76, s85, 0x2c000\n"
"buffer_store_dwordx4 v[228:231], v109, s[28:31], s76 offen sc1\n"
"s_add_u32 s76, s85, 0x4c000\n"
"buffer_store_dwordx4 v[232:235], v110, s[28:31], s76 offen sc1\n"
"s_add_u32 s76, s85, 0x6c000\n"
"buffer_store_dwordx4 v[236:239], v111, s[28:31], s76 offen sc1\n"
"s_waitcnt vmcnt(4)\n"
"buffer_store_dwordx4 v[104:107], v96, s[28:31], s69 offen offset:256 sc1\n"
"L_x2p0c%=:\n"
"v_cmp_ne_u32 vcc, s10, v100\n"
"s_nop 0\n"
"s_and_b32 s11, vcc_lo, 127\n"
"s_cbranch_scc1 L_x2p0r%=\n"
"L_x2p0d%=:\n"
"s_add_u32 s77, s88, 0\n"
"buffer_load_dwordx4 a[192:195], v124, s[28:31], s77 offen sc1\n"
"s_add_u32 s77, s88, 0x1000\n"
"buffer_load_dwordx4 a[196:199], v125, s[28:31], s77 offen sc1\n"
"s_add_u32 s77, s88, 0x2000\n"
"buffer_load_dwordx4 a[200:203], v126, s[28:31], s77 offen sc1\n"
"s_add_u32 s77, s88, 0x3000\n"
"buffer_load_dwordx4 a[204:207], v127, s[28:31], s77 offen sc1\n"
"s_lshr_b32 s11, s8, 1\n"
"s_lshl_b32 s11, s11, 2\n"
"s_and_b32 s12, s8, 1\n"
"s_lshl_b32 s12, s12, 5\n"
"v_bfe_u32 v4, v6, 3, 2\n"
"v_lshlrev_b32 v4, 6, v4\n"
"v_and_b32 v5, 7, v6\n"
"v_lshl_add_u32 v4, v5, 2, v4\n"
"v_add_u32 v4, s12, v4\n"
"s_set_gpr_idx_on s11, gpr_idx(SRC0)\n"
"v_mov_b32 v108, v128\n"
"v_mov_b32 v109, v129\n"
"v_mov_b32 v110, v130\n"
"v_mov_b32 v111, v131\n"
"v_mov_b32 v112, v160\n"
"v_mov_b32 v113, v161\n"
"v_mov_b32 v114, v162\n"
"v_mov_b32 v115, v163\n"
"v_mov_b32 v116, v192\n"
"v_mov_b32 v117, v193\n"
"v_mov_b32 v118, v194\n"
"v_mov_b32 v119, v195\n"
"v_mov_b32 v120, v224\n"
"v_mov_b32 v121, v225\n"
"v_mov_b32 v122, v226\n"
"v_mov_b32 v123, v227\n"
"s_set_gpr_idx_off\n"
"s_nop 1\n"
"ds_bpermute_b32 v108, v4, v108\n"
"ds_bpermute_b32 v109, v4, v109\n"
"ds_bpermute_b32 v110, v4, v110\n"
"ds_bpermute_b32 v111, v4, v111\n"
"ds_bpermute_b32 v112, v4, v112\n"
"ds_bpermute_b32 v113, v4, v113\n"
"ds_bpermute_b32 v114, v4, v114\n"
"ds_bpermute_b32 v115, v4, v115\n"
"ds_bpermute_b32 v116, v4, v116\n"
"ds_bpermute_b32 v117, v4, v117\n"
"ds_bpermute_b32 v118, v4, v118\n"
"ds_bpermute_b32 v119, v4, v119\n"
"ds_bpermute_b32 v120, v4, v120\n"
"ds_bpermute_b32 v121, v4, v121\n"
"ds_bpermute_b32 v122, v4, v122\n"
"ds_bpermute_b32 v123, v4, v123\n"
"L_final%=:\n"
"s_lshl_b32 s11, s9, 16\n"
"s_add_u32 s36, s60, s11\n"
"s_addc_u32 s37, s61, 0\n"
"s_and_b32 s37, s37, 0xffff\n"
"s_mov_b32 s38, 0x10000\n"
"s_mov_b32 s39, 0x20000\n"
"ds_read_b128 v[160:163], v8 offset:10240\n"
"ds_read_b128 v[164:167], v8 offset:10304\n"
"ds_read_b128 v[168:171], v8 offset:10752\n"
"ds_read_b128 v[172:175], v8 offset:10816\n"
"v_accvgpr_read_b32 v176, a0\n"
"v_accvgpr_read_b32 v177, a1\n"
"v_accvgpr_read_b32 v178, a2\n"
"v_accvgpr_read_b32 v179, a3\n"
"v_accvgpr_read_b32 v180, a4\n"
"v_accvgpr_read_b32 v181, a5\n"
"v_accvgpr_read_b32 v182, a6\n"
"v_accvgpr_read_b32 v183, a7\n"
"v_accvgpr_read_b32 v184, a8\n"
"v_accvgpr_read_b32 v185, a9\n"
"v_accvgpr_read_b32 v186, a10\n"
"v_accvgpr_read_b32 v187, a11\n"
"v_accvgpr_read_b32 v188, a12\n"
"v_accvgpr_read_b32 v189, a13\n"
"v_accvgpr_read_b32 v190, a14\n"
"v_accvgpr_read_b32 v191, a15\n"
"v_accvgpr_read_b32 v9, a17\n"
"s_sleep 6\n"
"buffer_load_dword v101, v97, s[28:31], s78 offen offset:128 sc1\n"
"buffer_load_dword v102, v97, s[28:31], s78 offen offset:256 sc1\n"
"s_waitcnt lgkmcnt(15)\n"
"v_dot2_f32_bf16 v128, v108, v98, 0\n"
"v_dot2_f32_bf16 v129, v108, v99, 0\n"
"v_dot2_f32_bf16 v130, v109, v98, 0\n"
"v_dot2_f32_bf16 v131, v109, v99, 0\n"
"v_dot2_f32_bf16 v132, v110, v98, 0\n"
"v_dot2_f32_bf16 v133, v110, v99, 0\n"
"v_dot2_f32_bf16 v134, v111, v98, 0\n"
"v_dot2_f32_bf16 v135, v111, v99, 0\n"
"v_dot2_f32_bf16 v136, v112, v98, 0\n"
"v_dot2_f32_bf16 v137, v112, v99, 0\n"
"s_waitcnt lgkmcnt(14)\n"
"v_dot2_f32_bf16 v138, v113, v98, 0\n"
"v_dot2_f32_bf16 v139, v113, v99, 0\n"
"s_waitcnt lgkmcnt(13)\n"
"v_dot2_f32_bf16 v140, v114, v98, 0\n"
"v_dot2_f32_bf16 v141, v114, v99, 0\n"
"s_waitcnt lgkmcnt(12)\n"
"v_dot2_f32_bf16 v142, v115, v98, 0\n"
"v_dot2_f32_bf16 v143, v115, v99, 0\n"
"s_waitcnt lgkmcnt(11)\n"
"v_dot2_f32_bf16 v144, v116, v98, 0\n"
"v_dot2_f32_bf16 v145, v116, v99, 0\n"
"s_waitcnt lgkmcnt(10)\n"
"v_dot2_f32_bf16 v146, v117, v98, 0\n"
"v_dot2_f32_bf16 v147, v117, v99, 0\n"
"s_waitcnt lgkmcnt(9)\n"
"v_dot2_f32_bf16 v148, v118, v98, 0\n"
"v_dot2_f32_bf16 v149, v118, v99, 0\n"
"s_waitcnt lgkmcnt(8)\n"
"v_dot2_f32_bf16 v150, v119, v98, 0\n"
"v_dot2_f32_bf16 v151, v119, v99, 0\n"
"s_waitcnt lgkmcnt(7)\n"
"v_dot2_f32_bf16 v152, v120, v98, 0\n"
"v_dot2_f32_bf16 v153, v120, v99, 0\n"
"s_waitcnt lgkmcnt(6)\n"
"v_dot2_f32_bf16 v154, v121, v98, 0\n"
"v_dot2_f32_bf16 v155, v121, v99, 0\n"
"s_waitcnt lgkmcnt(5)\n"
"v_dot2_f32_bf16 v156, v122, v98, 0\n"
"v_dot2_f32_bf16 v157, v122, v99, 0\n"
"s_waitcnt lgkmcnt(4)\n"
"v_dot2_f32_bf16 v158, v123, v98, 0\n"
"v_dot2_f32_bf16 v159, v123, v99, 0\n"
"L_x2p1c%=:\n"
"s_waitcnt vmcnt(1)\n"
"v_cmp_ne_u32 vcc, s10, v101\n"
"s_nop 0\n"
"s_and_b32 s11, vcc_lo, 127\n"
"s_cbranch_scc1 L_x2p1r%=\n"
"L_x2p1d%=:\n"
"s_add_u32 s77, s88, 0x4000\n"
"buffer_load_dwordx4 a[208:211], v124, s[28:31], s77 offen sc1\n"
"s_add_u32 s77, s88, 0x5000\n"
"buffer_load_dwordx4 a[212:215], v125, s[28:31], s77 offen sc1\n"
"s_add_u32 s77, s88, 0x6000\n"
"buffer_load_dwordx4 a[216:219], v126, s[28:31], s77 offen sc1\n"
"s_add_u32 s77, s88, 0x7000\n"
"buffer_load_dwordx4 a[220:223], v127, s[28:31], s77 offen sc1\n"
"L_x2p2c%=:\n"
"s_waitcnt vmcnt(4)\n"
"v_cmp_ne_u32 vcc, s10, v102\n"
"s_nop 0\n"
"s_and_b32 s11, vcc_lo, 127\n"
"s_cbranch_scc1 L_x2p2r%=\n"
"L_x2p2d%=:\n"
"s_add_u32 s77, s88, 0x8000\n"
"buffer_load_dwordx4 a[224:227], v124, s[28:31], s77 offen sc1\n"
"s_add_u32 s77, s88, 0x9000\n"
"buffer_load_dwordx4 a[228:231], v125, s[28:31], s77 offen sc1\n"
"s_add_u32 s77, s88, 0xa000\n"
"buffer_load_dwordx4 a[232:235], v126, s[28:31], s77 offen sc1\n"
"s_add_u32 s77, s88, 0xb000\n"
"buffer_load_dwordx4 a[236:239], v127, s[28:31], s77 offen sc1\n"
"s_add_u32 s77, s88, 0xc000\n"
"buffer_load_dwordx4 v[108:111], v124, s[28:31], s77 offen sc1\n"
"s_add_u32 s77, s88, 0xd000\n"
"buffer_load_dwordx4 v[112:115], v125, s[28:31], s77 offen sc1\n"
"s_add_u32 s77, s88, 0xe000\n"
"buffer_load_dwordx4 v[116:119], v126, s[28:31], s77 offen sc1\n"
"s_add_u32 s77, s88, 0xf000\n"
"buffer_load_dwordx4 v[120:123], v127, s[28:31], s77 offen sc1\n"
"v_accvgpr_read_b32 v240, a192\n"
"v_accvgpr_read_b32 v241, a193\n"
"v_accvgpr_read_b32 v242, a194\n"
"v_accvgpr_read_b32 v243, a195\n"
"v_accvgpr_read_b32 v244, a196\n"
"v_accvgpr_read_b32 v245, a197\n"
"v_accvgpr_read_b32 v246, a198\n"
"v_accvgpr_read_b32 v247, a199\n"
"v_accvgpr_read_b32 v248, a200\n"
"v_accvgpr_read_b32 v249, a201\n"
"v_accvgpr_read_b32 v250, a202\n"
"v_accvgpr_read_b32 v251, a203\n"
"v_accvgpr_read_b32 v252, a204\n"
"v_accvgpr_read_b32 v253, a205\n"
"v_accvgpr_read_b32 v254, a206\n"
"v_accvgpr_read_b32 v255, a207\n"
"v_dot2_f32_bf16 v128, v240, s70, v128\n"
"v_dot2_f32_bf16 v129, v240, s71, v129\n"
"v_dot2_f32_bf16 v130, v241, s70, v130\n"
"v_dot2_f32_bf16 v131, v241, s71, v131\n"
"v_dot2_f32_bf16 v132, v242, s70, v132\n"
"v_dot2_f32_bf16 v133, v242, s71, v133\n"
"v_dot2_f32_bf16 v134, v243, s70, v134\n"
"v_dot2_f32_bf16 v135, v243, s71, v135\n"
"v_dot2_f32_bf16 v128, v244, s70, v128\n"
"v_dot2_f32_bf16 v129, v244, s71, v129\n"
"v_dot2_f32_bf16 v130, v245, s70, v130\n"
"v_dot2_f32_bf16 v131, v245, s71, v131\n"
"v_dot2_f32_bf16 v132, v246, s70, v132\n"
"v_dot2_f32_bf16 v133, v246, s71, v133\n"
"v_dot2_f32_bf16 v134, v247, s70, v134\n"
"v_dot2_f32_bf16 v135, v247, s71, v135\n"
"v_dot2_f32_bf16 v128, v248, s70, v128\n"
"v_dot2_f32_bf16 v129, v248, s71, v129\n"
"v_dot2_f32_bf16 v130, v249, s70, v130\n"
"v_dot2_f32_bf16 v131, v249, s71, v131\n"
"v_dot2_f32_bf16 v132, v250, s70, v132\n"
"v_dot2_f32_bf16 v133, v250, s71, v133\n"
"v_dot2_f32_bf16 v134, v251, s70, v134\n"
"v_dot2_f32_bf16 v135, v251, s71, v135\n"
"v_dot2_f32_bf16 v128, v252, s70, v128\n"
"v_dot2_f32_bf16 v129, v252, s71, v129\n"
"v_dot2_f32_bf16 v130, v253, s70, v130\n"
"v_dot2_f32_bf16 v131, v253, s71, v131\n"
"v_dot2_f32_bf16 v132, v254, s70, v132\n"
"v_dot2_f32_bf16 v133, v254, s71, v133\n"
"v_dot2_f32_bf16 v134, v255, s70, v134\n"
"v_dot2_f32_bf16 v135, v255, s71, v135\n"
"s_waitcnt vmcnt(11)\n"
"v_accvgpr_read_b32 v240, a208\n"
"v_accvgpr_read_b32 v241, a209\n"
"v_accvgpr_read_b32 v242, a210\n"
"v_accvgpr_read_b32 v243, a211\n"
"s_waitcnt vmcnt(10)\n"
"v_accvgpr_read_b32 v244, a212\n"
"v_accvgpr_read_b32 v245, a213\n"
"v_accvgpr_read_b32 v246, a214\n"
"v_accvgpr_read_b32 v247, a215\n"
"s_waitcnt vmcnt(9)\n"
"v_accvgpr_read_b32 v248, a216\n"
"v_accvgpr_read_b32 v249, a217\n"
"v_accvgpr_read_b32 v250, a218\n"
"v_accvgpr_read_b32 v251, a219\n"
"s_waitcnt vmcnt(8)\n"
"v_accvgpr_read_b32 v252, a220\n"
"v_accvgpr_read_b32 v253, a221\n"
"v_accvgpr_read_b32 v254, a222\n"
"v_accvgpr_read_b32 v255, a223\n"
"v_dot2_f32_bf16 v136, v240, s70, v136\n"
"v_dot2_f32_bf16 v137, v240, s71, v137\n"
"v_dot2_f32_bf16 v138, v241, s70, v138\n"
"v_dot2_f32_bf16 v139, v241, s71, v139\n"
"v_dot2_f32_bf16 v140, v242, s70, v140\n"
"v_dot2_f32_bf16 v141, v242, s71, v141\n"
"v_dot2_f32_bf16 v142, v243, s70, v142\n"
"v_dot2_f32_bf16 v143, v243, s71, v143\n"
"v_dot2_f32_bf16 v136, v244, s70, v136\n"
"v_dot2_f32_bf16 v137, v244, s71, v137\n"
"v_dot2_f32_bf16 v138, v245, s70, v138\n"
"v_dot2_f32_bf16 v139, v245, s71, v139\n"
"v_dot2_f32_bf16 v140, v246, s70, v140\n"
"v_dot2_f32_bf16 v141, v246, s71, v141\n"
"v_dot2_f32_bf16 v142, v247, s70, v142\n"
"v_dot2_f32_bf16 v143, v247, s71, v143\n"
"v_dot2_f32_bf16 v136, v248, s70, v136\n"
"v_dot2_f32_bf16 v137, v248, s71, v137\n"
"v_dot2_f32_bf16 v138, v249, s70, v138\n"
"v_dot2_f32_bf16 v139, v249, s71, v139\n"
"v_dot2_f32_bf16 v140, v250, s70, v140\n"
"v_dot2_f32_bf16 v141, v250, s71, v141\n"
"v_dot2_f32_bf16 v142, v251, s70, v142\n"
"v_dot2_f32_bf16 v143, v251, s71, v143\n"
"v_dot2_f32_bf16 v136, v252, s70, v136\n"
"v_dot2_f32_bf16 v137, v252, s71, v137\n"
"v_dot2_f32_bf16 v138, v253, s70, v138\n"
"v_dot2_f32_bf16 v139, v253, s71, v139\n"
"v_dot2_f32_bf16 v140, v254, s70, v140\n"
"v_dot2_f32_bf16 v141, v254, s71, v141\n"
"v_dot2_f32_bf16 v142, v255, s70, v142\n"
"v_dot2_f32_bf16 v143, v255, s71, v143\n"
"s_waitcnt vmcnt(7)\n"
"v_accvgpr_read_b32 v240, a224\n"
"v_accvgpr_read_b32 v241, a225\n"
"v_accvgpr_read_b32 v242, a226\n"
"v_accvgpr_read_b32 v243, a227\n"
"s_waitcnt vmcnt(6)\n"
"v_accvgpr_read_b32 v244, a228\n"
"v_accvgpr_read_b32 v245, a229\n"
"v_accvgpr_read_b32 v246, a230\n"
"v_accvgpr_read_b32 v247, a231\n"
"s_waitcnt vmcnt(5)\n"
"v_accvgpr_read_b32 v248, a232\n"
"v_accvgpr_read_b32 v249, a233\n"
"v_accvgpr_read_b32 v250, a234\n"
"v_accvgpr_read_b32 v251, a235\n"
"s_waitcnt vmcnt(4)\n"
"v_accvgpr_read_b32 v252, a236\n"
"v_accvgpr_read_b32 v253, a237\n"
"v_accvgpr_read_b32 v254, a238\n"
"v_accvgpr_read_b32 v255, a239\n"
"v_dot2_f32_bf16 v144, v240, s70, v144\n"
"v_dot2_f32_bf16 v145, v240, s71, v145\n"
"v_dot2_f32_bf16 v146, v241, s70, v146\n"
"v_dot2_f32_bf16 v147, v241, s71, v147\n"
"v_dot2_f32_bf16 v148, v242, s70, v148\n"
"v_dot2_f32_bf16 v149, v242, s71, v149\n"
"v_dot2_f32_bf16 v150, v243, s70, v150\n"
"v_dot2_f32_bf16 v151, v243, s71, v151\n"
"v_dot2_f32_bf16 v144, v244, s70, v144\n"
"v_dot2_f32_bf16 v145, v244, s71, v145\n"
"v_dot2_f32_bf16 v146, v245, s70, v146\n"
"v_dot2_f32_bf16 v147, v245, s71, v147\n"
"v_dot2_f32_bf16 v148, v246, s70, v148\n"
"v_dot2_f32_bf16 v149, v246, s71, v149\n"
"v_dot2_f32_bf16 v150, v247, s70, v150\n"
"v_dot2_f32_bf16 v151, v247, s71, v151\n"
"v_dot2_f32_bf16 v144, v248, s70, v144\n"
"v_dot2_f32_bf16 v145, v248, s71, v145\n"
"v_dot2_f32_bf16 v146, v249, s70, v146\n"
"v_dot2_f32_bf16 v147, v249, s71, v147\n"
"v_dot2_f32_bf16 v148, v250, s70, v148\n"
"v_dot2_f32_bf16 v149, v250, s71, v149\n"
"v_dot2_f32_bf16 v150, v251, s70, v150\n"
"v_dot2_f32_bf16 v151, v251, s71, v151\n"
"v_dot2_f32_bf16 v144, v252, s70, v144\n"
"v_dot2_f32_bf16 v145, v252, s71, v145\n"
"v_dot2_f32_bf16 v146, v253, s70, v146\n"
"v_dot2_f32_bf16 v147, v253, s71, v147\n"
"v_dot2_f32_bf16 v148, v254, s70, v148\n"
"v_dot2_f32_bf16 v149, v254, s71, v149\n"
"v_dot2_f32_bf16 v150, v255, s70, v150\n"
"v_dot2_f32_bf16 v151, v255, s71, v151\n"
"v_permlane32_swap_b32 v128, v144\n"
"v_permlane32_swap_b32 v129, v145\n"
"v_permlane32_swap_b32 v130, v146\n"
"v_permlane32_swap_b32 v131, v147\n"
"v_permlane32_swap_b32 v132, v148\n"
"v_permlane32_swap_b32 v133, v149\n"
"v_permlane32_swap_b32 v134, v150\n"
"v_permlane32_swap_b32 v135, v151\n"
"v_pk_add_f32 v[128:129], v[128:129], v[144:145]\n"
"v_pk_add_f32 v[130:131], v[130:131], v[146:147]\n"
"v_pk_add_f32 v[132:133], v[132:133], v[148:149]\n"
"v_pk_add_f32 v[134:135], v[134:135], v[150:151]\n"
"; p3\n"
"L_x2v3c%=:\n"
"s_waitcnt vmcnt(3)\n"
"v_max3_u32 v4, v108, v109, v110\n"
"s_waitcnt vmcnt(2)\n"
"v_max3_u32 v5, v111, v112, v113\n"
"v_max3_u32 v4, v4, v114, v115\n"
"s_waitcnt vmcnt(1)\n"
"v_max3_u32 v5, v5, v116, v117\n"
"v_max3_u32 v4, v4, v118, v119\n"
"s_waitcnt vmcnt(0)\n"
"v_max3_u32 v5, v5, v120, v121\n"
"v_max3_u32 v4, v4, v122, v123\n"
"v_max_u32 v4, v4, v5\n"
"v_cmp_eq_u32 vcc, -1, v4\n"
"s_nop 0\n"
"s_cbranch_vccnz L_x2v3r%=\n"
"L_x2v3d%=:\n"
"v_dot2_f32_bf16 v152, v108, s70, v152\n"
"v_dot2_f32_bf16 v153, v108, s71, v153\n"
"v_dot2_f32_bf16 v154, v109, s70, v154\n"
"v_dot2_f32_bf16 v155, v109, s71, v155\n"
"v_dot2_f32_bf16 v156, v110, s70, v156\n"
"v_dot2_f32_bf16 v157, v110, s71, v157\n"
"v_dot2_f32_bf16 v158, v111, s70, v158\n"
"v_dot2_f32_bf16 v159, v111, s71, v159\n"
"v_dot2_f32_bf16 v152, v112, s70, v152\n"
"v_dot2_f32_bf16 v153, v112, s71, v153\n"
"v_dot2_f32_bf16 v154, v113, s70, v154\n"
"v_dot2_f32_bf16 v155, v113, s71, v155\n"
"v_dot2_f32_bf16 v156, v114, s70, v156\n"
"v_dot2_f32_bf16 v157, v114, s71, v157\n"
"v_dot2_f32_bf16 v158, v115, s70, v158\n"
"v_dot2_f32_bf16 v159, v115, s71, v159\n"
"v_dot2_f32_bf16 v152, v116, s70, v152\n"
"v_dot2_f32_bf16 v153, v116, s71, v153\n"
"v_dot2_f32_bf16 v154, v117, s70, v154\n"
"v_dot2_f32_bf16 v155, v117, s71, v155\n"
"v_dot2_f32_bf16 v156, v118, s70, v156\n"
"v_dot2_f32_bf16 v157, v118, s71, v157\n"
"v_dot2_f32_bf16 v158, v119, s70, v158\n"
"v_dot2_f32_bf16 v159, v119, s71, v159\n"
"v_dot2_f32_bf16 v152, v120, s70, v152\n"
"v_dot2_f32_bf16 v153, v120, s71, v153\n"
"v_dot2_f32_bf16 v154, v121, s70, v154\n"
"v_dot2_f32_bf16 v155, v121, s71, v155\n"
"v_dot2_f32_bf16 v156, v122, s70, v156\n"
"v_dot2_f32_bf16 v157, v122, s71, v157\n"
"v_dot2_f32_bf16 v158, v123, s70, v158\n"
"v_dot2_f32_bf16 v159, v123, s71, v159\n"
"v_permlane32_swap_b32 v136, v152\n"
"v_permlane32_swap_b32 v137, v153\n"
"v_permlane32_swap_b32 v138, v154\n"
"v_permlane32_swap_b32 v139, v155\n"
"v_permlane32_swap_b32 v140, v156\n"
"v_permlane32_swap_b32 v141, v157\n"
"v_permlane32_swap_b32 v142, v158\n"
"v_permlane32_swap_b32 v143, v159\n"
"v_pk_add_f32 v[136:137], v[136:137], v[152:153]\n"
"v_pk_add_f32 v[138:139], v[138:139], v[154:155]\n"
"v_pk_add_f32 v[140:141], v[140:141], v[156:157]\n"
"v_pk_add_f32 v[142:143], v[142:143], v[158:159]\n"
"s_waitcnt lgkmcnt(3)\n"
"v_pk_fma_f32 v[176:177], v[128:129], v[160:161], v[176:177]\n"
"v_pk_fma_f32 v[178:179], v[130:131], v[162:163], v[178:179]\n"
"s_waitcnt lgkmcnt(2)\n"
"v_pk_fma_f32 v[180:181], v[132:133], v[164:165], v[180:181]\n"
"v_pk_fma_f32 v[182:183], v[134:135], v[166:167], v[182:183]\n"
"s_waitcnt lgkmcnt(1)\n"
"v_pk_fma_f32 v[184:185], v[136:137], v[168:169], v[184:185]\n"
"v_pk_fma_f32 v[186:187], v[138:139], v[170:171], v[186:187]\n"
"s_waitcnt lgkmcnt(0)\n"
"v_pk_fma_f32 v[188:189], v[140:141], v[172:173], v[188:189]\n"
"v_pk_fma_f32 v[190:191], v[142:143], v[174:175], v[190:191]\n"
"v_cvt_pk_bf16_f32 v176, v176, v177\n"
"v_cvt_pk_bf16_f32 v177, v178, v179\n"
"v_cvt_pk_bf16_f32 v178, v180, v181\n"
"v_cvt_pk_bf16_f32 v179, v182, v183\n"
"v_cvt_pk_bf16_f32 v180, v184, v185\n"
"v_cvt_pk_bf16_f32 v181, v186, v187\n"
"v_cvt_pk_bf16_f32 v182, v188, v189\n"
"v_cvt_pk_bf16_f32 v183, v190, v191\n"
"buffer_store_dwordx2 v[176:177], v9, s[36:39], s68 offen\n"
"buffer_store_dwordx2 v[178:179], v9, s[36:39], s68 offen offset:32\n"
"buffer_store_dwordx2 v[180:181], v9, s[36:39], s68 offen offset:256\n"
"buffer_store_dwordx2 v[182:183], v9, s[36:39], s68 offen offset:288\n"
"s_branch L_end%=\n"
"L_rare0_%=:\n"
"s_mov_b32 s42, 0\n"
"s_branch L_rareP0_%=\n"
"L_rare1_%=:\n"
"s_mov_b32 s42, 1\n"
"s_branch L_rareP1_%=\n"
"L_rare2_%=:\n"
"s_mov_b32 s42, 2\n"
"s_branch L_rareP0_%=\n"
"L_rare3_%=:\n"
"s_mov_b32 s42, 3\n"
"s_branch L_rareP1_%=\n"
"L_rare4_%=:\n"
"s_mov_b32 s42, 4\n"
"s_branch L_rareP0_%=\n"
"L_rare5_%=:\n"
"s_mov_b32 s42, 5\n"
"s_branch L_rareP1_%=\n"
"L_rare6_%=:\n"
"s_mov_b32 s42, 6\n"
"s_branch L_rareP0_%=\n"
"L_rare7_%=:\n"
"s_mov_b32 s42, 7\n"
"s_branch L_rareP1_%=\n"
"L_rareP0_%=:\n"
"s_nop 15\n"
"v_max3_f32 v20, v128, v129, v130\n"
"v_max3_f32 v16, v143, v160, v161\n"
"v_max3_f32 v20, v20, v131, v132\n"
"v_max3_f32 v16, v16, v162, v163\n"
"v_max3_f32 v20, v20, v133, v134\n"
"v_max3_f32 v16, v16, v164, v165\n"
"v_max3_f32 v20, v20, v135, v136\n"
"v_max3_f32 v16, v16, v166, v167\n"
"v_max3_f32 v20, v20, v137, v138\n"
"v_max3_f32 v16, v16, v168, v169\n"
"v_max3_f32 v20, v20, v139, v140\n"
"v_max3_f32 v16, v16, v170, v171\n"
"v_max3_f32 v20, v20, v141, v142\n"
"v_max3_f32 v16, v16, v172, v173\n"
"v_max3_f32 v16, v16, v174, v175\n"
"v_max_f32 v20, v20, v16\n"
"v_mov_b32 v16, v20\n"
"s_nop 1\n"
"v_permlane32_swap_b32 v20, v16\n"
"v_max_f32 v20, v20, v16\n"
"v_add_f32 v20, 0x42a00000, v20\n"
"v_max_f32 v20, 0, v20\n"
"v_sub_f32 v32, v32, v20\n"
"v_sub_f32 v33, v33, v20\n"
"v_sub_f32 v34, v34, v20\n"
"v_sub_f32 v35, v35, v20\n"
"v_sub_f32 v36, v36, v20\n"
"v_sub_f32 v37, v37, v20\n"
"v_sub_f32 v38, v38, v20\n"
"v_sub_f32 v39, v39, v20\n"
"v_sub_f32 v40, v40, v20\n"
"v_sub_f32 v41, v41, v20\n"
"v_sub_f32 v42, v42, v20\n"
"v_sub_f32 v43, v43, v20\n"
"v_sub_f32 v44, v44, v20\n"
"v_sub_f32 v45, v45, v20\n"
"v_sub_f32 v46, v46, v20\n"
"v_sub_f32 v47, v47, v20\n"
"v_sub_f32 v128, v128, v20\n"
"v_sub_f32 v129, v129, v20\n"
"v_sub_f32 v130, v130, v20\n"
"v_sub_f32 v131, v131, v20\n"
"v_sub_f32 v132, v132, v20\n"
"v_sub_f32 v133, v133, v20\n"
"v_sub_f32 v134, v134, v20\n"
"v_sub_f32 v135, v135, v20\n"
"v_sub_f32 v136, v136, v20\n"
"v_sub_f32 v137, v137, v20\n"
"v_sub_f32 v138, v138, v20\n"
"v_sub_f32 v139, v139, v20\n"
"v_sub_f32 v140, v140, v20\n"
"v_sub_f32 v141, v141, v20\n"
"v_sub_f32 v142, v142, v20\n"
"v_sub_f32 v143, v143, v20\n"
"v_sub_f32 v160, v160, v20\n"
"v_sub_f32 v161, v161, v20\n"
"v_sub_f32 v162, v162, v20\n"
"v_sub_f32 v163, v163, v20\n"
"v_sub_f32 v164, v164, v20\n"
"v_sub_f32 v165, v165, v20\n"
"v_sub_f32 v166, v166, v20\n"
"v_sub_f32 v167, v167, v20\n"
"v_sub_f32 v168, v168, v20\n"
"v_sub_f32 v169, v169, v20\n"
"v_sub_f32 v170, v170, v20\n"
"v_sub_f32 v171, v171, v20\n"
"v_sub_f32 v172, v172, v20\n"
"v_sub_f32 v173, v173, v20\n"
"v_sub_f32 v174, v174, v20\n"
"v_sub_f32 v175, v175, v20\n"
"v_sub_f32 v192, v192, v20\n"
"v_sub_f32 v193, v193, v20\n"
"v_sub_f32 v194, v194, v20\n"
"v_sub_f32 v195, v195, v20\n"
"v_sub_f32 v196, v196, v20\n"
"v_sub_f32 v197, v197, v20\n"
"v_sub_f32 v198, v198, v20\n"
"v_sub_f32 v199, v199, v20\n"
"v_sub_f32 v200, v200, v20\n"
"v_sub_f32 v201, v201, v20\n"
"v_sub_f32 v202, v202, v20\n"
"v_sub_f32 v203, v203, v20\n"
"v_sub_f32 v204, v204, v20\n"
"v_sub_f32 v205, v205, v20\n"
"v_sub_f32 v206, v206, v20\n"
"v_sub_f32 v207, v207, v20\n"
"v_sub_f32 v224, v224, v20\n"
"v_sub_f32 v225, v225, v20\n"
"v_sub_f32 v226, v226, v20\n"
"v_sub_f32 v227, v227, v20\n"
"v_sub_f32 v228, v228, v20\n"
"v_sub_f32 v229, v229, v20\n"
"v_sub_f32 v230, v230, v20\n"
"v_sub_f32 v231, v231, v20\n"
"v_sub_f32 v232, v232, v20\n"
"v_sub_f32 v233, v233, v20\n"
"v_sub_f32 v234, v234, v20\n"
"v_sub_f32 v235, v235, v20\n"
"v_sub_f32 v236, v236, v20\n"
"v_sub_f32 v237, v237, v20\n"
"v_sub_f32 v238, v238, v20\n"
"v_sub_f32 v239, v239, v20\n"
"v_mul_f32 v16, -0.5, v20\n"
"v_exp_f32 v16, v16\n"
"s_nop 0\n"
"v_mul_f32 v22, v22, v16\n"
"v_mul_f32 v22, v22, v16\n"
"v_accvgpr_read_b32 v24, a0\n"
"v_mul_f32 v24, v24, v16\n"
"v_mul_f32 v24, v24, v16\n"
"v_accvgpr_write_b32 a0, v24\n"
"v_accvgpr_read_b32 v24, a1\n"
"v_mul_f32 v24, v24, v16\n"
"v_mul_f32 v24, v24, v16\n"
"v_accvgpr_write_b32 a1, v24\n"
"v_accvgpr_read_b32 v24, a2\n"
"v_mul_f32 v24, v24, v16\n"
"v_mul_f32 v24, v24, v16\n"
"v_accvgpr_write_b32 a2, v24\n"
"v_accvgpr_read_b32 v24, a3\n"
"v_mul_f32 v24, v24, v16\n"
"v_mul_f32 v24, v24, v16\n"
"v_accvgpr_write_b32 a3, v24\n"
"v_accvgpr_read_b32 v24, a4\n"
"v_mul_f32 v24, v24, v16\n"
"v_mul_f32 v24, v24, v16\n"
"v_accvgpr_write_b32 a4, v24\n"
"v_accvgpr_read_b32 v24, a5\n"
"v_mul_f32 v24, v24, v16\n"
"v_mul_f32 v24, v24, v16\n"
"v_accvgpr_write_b32 a5, v24\n"
"v_accvgpr_read_b32 v24, a6\n"
"v_mul_f32 v24, v24, v16\n"
"v_mul_f32 v24, v24, v16\n"
"v_accvgpr_write_b32 a6, v24\n"
"v_accvgpr_read_b32 v24, a7\n"
"v_mul_f32 v24, v24, v16\n"
"v_mul_f32 v24, v24, v16\n"
"v_accvgpr_write_b32 a7, v24\n"
"v_accvgpr_read_b32 v24, a8\n"
"v_mul_f32 v24, v24, v16\n"
"v_mul_f32 v24, v24, v16\n"
"v_accvgpr_write_b32 a8, v24\n"
"v_accvgpr_read_b32 v24, a9\n"
"v_mul_f32 v24, v24, v16\n"
"v_mul_f32 v24, v24, v16\n"
"v_accvgpr_write_b32 a9, v24\n"
"v_accvgpr_read_b32 v24, a10\n"
"v_mul_f32 v24, v24, v16\n"
"v_mul_f32 v24, v24, v16\n"
"v_accvgpr_write_b32 a10, v24\n"
"v_accvgpr_read_b32 v24, a11\n"
"v_mul_f32 v24, v24, v16\n"
"v_mul_f32 v24, v24, v16\n"
"v_accvgpr_write_b32 a11, v24\n"
"v_accvgpr_read_b32 v24, a12\n"
"v_mul_f32 v24, v24, v16\n"
"v_mul_f32 v24, v24, v16\n"
"v_accvgpr_write_b32 a12, v24\n"
"v_accvgpr_read_b32 v24, a13\n"
"v_mul_f32 v24, v24, v16\n"
"v_mul_f32 v24, v24, v16\n"
"v_accvgpr_write_b32 a13, v24\n"
"v_accvgpr_read_b32 v24, a14\n"
"v_mul_f32 v24, v24, v16\n"
"v_mul_f32 v24, v24, v16\n"
"v_accvgpr_write_b32 a14, v24\n"
"v_accvgpr_read_b32 v24, a15\n"
"v_mul_f32 v24, v24, v16\n"
"v_mul_f32 v24, v24, v16\n"
"v_accvgpr_write_b32 a15, v24\n"
"v_accvgpr_read_b32 v24, a32\n"
"v_mul_f32 v24, v24, v16\n"
"v_mul_f32 v24, v24, v16\n"
"v_accvgpr_write_b32 a32, v24\n"
"v_accvgpr_read_b32 v24, a33\n"
"v_mul_f32 v24, v24, v16\n"
"v_mul_f32 v24, v24, v16\n"
"v_accvgpr_write_b32 a33, v24\n"
"v_accvgpr_read_b32 v24, a34\n"
"v_mul_f32 v24, v24, v16\n"
"v_mul_f32 v24, v24, v16\n"
"v_accvgpr_write_b32 a34, v24\n"
"v_accvgpr_read_b32 v24, a35\n"
"v_mul_f32 v24, v24, v16\n"
"v_mul_f32 v24, v24, v16\n"
"v_accvgpr_write_b32 a35, v24\n"
"v_accvgpr_read_b32 v24, a36\n"
"v_mul_f32 v24, v24, v16\n"
"v_mul_f32 v24, v24, v16\n"
"v_accvgpr_write_b32 a36, v24\n"
"v_accvgpr_read_b32 v24, a37\n"
"v_mul_f32 v24, v24, v16\n"
"v_mul_f32 v24, v24, v16\n"
"v_accvgpr_write_b32 a37, v24\n"
"v_accvgpr_read_b32 v24, a38\n"
"v_mul_f32 v24, v24, v16\n"
"v_mul_f32 v24, v24, v16\n"
"v_accvgpr_write_b32 a38, v24\n"
"v_accvgpr_read_b32 v24, a39\n"
"v_mul_f32 v24, v24, v16\n"
"v_mul_f32 v24, v24, v16\n"
"v_accvgpr_write_b32 a39, v24\n"
"v_accvgpr_read_b32 v24, a40\n"
"v_mul_f32 v24, v24, v16\n"
"v_mul_f32 v24, v24, v16\n"
"v_accvgpr_write_b32 a40, v24\n"
"v_accvgpr_read_b32 v24, a41\n"
"v_mul_f32 v24, v24, v16\n"
"v_mul_f32 v24, v24, v16\n"
"v_accvgpr_write_b32 a41, v24\n"
"v_accvgpr_read_b32 v24, a42\n"
"v_mul_f32 v24, v24, v16\n"
"v_mul_f32 v24, v24, v16\n"
"v_accvgpr_write_b32 a42, v24\n"
"v_accvgpr_read_b32 v24, a43\n"
"v_mul_f32 v24, v24, v16\n"
"v_mul_f32 v24, v24, v16\n"
"v_accvgpr_write_b32 a43, v24\n"
"v_accvgpr_read_b32 v24, a44\n"
"v_mul_f32 v24, v24, v16\n"
"v_mul_f32 v24, v24, v16\n"
"v_accvgpr_write_b32 a44, v24\n"
"v_accvgpr_read_b32 v24, a45\n"
"v_mul_f32 v24, v24, v16\n"
"v_mul_f32 v24, v24, v16\n"
"v_accvgpr_write_b32 a45, v24\n"
"v_accvgpr_read_b32 v24, a46\n"
"v_mul_f32 v24, v24, v16\n"
"v_mul_f32 v24, v24, v16\n"
"v_accvgpr_write_b32 a46, v24\n"
"v_accvgpr_read_b32 v24, a47\n"
"v_mul_f32 v24, v24, v16\n"
"v_mul_f32 v24, v24, v16\n"
"v_accvgpr_write_b32 a47, v24\n"
"v_accvgpr_read_b32 v24, a64\n"
"v_mul_f32 v24, v24, v16\n"
"v_mul_f32 v24, v24, v16\n"
"v_accvgpr_write_b32 a64, v24\n"
"v_accvgpr_read_b32 v24, a65\n"
"v_mul_f32 v24, v24, v16\n"
"v_mul_f32 v24, v24, v16\n"
"v_accvgpr_write_b32 a65, v24\n"
"v_accvgpr_read_b32 v24, a66\n"
"v_mul_f32 v24, v24, v16\n"
"v_mul_f32 v24, v24, v16\n"
"v_accvgpr_write_b32 a66, v24\n"
"v_accvgpr_read_b32 v24, a67\n"
"v_mul_f32 v24, v24, v16\n"
"v_mul_f32 v24, v24, v16\n"
"v_accvgpr_write_b32 a67, v24\n"
"v_accvgpr_read_b32 v24, a68\n"
"v_mul_f32 v24, v24, v16\n"
"v_mul_f32 v24, v24, v16\n"
"v_accvgpr_write_b32 a68, v24\n"
"v_accvgpr_read_b32 v24, a69\n"
"v_mul_f32 v24, v24, v16\n"
"v_mul_f32 v24, v24, v16\n"
"v_accvgpr_write_b32 a69, v24\n"
"v_accvgpr_read_b32 v24, a70\n"
"v_mul_f32 v24, v24, v16\n"
"v_mul_f32 v24, v24, v16\n"
"v_accvgpr_write_b32 a70, v24\n"
"v_accvgpr_read_b32 v24, a71\n"
"v_mul_f32 v24, v24, v16\n"
"v_mul_f32 v24, v24, v16\n"
"v_accvgpr_write_b32 a71, v24\n"
"v_accvgpr_read_b32 v24, a72\n"
"v_mul_f32 v24, v24, v16\n"
"v_mul_f32 v24, v24, v16\n"
"v_accvgpr_write_b32 a72, v24\n"
"v_accvgpr_read_b32 v24, a73\n"
"v_mul_f32 v24, v24, v16\n"
"v_mul_f32 v24, v24, v16\n"
"v_accvgpr_write_b32 a73, v24\n"
"v_accvgpr_read_b32 v24, a74\n"
"v_mul_f32 v24, v24, v16\n"
"v_mul_f32 v24, v24, v16\n"
"v_accvgpr_write_b32 a74, v24\n"
"v_accvgpr_read_b32 v24, a75\n"
"v_mul_f32 v24, v24, v16\n"
"v_mul_f32 v24, v24, v16\n"
"v_accvgpr_write_b32 a75, v24\n"
"v_accvgpr_read_b32 v24, a76\n"
"v_mul_f32 v24, v24, v16\n"
"v_mul_f32 v24, v24, v16\n"
"v_accvgpr_write_b32 a76, v24\n"
"v_accvgpr_read_b32 v24, a77\n"
"v_mul_f32 v24, v24, v16\n"
"v_mul_f32 v24, v24, v16\n"
"v_accvgpr_write_b32 a77, v24\n"
"v_accvgpr_read_b32 v24, a78\n"
"v_mul_f32 v24, v24, v16\n"
"v_mul_f32 v24, v24, v16\n"
"v_accvgpr_write_b32 a78, v24\n"
"v_accvgpr_read_b32 v24, a79\n"
"v_mul_f32 v24, v24, v16\n"
"v_mul_f32 v24, v24, v16\n"
"v_accvgpr_write_b32 a79, v24\n"
"v_accvgpr_read_b32 v24, a96\n"
"v_mul_f32 v24, v24, v16\n"
"v_mul_f32 v24, v24, v16\n"
"v_accvgpr_write_b32 a96, v24\n"
"v_accvgpr_read_b32 v24, a97\n"
"v_mul_f32 v24, v24, v16\n"
"v_mul_f32 v24, v24, v16\n"
"v_accvgpr_write_b32 a97, v24\n"
"v_accvgpr_read_b32 v24, a98\n"
"v_mul_f32 v24, v24, v16\n"
"v_mul_f32 v24, v24, v16\n"
"v_accvgpr_write_b32 a98, v24\n"
"v_accvgpr_read_b32 v24, a99\n"
"v_mul_f32 v24, v24, v16\n"
"v_mul_f32 v24, v24, v16\n"
"v_accvgpr_write_b32 a99, v24\n"
"v_accvgpr_read_b32 v24, a100\n"
"v_mul_f32 v24, v24, v16\n"
"v_mul_f32 v24, v24, v16\n"
"v_accvgpr_write_b32 a100, v24\n"
"v_accvgpr_read_b32 v24, a101\n"
"v_mul_f32 v24, v24, v16\n"
"v_mul_f32 v24, v24, v16\n"
"v_accvgpr_write_b32 a101, v24\n"
"v_accvgpr_read_b32 v24, a102\n"
"v_mul_f32 v24, v24, v16\n"
"v_mul_f32 v24, v24, v16\n"
"v_accvgpr_write_b32 a102, v24\n"
"v_accvgpr_read_b32 v24, a103\n"
"v_mul_f32 v24, v24, v16\n"
"v_mul_f32 v24, v24, v16\n"
"v_accvgpr_write_b32 a103, v24\n"
"v_accvgpr_read_b32 v24, a104\n"
"v_mul_f32 v24, v24, v16\n"
"v_mul_f32 v24, v24, v16\n"
"v_accvgpr_write_b32 a104, v24\n"
"v_accvgpr_read_b32 v24, a105\n"
"v_mul_f32 v24, v24, v16\n"
"v_mul_f32 v24, v24, v16\n"
"v_accvgpr_write_b32 a105, v24\n"
"v_accvgpr_read_b32 v24, a106\n"
"v_mul_f32 v24, v24, v16\n"
"v_mul_f32 v24, v24, v16\n"
"v_accvgpr_write_b32 a106, v24\n"
"v_accvgpr_read_b32 v24, a107\n"
"v_mul_f32 v24, v24, v16\n"
"v_mul_f32 v24, v24, v16\n"
"v_accvgpr_write_b32 a107, v24\n"
"v_accvgpr_read_b32 v24, a108\n"
"v_mul_f32 v24, v24, v16\n"
"v_mul_f32 v24, v24, v16\n"
"v_accvgpr_write_b32 a108, v24\n"
"v_accvgpr_read_b32 v24, a109\n"
"v_mul_f32 v24, v24, v16\n"
"v_mul_f32 v24, v24, v16\n"
"v_accvgpr_write_b32 a109, v24\n"
"v_accvgpr_read_b32 v24, a110\n"
"v_mul_f32 v24, v24, v16\n"
"v_mul_f32 v24, v24, v16\n"
"v_accvgpr_write_b32 a110, v24\n"
"v_accvgpr_read_b32 v24, a111\n"
"v_mul_f32 v24, v24, v16\n"
"v_mul_f32 v24, v24, v16\n"
"v_accvgpr_write_b32 a111, v24\n"
"v_max3_f32 v21, v144, v145, v146\n"
"v_max3_f32 v17, v159, v176, v177\n"
"v_max3_f32 v21, v21, v147, v148\n"
"v_max3_f32 v17, v17, v178, v179\n"
"v_max3_f32 v21, v21, v149, v150\n"
"v_max3_f32 v17, v17, v180, v181\n"
"v_max3_f32 v21, v21, v151, v152\n"
"v_max3_f32 v17, v17, v182, v183\n"
"v_max3_f32 v21, v21, v153, v154\n"
"v_max3_f32 v17, v17, v184, v185\n"
"v_max3_f32 v21, v21, v155, v156\n"
"v_max3_f32 v17, v17, v186, v187\n"
"v_max3_f32 v21, v21, v157, v158\n"
"v_max3_f32 v17, v17, v188, v189\n"
"v_max3_f32 v17, v17, v190, v191\n"
"v_max_f32 v21, v21, v17\n"
"v_mov_b32 v17, v21\n"
"s_nop 1\n"
"v_permlane32_swap_b32 v21, v17\n"
"v_max_f32 v21, v21, v17\n"
"v_add_f32 v21, 0x42a00000, v21\n"
"v_max_f32 v21, 0, v21\n"
"v_sub_f32 v48, v48, v21\n"
"v_sub_f32 v49, v49, v21\n"
"v_sub_f32 v50, v50, v21\n"
"v_sub_f32 v51, v51, v21\n"
"v_sub_f32 v52, v52, v21\n"
"v_sub_f32 v53, v53, v21\n"
"v_sub_f32 v54, v54, v21\n"
"v_sub_f32 v55, v55, v21\n"
"v_sub_f32 v56, v56, v21\n"
"v_sub_f32 v57, v57, v21\n"
"v_sub_f32 v58, v58, v21\n"
"v_sub_f32 v59, v59, v21\n"
"v_sub_f32 v60, v60, v21\n"
"v_sub_f32 v61, v61, v21\n"
"v_sub_f32 v62, v62, v21\n"
"v_sub_f32 v63, v63, v21\n"
"v_sub_f32 v144, v144, v21\n"
"v_sub_f32 v145, v145, v21\n"
"v_sub_f32 v146, v146, v21\n"
"v_sub_f32 v147, v147, v21\n"
"v_sub_f32 v148, v148, v21\n"
"v_sub_f32 v149, v149, v21\n"
"v_sub_f32 v150, v150, v21\n"
"v_sub_f32 v151, v151, v21\n"
"v_sub_f32 v152, v152, v21\n"
"v_sub_f32 v153, v153, v21\n"
"v_sub_f32 v154, v154, v21\n"
"v_sub_f32 v155, v155, v21\n"
"v_sub_f32 v156, v156, v21\n"
"v_sub_f32 v157, v157, v21\n"
"v_sub_f32 v158, v158, v21\n"
"v_sub_f32 v159, v159, v21\n"
"v_sub_f32 v176, v176, v21\n"
"v_sub_f32 v177, v177, v21\n"
"v_sub_f32 v178, v178, v21\n"
"v_sub_f32 v179, v179, v21\n"
"v_sub_f32 v180, v180, v21\n"
"v_sub_f32 v181, v181, v21\n"
"v_sub_f32 v182, v182, v21\n"
"v_sub_f32 v183, v183, v21\n"
"v_sub_f32 v184, v184, v21\n"
"v_sub_f32 v185, v185, v21\n"
"v_sub_f32 v186, v186, v21\n"
"v_sub_f32 v187, v187, v21\n"
"v_sub_f32 v188, v188, v21\n"
"v_sub_f32 v189, v189, v21\n"
"v_sub_f32 v190, v190, v21\n"
"v_sub_f32 v191, v191, v21\n"
"v_sub_f32 v208, v208, v21\n"
"v_sub_f32 v209, v209, v21\n"
"v_sub_f32 v210, v210, v21\n"
"v_sub_f32 v211, v211, v21\n"
"v_sub_f32 v212, v212, v21\n"
"v_sub_f32 v213, v213, v21\n"
"v_sub_f32 v214, v214, v21\n"
"v_sub_f32 v215, v215, v21\n"
"v_sub_f32 v216, v216, v21\n"
"v_sub_f32 v217, v217, v21\n"
"v_sub_f32 v218, v218, v21\n"
"v_sub_f32 v219, v219, v21\n"
"v_sub_f32 v220, v220, v21\n"
"v_sub_f32 v221, v221, v21\n"
"v_sub_f32 v222, v222, v21\n"
"v_sub_f32 v223, v223, v21\n"
"v_sub_f32 v240, v240, v21\n"
"v_sub_f32 v241, v241, v21\n"
"v_sub_f32 v242, v242, v21\n"
"v_sub_f32 v243, v243, v21\n"
"v_sub_f32 v244, v244, v21\n"
"v_sub_f32 v245, v245, v21\n"
"v_sub_f32 v246, v246, v21\n"
"v_sub_f32 v247, v247, v21\n"
"v_sub_f32 v248, v248, v21\n"
"v_sub_f32 v249, v249, v21\n"
"v_sub_f32 v250, v250, v21\n"
"v_sub_f32 v251, v251, v21\n"
"v_sub_f32 v252, v252, v21\n"
"v_sub_f32 v253, v253, v21\n"
"v_sub_f32 v254, v254, v21\n"
"v_sub_f32 v255, v255, v21\n"
"v_mul_f32 v17, -0.5, v21\n"
"v_exp_f32 v17, v17\n"
"s_nop 0\n"
"v_mul_f32 v23, v23, v17\n"
"v_mul_f32 v23, v23, v17\n"
"v_accvgpr_read_b32 v24, a16\n"
"v_mul_f32 v24, v24, v17\n"
"v_mul_f32 v24, v24, v17\n"
"v_accvgpr_write_b32 a16, v24\n"
"v_accvgpr_read_b32 v24, a17\n"
"v_mul_f32 v24, v24, v17\n"
"v_mul_f32 v24, v24, v17\n"
"v_accvgpr_write_b32 a17, v24\n"
"v_accvgpr_read_b32 v24, a18\n"
"v_mul_f32 v24, v24, v17\n"
"v_mul_f32 v24, v24, v17\n"
"v_accvgpr_write_b32 a18, v24\n"
"v_accvgpr_read_b32 v24, a19\n"
"v_mul_f32 v24, v24, v17\n"
"v_mul_f32 v24, v24, v17\n"
"v_accvgpr_write_b32 a19, v24\n"
"v_accvgpr_read_b32 v24, a20\n"
"v_mul_f32 v24, v24, v17\n"
"v_mul_f32 v24, v24, v17\n"
"v_accvgpr_write_b32 a20, v24\n"
"v_accvgpr_read_b32 v24, a21\n"
"v_mul_f32 v24, v24, v17\n"
"v_mul_f32 v24, v24, v17\n"
"v_accvgpr_write_b32 a21, v24\n"
"v_accvgpr_read_b32 v24, a22\n"
"v_mul_f32 v24, v24, v17\n"
"v_mul_f32 v24, v24, v17\n"
"v_accvgpr_write_b32 a22, v24\n"
"v_accvgpr_read_b32 v24, a23\n"
"v_mul_f32 v24, v24, v17\n"
"v_mul_f32 v24, v24, v17\n"
"v_accvgpr_write_b32 a23, v24\n"
"v_accvgpr_read_b32 v24, a24\n"
"v_mul_f32 v24, v24, v17\n"
"v_mul_f32 v24, v24, v17\n"
"v_accvgpr_write_b32 a24, v24\n"
"v_accvgpr_read_b32 v24, a25\n"
"v_mul_f32 v24, v24, v17\n"
"v_mul_f32 v24, v24, v17\n"
"v_accvgpr_write_b32 a25, v24\n"
"v_accvgpr_read_b32 v24, a26\n"
"v_mul_f32 v24, v24, v17\n"
"v_mul_f32 v24, v24, v17\n"
"v_accvgpr_write_b32 a26, v24\n"
"v_accvgpr_read_b32 v24, a27\n"
"v_mul_f32 v24, v24, v17\n"
"v_mul_f32 v24, v24, v17\n"
"v_accvgpr_write_b32 a27, v24\n"
"v_accvgpr_read_b32 v24, a28\n"
"v_mul_f32 v24, v24, v17\n"
"v_mul_f32 v24, v24, v17\n"
"v_accvgpr_write_b32 a28, v24\n"
"v_accvgpr_read_b32 v24, a29\n"
"v_mul_f32 v24, v24, v17\n"
"v_mul_f32 v24, v24, v17\n"
"v_accvgpr_write_b32 a29, v24\n"
"v_accvgpr_read_b32 v24, a30\n"
"v_mul_f32 v24, v24, v17\n"
"v_mul_f32 v24, v24, v17\n"
"v_accvgpr_write_b32 a30, v24\n"
"v_accvgpr_read_b32 v24, a31\n"
"v_mul_f32 v24, v24, v17\n"
"v_mul_f32 v24, v24, v17\n"
"v_accvgpr_write_b32 a31, v24\n"
"v_accvgpr_read_b32 v24, a48\n"
"v_mul_f32 v24, v24, v17\n"
"v_mul_f32 v24, v24, v17\n"
"v_accvgpr_write_b32 a48, v24\n"
"v_accvgpr_read_b32 v24, a49\n"
"v_mul_f32 v24, v24, v17\n"
"v_mul_f32 v24, v24, v17\n"
"v_accvgpr_write_b32 a49, v24\n"
"v_accvgpr_read_b32 v24, a50\n"
"v_mul_f32 v24, v24, v17\n"
"v_mul_f32 v24, v24, v17\n"
"v_accvgpr_write_b32 a50, v24\n"
"v_accvgpr_read_b32 v24, a51\n"
"v_mul_f32 v24, v24, v17\n"
"v_mul_f32 v24, v24, v17\n"
"v_accvgpr_write_b32 a51, v24\n"
"v_accvgpr_read_b32 v24, a52\n"
"v_mul_f32 v24, v24, v17\n"
"v_mul_f32 v24, v24, v17\n"
"v_accvgpr_write_b32 a52, v24\n"
"v_accvgpr_read_b32 v24, a53\n"
"v_mul_f32 v24, v24, v17\n"
"v_mul_f32 v24, v24, v17\n"
"v_accvgpr_write_b32 a53, v24\n"
"v_accvgpr_read_b32 v24, a54\n"
"v_mul_f32 v24, v24, v17\n"
"v_mul_f32 v24, v24, v17\n"
"v_accvgpr_write_b32 a54, v24\n"
"v_accvgpr_read_b32 v24, a55\n"
"v_mul_f32 v24, v24, v17\n"
"v_mul_f32 v24, v24, v17\n"
"v_accvgpr_write_b32 a55, v24\n"
"v_accvgpr_read_b32 v24, a56\n"
"v_mul_f32 v24, v24, v17\n"
"v_mul_f32 v24, v24, v17\n"
"v_accvgpr_write_b32 a56, v24\n"
"v_accvgpr_read_b32 v24, a57\n"
"v_mul_f32 v24, v24, v17\n"
"v_mul_f32 v24, v24, v17\n"
"v_accvgpr_write_b32 a57, v24\n"
"v_accvgpr_read_b32 v24, a58\n"
"v_mul_f32 v24, v24, v17\n"
"v_mul_f32 v24, v24, v17\n"
"v_accvgpr_write_b32 a58, v24\n"
"v_accvgpr_read_b32 v24, a59\n"
"v_mul_f32 v24, v24, v17\n"
"v_mul_f32 v24, v24, v17\n"
"v_accvgpr_write_b32 a59, v24\n"
"v_accvgpr_read_b32 v24, a60\n"
"v_mul_f32 v24, v24, v17\n"
"v_mul_f32 v24, v24, v17\n"
"v_accvgpr_write_b32 a60, v24\n"
"v_accvgpr_read_b32 v24, a61\n"
"v_mul_f32 v24, v24, v17\n"
"v_mul_f32 v24, v24, v17\n"
"v_accvgpr_write_b32 a61, v24\n"
"v_accvgpr_read_b32 v24, a62\n"
"v_mul_f32 v24, v24, v17\n"
"v_mul_f32 v24, v24, v17\n"
"v_accvgpr_write_b32 a62, v24\n"
"v_accvgpr_read_b32 v24, a63\n"
"v_mul_f32 v24, v24, v17\n"
"v_mul_f32 v24, v24, v17\n"
"v_accvgpr_write_b32 a63, v24\n"
"v_accvgpr_read_b32 v24, a80\n"
"v_mul_f32 v24, v24, v17\n"
"v_mul_f32 v24, v24, v17\n"
"v_accvgpr_write_b32 a80, v24\n"
"v_accvgpr_read_b32 v24, a81\n"
"v_mul_f32 v24, v24, v17\n"
"v_mul_f32 v24, v24, v17\n"
"v_accvgpr_write_b32 a81, v24\n"
"v_accvgpr_read_b32 v24, a82\n"
"v_mul_f32 v24, v24, v17\n"
"v_mul_f32 v24, v24, v17\n"
"v_accvgpr_write_b32 a82, v24\n"
"v_accvgpr_read_b32 v24, a83\n"
"v_mul_f32 v24, v24, v17\n"
"v_mul_f32 v24, v24, v17\n"
"v_accvgpr_write_b32 a83, v24\n"
"v_accvgpr_read_b32 v24, a84\n"
"v_mul_f32 v24, v24, v17\n"
"v_mul_f32 v24, v24, v17\n"
"v_accvgpr_write_b32 a84, v24\n"
"v_accvgpr_read_b32 v24, a85\n"
"v_mul_f32 v24, v24, v17\n"
"v_mul_f32 v24, v24, v17\n"
"v_accvgpr_write_b32 a85, v24\n"
"v_accvgpr_read_b32 v24, a86\n"
"v_mul_f32 v24, v24, v17\n"
"v_mul_f32 v24, v24, v17\n"
"v_accvgpr_write_b32 a86, v24\n"
"v_accvgpr_read_b32 v24, a87\n"
"v_mul_f32 v24, v24, v17\n"
"v_mul_f32 v24, v24, v17\n"
"v_accvgpr_write_b32 a87, v24\n"
"v_accvgpr_read_b32 v24, a88\n"
"v_mul_f32 v24, v24, v17\n"
"v_mul_f32 v24, v24, v17\n"
"v_accvgpr_write_b32 a88, v24\n"
"v_accvgpr_read_b32 v24, a89\n"
"v_mul_f32 v24, v24, v17\n"
"v_mul_f32 v24, v24, v17\n"
"v_accvgpr_write_b32 a89, v24\n"
"v_accvgpr_read_b32 v24, a90\n"
"v_mul_f32 v24, v24, v17\n"
"v_mul_f32 v24, v24, v17\n"
"v_accvgpr_write_b32 a90, v24\n"
"v_accvgpr_read_b32 v24, a91\n"
"v_mul_f32 v24, v24, v17\n"
"v_mul_f32 v24, v24, v17\n"
"v_accvgpr_write_b32 a91, v24\n"
"v_accvgpr_read_b32 v24, a92\n"
"v_mul_f32 v24, v24, v17\n"
"v_mul_f32 v24, v24, v17\n"
"v_accvgpr_write_b32 a92, v24\n"
"v_accvgpr_read_b32 v24, a93\n"
"v_mul_f32 v24, v24, v17\n"
"v_mul_f32 v24, v24, v17\n"
"v_accvgpr_write_b32 a93, v24\n"
"v_accvgpr_read_b32 v24, a94\n"
"v_mul_f32 v24, v24, v17\n"
"v_mul_f32 v24, v24, v17\n"
"v_accvgpr_write_b32 a94, v24\n"
"v_accvgpr_read_b32 v24, a95\n"
"v_mul_f32 v24, v24, v17\n"
"v_mul_f32 v24, v24, v17\n"
"v_accvgpr_write_b32 a95, v24\n"
"v_accvgpr_read_b32 v24, a112\n"
"v_mul_f32 v24, v24, v17\n"
"v_mul_f32 v24, v24, v17\n"
"v_accvgpr_write_b32 a112, v24\n"
"v_accvgpr_read_b32 v24, a113\n"
"v_mul_f32 v24, v24, v17\n"
"v_mul_f32 v24, v24, v17\n"
"v_accvgpr_write_b32 a113, v24\n"
"v_accvgpr_read_b32 v24, a114\n"
"v_mul_f32 v24, v24, v17\n"
"v_mul_f32 v24, v24, v17\n"
"v_accvgpr_write_b32 a114, v24\n"
"v_accvgpr_read_b32 v24, a115\n"
"v_mul_f32 v24, v24, v17\n"
"v_mul_f32 v24, v24, v17\n"
"v_accvgpr_write_b32 a115, v24\n"
"v_accvgpr_read_b32 v24, a116\n"
"v_mul_f32 v24, v24, v17\n"
"v_mul_f32 v24, v24, v17\n"
"v_accvgpr_write_b32 a116, v24\n"
"v_accvgpr_read_b32 v24, a117\n"
"v_mul_f32 v24, v24, v17\n"
"v_mul_f32 v24, v24, v17\n"
"v_accvgpr_write_b32 a117, v24\n"
"v_accvgpr_read_b32 v24, a118\n"
"v_mul_f32 v24, v24, v17\n"
"v_mul_f32 v24, v24, v17\n"
"v_accvgpr_write_b32 a118, v24\n"
"v_accvgpr_read_b32 v24, a119\n"
"v_mul_f32 v24, v24, v17\n"
"v_mul_f32 v24, v24, v17\n"
"v_accvgpr_write_b32 a119, v24\n"
"v_accvgpr_read_b32 v24, a120\n"
"v_mul_f32 v24, v24, v17\n"
"v_mul_f32 v24, v24, v17\n"
"v_accvgpr_write_b32 a120, v24\n"
"v_accvgpr_read_b32 v24, a121\n"
"v_mul_f32 v24, v24, v17\n"
"v_mul_f32 v24, v24, v17\n"
"v_accvgpr_write_b32 a121, v24\n"
"v_accvgpr_read_b32 v24, a122\n"
"v_mul_f32 v24, v24, v17\n"
"v_mul_f32 v24, v24, v17\n"
"v_accvgpr_write_b32 a122, v24\n"
"v_accvgpr_read_b32 v24, a123\n"
"v_mul_f32 v24, v24, v17\n"
"v_mul_f32 v24, v24, v17\n"
"v_accvgpr_write_b32 a123, v24\n"
"v_accvgpr_read_b32 v24, a124\n"
"v_mul_f32 v24, v24, v17\n"
"v_mul_f32 v24, v24, v17\n"
"v_accvgpr_write_b32 a124, v24\n"
"v_accvgpr_read_b32 v24, a125\n"
"v_mul_f32 v24, v24, v17\n"
"v_mul_f32 v24, v24, v17\n"
"v_accvgpr_write_b32 a125, v24\n"
"v_accvgpr_read_b32 v24, a126\n"
"v_mul_f32 v24, v24, v17\n"
"v_mul_f32 v24, v24, v17\n"
"v_accvgpr_write_b32 a126, v24\n"
"v_accvgpr_read_b32 v24, a127\n"
"v_mul_f32 v24, v24, v17\n"
"v_mul_f32 v24, v24, v17\n"
"v_accvgpr_write_b32 a127, v24\n"
"v_exp_f32 v16, v128\n"
"v_exp_f32 v17, v129\n"
"v_exp_f32 v18, v130\n"
"v_add_f32 v20, v16, v17\n"
"v_cvt_pk_bf16_f32 v64, v16, v17\n"
"v_exp_f32 v19, v131\n"
"v_add_f32 v20, v20, v18\n"
"v_exp_f32 v16, v132\n"
"v_add_f32 v20, v20, v19\n"
"v_cvt_pk_bf16_f32 v65, v18, v19\n"
"v_exp_f32 v17, v133\n"
"v_add_f32 v20, v20, v16\n"
"v_exp_f32 v18, v134\n"
"v_add_f32 v20, v20, v17\n"
"v_cvt_pk_bf16_f32 v66, v16, v17\n"
"v_exp_f32 v19, v135\n"
"v_add_f32 v20, v20, v18\n"
"v_exp_f32 v16, v136\n"
"v_add_f32 v20, v20, v19\n"
"v_cvt_pk_bf16_f32 v67, v18, v19\n"
"v_exp_f32 v17, v137\n"
"v_add_f32 v20, v20, v16\n"
"v_exp_f32 v18, v138\n"
"v_add_f32 v20, v20, v17\n"
"v_cvt_pk_bf16_f32 v68, v16, v17\n"
"v_exp_f32 v19, v139\n"
"v_add_f32 v20, v20, v18\n"
"v_exp_f32 v16, v140\n"
"v_add_f32 v20, v20, v19\n"
"v_cvt_pk_bf16_f32 v69, v18, v19\n"
"v_exp_f32 v17, v141\n"
"v_add_f32 v20, v20, v16\n"
"v_exp_f32 v18, v142\n"
"v_add_f32 v20, v20, v17\n"
"v_cvt_pk_bf16_f32 v70, v16, v17\n"
"v_exp_f32 v19, v143\n"
"v_add_f32 v20, v20, v18\n"
"v_exp_f32 v16, v144\n"
"v_add_f32 v20, v20, v19\n"
"v_cvt_pk_bf16_f32 v71, v18, v19\n"
"v_exp_f32 v17, v145\n"
"v_exp_f32 v18, v146\n"
"v_add_f32 v21, v16, v17\n"
"v_cvt_pk_bf16_f32 v72, v16, v17\n"
"v_exp_f32 v19, v147\n"
"v_add_f32 v21, v21, v18\n"
"v_exp_f32 v16, v148\n"
"v_add_f32 v21, v21, v19\n"
"v_cvt_pk_bf16_f32 v73, v18, v19\n"
"v_exp_f32 v17, v149\n"
"v_add_f32 v21, v21, v16\n"
"v_exp_f32 v18, v150\n"
"v_add_f32 v21, v21, v17\n"
"v_cvt_pk_bf16_f32 v74, v16, v17\n"
"v_exp_f32 v19, v151\n"
"v_add_f32 v21, v21, v18\n"
"v_exp_f32 v16, v152\n"
"v_add_f32 v21, v21, v19\n"
"v_cvt_pk_bf16_f32 v75, v18, v19\n"
"v_exp_f32 v17, v153\n"
"v_add_f32 v21, v21, v16\n"
"v_exp_f32 v18, v154\n"
"v_add_f32 v21, v21, v17\n"
"v_cvt_pk_bf16_f32 v76, v16, v17\n"
"v_exp_f32 v19, v155\n"
"v_add_f32 v21, v21, v18\n"
"v_exp_f32 v16, v156\n"
"v_add_f32 v21, v21, v19\n"
"v_cvt_pk_bf16_f32 v77, v18, v19\n"
"v_exp_f32 v17, v157\n"
"v_add_f32 v21, v21, v16\n"
"v_exp_f32 v18, v158\n"
"v_add_f32 v21, v21, v17\n"
"v_cvt_pk_bf16_f32 v78, v16, v17\n"
"v_exp_f32 v19, v159\n"
"v_add_f32 v21, v21, v18\n"
"v_add_f32 v21, v21, v19\n"
"v_cvt_pk_bf16_f32 v79, v18, v19\n"
"v_exp_f32 v16, v160\n"
"v_exp_f32 v17, v161\n"
"v_add_f32 v20, v20, v16\n"
"v_exp_f32 v18, v162\n"
"v_add_f32 v20, v20, v17\n"
"v_cvt_pk_bf16_f32 v96, v16, v17\n"
"v_exp_f32 v19, v163\n"
"v_add_f32 v20, v20, v18\n"
"v_exp_f32 v16, v164\n"
"v_add_f32 v20, v20, v19\n"
"v_cvt_pk_bf16_f32 v97, v18, v19\n"
"v_exp_f32 v17, v165\n"
"v_add_f32 v20, v20, v16\n"
"v_exp_f32 v18, v166\n"
"v_add_f32 v20, v20, v17\n"
"v_cvt_pk_bf16_f32 v98, v16, v17\n"
"v_exp_f32 v19, v167\n"
"v_add_f32 v20, v20, v18\n"
"v_exp_f32 v16, v168\n"
"v_add_f32 v20, v20, v19\n"
"v_cvt_pk_bf16_f32 v99, v18, v19\n"
"v_exp_f32 v17, v169\n"
"v_add_f32 v20, v20, v16\n"
"v_exp_f32 v18, v170\n"
"v_add_f32 v20, v20, v17\n"
"v_cvt_pk_bf16_f32 v100, v16, v17\n"
"v_exp_f32 v19, v171\n"
"v_add_f32 v20, v20, v18\n"
"v_exp_f32 v16, v172\n"
"v_add_f32 v20, v20, v19\n"
"v_cvt_pk_bf16_f32 v101, v18, v19\n"
"v_exp_f32 v17, v173\n"
"v_add_f32 v20, v20, v16\n"
"v_exp_f32 v18, v174\n"
"v_add_f32 v20, v20, v17\n"
"v_cvt_pk_bf16_f32 v102, v16, v17\n"
"v_exp_f32 v19, v175\n"
"v_add_f32 v20, v20, v18\n"
"v_exp_f32 v16, v176\n"
"v_add_f32 v20, v20, v19\n"
"v_cvt_pk_bf16_f32 v103, v18, v19\n"
"v_exp_f32 v17, v177\n"
"v_add_f32 v21, v21, v16\n"
"v_exp_f32 v18, v178\n"
"v_add_f32 v21, v21, v17\n"
"v_cvt_pk_bf16_f32 v104, v16, v17\n"
"v_exp_f32 v19, v179\n"
"v_add_f32 v21, v21, v18\n"
"v_exp_f32 v16, v180\n"
"v_add_f32 v21, v21, v19\n"
"v_cvt_pk_bf16_f32 v105, v18, v19\n"
"v_exp_f32 v17, v181\n"
"v_add_f32 v21, v21, v16\n"
"v_exp_f32 v18, v182\n"
"v_add_f32 v21, v21, v17\n"
"v_cvt_pk_bf16_f32 v106, v16, v17\n"
"v_exp_f32 v19, v183\n"
"v_add_f32 v21, v21, v18\n"
"v_exp_f32 v16, v184\n"
"v_add_f32 v21, v21, v19\n"
"v_cvt_pk_bf16_f32 v107, v18, v19\n"
"v_exp_f32 v17, v185\n"
"v_add_f32 v21, v21, v16\n"
"v_exp_f32 v18, v186\n"
"v_add_f32 v21, v21, v17\n"
"v_cvt_pk_bf16_f32 v108, v16, v17\n"
"v_exp_f32 v19, v187\n"
"v_add_f32 v21, v21, v18\n"
"v_exp_f32 v16, v188\n"
"v_add_f32 v21, v21, v19\n"
"v_cvt_pk_bf16_f32 v109, v18, v19\n"
"v_exp_f32 v17, v189\n"
"v_add_f32 v21, v21, v16\n"
"v_exp_f32 v18, v190\n"
"v_add_f32 v21, v21, v17\n"
"v_cvt_pk_bf16_f32 v110, v16, v17\n"
"v_exp_f32 v19, v191\n"
"v_add_f32 v21, v21, v18\n"
"v_add_f32 v21, v21, v19\n"
"v_cvt_pk_bf16_f32 v111, v18, v19\n"
"s_nop 7\n"
"s_cmp_eq_u32 s42, 0\n"
"s_cbranch_scc1 L_back0_%=\n"
"s_cmp_eq_u32 s42, 2\n"
"s_cbranch_scc1 L_back2_%=\n"
"s_cmp_eq_u32 s42, 4\n"
"s_cbranch_scc1 L_back4_%=\n"
"s_cmp_eq_u32 s42, 6\n"
"s_cbranch_scc1 L_back6_%=\n"
"L_rareP1_%=:\n"
"s_nop 15\n"
"v_max3_f32 v20, v192, v193, v194\n"
"v_max3_f32 v16, v207, v224, v225\n"
"v_max3_f32 v20, v20, v195, v196\n"
"v_max3_f32 v16, v16, v226, v227\n"
"v_max3_f32 v20, v20, v197, v198\n"
"v_max3_f32 v16, v16, v228, v229\n"
"v_max3_f32 v20, v20, v199, v200\n"
"v_max3_f32 v16, v16, v230, v231\n"
"v_max3_f32 v20, v20, v201, v202\n"
"v_max3_f32 v16, v16, v232, v233\n"
"v_max3_f32 v20, v20, v203, v204\n"
"v_max3_f32 v16, v16, v234, v235\n"
"v_max3_f32 v20, v20, v205, v206\n"
"v_max3_f32 v16, v16, v236, v237\n"
"v_max3_f32 v16, v16, v238, v239\n"
"v_max_f32 v20, v20, v16\n"
"v_mov_b32 v16, v20\n"
"s_nop 1\n"
"v_permlane32_swap_b32 v20, v16\n"
"v_max_f32 v20, v20, v16\n"
"v_add_f32 v20, 0x42a00000, v20\n"
"v_max_f32 v20, 0, v20\n"
"v_sub_f32 v32, v32, v20\n"
"v_sub_f32 v33, v33, v20\n"
"v_sub_f32 v34, v34, v20\n"
"v_sub_f32 v35, v35, v20\n"
"v_sub_f32 v36, v36, v20\n"
"v_sub_f32 v37, v37, v20\n"
"v_sub_f32 v38, v38, v20\n"
"v_sub_f32 v39, v39, v20\n"
"v_sub_f32 v40, v40, v20\n"
"v_sub_f32 v41, v41, v20\n"
"v_sub_f32 v42, v42, v20\n"
"v_sub_f32 v43, v43, v20\n"
"v_sub_f32 v44, v44, v20\n"
"v_sub_f32 v45, v45, v20\n"
"v_sub_f32 v46, v46, v20\n"
"v_sub_f32 v47, v47, v20\n"
"v_sub_f32 v128, v128, v20\n"
"v_sub_f32 v129, v129, v20\n"
"v_sub_f32 v130, v130, v20\n"
"v_sub_f32 v131, v131, v20\n"
"v_sub_f32 v132, v132, v20\n"
"v_sub_f32 v133, v133, v20\n"
"v_sub_f32 v134, v134, v20\n"
"v_sub_f32 v135, v135, v20\n"
"v_sub_f32 v136, v136, v20\n"
"v_sub_f32 v137, v137, v20\n"
"v_sub_f32 v138, v138, v20\n"
"v_sub_f32 v139, v139, v20\n"
"v_sub_f32 v140, v140, v20\n"
"v_sub_f32 v141, v141, v20\n"
"v_sub_f32 v142, v142, v20\n"
"v_sub_f32 v143, v143, v20\n"
"v_sub_f32 v160, v160, v20\n"
"v_sub_f32 v161, v161, v20\n"
"v_sub_f32 v162, v162, v20\n"
"v_sub_f32 v163, v163, v20\n"
"v_sub_f32 v164, v164, v20\n"
"v_sub_f32 v165, v165, v20\n"
"v_sub_f32 v166, v166, v20\n"
"v_sub_f32 v167, v167, v20\n"
"v_sub_f32 v168, v168, v20\n"
"v_sub_f32 v169, v169, v20\n"
"v_sub_f32 v170, v170, v20\n"
"v_sub_f32 v171, v171, v20\n"
"v_sub_f32 v172, v172, v20\n"
"v_sub_f32 v173, v173, v20\n"
"v_sub_f32 v174, v174, v20\n"
"v_sub_f32 v175, v175, v20\n"
"v_sub_f32 v192, v192, v20\n"
"v_sub_f32 v193, v193, v20\n"
"v_sub_f32 v194, v194, v20\n"
"v_sub_f32 v195, v195, v20\n"
"v_sub_f32 v196, v196, v20\n"
"v_sub_f32 v197, v197, v20\n"
"v_sub_f32 v198, v198, v20\n"
"v_sub_f32 v199, v199, v20\n"
"v_sub_f32 v200, v200, v20\n"
"v_sub_f32 v201, v201, v20\n"
"v_sub_f32 v202, v202, v20\n"
"v_sub_f32 v203, v203, v20\n"
"v_sub_f32 v204, v204, v20\n"
"v_sub_f32 v205, v205, v20\n"
"v_sub_f32 v206, v206, v20\n"
"v_sub_f32 v207, v207, v20\n"
"v_sub_f32 v224, v224, v20\n"
"v_sub_f32 v225, v225, v20\n"
"v_sub_f32 v226, v226, v20\n"
"v_sub_f32 v227, v227, v20\n"
"v_sub_f32 v228, v228, v20\n"
"v_sub_f32 v229, v229, v20\n"
"v_sub_f32 v230, v230, v20\n"
"v_sub_f32 v231, v231, v20\n"
"v_sub_f32 v232, v232, v20\n"
"v_sub_f32 v233, v233, v20\n"
"v_sub_f32 v234, v234, v20\n"
"v_sub_f32 v235, v235, v20\n"
"v_sub_f32 v236, v236, v20\n"
"v_sub_f32 v237, v237, v20\n"
"v_sub_f32 v238, v238, v20\n"
"v_sub_f32 v239, v239, v20\n"
"v_mul_f32 v16, -0.5, v20\n"
"v_exp_f32 v16, v16\n"
"s_nop 0\n"
"v_mul_f32 v22, v22, v16\n"
"v_mul_f32 v22, v22, v16\n"
"v_accvgpr_read_b32 v24, a0\n"
"v_mul_f32 v24, v24, v16\n"
"v_mul_f32 v24, v24, v16\n"
"v_accvgpr_write_b32 a0, v24\n"
"v_accvgpr_read_b32 v24, a1\n"
"v_mul_f32 v24, v24, v16\n"
"v_mul_f32 v24, v24, v16\n"
"v_accvgpr_write_b32 a1, v24\n"
"v_accvgpr_read_b32 v24, a2\n"
"v_mul_f32 v24, v24, v16\n"
"v_mul_f32 v24, v24, v16\n"
"v_accvgpr_write_b32 a2, v24\n"
"v_accvgpr_read_b32 v24, a3\n"
"v_mul_f32 v24, v24, v16\n"
"v_mul_f32 v24, v24, v16\n"
"v_accvgpr_write_b32 a3, v24\n"
"v_accvgpr_read_b32 v24, a4\n"
"v_mul_f32 v24, v24, v16\n"
"v_mul_f32 v24, v24, v16\n"
"v_accvgpr_write_b32 a4, v24\n"
"v_accvgpr_read_b32 v24, a5\n"
"v_mul_f32 v24, v24, v16\n"
"v_mul_f32 v24, v24, v16\n"
"v_accvgpr_write_b32 a5, v24\n"
"v_accvgpr_read_b32 v24, a6\n"
"v_mul_f32 v24, v24, v16\n"
"v_mul_f32 v24, v24, v16\n"
"v_accvgpr_write_b32 a6, v24\n"
"v_accvgpr_read_b32 v24, a7\n"
"v_mul_f32 v24, v24, v16\n"
"v_mul_f32 v24, v24, v16\n"
"v_accvgpr_write_b32 a7, v24\n"
"v_accvgpr_read_b32 v24, a8\n"
"v_mul_f32 v24, v24, v16\n"
"v_mul_f32 v24, v24, v16\n"
"v_accvgpr_write_b32 a8, v24\n"
"v_accvgpr_read_b32 v24, a9\n"
"v_mul_f32 v24, v24, v16\n"
"v_mul_f32 v24, v24, v16\n"
"v_accvgpr_write_b32 a9, v24\n"
"v_accvgpr_read_b32 v24, a10\n"
"v_mul_f32 v24, v24, v16\n"
"v_mul_f32 v24, v24, v16\n"
"v_accvgpr_write_b32 a10, v24\n"
"v_accvgpr_read_b32 v24, a11\n"
"v_mul_f32 v24, v24, v16\n"
"v_mul_f32 v24, v24, v16\n"
"v_accvgpr_write_b32 a11, v24\n"
"v_accvgpr_read_b32 v24, a12\n"
"v_mul_f32 v24, v24, v16\n"
"v_mul_f32 v24, v24, v16\n"
"v_accvgpr_write_b32 a12, v24\n"
"v_accvgpr_read_b32 v24, a13\n"
"v_mul_f32 v24, v24, v16\n"
"v_mul_f32 v24, v24, v16\n"
"v_accvgpr_write_b32 a13, v24\n"
"v_accvgpr_read_b32 v24, a14\n"
"v_mul_f32 v24, v24, v16\n"
"v_mul_f32 v24, v24, v16\n"
"v_accvgpr_write_b32 a14, v24\n"
"v_accvgpr_read_b32 v24, a15\n"
"v_mul_f32 v24, v24, v16\n"
"v_mul_f32 v24, v24, v16\n"
"v_accvgpr_write_b32 a15, v24\n"
"v_accvgpr_read_b32 v24, a32\n"
"v_mul_f32 v24, v24, v16\n"
"v_mul_f32 v24, v24, v16\n"
"v_accvgpr_write_b32 a32, v24\n"
"v_accvgpr_read_b32 v24, a33\n"
"v_mul_f32 v24, v24, v16\n"
"v_mul_f32 v24, v24, v16\n"
"v_accvgpr_write_b32 a33, v24\n"
"v_accvgpr_read_b32 v24, a34\n"
"v_mul_f32 v24, v24, v16\n"
"v_mul_f32 v24, v24, v16\n"
"v_accvgpr_write_b32 a34, v24\n"
"v_accvgpr_read_b32 v24, a35\n"
"v_mul_f32 v24, v24, v16\n"
"v_mul_f32 v24, v24, v16\n"
"v_accvgpr_write_b32 a35, v24\n"
"v_accvgpr_read_b32 v24, a36\n"
"v_mul_f32 v24, v24, v16\n"
"v_mul_f32 v24, v24, v16\n"
"v_accvgpr_write_b32 a36, v24\n"
"v_accvgpr_read_b32 v24, a37\n"
"v_mul_f32 v24, v24, v16\n"
"v_mul_f32 v24, v24, v16\n"
"v_accvgpr_write_b32 a37, v24\n"
"v_accvgpr_read_b32 v24, a38\n"
"v_mul_f32 v24, v24, v16\n"
"v_mul_f32 v24, v24, v16\n"
"v_accvgpr_write_b32 a38, v24\n"
"v_accvgpr_read_b32 v24, a39\n"
"v_mul_f32 v24, v24, v16\n"
"v_mul_f32 v24, v24, v16\n"
"v_accvgpr_write_b32 a39, v24\n"
"v_accvgpr_read_b32 v24, a40\n"
"v_mul_f32 v24, v24, v16\n"
"v_mul_f32 v24, v24, v16\n"
"v_accvgpr_write_b32 a40, v24\n"
"v_accvgpr_read_b32 v24, a41\n"
"v_mul_f32 v24, v24, v16\n"
"v_mul_f32 v24, v24, v16\n"
"v_accvgpr_write_b32 a41, v24\n"
"v_accvgpr_read_b32 v24, a42\n"
"v_mul_f32 v24, v24, v16\n"
"v_mul_f32 v24, v24, v16\n"
"v_accvgpr_write_b32 a42, v24\n"
"v_accvgpr_read_b32 v24, a43\n"
"v_mul_f32 v24, v24, v16\n"
"v_mul_f32 v24, v24, v16\n"
"v_accvgpr_write_b32 a43, v24\n"
"v_accvgpr_read_b32 v24, a44\n"
"v_mul_f32 v24, v24, v16\n"
"v_mul_f32 v24, v24, v16\n"
"v_accvgpr_write_b32 a44, v24\n"
"v_accvgpr_read_b32 v24, a45\n"
"v_mul_f32 v24, v24, v16\n"
"v_mul_f32 v24, v24, v16\n"
"v_accvgpr_write_b32 a45, v24\n"
"v_accvgpr_read_b32 v24, a46\n"
"v_mul_f32 v24, v24, v16\n"
"v_mul_f32 v24, v24, v16\n"
"v_accvgpr_write_b32 a46, v24\n"
"v_accvgpr_read_b32 v24, a47\n"
"v_mul_f32 v24, v24, v16\n"
"v_mul_f32 v24, v24, v16\n"
"v_accvgpr_write_b32 a47, v24\n"
"v_accvgpr_read_b32 v24, a64\n"
"v_mul_f32 v24, v24, v16\n"
"v_mul_f32 v24, v24, v16\n"
"v_accvgpr_write_b32 a64, v24\n"
"v_accvgpr_read_b32 v24, a65\n"
"v_mul_f32 v24, v24, v16\n"
"v_mul_f32 v24, v24, v16\n"
"v_accvgpr_write_b32 a65, v24\n"
"v_accvgpr_read_b32 v24, a66\n"
"v_mul_f32 v24, v24, v16\n"
"v_mul_f32 v24, v24, v16\n"
"v_accvgpr_write_b32 a66, v24\n"
"v_accvgpr_read_b32 v24, a67\n"
"v_mul_f32 v24, v24, v16\n"
"v_mul_f32 v24, v24, v16\n"
"v_accvgpr_write_b32 a67, v24\n"
"v_accvgpr_read_b32 v24, a68\n"
"v_mul_f32 v24, v24, v16\n"
"v_mul_f32 v24, v24, v16\n"
"v_accvgpr_write_b32 a68, v24\n"
"v_accvgpr_read_b32 v24, a69\n"
"v_mul_f32 v24, v24, v16\n"
"v_mul_f32 v24, v24, v16\n"
"v_accvgpr_write_b32 a69, v24\n"
"v_accvgpr_read_b32 v24, a70\n"
"v_mul_f32 v24, v24, v16\n"
"v_mul_f32 v24, v24, v16\n"
"v_accvgpr_write_b32 a70, v24\n"
"v_accvgpr_read_b32 v24, a71\n"
"v_mul_f32 v24, v24, v16\n"
"v_mul_f32 v24, v24, v16\n"
"v_accvgpr_write_b32 a71, v24\n"
"v_accvgpr_read_b32 v24, a72\n"
"v_mul_f32 v24, v24, v16\n"
"v_mul_f32 v24, v24, v16\n"
"v_accvgpr_write_b32 a72, v24\n"
"v_accvgpr_read_b32 v24, a73\n"
"v_mul_f32 v24, v24, v16\n"
"v_mul_f32 v24, v24, v16\n"
"v_accvgpr_write_b32 a73, v24\n"
"v_accvgpr_read_b32 v24, a74\n"
"v_mul_f32 v24, v24, v16\n"
"v_mul_f32 v24, v24, v16\n"
"v_accvgpr_write_b32 a74, v24\n"
"v_accvgpr_read_b32 v24, a75\n"
"v_mul_f32 v24, v24, v16\n"
"v_mul_f32 v24, v24, v16\n"
"v_accvgpr_write_b32 a75, v24\n"
"v_accvgpr_read_b32 v24, a76\n"
"v_mul_f32 v24, v24, v16\n"
"v_mul_f32 v24, v24, v16\n"
"v_accvgpr_write_b32 a76, v24\n"
"v_accvgpr_read_b32 v24, a77\n"
"v_mul_f32 v24, v24, v16\n"
"v_mul_f32 v24, v24, v16\n"
"v_accvgpr_write_b32 a77, v24\n"
"v_accvgpr_read_b32 v24, a78\n"
"v_mul_f32 v24, v24, v16\n"
"v_mul_f32 v24, v24, v16\n"
"v_accvgpr_write_b32 a78, v24\n"
"v_accvgpr_read_b32 v24, a79\n"
"v_mul_f32 v24, v24, v16\n"
"v_mul_f32 v24, v24, v16\n"
"v_accvgpr_write_b32 a79, v24\n"
"v_accvgpr_read_b32 v24, a96\n"
"v_mul_f32 v24, v24, v16\n"
"v_mul_f32 v24, v24, v16\n"
"v_accvgpr_write_b32 a96, v24\n"
"v_accvgpr_read_b32 v24, a97\n"
"v_mul_f32 v24, v24, v16\n"
"v_mul_f32 v24, v24, v16\n"
"v_accvgpr_write_b32 a97, v24\n"
"v_accvgpr_read_b32 v24, a98\n"
"v_mul_f32 v24, v24, v16\n"
"v_mul_f32 v24, v24, v16\n"
"v_accvgpr_write_b32 a98, v24\n"
"v_accvgpr_read_b32 v24, a99\n"
"v_mul_f32 v24, v24, v16\n"
"v_mul_f32 v24, v24, v16\n"
"v_accvgpr_write_b32 a99, v24\n"
"v_accvgpr_read_b32 v24, a100\n"
"v_mul_f32 v24, v24, v16\n"
"v_mul_f32 v24, v24, v16\n"
"v_accvgpr_write_b32 a100, v24\n"
"v_accvgpr_read_b32 v24, a101\n"
"v_mul_f32 v24, v24, v16\n"
"v_mul_f32 v24, v24, v16\n"
"v_accvgpr_write_b32 a101, v24\n"
"v_accvgpr_read_b32 v24, a102\n"
"v_mul_f32 v24, v24, v16\n"
"v_mul_f32 v24, v24, v16\n"
"v_accvgpr_write_b32 a102, v24\n"
"v_accvgpr_read_b32 v24, a103\n"
"v_mul_f32 v24, v24, v16\n"
"v_mul_f32 v24, v24, v16\n"
"v_accvgpr_write_b32 a103, v24\n"
"v_accvgpr_read_b32 v24, a104\n"
"v_mul_f32 v24, v24, v16\n"
"v_mul_f32 v24, v24, v16\n"
"v_accvgpr_write_b32 a104, v24\n"
"v_accvgpr_read_b32 v24, a105\n"
"v_mul_f32 v24, v24, v16\n"
"v_mul_f32 v24, v24, v16\n"
"v_accvgpr_write_b32 a105, v24\n"
"v_accvgpr_read_b32 v24, a106\n"
"v_mul_f32 v24, v24, v16\n"
"v_mul_f32 v24, v24, v16\n"
"v_accvgpr_write_b32 a106, v24\n"
"v_accvgpr_read_b32 v24, a107\n"
"v_mul_f32 v24, v24, v16\n"
"v_mul_f32 v24, v24, v16\n"
"v_accvgpr_write_b32 a107, v24\n"
"v_accvgpr_read_b32 v24, a108\n"
"v_mul_f32 v24, v24, v16\n"
"v_mul_f32 v24, v24, v16\n"
"v_accvgpr_write_b32 a108, v24\n"
"v_accvgpr_read_b32 v24, a109\n"
"v_mul_f32 v24, v24, v16\n"
"v_mul_f32 v24, v24, v16\n"
"v_accvgpr_write_b32 a109, v24\n"
"v_accvgpr_read_b32 v24, a110\n"
"v_mul_f32 v24, v24, v16\n"
"v_mul_f32 v24, v24, v16\n"
"v_accvgpr_write_b32 a110, v24\n"
"v_accvgpr_read_b32 v24, a111\n"
"v_mul_f32 v24, v24, v16\n"
"v_mul_f32 v24, v24, v16\n"
"v_accvgpr_write_b32 a111, v24\n"
"v_max3_f32 v21, v208, v209, v210\n"
"v_max3_f32 v17, v223, v240, v241\n"
"v_max3_f32 v21, v21, v211, v212\n"
"v_max3_f32 v17, v17, v242, v243\n"
"v_max3_f32 v21, v21, v213, v214\n"
"v_max3_f32 v17, v17, v244, v245\n"
"v_max3_f32 v21, v21, v215, v216\n"
"v_max3_f32 v17, v17, v246, v247\n"
"v_max3_f32 v21, v21, v217, v218\n"
"v_max3_f32 v17, v17, v248, v249\n"
"v_max3_f32 v21, v21, v219, v220\n"
"v_max3_f32 v17, v17, v250, v251\n"
"v_max3_f32 v21, v21, v221, v222\n"
"v_max3_f32 v17, v17, v252, v253\n"
"v_max3_f32 v17, v17, v254, v255\n"
"v_max_f32 v21, v21, v17\n"
"v_mov_b32 v17, v21\n"
"s_nop 1\n"
"v_permlane32_swap_b32 v21, v17\n"
"v_max_f32 v21, v21, v17\n"
"v_add_f32 v21, 0x42a00000, v21\n"
"v_max_f32 v21, 0, v21\n"
"v_sub_f32 v48, v48, v21\n"
"v_sub_f32 v49, v49, v21\n"
"v_sub_f32 v50, v50, v21\n"
"v_sub_f32 v51, v51, v21\n"
"v_sub_f32 v52, v52, v21\n"
"v_sub_f32 v53, v53, v21\n"
"v_sub_f32 v54, v54, v21\n"
"v_sub_f32 v55, v55, v21\n"
"v_sub_f32 v56, v56, v21\n"
"v_sub_f32 v57, v57, v21\n"
"v_sub_f32 v58, v58, v21\n"
"v_sub_f32 v59, v59, v21\n"
"v_sub_f32 v60, v60, v21\n"
"v_sub_f32 v61, v61, v21\n"
"v_sub_f32 v62, v62, v21\n"
"v_sub_f32 v63, v63, v21\n"
"v_sub_f32 v144, v144, v21\n"
"v_sub_f32 v145, v145, v21\n"
"v_sub_f32 v146, v146, v21\n"
"v_sub_f32 v147, v147, v21\n"
"v_sub_f32 v148, v148, v21\n"
"v_sub_f32 v149, v149, v21\n"
"v_sub_f32 v150, v150, v21\n"
"v_sub_f32 v151, v151, v21\n"
"v_sub_f32 v152, v152, v21\n"
"v_sub_f32 v153, v153, v21\n"
"v_sub_f32 v154, v154, v21\n"
"v_sub_f32 v155, v155, v21\n"
"v_sub_f32 v156, v156, v21\n"
"v_sub_f32 v157, v157, v21\n"
"v_sub_f32 v158, v158, v21\n"
"v_sub_f32 v159, v159, v21\n"
"v_sub_f32 v176, v176, v21\n"
"v_sub_f32 v177, v177, v21\n"
"v_sub_f32 v178, v178, v21\n"
"v_sub_f32 v179, v179, v21\n"
"v_sub_f32 v180, v180, v21\n"
"v_sub_f32 v181, v181, v21\n"
"v_sub_f32 v182, v182, v21\n"
"v_sub_f32 v183, v183, v21\n"
"v_sub_f32 v184, v184, v21\n"
"v_sub_f32 v185, v185, v21\n"
"v_sub_f32 v186, v186, v21\n"
"v_sub_f32 v187, v187, v21\n"
"v_sub_f32 v188, v188, v21\n"
"v_sub_f32 v189, v189, v21\n"
"v_sub_f32 v190, v190, v21\n"
"v_sub_f32 v191, v191, v21\n"
"v_sub_f32 v208, v208, v21\n"
"v_sub_f32 v209, v209, v21\n"
"v_sub_f32 v210, v210, v21\n"
"v_sub_f32 v211, v211, v21\n"
"v_sub_f32 v212, v212, v21\n"
"v_sub_f32 v213, v213, v21\n"
"v_sub_f32 v214, v214, v21\n"
"v_sub_f32 v215, v215, v21\n"
"v_sub_f32 v216, v216, v21\n"
"v_sub_f32 v217, v217, v21\n"
"v_sub_f32 v218, v218, v21\n"
"v_sub_f32 v219, v219, v21\n"
"v_sub_f32 v220, v220, v21\n"
"v_sub_f32 v221, v221, v21\n"
"v_sub_f32 v222, v222, v21\n"
"v_sub_f32 v223, v223, v21\n"
"v_sub_f32 v240, v240, v21\n"
"v_sub_f32 v241, v241, v21\n"
"v_sub_f32 v242, v242, v21\n"
"v_sub_f32 v243, v243, v21\n"
"v_sub_f32 v244, v244, v21\n"
"v_sub_f32 v245, v245, v21\n"
"v_sub_f32 v246, v246, v21\n"
"v_sub_f32 v247, v247, v21\n"
"v_sub_f32 v248, v248, v21\n"
"v_sub_f32 v249, v249, v21\n"
"v_sub_f32 v250, v250, v21\n"
"v_sub_f32 v251, v251, v21\n"
"v_sub_f32 v252, v252, v21\n"
"v_sub_f32 v253, v253, v21\n"
"v_sub_f32 v254, v254, v21\n"
"v_sub_f32 v255, v255, v21\n"
"v_mul_f32 v17, -0.5, v21\n"
"v_exp_f32 v17, v17\n"
"s_nop 0\n"
"v_mul_f32 v23, v23, v17\n"
"v_mul_f32 v23, v23, v17\n"
"v_accvgpr_read_b32 v24, a16\n"
"v_mul_f32 v24, v24, v17\n"
"v_mul_f32 v24, v24, v17\n"
"v_accvgpr_write_b32 a16, v24\n"
"v_accvgpr_read_b32 v24, a17\n"
"v_mul_f32 v24, v24, v17\n"
"v_mul_f32 v24, v24, v17\n"
"v_accvgpr_write_b32 a17, v24\n"
"v_accvgpr_read_b32 v24, a18\n"
"v_mul_f32 v24, v24, v17\n"
"v_mul_f32 v24, v24, v17\n"
"v_accvgpr_write_b32 a18, v24\n"
"v_accvgpr_read_b32 v24, a19\n"
"v_mul_f32 v24, v24, v17\n"
"v_mul_f32 v24, v24, v17\n"
"v_accvgpr_write_b32 a19, v24\n"
"v_accvgpr_read_b32 v24, a20\n"
"v_mul_f32 v24, v24, v17\n"
"v_mul_f32 v24, v24, v17\n"
"v_accvgpr_write_b32 a20, v24\n"
"v_accvgpr_read_b32 v24, a21\n"
"v_mul_f32 v24, v24, v17\n"
"v_mul_f32 v24, v24, v17\n"
"v_accvgpr_write_b32 a21, v24\n"
"v_accvgpr_read_b32 v24, a22\n"
"v_mul_f32 v24, v24, v17\n"
"v_mul_f32 v24, v24, v17\n"
"v_accvgpr_write_b32 a22, v24\n"
"v_accvgpr_read_b32 v24, a23\n"
"v_mul_f32 v24, v24, v17\n"
"v_mul_f32 v24, v24, v17\n"
"v_accvgpr_write_b32 a23, v24\n"
"v_accvgpr_read_b32 v24, a24\n"
"v_mul_f32 v24, v24, v17\n"
"v_mul_f32 v24, v24, v17\n"
"v_accvgpr_write_b32 a24, v24\n"
"v_accvgpr_read_b32 v24, a25\n"
"v_mul_f32 v24, v24, v17\n"
"v_mul_f32 v24, v24, v17\n"
"v_accvgpr_write_b32 a25, v24\n"
"v_accvgpr_read_b32 v24, a26\n"
"v_mul_f32 v24, v24, v17\n"
"v_mul_f32 v24, v24, v17\n"
"v_accvgpr_write_b32 a26, v24\n"
"v_accvgpr_read_b32 v24, a27\n"
"v_mul_f32 v24, v24, v17\n"
"v_mul_f32 v24, v24, v17\n"
"v_accvgpr_write_b32 a27, v24\n"
"v_accvgpr_read_b32 v24, a28\n"
"v_mul_f32 v24, v24, v17\n"
"v_mul_f32 v24, v24, v17\n"
"v_accvgpr_write_b32 a28, v24\n"
"v_accvgpr_read_b32 v24, a29\n"
"v_mul_f32 v24, v24, v17\n"
"v_mul_f32 v24, v24, v17\n"
"v_accvgpr_write_b32 a29, v24\n"
"v_accvgpr_read_b32 v24, a30\n"
"v_mul_f32 v24, v24, v17\n"
"v_mul_f32 v24, v24, v17\n"
"v_accvgpr_write_b32 a30, v24\n"
"v_accvgpr_read_b32 v24, a31\n"
"v_mul_f32 v24, v24, v17\n"
"v_mul_f32 v24, v24, v17\n"
"v_accvgpr_write_b32 a31, v24\n"
"v_accvgpr_read_b32 v24, a48\n"
"v_mul_f32 v24, v24, v17\n"
"v_mul_f32 v24, v24, v17\n"
"v_accvgpr_write_b32 a48, v24\n"
"v_accvgpr_read_b32 v24, a49\n"
"v_mul_f32 v24, v24, v17\n"
"v_mul_f32 v24, v24, v17\n"
"v_accvgpr_write_b32 a49, v24\n"
"v_accvgpr_read_b32 v24, a50\n"
"v_mul_f32 v24, v24, v17\n"
"v_mul_f32 v24, v24, v17\n"
"v_accvgpr_write_b32 a50, v24\n"
"v_accvgpr_read_b32 v24, a51\n"
"v_mul_f32 v24, v24, v17\n"
"v_mul_f32 v24, v24, v17\n"
"v_accvgpr_write_b32 a51, v24\n"
"v_accvgpr_read_b32 v24, a52\n"
"v_mul_f32 v24, v24, v17\n"
"v_mul_f32 v24, v24, v17\n"
"v_accvgpr_write_b32 a52, v24\n"
"v_accvgpr_read_b32 v24, a53\n"
"v_mul_f32 v24, v24, v17\n"
"v_mul_f32 v24, v24, v17\n"
"v_accvgpr_write_b32 a53, v24\n"
"v_accvgpr_read_b32 v24, a54\n"
"v_mul_f32 v24, v24, v17\n"
"v_mul_f32 v24, v24, v17\n"
"v_accvgpr_write_b32 a54, v24\n"
"v_accvgpr_read_b32 v24, a55\n"
"v_mul_f32 v24, v24, v17\n"
"v_mul_f32 v24, v24, v17\n"
"v_accvgpr_write_b32 a55, v24\n"
"v_accvgpr_read_b32 v24, a56\n"
"v_mul_f32 v24, v24, v17\n"
"v_mul_f32 v24, v24, v17\n"
"v_accvgpr_write_b32 a56, v24\n"
"v_accvgpr_read_b32 v24, a57\n"
"v_mul_f32 v24, v24, v17\n"
"v_mul_f32 v24, v24, v17\n"
"v_accvgpr_write_b32 a57, v24\n"
"v_accvgpr_read_b32 v24, a58\n"
"v_mul_f32 v24, v24, v17\n"
"v_mul_f32 v24, v24, v17\n"
"v_accvgpr_write_b32 a58, v24\n"
"v_accvgpr_read_b32 v24, a59\n"
"v_mul_f32 v24, v24, v17\n"
"v_mul_f32 v24, v24, v17\n"
"v_accvgpr_write_b32 a59, v24\n"
"v_accvgpr_read_b32 v24, a60\n"
"v_mul_f32 v24, v24, v17\n"
"v_mul_f32 v24, v24, v17\n"
"v_accvgpr_write_b32 a60, v24\n"
"v_accvgpr_read_b32 v24, a61\n"
"v_mul_f32 v24, v24, v17\n"
"v_mul_f32 v24, v24, v17\n"
"v_accvgpr_write_b32 a61, v24\n"
"v_accvgpr_read_b32 v24, a62\n"
"v_mul_f32 v24, v24, v17\n"
"v_mul_f32 v24, v24, v17\n"
"v_accvgpr_write_b32 a62, v24\n"
"v_accvgpr_read_b32 v24, a63\n"
"v_mul_f32 v24, v24, v17\n"
"v_mul_f32 v24, v24, v17\n"
"v_accvgpr_write_b32 a63, v24\n"
"v_accvgpr_read_b32 v24, a80\n"
"v_mul_f32 v24, v24, v17\n"
"v_mul_f32 v24, v24, v17\n"
"v_accvgpr_write_b32 a80, v24\n"
"v_accvgpr_read_b32 v24, a81\n"
"v_mul_f32 v24, v24, v17\n"
"v_mul_f32 v24, v24, v17\n"
"v_accvgpr_write_b32 a81, v24\n"
"v_accvgpr_read_b32 v24, a82\n"
"v_mul_f32 v24, v24, v17\n"
"v_mul_f32 v24, v24, v17\n"
"v_accvgpr_write_b32 a82, v24\n"
"v_accvgpr_read_b32 v24, a83\n"
"v_mul_f32 v24, v24, v17\n"
"v_mul_f32 v24, v24, v17\n"
"v_accvgpr_write_b32 a83, v24\n"
"v_accvgpr_read_b32 v24, a84\n"
"v_mul_f32 v24, v24, v17\n"
"v_mul_f32 v24, v24, v17\n"
"v_accvgpr_write_b32 a84, v24\n"
"v_accvgpr_read_b32 v24, a85\n"
"v_mul_f32 v24, v24, v17\n"
"v_mul_f32 v24, v24, v17\n"
"v_accvgpr_write_b32 a85, v24\n"
"v_accvgpr_read_b32 v24, a86\n"
"v_mul_f32 v24, v24, v17\n"
"v_mul_f32 v24, v24, v17\n"
"v_accvgpr_write_b32 a86, v24\n"
"v_accvgpr_read_b32 v24, a87\n"
"v_mul_f32 v24, v24, v17\n"
"v_mul_f32 v24, v24, v17\n"
"v_accvgpr_write_b32 a87, v24\n"
"v_accvgpr_read_b32 v24, a88\n"
"v_mul_f32 v24, v24, v17\n"
"v_mul_f32 v24, v24, v17\n"
"v_accvgpr_write_b32 a88, v24\n"
"v_accvgpr_read_b32 v24, a89\n"
"v_mul_f32 v24, v24, v17\n"
"v_mul_f32 v24, v24, v17\n"
"v_accvgpr_write_b32 a89, v24\n"
"v_accvgpr_read_b32 v24, a90\n"
"v_mul_f32 v24, v24, v17\n"
"v_mul_f32 v24, v24, v17\n"
"v_accvgpr_write_b32 a90, v24\n"
"v_accvgpr_read_b32 v24, a91\n"
"v_mul_f32 v24, v24, v17\n"
"v_mul_f32 v24, v24, v17\n"
"v_accvgpr_write_b32 a91, v24\n"
"v_accvgpr_read_b32 v24, a92\n"
"v_mul_f32 v24, v24, v17\n"
"v_mul_f32 v24, v24, v17\n"
"v_accvgpr_write_b32 a92, v24\n"
"v_accvgpr_read_b32 v24, a93\n"
"v_mul_f32 v24, v24, v17\n"
"v_mul_f32 v24, v24, v17\n"
"v_accvgpr_write_b32 a93, v24\n"
"v_accvgpr_read_b32 v24, a94\n"
"v_mul_f32 v24, v24, v17\n"
"v_mul_f32 v24, v24, v17\n"
"v_accvgpr_write_b32 a94, v24\n"
"v_accvgpr_read_b32 v24, a95\n"
"v_mul_f32 v24, v24, v17\n"
"v_mul_f32 v24, v24, v17\n"
"v_accvgpr_write_b32 a95, v24\n"
"v_accvgpr_read_b32 v24, a112\n"
"v_mul_f32 v24, v24, v17\n"
"v_mul_f32 v24, v24, v17\n"
"v_accvgpr_write_b32 a112, v24\n"
"v_accvgpr_read_b32 v24, a113\n"
"v_mul_f32 v24, v24, v17\n"
"v_mul_f32 v24, v24, v17\n"
"v_accvgpr_write_b32 a113, v24\n"
"v_accvgpr_read_b32 v24, a114\n"
"v_mul_f32 v24, v24, v17\n"
"v_mul_f32 v24, v24, v17\n"
"v_accvgpr_write_b32 a114, v24\n"
"v_accvgpr_read_b32 v24, a115\n"
"v_mul_f32 v24, v24, v17\n"
"v_mul_f32 v24, v24, v17\n"
"v_accvgpr_write_b32 a115, v24\n"
"v_accvgpr_read_b32 v24, a116\n"
"v_mul_f32 v24, v24, v17\n"
"v_mul_f32 v24, v24, v17\n"
"v_accvgpr_write_b32 a116, v24\n"
"v_accvgpr_read_b32 v24, a117\n"
"v_mul_f32 v24, v24, v17\n"
"v_mul_f32 v24, v24, v17\n"
"v_accvgpr_write_b32 a117, v24\n"
"v_accvgpr_read_b32 v24, a118\n"
"v_mul_f32 v24, v24, v17\n"
"v_mul_f32 v24, v24, v17\n"
"v_accvgpr_write_b32 a118, v24\n"
"v_accvgpr_read_b32 v24, a119\n"
"v_mul_f32 v24, v24, v17\n"
"v_mul_f32 v24, v24, v17\n"
"v_accvgpr_write_b32 a119, v24\n"
"v_accvgpr_read_b32 v24, a120\n"
"v_mul_f32 v24, v24, v17\n"
"v_mul_f32 v24, v24, v17\n"
"v_accvgpr_write_b32 a120, v24\n"
"v_accvgpr_read_b32 v24, a121\n"
"v_mul_f32 v24, v24, v17\n"
"v_mul_f32 v24, v24, v17\n"
"v_accvgpr_write_b32 a121, v24\n"
"v_accvgpr_read_b32 v24, a122\n"
"v_mul_f32 v24, v24, v17\n"
"v_mul_f32 v24, v24, v17\n"
"v_accvgpr_write_b32 a122, v24\n"
"v_accvgpr_read_b32 v24, a123\n"
"v_mul_f32 v24, v24, v17\n"
"v_mul_f32 v24, v24, v17\n"
"v_accvgpr_write_b32 a123, v24\n"
"v_accvgpr_read_b32 v24, a124\n"
"v_mul_f32 v24, v24, v17\n"
"v_mul_f32 v24, v24, v17\n"
"v_accvgpr_write_b32 a124, v24\n"
"v_accvgpr_read_b32 v24, a125\n"
"v_mul_f32 v24, v24, v17\n"
"v_mul_f32 v24, v24, v17\n"
"v_accvgpr_write_b32 a125, v24\n"
"v_accvgpr_read_b32 v24, a126\n"
"v_mul_f32 v24, v24, v17\n"
"v_mul_f32 v24, v24, v17\n"
"v_accvgpr_write_b32 a126, v24\n"
"v_accvgpr_read_b32 v24, a127\n"
"v_mul_f32 v24, v24, v17\n"
"v_mul_f32 v24, v24, v17\n"
"v_accvgpr_write_b32 a127, v24\n"
"v_exp_f32 v16, v192\n"
"v_exp_f32 v17, v193\n"
"v_exp_f32 v18, v194\n"
"v_add_f32 v20, v16, v17\n"
"v_cvt_pk_bf16_f32 v80, v16, v17\n"
"v_exp_f32 v19, v195\n"
"v_add_f32 v20, v20, v18\n"
"v_exp_f32 v16, v196\n"
"v_add_f32 v20, v20, v19\n"
"v_cvt_pk_bf16_f32 v81, v18, v19\n"
"v_exp_f32 v17, v197\n"
"v_add_f32 v20, v20, v16\n"
"v_exp_f32 v18, v198\n"
"v_add_f32 v20, v20, v17\n"
"v_cvt_pk_bf16_f32 v82, v16, v17\n"
"v_exp_f32 v19, v199\n"
"v_add_f32 v20, v20, v18\n"
"v_exp_f32 v16, v200\n"
"v_add_f32 v20, v20, v19\n"
"v_cvt_pk_bf16_f32 v83, v18, v19\n"
"v_exp_f32 v17, v201\n"
"v_add_f32 v20, v20, v16\n"
"v_exp_f32 v18, v202\n"
"v_add_f32 v20, v20, v17\n"
"v_cvt_pk_bf16_f32 v84, v16, v17\n"
"v_exp_f32 v19, v203\n"
"v_add_f32 v20, v20, v18\n"
"v_exp_f32 v16, v204\n"
"v_add_f32 v20, v20, v19\n"
"v_cvt_pk_bf16_f32 v85, v18, v19\n"
"v_exp_f32 v17, v205\n"
"v_add_f32 v20, v20, v16\n"
"v_exp_f32 v18, v206\n"
"v_add_f32 v20, v20, v17\n"
"v_cvt_pk_bf16_f32 v86, v16, v17\n"
"v_exp_f32 v19, v207\n"
"v_add_f32 v20, v20, v18\n"
"v_exp_f32 v16, v208\n"
"v_add_f32 v20, v20, v19\n"
"v_cvt_pk_bf16_f32 v87, v18, v19\n"
"v_exp_f32 v17, v209\n"
"v_exp_f32 v18, v210\n"
"v_add_f32 v21, v16, v17\n"
"v_cvt_pk_bf16_f32 v88, v16, v17\n"
"v_exp_f32 v19, v211\n"
"v_add_f32 v21, v21, v18\n"
"v_exp_f32 v16, v212\n"
"v_add_f32 v21, v21, v19\n"
"v_cvt_pk_bf16_f32 v89, v18, v19\n"
"v_exp_f32 v17, v213\n"
"v_add_f32 v21, v21, v16\n"
"v_exp_f32 v18, v214\n"
"v_add_f32 v21, v21, v17\n"
"v_cvt_pk_bf16_f32 v90, v16, v17\n"
"v_exp_f32 v19, v215\n"
"v_add_f32 v21, v21, v18\n"
"v_exp_f32 v16, v216\n"
"v_add_f32 v21, v21, v19\n"
"v_cvt_pk_bf16_f32 v91, v18, v19\n"
"v_exp_f32 v17, v217\n"
"v_add_f32 v21, v21, v16\n"
"v_exp_f32 v18, v218\n"
"v_add_f32 v21, v21, v17\n"
"v_cvt_pk_bf16_f32 v92, v16, v17\n"
"v_exp_f32 v19, v219\n"
"v_add_f32 v21, v21, v18\n"
"v_exp_f32 v16, v220\n"
"v_add_f32 v21, v21, v19\n"
"v_cvt_pk_bf16_f32 v93, v18, v19\n"
"v_exp_f32 v17, v221\n"
"v_add_f32 v21, v21, v16\n"
"v_exp_f32 v18, v222\n"
"v_add_f32 v21, v21, v17\n"
"v_cvt_pk_bf16_f32 v94, v16, v17\n"
"v_exp_f32 v19, v223\n"
"v_add_f32 v21, v21, v18\n"
"v_add_f32 v21, v21, v19\n"
"v_cvt_pk_bf16_f32 v95, v18, v19\n"
"v_exp_f32 v16, v224\n"
"v_exp_f32 v17, v225\n"
"v_add_f32 v20, v20, v16\n"
"v_exp_f32 v18, v226\n"
"v_add_f32 v20, v20, v17\n"
"v_cvt_pk_bf16_f32 v96, v16, v17\n"
"v_exp_f32 v19, v227\n"
"v_add_f32 v20, v20, v18\n"
"v_exp_f32 v16, v228\n"
"v_add_f32 v20, v20, v19\n"
"v_cvt_pk_bf16_f32 v97, v18, v19\n"
"v_exp_f32 v17, v229\n"
"v_add_f32 v20, v20, v16\n"
"v_exp_f32 v18, v230\n"
"v_add_f32 v20, v20, v17\n"
"v_cvt_pk_bf16_f32 v98, v16, v17\n"
"v_exp_f32 v19, v231\n"
"v_add_f32 v20, v20, v18\n"
"v_exp_f32 v16, v232\n"
"v_add_f32 v20, v20, v19\n"
"v_cvt_pk_bf16_f32 v99, v18, v19\n"
"v_exp_f32 v17, v233\n"
"v_add_f32 v20, v20, v16\n"
"v_exp_f32 v18, v234\n"
"v_add_f32 v20, v20, v17\n"
"v_cvt_pk_bf16_f32 v100, v16, v17\n"
"v_exp_f32 v19, v235\n"
"v_add_f32 v20, v20, v18\n"
"v_exp_f32 v16, v236\n"
"v_add_f32 v20, v20, v19\n"
"v_cvt_pk_bf16_f32 v101, v18, v19\n"
"v_exp_f32 v17, v237\n"
"v_add_f32 v20, v20, v16\n"
"v_exp_f32 v18, v238\n"
"v_add_f32 v20, v20, v17\n"
"v_cvt_pk_bf16_f32 v102, v16, v17\n"
"v_exp_f32 v19, v239\n"
"v_add_f32 v20, v20, v18\n"
"v_exp_f32 v16, v240\n"
"v_add_f32 v20, v20, v19\n"
"v_cvt_pk_bf16_f32 v103, v18, v19\n"
"v_exp_f32 v17, v241\n"
"v_add_f32 v21, v21, v16\n"
"v_exp_f32 v18, v242\n"
"v_add_f32 v21, v21, v17\n"
"v_cvt_pk_bf16_f32 v104, v16, v17\n"
"v_exp_f32 v19, v243\n"
"v_add_f32 v21, v21, v18\n"
"v_exp_f32 v16, v244\n"
"v_add_f32 v21, v21, v19\n"
"v_cvt_pk_bf16_f32 v105, v18, v19\n"
"v_exp_f32 v17, v245\n"
"v_add_f32 v21, v21, v16\n"
"v_exp_f32 v18, v246\n"
"v_add_f32 v21, v21, v17\n"
"v_cvt_pk_bf16_f32 v106, v16, v17\n"
"v_exp_f32 v19, v247\n"
"v_add_f32 v21, v21, v18\n"
"v_exp_f32 v16, v248\n"
"v_add_f32 v21, v21, v19\n"
"v_cvt_pk_bf16_f32 v107, v18, v19\n"
"v_exp_f32 v17, v249\n"
"v_add_f32 v21, v21, v16\n"
"v_exp_f32 v18, v250\n"
"v_add_f32 v21, v21, v17\n"
"v_cvt_pk_bf16_f32 v108, v16, v17\n"
"v_exp_f32 v19, v251\n"
"v_add_f32 v21, v21, v18\n"
"v_exp_f32 v16, v252\n"
"v_add_f32 v21, v21, v19\n"
"v_cvt_pk_bf16_f32 v109, v18, v19\n"
"v_exp_f32 v17, v253\n"
"v_add_f32 v21, v21, v16\n"
"v_exp_f32 v18, v254\n"
"v_add_f32 v21, v21, v17\n"
"v_cvt_pk_bf16_f32 v110, v16, v17\n"
"v_exp_f32 v19, v255\n"
"v_add_f32 v21, v21, v18\n"
"v_add_f32 v21, v21, v19\n"
"v_cvt_pk_bf16_f32 v111, v18, v19\n"
"s_nop 7\n"
"s_cmp_eq_u32 s42, 1\n"
"s_cbranch_scc1 L_back1_%=\n"
"s_cmp_eq_u32 s42, 3\n"
"s_cbranch_scc1 L_back3_%=\n"
"s_cmp_eq_u32 s42, 5\n"
"s_cbranch_scc1 L_back5_%=\n"
"s_cmp_eq_u32 s42, 7\n"
"s_cbranch_scc1 L_back7_%=\n"
"L_mgrb%=:\n"
"s_cmp_eq_u32 s13, 0\n"
"s_cbranch_scc1 L_mgdb%=\n"
"s_sub_u32 s13, s13, 1\n"
"s_mov_b32 s12, 0x22000\n"
"buffer_load_dwordx4 v[32:35], v11, s[28:31], s12 offen sc1\n"
"s_mov_b32 s12, 0x23000\n"
"buffer_load_dwordx4 v[36:39], v11, s[28:31], s12 offen sc1\n"
"s_mov_b32 s12, 0x28000\n"
"buffer_load_dwordx4 v[40:43], v11, s[28:31], s12 offen sc1\n"
"s_mov_b32 s12, 0x29000\n"
"buffer_load_dwordx4 v[44:47], v11, s[28:31], s12 offen sc1\n"
"s_waitcnt vmcnt(0)\n"
"v_xor_b32 v4, s10, v33\n"
"v_bitop3_b32 v4, v35, s10, v4 bitop3:0xbe\n"
"v_bitop3_b32 v4, v37, s10, v4 bitop3:0xbe\n"
"v_bitop3_b32 v4, v39, s10, v4 bitop3:0xbe\n"
"v_bitop3_b32 v4, v41, s10, v4 bitop3:0xbe\n"
"v_bitop3_b32 v4, v43, s10, v4 bitop3:0xbe\n"
"v_bitop3_b32 v4, v45, s10, v4 bitop3:0xbe\n"
"v_bitop3_b32 v4, v47, s10, v4 bitop3:0xbe\n"
"v_cmp_ne_u32 vcc, 0, v4\n"
"s_nop 0\n"
"s_cbranch_vccnz L_mgrb%=\n"
"s_branch L_mgdb%=\n"
"L_mgrc%=:\n"
"s_cmp_eq_u32 s13, 0\n"
"s_cbranch_scc1 L_mgdc%=\n"
"s_sub_u32 s13, s13, 1\n"
"s_mov_b32 s12, 0x24000\n"
"buffer_load_dwordx4 v[64:67], v11, s[28:31], s12 offen sc1\n"
"s_mov_b32 s12, 0x25000\n"
"buffer_load_dwordx4 v[68:71], v11, s[28:31], s12 offen sc1\n"
"s_mov_b32 s12, 0x2a000\n"
"buffer_load_dwordx4 v[72:75], v11, s[28:31], s12 offen sc1\n"
"s_mov_b32 s12, 0x2b000\n"
"buffer_load_dwordx4 v[76:79], v11, s[28:31], s12 offen sc1\n"
"s_waitcnt vmcnt(0)\n"
"v_xor_b32 v4, s10, v65\n"
"v_bitop3_b32 v4, v67, s10, v4 bitop3:0xbe\n"
"v_bitop3_b32 v4, v69, s10, v4 bitop3:0xbe\n"
"v_bitop3_b32 v4, v71, s10, v4 bitop3:0xbe\n"
"v_bitop3_b32 v4, v73, s10, v4 bitop3:0xbe\n"
"v_bitop3_b32 v4, v75, s10, v4 bitop3:0xbe\n"
"v_bitop3_b32 v4, v77, s10, v4 bitop3:0xbe\n"
"v_bitop3_b32 v4, v79, s10, v4 bitop3:0xbe\n"
"v_cmp_ne_u32 vcc, 0, v4\n"
"s_nop 0\n"
"s_cbranch_vccnz L_mgrc%=\n"
"s_branch L_mgdc%=\n"
"L_x1p0r%=:\n"
"s_cmp_eq_u32 s14, 0\n"
"s_cbranch_scc1 L_x1p0d%=\n"
"s_sub_u32 s14, s14, 1\n"
"s_sleep 2\n"
"buffer_load_dword v100, v97, s[28:31], s78 offen sc1\n"
"s_waitcnt vmcnt(0)\n"
"s_branch L_x1p0c%=\n"
"L_x1p1r%=:\n"
"s_cmp_eq_u32 s14, 0\n"
"s_cbranch_scc1 L_x1p1d%=\n"
"s_sub_u32 s14, s14, 1\n"
"s_sleep 2\n"
"buffer_load_dword v101, v97, s[28:31], s78 offen offset:128 sc1\n"
"s_waitcnt vmcnt(0)\n"
"s_branch L_x1p1c%=\n"
"L_x1v2r%=:\n"
"s_cmp_eq_u32 s14, 0\n"
"s_cbranch_scc1 L_x1v2d%=\n"
"s_sub_u32 s14, s14, 1\n"
"s_sleep 2\n"
"v_max3_u32 v4, v240, v241, v242\n"
"v_max_u32 v4, v4, v243\n"
"v_cmp_eq_u32 vcc, -1, v4\n"
"s_nop 1\n"
"s_add_u32 s77, s88, 0x8000\n"
"s_and_b64 exec, vcc, vcc\n"
"s_cbranch_scc0 L_x1v2s0%=\n"
"buffer_load_dwordx4 v[240:243], v124, s[28:31], s77 offen sc1\n"
"L_x1v2s0%=:\n"
"s_mov_b64 exec, -1\n"
"v_max3_u32 v4, v244, v245, v246\n"
"v_max_u32 v4, v4, v247\n"
"v_cmp_eq_u32 vcc, -1, v4\n"
"s_nop 1\n"
"s_add_u32 s77, s88, 0x9000\n"
"s_and_b64 exec, vcc, vcc\n"
"s_cbranch_scc0 L_x1v2s1%=\n"
"buffer_load_dwordx4 v[244:247], v125, s[28:31], s77 offen sc1\n"
"L_x1v2s1%=:\n"
"s_mov_b64 exec, -1\n"
"v_max3_u32 v4, v248, v249, v250\n"
"v_max_u32 v4, v4, v251\n"
"v_cmp_eq_u32 vcc, -1, v4\n"
"s_nop 1\n"
"s_add_u32 s77, s88, 0xa000\n"
"s_and_b64 exec, vcc, vcc\n"
"s_cbranch_scc0 L_x1v2s2%=\n"
"buffer_load_dwordx4 v[248:251], v126, s[28:31], s77 offen sc1\n"
"L_x1v2s2%=:\n"
"s_mov_b64 exec, -1\n"
"v_max3_u32 v4, v252, v253, v254\n"
"v_max_u32 v4, v4, v255\n"
"v_cmp_eq_u32 vcc, -1, v4\n"
"s_nop 1\n"
"s_add_u32 s77, s88, 0xb000\n"
"s_and_b64 exec, vcc, vcc\n"
"s_cbranch_scc0 L_x1v2s3%=\n"
"buffer_load_dwordx4 v[252:255], v127, s[28:31], s77 offen sc1\n"
"L_x1v2s3%=:\n"
"s_mov_b64 exec, -1\n"
"s_waitcnt vmcnt(0)\n"
"s_branch L_x1v2c%=\n"
"L_x1v3r%=:\n"
"s_cmp_eq_u32 s14, 0\n"
"s_cbranch_scc1 L_x1v3d%=\n"
"s_sub_u32 s14, s14, 1\n"
"s_sleep 2\n"
"v_max3_u32 v4, v108, v109, v110\n"
"v_max_u32 v4, v4, v111\n"
"v_cmp_eq_u32 vcc, -1, v4\n"
"s_nop 1\n"
"s_add_u32 s77, s88, 0xc000\n"
"s_and_b64 exec, vcc, vcc\n"
"s_cbranch_scc0 L_x1v3s0%=\n"
"buffer_load_dwordx4 v[108:111], v124, s[28:31], s77 offen sc1\n"
"L_x1v3s0%=:\n"
"s_mov_b64 exec, -1\n"
"v_max3_u32 v4, v112, v113, v114\n"
"v_max_u32 v4, v4, v115\n"
"v_cmp_eq_u32 vcc, -1, v4\n"
"s_nop 1\n"
"s_add_u32 s77, s88, 0xd000\n"
"s_and_b64 exec, vcc, vcc\n"
"s_cbranch_scc0 L_x1v3s1%=\n"
"buffer_load_dwordx4 v[112:115], v125, s[28:31], s77 offen sc1\n"
"L_x1v3s1%=:\n"
"s_mov_b64 exec, -1\n"
"v_max3_u32 v4, v116, v117, v118\n"
"v_max_u32 v4, v4, v119\n"
"v_cmp_eq_u32 vcc, -1, v4\n"
"s_nop 1\n"
"s_add_u32 s77, s88, 0xe000\n"
"s_and_b64 exec, vcc, vcc\n"
"s_cbranch_scc0 L_x1v3s2%=\n"
"buffer_load_dwordx4 v[116:119], v126, s[28:31], s77 offen sc1\n"
"L_x1v3s2%=:\n"
"s_mov_b64 exec, -1\n"
"v_max3_u32 v4, v120, v121, v122\n"
"v_max_u32 v4, v4, v123\n"
"v_cmp_eq_u32 vcc, -1, v4\n"
"s_nop 1\n"
"s_add_u32 s77, s88, 0xf000\n"
"s_and_b64 exec, vcc, vcc\n"
"s_cbranch_scc0 L_x1v3s3%=\n"
"buffer_load_dwordx4 v[120:123], v127, s[28:31], s77 offen sc1\n"
"L_x1v3s3%=:\n"
"s_mov_b64 exec, -1\n"
"s_waitcnt vmcnt(0)\n"
"s_branch L_x1v3c%=\n"
"L_eu%=:\n"
"v_lshlrev_b32 v4, 9, v6\n"
"s_mov_b32 s12, 0x10000\n"
"s_mov_b32 s11, 0x40000\n"
"L_tok%=:\n"
"buffer_load_dword v16, v4, s[28:31], s11 offen sc1\n"
"buffer_load_dword v17, v4, s[28:31], s11 offen offset:128 sc1\n"
"buffer_load_dword v18, v4, s[28:31], s11 offen offset:256 sc1\n"
"buffer_load_dword v19, v4, s[28:31], s11 offen offset:384 sc1\n"
"s_waitcnt vmcnt(0)\n"
"v_xor_b32 v16, s10, v16\n"
"v_bitop3_b32 v16, v17, s10, v16 bitop3:0xbe\n"
"v_bitop3_b32 v16, v18, s10, v16 bitop3:0xbe\n"
"v_bitop3_b32 v16, v19, s10, v16 bitop3:0xbe\n"
"v_cmp_ne_u32 vcc, 0, v16\n"
"s_sub_u32 s12, s12, 1\n"
"s_cbranch_scc1 L_eud%=\n"
"s_cbranch_vccnz L_tok%=\n"
"buffer_store_dword v104, off, s[28:31], 0 sc1\n"
"s_branch L_eud%=\n"
"L_h0r%=:\n"
"s_cmp_eq_u32 s14, 0\n"
"s_cbranch_scc1 L_h0d%=\n"
"s_sub_u32 s14, s14, 1\n"
"s_sleep 2\n"
"v_max3_u32 v4, v192, v193, v194\n"
"v_max_u32 v4, v4, v195\n"
"v_cmp_eq_u32 vcc, -1, v4\n"
"s_nop 1\n"
"s_add_u32 s77, s89, 0\n"
"s_and_b64 exec, vcc, vcc\n"
"s_cbranch_scc0 L_h0s0%=\n"
"buffer_load_dwordx4 v[192:195], v7, s[28:31], s77 offen sc1\n"
"L_h0s0%=:\n"
"s_mov_b64 exec, -1\n"
"v_max3_u32 v4, v196, v197, v198\n"
"v_max_u32 v4, v4, v199\n"
"v_cmp_eq_u32 vcc, -1, v4\n"
"s_nop 1\n"
"s_add_u32 s77, s89, 0x4000\n"
"s_and_b64 exec, vcc, vcc\n"
"s_cbranch_scc0 L_h0s1%=\n"
"buffer_load_dwordx4 v[196:199], v7, s[28:31], s77 offen sc1\n"
"L_h0s1%=:\n"
"s_mov_b64 exec, -1\n"
"v_max3_u32 v4, v200, v201, v202\n"
"v_max_u32 v4, v4, v203\n"
"v_cmp_eq_u32 vcc, -1, v4\n"
"s_nop 1\n"
"s_add_u32 s77, s89, 0x8000\n"
"s_and_b64 exec, vcc, vcc\n"
"s_cbranch_scc0 L_h0s2%=\n"
"buffer_load_dwordx4 v[200:203], v7, s[28:31], s77 offen sc1\n"
"L_h0s2%=:\n"
"s_mov_b64 exec, -1\n"
"v_max3_u32 v4, v204, v205, v206\n"
"v_max_u32 v4, v4, v207\n"
"v_cmp_eq_u32 vcc, -1, v4\n"
"s_nop 1\n"
"s_add_u32 s77, s89, 0xc000\n"
"s_and_b64 exec, vcc, vcc\n"
"s_cbranch_scc0 L_h0s3%=\n"
"buffer_load_dwordx4 v[204:207], v7, s[28:31], s77 offen sc1\n"
"L_h0s3%=:\n"
"s_mov_b64 exec, -1\n"
"s_waitcnt vmcnt(0)\n"
"s_branch L_h0c%=\n"
"L_hq1r%=:\n"
"s_cmp_eq_u32 s14, 0\n"
"s_cbranch_scc1 L_hq1d%=\n"
"s_sub_u32 s14, s14, 1\n"
"s_sleep 2\n"
"s_add_u32 s77, s90, 0\n"
"buffer_load_dwordx4 a[64:67], v7, s[28:31], s77 offen sc1\n"
"s_add_u32 s77, s90, 0x4000\n"
"buffer_load_dwordx4 a[68:71], v7, s[28:31], s77 offen sc1\n"
"s_add_u32 s77, s90, 0x8000\n"
"buffer_load_dwordx4 a[72:75], v7, s[28:31], s77 offen sc1\n"
"s_add_u32 s77, s90, 0xc000\n"
"buffer_load_dwordx4 a[76:79], v7, s[28:31], s77 offen sc1\n"
"s_waitcnt vmcnt(0)\n"
"v_accvgpr_read_b32 v4, a64\n"
"v_accvgpr_read_b32 v5, a65\n"
"v_max_u32 v6, v4, v5\n"
"v_accvgpr_read_b32 v4, a66\n"
"v_accvgpr_read_b32 v5, a67\n"
"v_max3_u32 v6, v6, v4, v5\n"
"v_accvgpr_read_b32 v4, a68\n"
"v_accvgpr_read_b32 v5, a69\n"
"v_max3_u32 v6, v6, v4, v5\n"
"v_accvgpr_read_b32 v4, a70\n"
"v_accvgpr_read_b32 v5, a71\n"
"v_max3_u32 v6, v6, v4, v5\n"
"v_accvgpr_read_b32 v4, a72\n"
"v_accvgpr_read_b32 v5, a73\n"
"v_max3_u32 v6, v6, v4, v5\n"
"v_accvgpr_read_b32 v4, a74\n"
"v_accvgpr_read_b32 v5, a75\n"
"v_max3_u32 v6, v6, v4, v5\n"
"v_accvgpr_read_b32 v4, a76\n"
"v_accvgpr_read_b32 v5, a77\n"
"v_max3_u32 v6, v6, v4, v5\n"
"v_accvgpr_read_b32 v4, a78\n"
"v_accvgpr_read_b32 v5, a79\n"
"v_max3_u32 v6, v6, v4, v5\n"
"v_cmp_eq_u32 vcc, -1, v6\n"
"s_nop 0\n"
"s_cbranch_vccnz L_hq1r%=\n"
"s_branch L_hq1d%=\n"
"L_hq2r%=:\n"
"s_cmp_eq_u32 s14, 0\n"
"s_cbranch_scc1 L_hq2d%=\n"
"s_sub_u32 s14, s14, 1\n"
"s_sleep 2\n"
"s_add_u32 s77, s91, 0\n"
"buffer_load_dwordx4 a[80:83], v7, s[28:31], s77 offen sc1\n"
"s_add_u32 s77, s91, 0x4000\n"
"buffer_load_dwordx4 a[84:87], v7, s[28:31], s77 offen sc1\n"
"s_add_u32 s77, s91, 0x8000\n"
"buffer_load_dwordx4 a[88:91], v7, s[28:31], s77 offen sc1\n"
"s_add_u32 s77, s91, 0xc000\n"
"buffer_load_dwordx4 a[92:95], v7, s[28:31], s77 offen sc1\n"
"s_waitcnt vmcnt(0)\n"
"v_accvgpr_read_b32 v4, a80\n"
"v_accvgpr_read_b32 v5, a81\n"
"v_max_u32 v6, v4, v5\n"
"v_accvgpr_read_b32 v4, a82\n"
"v_accvgpr_read_b32 v5, a83\n"
"v_max3_u32 v6, v6, v4, v5\n"
"v_accvgpr_read_b32 v4, a84\n"
"v_accvgpr_read_b32 v5, a85\n"
"v_max3_u32 v6, v6, v4, v5\n"
"v_accvgpr_read_b32 v4, a86\n"
"v_accvgpr_read_b32 v5, a87\n"
"v_max3_u32 v6, v6, v4, v5\n"
"v_accvgpr_read_b32 v4, a88\n"
"v_accvgpr_read_b32 v5, a89\n"
"v_max3_u32 v6, v6, v4, v5\n"
"v_accvgpr_read_b32 v4, a90\n"
"v_accvgpr_read_b32 v5, a91\n"
"v_max3_u32 v6, v6, v4, v5\n"
"v_accvgpr_read_b32 v4, a92\n"
"v_accvgpr_read_b32 v5, a93\n"
"v_max3_u32 v6, v6, v4, v5\n"
"v_accvgpr_read_b32 v4, a94\n"
"v_accvgpr_read_b32 v5, a95\n"
"v_max3_u32 v6, v6, v4, v5\n"
"v_cmp_eq_u32 vcc, -1, v6\n"
"s_nop 0\n"
"s_cbranch_vccnz L_hq2r%=\n"
"s_branch L_hq2d%=\n"
"L_hq3r%=:\n"
"s_cmp_eq_u32 s14, 0\n"
"s_cbranch_scc1 L_hq3d%=\n"
"s_sub_u32 s14, s14, 1\n"
"s_sleep 2\n"
"s_add_u32 s77, s92, 0\n"
"buffer_load_dwordx4 a[96:99], v7, s[28:31], s77 offen sc1\n"
"s_add_u32 s77, s92, 0x4000\n"
"buffer_load_dwordx4 a[100:103], v7, s[28:31], s77 offen sc1\n"
"s_add_u32 s77, s92, 0x8000\n"
"buffer_load_dwordx4 a[104:107], v7, s[28:31], s77 offen sc1\n"
"s_add_u32 s77, s92, 0xc000\n"
"buffer_load_dwordx4 a[108:111], v7, s[28:31], s77 offen sc1\n"
"s_waitcnt vmcnt(0)\n"
"v_accvgpr_read_b32 v4, a96\n"
"v_accvgpr_read_b32 v5, a97\n"
"v_max_u32 v6, v4, v5\n"
"v_accvgpr_read_b32 v4, a98\n"
"v_accvgpr_read_b32 v5, a99\n"
"v_max3_u32 v6, v6, v4, v5\n"
"v_accvgpr_read_b32 v4, a100\n"
"v_accvgpr_read_b32 v5, a101\n"
"v_max3_u32 v6, v6, v4, v5\n"
"v_accvgpr_read_b32 v4, a102\n"
"v_accvgpr_read_b32 v5, a103\n"
"v_max3_u32 v6, v6, v4, v5\n"
"v_accvgpr_read_b32 v4, a104\n"
"v_accvgpr_read_b32 v5, a105\n"
"v_max3_u32 v6, v6, v4, v5\n"
"v_accvgpr_read_b32 v4, a106\n"
"v_accvgpr_read_b32 v5, a107\n"
"v_max3_u32 v6, v6, v4, v5\n"
"v_accvgpr_read_b32 v4, a108\n"
"v_accvgpr_read_b32 v5, a109\n"
"v_max3_u32 v6, v6, v4, v5\n"
"v_accvgpr_read_b32 v4, a110\n"
"v_accvgpr_read_b32 v5, a111\n"
"v_max3_u32 v6, v6, v4, v5\n"
"v_cmp_eq_u32 vcc, -1, v6\n"
"s_nop 0\n"
"s_cbranch_vccnz L_hq3r%=\n"
"s_branch L_hq3d%=\n"
"L_x2p0r%=:\n"
"s_cmp_eq_u32 s14, 0\n"
"s_cbranch_scc1 L_x2p0d%=\n"
"s_sub_u32 s14, s14, 1\n"
"s_sleep 2\n"
"buffer_load_dword v100, v97, s[28:31], s78 offen sc1\n"
"s_waitcnt vmcnt(0)\n"
"s_branch L_x2p0c%=\n"
"L_sk0r%=:\n"
"s_cmp_eq_u32 s14, 0\n"
"s_cbranch_scc1 L_sk0d%=\n"
"s_sub_u32 s14, s14, 1\n"
"s_sleep 2\n"
"buffer_load_dword v100, v97, s[28:31], s78 offen sc1\n"
"s_waitcnt vmcnt(0)\n"
"s_branch L_sk0c%=\n"
"L_skip%=:\n"
"v_accvgpr_read_b32 v108, a18\n"
"v_accvgpr_read_b32 v109, a19\n"
"v_accvgpr_read_b32 v110, a20\n"
"v_accvgpr_read_b32 v111, a21\n"
"v_accvgpr_read_b32 v124, a22\n"
"v_accvgpr_read_b32 v125, a23\n"
"v_accvgpr_read_b32 v126, a24\n"
"v_accvgpr_read_b32 v127, a25\n"
"v_accvgpr_read_b32 v98, a26\n"
"v_accvgpr_read_b32 v96, a27\n"
"v_accvgpr_read_b32 v97, a28\n"
"v_lshrrev_b32 v4, 5, v6\n"
"v_lshl_or_b32 v127, v4, 30, v127\n"
"s_cmp_eq_u32 s8, 7\n"
"s_cselect_b32 s13, 0, -1\n"
"v_and_b32 v98, s13, v98\n"
"v_lshlrev_b32 v99, 16, v98\n"
"v_mov_b32 v104, s10\n"
"v_mov_b32 v105, s10\n"
"v_mov_b32 v106, s10\n"
"v_mov_b32 v107, s10\n"
"s_add_u32 s69, s69, 0x100000\n"
"s_add_u32 s78, s78, 0x100000\n"
"s_lshl_b32 s88, s9, 19\n"
"s_lshl_b32 s15, s8, 16\n"
"s_add_u32 s88, s88, s15\n"
"s_lshl_b32 s15, s7, 9\n"
"s_add_u32 s88, s88, s15\n"
"s_add_u32 s88, s88, 0x1800000\n"
"s_mov_b32 s14, 0x10000\n"
"v_mov_b32 v108, 0\n"
"v_mov_b32 v109, 0\n"
"v_mov_b32 v110, 0\n"
"v_mov_b32 v111, 0\n"
"v_mov_b32 v112, 0\n"
"v_mov_b32 v113, 0\n"
"v_mov_b32 v114, 0\n"
"v_mov_b32 v115, 0\n"
"v_mov_b32 v116, 0\n"
"v_mov_b32 v117, 0\n"
"v_mov_b32 v118, 0\n"
"v_mov_b32 v119, 0\n"
"v_mov_b32 v120, 0\n"
"v_mov_b32 v121, 0\n"
"v_mov_b32 v122, 0\n"
"v_mov_b32 v123, 0\n"
"s_sleep 127\n"
"s_sleep 127\n"
"s_sleep 40\n"
"buffer_load_dword v100, v97, s[28:31], s78 offen sc1\n"
"s_waitcnt vmcnt(0)\n"
"L_sk0c%=:\n"
"v_cmp_ne_u32 vcc, s10, v100\n"
"s_nop 0\n"
"s_and_b32 s11, vcc_lo, 127\n"
"s_cbranch_scc1 L_sk0r%=\n"
"L_sk0d%=:\n"
"s_add_u32 s77, s88, 0\n"
"buffer_load_dwordx4 a[192:195], v124, s[28:31], s77 offen sc1\n"
"s_add_u32 s77, s88, 0x1000\n"
"buffer_load_dwordx4 a[196:199], v125, s[28:31], s77 offen sc1\n"
"s_add_u32 s77, s88, 0x2000\n"
"buffer_load_dwordx4 a[200:203], v126, s[28:31], s77 offen sc1\n"
"s_add_u32 s77, s88, 0x3000\n"
"buffer_load_dwordx4 a[204:207], v127, s[28:31], s77 offen sc1\n"
"s_waitcnt vmcnt(0)\n"
"s_branch L_final%=\n"
"L_x2p1r%=:\n"
"s_cmp_eq_u32 s14, 0\n"
"s_cbranch_scc1 L_x2p1d%=\n"
"s_sub_u32 s14, s14, 1\n"
"s_sleep 2\n"
"buffer_load_dword v101, v97, s[28:31], s78 offen offset:128 sc1\n"
"s_waitcnt vmcnt(0)\n"
"s_branch L_x2p1c%=\n"
"L_x2p2r%=:\n"
"s_cmp_eq_u32 s14, 0\n"
"s_cbranch_scc1 L_x2p2d%=\n"
"s_sub_u32 s14, s14, 1\n"
"s_sleep 2\n"
"buffer_load_dword v102, v97, s[28:31], s78 offen offset:256 sc1\n"
"s_waitcnt vmcnt(0)\n"
"s_branch L_x2p2c%=\n"
"L_x2v3r%=:\n"
"s_cmp_eq_u32 s14, 0\n"
"s_cbranch_scc1 L_x2v3d%=\n"
"s_sub_u32 s14, s14, 1\n"
"s_sleep 2\n"
"v_max3_u32 v4, v108, v109, v110\n"
"v_max_u32 v4, v4, v111\n"
"v_cmp_eq_u32 vcc, -1, v4\n"
"s_nop 1\n"
"s_add_u32 s77, s88, 0xc000\n"
"s_and_b64 exec, vcc, vcc\n"
"s_cbranch_scc0 L_x2v3s0%=\n"
"buffer_load_dwordx4 v[108:111], v124, s[28:31], s77 offen sc1\n"
"L_x2v3s0%=:\n"
"s_mov_b64 exec, -1\n"
"v_max3_u32 v4, v112, v113, v114\n"
"v_max_u32 v4, v4, v115\n"
"v_cmp_eq_u32 vcc, -1, v4\n"
"s_nop 1\n"
"s_add_u32 s77, s88, 0xd000\n"
"s_and_b64 exec, vcc, vcc\n"
"s_cbranch_scc0 L_x2v3s1%=\n"
"buffer_load_dwordx4 v[112:115], v125, s[28:31], s77 offen sc1\n"
"L_x2v3s1%=:\n"
"s_mov_b64 exec, -1\n"
"v_max3_u32 v4, v116, v117, v118\n"
"v_max_u32 v4, v4, v119\n"
"v_cmp_eq_u32 vcc, -1, v4\n"
"s_nop 1\n"
"s_add_u32 s77, s88, 0xe000\n"
"s_and_b64 exec, vcc, vcc\n"
"s_cbranch_scc0 L_x2v3s2%=\n"
"buffer_load_dwordx4 v[116:119], v126, s[28:31], s77 offen sc1\n"
"L_x2v3s2%=:\n"
"s_mov_b64 exec, -1\n"
"v_max3_u32 v4, v120, v121, v122\n"
"v_max_u32 v4, v4, v123\n"
"v_cmp_eq_u32 vcc, -1, v4\n"
"s_nop 1\n"
"s_add_u32 s77, s88, 0xf000\n"
"s_and_b64 exec, vcc, vcc\n"
"s_cbranch_scc0 L_x2v3s3%=\n"
"buffer_load_dwordx4 v[120:123], v127, s[28:31], s77 offen sc1\n"
"L_x2v3s3%=:\n"
"s_mov_b64 exec, -1\n"
"s_waitcnt vmcnt(0)\n"
"s_branch L_x2v3c%=\n"
"L_end%=:\n"
  :: "{v0}"(threadIdx.x), "{s[4:5]}"(__builtin_amdgcn_kernarg_segment_ptr()), "{s6}"(blockIdx.x) : "v1","v2","v3","v4","v5","v6","v7","v8","v9","v10","v11","v12","v13","v14","v15","v16","v17","v18","v19","v20","v21","v22","v23","v24","v25","v26","v27","v28","v29","v30","v31","v32","v33","v34","v35","v36","v37","v38","v39","v40","v41","v42","v43","v44","v45","v46","v47","v48","v49","v50","v51","v52","v53","v54","v55","v56","v57","v58","v59","v60","v61","v62","v63","v64","v65","v66","v67","v68","v69","v70","v71","v72","v73","v74","v75","v76","v77","v78","v79","v80","v81","v82","v83","v84","v85","v86","v87","v88","v89","v90","v91","v92","v93","v94","v95","v96","v97","v98","v99","v100","v101","v102","v103","v104","v105","v106","v107","v108","v109","v110","v111","v112","v113","v114","v115","v116","v117","v118","v119","v120","v121","v122","v123","v124","v125","v126","v127","v128","v129","v130","v131","v132","v133","v134","v135","v136","v137","v138","v139","v140","v141","v142","v143","v144","v145","v146","v147","v148","v149","v150","v151","v152","v153","v154","v155","v156","v157","v158","v159","v160","v161","v162","v163","v164","v165","v166","v167","v168","v169","v170","v171","v172","v173","v174","v175","v176","v177","v178","v179","v180","v181","v182","v183","v184","v185","v186","v187","v188","v189","v190","v191","v192","v193","v194","v195","v196","v197","v198","v199","v200","v201","v202","v203","v204","v205","v206","v207","v208","v209","v210","v211","v212","v213","v214","v215","v216","v217","v218","v219","v220","v221","v222","v223","v224","v225","v226","v227","v228","v229","v230","v231","v232","v233","v234","v235","v236","v237","v238","v239","v240","v241","v242","v243","v244","v245","v246","v247","v248","v249","v250","v251","v252","v253","v254","v255","a0","a1","a2","a3","a4","a5","a6","a7","a8","a9","a10","a11","a12","a13","a14","a15","a16","a17","a18","a19","a20","a21","a22","a23","a24","a25","a26","a27","a28","a29","a30","a31","a32","a33","a34","a35","a36","a37","a38","a39","a40","a41","a42","a43","a44","a45","a46","a47","a48","a49","a50","a51","a52","a53","a54","a55","a56","a57","a58","a59","a60","a61","a62","a63","a64","a65","a66","a67","a68","a69","a70","a71","a72","a73","a74","a75","a76","a77","a78","a79","a80","a81","a82","a83","a84","a85","a86","a87","a88","a89","a90","a91","a92","a93","a94","a95","a96","a97","a98","a99","a100","a101","a102","a103","a104","a105","a106","a107","a108","a109","a110","a111","a112","a113","a114","a115","a116","a117","a118","a119","a120","a121","a122","a123","a124","a125","a126","a127","a128","a129","a130","a131","a132","a133","a134","a135","a136","a137","a138","a139","a140","a141","a142","a143","a144","a145","a146","a147","a148","a149","a150","a151","a152","a153","a154","a155","a156","a157","a158","a159","a160","a161","a162","a163","a164","a165","a166","a167","a168","a169","a170","a171","a172","a173","a174","a175","a176","a177","a178","a179","a180","a181","a182","a183","a184","a185","a186","a187","a188","a189","a190","a191","a192","a193","a194","a195","a196","a197","a198","a199","a200","a201","a202","a203","a204","a205","a206","a207","a208","a209","a210","a211","a212","a213","a214","a215","a216","a217","a218","a219","a220","a221","a222","a223","a224","a225","a226","a227","a228","a229","a230","a231","a232","a233","a234","a235","a236","a237","a238","a239","a240","a241","a242","a243","a244","a245","a246","a247","a248","a249","a250","a251","a252","a253","a254","a255","s7","s8","s9","s10","s11","s12","s13","s14","s15","s16","s17","s18","s19","s20","s21","s22","s23","s24","s25","s26","s27","s28","s29","s30","s31","s33","s34","s35","s36","s37","s38","s39","s40","s41","s42","s43","s44","s45","s46","s47","s48","s49","s50","s51","s52","s53","s54","s55","s56","s57","s58","s59","s60","s61","s62","s63","s64","s65","s66","s67","s68","s69","s70","s71","s72","s73","s74","s75","s76","s77","s78","s79","s80","s81","s82","s83","s84","s85","s86","s87","s88","s89","s90","s91","s92","s93","s94","s95","s96","s97","s98","s99","vcc","scc","memory");
}
extern "C" void kernel_launch(void* const* d_in, const int* in_sizes, int n_in, void* d_out, int out_size, void* d_ws, size_t ws_size, hipStream_t stream){
  (void)hipFuncSetAttribute((const void*)main_kernel, hipFuncAttributeMaxDynamicSharedMemorySize, 163840);
  main_kernel<<<256,256,163840,stream>>>(d_in[0],d_in[1],d_in[2],d_in[3],d_in[4],d_in[6],d_in[7],d_in[8],d_out,d_ws,d_in[9],d_in[10]);
}
```
